```python
import torch

def _softmax(x, axis=-1):
    return torch.softmax(x, dim=axis)
def _gelu(x):
    return torch.nn.functional.gelu(x, approximate="tanh")
def _silu(x):
    return torch.nn.functional.silu(x)
def _sigmoid(x):
    return torch.sigmoid(x)
def _layernorm(x, axis=-1, eps=1e-5):
    m = x.mean(dim=axis, keepdim=True)
    v = x.var(dim=axis, keepdim=True, unbiased=False)
    return (x - m) / torch.sqrt(v + eps)
def _scatter_add(tgt, idx, src, axis=0):
    return tgt.clone().index_add_(axis, idx, src)
def _segment_sum(src, seg, n):
    out = torch.zeros((n,) + src.shape[1:], dtype=src.dtype)
    return out.index_add_(0, seg, src)
def _pad(x, pw):
    flat = tuple(v for pair in reversed(pw) for v in pair)
    return torch.nn.functional.pad(x, flat)

kernel_name = 'concat_heads__mha_block_cached__gqa_block_cached__near_ridge'

def setup_inputs():

    _w = (lambda a: (lambda t: t.to(torch.bfloat16) if t.is_floating_point() else t)(torch.as_tensor(a)))
    def _A_setup():
        B, Sq, D = 8, 192, 640
        H, Sk, Dh = 32, 512, 128
        return {
            "x": _w((lambda *s: torch.randn(*s, dtype=torch.bfloat16) * 0.8)(B, Sq, D)),
            "k_cache": _w((lambda *s: torch.randn(*s, dtype=torch.bfloat16) * 0.8)(B, H, Sk, Dh)),
            "v_cache": _w((lambda *s: torch.randn(*s, dtype=torch.bfloat16) * 0.05)(B, H, Sk, Dh)),
            "wq": _w((lambda *s: torch.randn(*s, dtype=torch.bfloat16) * 0.8)(D, H * Dh) * (D ** -0.5)),
            "wo": _w((lambda *s: torch.randn(*s, dtype=torch.bfloat16) * 0.05)(H * Dh, D) * ((H * Dh) ** -0.5)),
        }
    _A_in = _A_setup()

    def _B_setup():
        B, Sq, D = 8, 192, 640
        Hq, Hkv, Sk, Dh = 32, 32, 512, 128
        assert Hq % Hkv == 0, (Hq, Hkv)
        return {
            "x": _w((lambda *s: torch.randn(*s, dtype=torch.bfloat16) * 0.8)(B, Sq, D)),
            "k_cache": _w((lambda *s: torch.randn(*s, dtype=torch.bfloat16) * 0.8)(B, Hkv, Sk, Dh)),
            "v_cache": _w((lambda *s: torch.randn(*s, dtype=torch.bfloat16) * 0.05)(B, Hkv, Sk, Dh)),
            "wq": _w((lambda *s: torch.randn(*s, dtype=torch.bfloat16) * 0.8)(D, Hq * Dh) * (D ** -0.5)),
            "wo": _w((lambda *s: torch.randn(*s, dtype=torch.bfloat16) * 0.05)(Hq * Dh, D) * ((Hq * Dh) ** -0.5)),
        }
    _B_in = _B_setup()

    return {
        "A_x": _A_in["x"],
        "A_k_cache": _A_in["k_cache"],
        "A_v_cache": _A_in["v_cache"],
        "A_wq": _A_in["wq"],
        "A_wo": _A_in["wo"],
        "B_x": _B_in["x"],
        "B_k_cache": _B_in["k_cache"],
        "B_v_cache": _B_in["v_cache"],
        "B_wq": _B_in["wq"],
        "B_wo": _B_in["wo"],
    }

def reference(A_x, A_k_cache, A_v_cache, A_wq, A_wo, B_x, B_k_cache, B_v_cache, B_wq, B_wo):
    def _A_ref(x, k_cache, v_cache, wq, wo):
        B, Sq, D = x.shape
        H, Dh = 32, 128
        q = (lambda x, *d: x.permute(*d))((x @ wq).reshape(B, Sq, H, Dh), 0, 2, 1, 3)
        scores = torch.einsum("bhqd,bhkd->bhqk", q, k_cache) * (Dh ** -0.5)
        p = _softmax(scores, axis=-1)
        o = torch.einsum("bhqk,bhkd->bhqd", p, v_cache)
        return (lambda x, *d: x.permute(*d))(o, 0, 2, 1, 3).reshape(B, Sq, H * Dh) @ wo

    def _B_ref(x, k_cache, v_cache, wq, wo):
        B, Sq, D = x.shape
        Hq, Dh = 32, 128
        Hkv = k_cache.shape[1]
        rep = Hq // Hkv
        q = (lambda x, *d: x.permute(*d))((x @ wq).reshape(B, Sq, Hq, Dh), 0, 2, 1, 3)
        k = (lambda x, n, axis: torch.repeat_interleave(x, n, dim=axis))(k_cache, rep, axis=1)
        v = (lambda x, n, axis: torch.repeat_interleave(x, n, dim=axis))(v_cache, rep, axis=1)
        scores = torch.einsum("bhqd,bhkd->bhqk", q, k) * (Dh ** -0.5)
        p = _softmax(scores, axis=-1)
        o = torch.einsum("bhqk,bhkd->bhqd", p, v)
        return (lambda x, *d: x.permute(*d))(o, 0, 2, 1, 3).reshape(B, Sq, Hq * Dh) @ wo

    a = _A_ref(A_x, A_k_cache, A_v_cache, A_wq, A_wo)
    b = _B_ref(B_x, B_k_cache, B_v_cache, B_wq, B_wo)
    return torch.concatenate([a, b], axis=-1)
```

```cpp
#include <hip/hip_runtime.h>
#include <hip/hip_bf16.h>
typedef __attribute__((ext_vector_type(8))) short bf16x8;
typedef __attribute__((ext_vector_type(16))) float f32x16;
typedef unsigned short u16;
#define DI __device__ __forceinline__
DI u16 f2bf(float x){ unsigned u=__float_as_uint(x); u+=0x7fffu+((u>>16)&1u); return (u16)(u>>16);}
DI int crow(int reg,int h){return (reg&3)+8*(reg>>2)+4*h;}

__global__ __launch_bounds__(256) void gemm_naive(const u16* A,int lda,const u16* B,int ldb,u16* C,int ldc,int M,int N,int K){
  int wave=(blockIdx.x*256+threadIdx.x)>>6, lane=threadIdx.x&63, r=lane&31,h=lane>>5;
  int tn=N/32; int tm_i=wave/tn, tn_i=wave%tn; if(tm_i*32>=M) return;
  f32x16 acc; for(int i=0;i<16;i++)acc[i]=0.f;
  const u16* ap=A+(size_t)(tm_i*32+r)*lda+8*h;
  const u16* bp=B+(size_t)(8*h)*ldb+tn_i*32+r;
  for(int k=0;k<K;k+=16){
    bf16x8 a=*(const bf16x8*)(ap+k);
    bf16x8 b; for(int j=0;j<8;j++) b[j]=bp[(size_t)(k+j)*ldb];
    acc=__builtin_amdgcn_mfma_f32_32x32x16_bf16(a,b,acc,0,0,0);
  }
  for(int i=0;i<16;i++) C[(size_t)(tm_i*32+crow(i,h))*ldc+tn_i*32+r]=f2bf(acc[i]);
}
__global__ __launch_bounds__(64) void attn_naive(const u16* Q,const u16* Kc,const u16* Vc,u16* O){
  int id=blockIdx.x; int qb=id%6, bh=id/6, hh=bh%32, b=bh/32;
  int lane=threadIdx.x,r=lane&31,h=lane>>5;
  const u16* q=Q+(size_t)(b*192+qb*32+r)*4096+hh*128+8*h;
  bf16x8 qf[8]; for(int s=0;s<8;s++) qf[s]=*(const bf16x8*)(q+16*s);
  const u16* kp=Kc+(size_t)bh*512*128; const u16* vp=Vc+(size_t)bh*512*128;
  f32x16 o[4]; for(int d=0;d<4;d++)for(int i=0;i<16;i++)o[d][i]=0.f;
  float m=-1e30f,l=0.f; const float c=0.12751743082f;
  for(int t=0;t<16;t++){
    f32x16 s; for(int i=0;i<16;i++)s[i]=0.f;
    for(int k=0;k<8;k++){ bf16x8 kf=*(const bf16x8*)(kp+(size_t)(t*32+r)*128+16*k+8*h);
      s=__builtin_amdgcn_mfma_f32_32x32x16_bf16(kf,qf[k],s,0,0,0);}
    float mx=-1e30f; for(int i=0;i<16;i++) mx=fmaxf(mx,s[i]);
    mx=fmaxf(mx,__shfl_xor(mx,32)); float mn=fmaxf(m,mx*c); float al=exp2f(m-mn); m=mn;
    float rs=0; float p[16]; for(int i=0;i<16;i++){p[i]=exp2f(s[i]*c-mn); rs+=p[i];}
    rs+=__shfl_xor(rs,32); l=l*al+rs;
    for(int d=0;d<4;d++)for(int i=0;i<16;i++)o[d][i]*=al;
    for(int ks=0;ks<2;ks++){
      bf16x8 pf; for(int j=0;j<8;j++) pf[j]=f2bf(p[8*ks+j]);
      for(int d=0;d<4;d++){ bf16x8 vf; for(int j=0;j<8;j++) vf[j]=vp[(size_t)(t*32+16*ks+8*(j>>2)+4*h+(j&3))*128+d*32+r];
        o[d]=__builtin_amdgcn_mfma_f32_32x32x16_bf16(vf,pf,o[d],0,0,0);}
    }
  }
  float il=1.f/l;
  u16* op=O+(size_t)(b*192+qb*32+r)*4096+hh*128;
  for(int d=0;d<4;d++)for(int i=0;i<16;i++) op[d*32+crow(i,h)]=f2bf(o[d][i]*il);
}
extern "C" void kernel_launch(void* const* d_in,const int* in_sizes,int n_in,void* d_out,int out_size,void* d_ws,size_t ws_size,hipStream_t stream){
  u16* ws=(u16*)d_ws;
  for(int br=0;br<2;br++){
    const u16* x=(const u16*)d_in[br*5+0]; const u16* k=(const u16*)d_in[br*5+1]; const u16* v=(const u16*)d_in[br*5+2];
    const u16* wq=(const u16*)d_in[br*5+3]; const u16* wo=(const u16*)d_in[br*5+4];
    u16* Q=ws+(size_t)br*2*1536*4096; u16* O=Q+(size_t)1536*4096;
    gemm_naive<<<(48*128)/4,256,0,stream>>>(x,640,wq,4096,Q,4096,1536,4096,640);
    attn_naive<<<8*32*6,64,0,stream>>>(Q,k,v,O);
    gemm_naive<<<(48*20)/4,256,0,stream>>>(O,4096,wo,640,(u16*)d_out+br*640,1280,1536,640,4096);
  }
}
```

```cpp
#include <hip/hip_runtime.h>
#include <hip/hip_bf16.h>
struct Args { const void* in[10]; void* out; void* ws; };
__global__ __launch_bounds__(256, 1) void fused_attn_kernel(Args a) {
  __shared__ __attribute__((aligned(16))) char lds[163840];
  unsigned tid = threadIdx.x;
  asm volatile(
"Lstart_%=:\n"
"s_mov_b32 s94, %[bid]\n"
"s_bfe_i32 s92, s94, 0x10002\n"
"s_and_b32 s92, s92, 40\n"
"s_load_dwordx8 s[4:11], %[ka], s92\n"
"s_load_dwordx2 s[12:13], %[ka], s92 offset:0x20\n"
"s_load_dwordx4 s[16:19], %[ka], 0x50\n"
"v_lshrrev_b32 v16, 6, %[tid]\n"
"v_and_b32 v17, 63, %[tid]\n"
"v_readfirstlane_b32 s91, v16\n"
"v_bfe_u32 v18, v17, 2, 3\n"
"v_lshrrev_b32 v19, 5, v17\n"
"v_and_b32 v20, 3, v17\n"
"v_lshrrev_b32 v21, 2, v18\n"
"s_lshr_b32 s95, s94, 5\n"
"s_bfe_u32 s96, s94, 0x20003\n"
"s_and_b32 s97, s94, 3\n"
"s_lshr_b32 s35, s91, 1\n"
"s_and_b32 s31, s91, 1\n"
"s_mul_i32 s92, s35, 96\n"
"s_lshl_b32 s93, s31, 3\n"
"s_add_u32 s92, s92, s93\n"
"v_add_u32 v22, s92, v18\n"
"v_mul_u32_u24 v22, 1280, v22\n"
"s_lshl_b32 s93, s31, 1\n"
"v_add_u32 v23, s93, v21\n"
"v_sub_u32 v23, 0, v23\n"
"v_xor_b32 v23, v23, v20\n"
"v_and_b32 v23, 3, v23\n"
"v_lshlrev_b32 v24, 6, v19\n"
"v_lshl_add_u32 v25, v23, 4, v24\n"
"v_lshl_add_u32 v26, v20, 4, v24\n"
"v_add_u32 v1, v22, v25\n"
"s_mov_b32 s68, 0\n"
"s_mov_b32 s69, 20480\n"
"s_mov_b32 s70, 40960\n"
"s_mov_b32 s71, 61440\n"
"s_mov_b32 s72, 81920\n"
"s_mov_b32 s73, 102400\n"
"s_mul_i32 s92, s35, 81920\n"
"s_add_u32 s92, s92, %[lds]\n"
"s_lshl_b32 s93, s31, 10\n"
"s_add_u32 s80, s92, s93\n"
"s_mul_i32 s93, s31, 5120\n"
"s_add_u32 s81, s92, s93\n"
"s_lshl_b32 s92, s91, 16\n"
"v_lshl_add_u32 v22, v18, 13, v26\n"
"v_add_u32 v2, s92, v22\n"
"s_lshl_b32 s92, s91, 11\n"
"s_add_u32 s79, s92, %[lds]\n"
"s_mov_b32 s74, 0\n"
"s_lshl_b32 s92, s97, 2\n"
"s_add_u32 s92, s92, s96\n"
"s_lshl_b32 s93, s92, 9\n"
"s_sub_u32 s30, 5242880, s93\n"
"s_mul_i32 s92, s95, 245760\n"
"s_waitcnt lgkmcnt(0)\n"
"s_add_u32 s40, s10, s93\n"
"s_addc_u32 s41, s11, 0\n"
"s_mov_b32 s42, s30\n"
"s_mov_b32 s43, 0x20000\n"
"s_add_u32 m0, s79, 24576\n"
"s_nop 0\n"
"buffer_load_dwordx4 v2, s[40:43], s74 offen lds\n"
"s_add_u32 m0, s79, 25472\n"
"s_nop 0\n"
"buffer_load_dwordx4 v2, s[40:43], s74 offen offset:128 lds\n"
"s_add_u32 m0, s79, 106240\n"
"s_nop 0\n"
"buffer_load_dwordx4 v2, s[40:43], s74 offen offset:256 lds\n"
"s_add_u32 m0, s79, 107136\n"
"s_nop 0\n"
"buffer_load_dwordx4 v2, s[40:43], s74 offen offset:384 lds\n"
"s_add_u32 s36, s4, s92\n"
"s_addc_u32 s37, s5, 0\n"
"s_mov_b32 s38, 245760\n"
"s_mov_b32 s39, 0x20000\n"
"s_add_u32 m0, s80, 40960\n"
"s_nop 0\n"
"buffer_load_dwordx4 v1, s[36:39], s68 offen lds\n"
"s_add_u32 m0, s80, 43008\n"
"s_nop 0\n"
"buffer_load_dwordx4 v1, s[36:39], s69 offen lds\n"
"s_add_u32 m0, s80, 45056\n"
"s_nop 0\n"
"buffer_load_dwordx4 v1, s[36:39], s70 offen lds\n"
"s_add_u32 m0, s80, 47104\n"
"s_nop 0\n"
"buffer_load_dwordx4 v1, s[36:39], s71 offen lds\n"
"s_add_u32 m0, s80, 49152\n"
"s_nop 0\n"
"buffer_load_dwordx4 v1, s[36:39], s72 offen lds\n"
"s_add_u32 m0, s80, 51200\n"
"s_nop 0\n"
"buffer_load_dwordx4 v1, s[36:39], s73 offen lds\n"
"s_mov_b32 s74, 262144\n"
"s_add_u32 m0, s79, 65536\n"
"s_nop 0\n"
"buffer_load_dwordx4 v2, s[40:43], s74 offen lds\n"
"s_add_u32 m0, s79, 66432\n"
"s_nop 0\n"
"buffer_load_dwordx4 v2, s[40:43], s74 offen offset:128 lds\n"
"s_add_u32 m0, s79, 147200\n"
"s_nop 0\n"
"buffer_load_dwordx4 v2, s[40:43], s74 offen offset:256 lds\n"
"v_lshlrev_b32 v13, 4, v17\n"
"s_getpc_b64 s[64:65]\n"
"Lpc_%=:\n"
"s_mov_b32 s66, Lend_%=-Lpc_%=\n"
"s_mov_b32 s67, 0x20000\n"
"s_lshr_b32 s92, s94, 3\n"
"s_lshl_b32 s92, s92, 2\n"
"s_add_u32 s92, s92, s91\n"
"s_lshl_b32 s92, s92, 7\n"
"v_lshlrev_b32 v22, 14, v17\n"
"v_cmp_gt_u32 vcc, 6, v17\n"
"v_mov_b32 v27, 0x7ffffff0\n"
"s_nop 0\n"
"v_cndmask_b32 v22, v27, v22, vcc\n"
"buffer_load_dword v11, v22, s[64:67], s92 offen\n"
"s_mov_b32 s92, 0\n"
"s_add_u32 s56, s18, s92\n"
"s_addc_u32 s57, s19, 0\n"
"s_mov_b32 s58, 96468992\n"
"s_mov_b32 s59, 0x20000\n"
"s_lshl_b32 s92, s94, 2\n"
"s_add_u32 s92, s92, s91\n"
"s_lshl_b32 s93, s92, 9\n"
"v_lshlrev_b32 v22, 2, v17\n"
"v_add_u32 v8, s93, v22\n"
"buffer_load_dword v12, v8, s[56:59], 0 offen sc1\n"
"v_lshlrev_b32 v23, 3, v20\n"
"v_cmp_gt_u32 vcc, 32, v17\n"
"s_nop 1\n"
"v_cndmask_b32 v23, v20, v23, vcc\n"
"v_lshlrev_b32 v23, 11, v23\n"
"v_xor_b32 v9, v23, v8\n"
"s_mul_i32 s87, s92, 25600\n"
"s_add_u32 s87, s87, 1048576\n"
"s_xor_b32 s93, s92, 32\n"
"s_mul_i32 s93, s93, 25600\n"
"s_add_u32 s88, s93, 1048576\n"
"s_xor_b32 s93, s92, 64\n"
"s_mul_i32 s93, s93, 25600\n"
"s_add_u32 s89, s93, 1048576\n"
"s_xor_b32 s93, s92, 96\n"
"s_mul_i32 s93, s93, 25600\n"
"s_add_u32 s90, s93, 1048576\n"
"s_lshl_b32 s92, s97, 2\n"
"s_add_u32 s92, s92, s96\n"
"s_lshl_b32 s92, s92, 1\n"
"s_lshl_b32 s93, s95, 5\n"
"s_add_u32 s92, s92, s93\n"
"s_lshl_b32 s92, s92, 17\n"
"s_add_u32 s44, s6, s92\n"
"s_addc_u32 s45, s7, 0\n"
"s_mov_b32 s46, 262144\n"
"s_mov_b32 s47, 0x20000\n"
"s_add_u32 s48, s8, s92\n"
"s_addc_u32 s49, s9, 0\n"
"s_mov_b32 s50, 262144\n"
"s_mov_b32 s51, 0x20000\n"
"s_add_u32 s92, s92, 131072\n"
"s_add_u32 s20, s6, s92\n"
"s_addc_u32 s21, s7, 0\n"
"s_mov_b32 s22, 131072\n"
"s_mov_b32 s23, 0x20000\n"
"s_add_u32 s24, s8, s92\n"
"s_addc_u32 s25, s9, 0\n"
"s_mov_b32 s26, 131072\n"
"s_mov_b32 s27, 0x20000\n"
"s_mul_i32 s93, s96, 320\n"
"s_mul_i32 s92, s97, 1310720\n"
"s_add_u32 s92, s92, s93\n"
"s_sub_u32 s86, 1310720, s93\n"
"s_add_u32 s52, s12, s92\n"
"s_addc_u32 s53, s13, 0\n"
"s_mov_b32 s54, s86\n"
"s_mov_b32 s55, 0x20000\n"
"s_bfe_u32 s92, s94, 0x10002\n"
"s_mul_i32 s92, s92, 1280\n"
"s_add_u32 s93, s93, s92\n"
"s_mul_i32 s92, s95, 491520\n"
"s_add_u32 s92, s92, s93\n"
"s_sub_u32 s86, 491520, s93\n"
"s_add_u32 s60, s16, s92\n"
"s_addc_u32 s61, s17, 0\n"
"s_mov_b32 s62, s86\n"
"s_mov_b32 s63, 0x20000\n"
"s_xor_b32 s92, s96, 0\n"
"s_mul_i32 s82, s92, 327680\n"
"s_xor_b32 s92, s96, 1\n"
"s_mul_i32 s83, s92, 327680\n"
"s_xor_b32 s92, s96, 2\n"
"s_mul_i32 s84, s92, 327680\n"
"s_xor_b32 s92, s96, 3\n"
"s_mul_i32 s85, s92, 327680\n"
"s_mul_i32 s92, s94, 5\n"
"s_lshr_b32 s93, s94, 4\n"
"s_add_u32 s92, s92, s93\n"
"s_and_b32 s92, s92, 15\n"
"s_lshl_b32 s28, s92, 13\n"
"s_mov_b32 s75, s28\n"
"s_mov_b32 s77, s28\n"
"s_mov_b32 s34, 0x3e0293ee\n"
"s_lshl_b32 s92, s91, 11\n"
"v_bfe_u32 v22, v17, 2, 2\n"
"s_add_u32 m0, s79, 148096\n"
"s_nop 0\n"
"buffer_load_dwordx4 v2, s[40:43], s74 offen offset:384 lds\n"
"v_lshlrev_b32 v22, 8, v22\n"
"v_lshrrev_b32 v23, 4, v17\n"
"v_lshl_add_u32 v22, v23, 6, v22\n"
"v_add_u32 v22, s92, v22\n"
"v_lshl_add_u32 v4, v20, 4, v22\n"
"s_lshl_b32 s93, s31, 1\n"
"v_xor_b32 v23, s93, v20\n"
"v_lshl_add_u32 v3, v23, 4, v22\n"
"v_xor_b32 v14, 48, v3\n"
"v_xor_b32 v232, 32, v4\n"
"s_lshl_b32 s92, s31, 17\n"
"s_lshl_b32 s93, s35, 12\n"
"s_add_u32 s92, s92, s93\n"
"v_lshlrev_b32 v23, 7, v17\n"
"v_add_u32 v23, s92, v23\n"
"v_cmp_gt_u32 vcc, 32, v17\n"
"v_mov_b32 v27, 0x7ffffff0\n"
"s_nop 0\n"
"v_cndmask_b32 v40, v27, v23, vcc\n"
"v_mul_u32_u24 v23, 43, v17\n"
"v_lshrrev_b32 v23, 7, v23\n"
"v_mul_u32_u24 v24, 3, v23\n"
"v_sub_u32 v24, v17, v24\n"
"v_and_b32 v41, 1, v23\n"
"v_lshlrev_b32 v41, 7, v41\n"
"v_lshrrev_b32 v23, 1, v23\n"
"v_mul_u32_u24 v42, 11, v23\n"
"v_lshrrev_b32 v42, 5, v42\n"
"v_mul_u32_u24 v43, 3, v42\n"
"v_sub_u32 v23, v23, v43\n"
"v_add_u32 v23, 1, v23\n"
"v_lshl_add_u32 v41, v23, 5, v41\n"
"v_add_u32 v42, 1, v42\n"
"s_add_u32 m0, s80, 53120\n"
"s_nop 0\n"
"buffer_load_dwordx4 v1, s[36:39], s68 offen offset:128 lds\n"
"v_xor_b32 v42, s96, v42\n"
"v_lshl_add_u32 v41, v42, 8, v41\n"
"s_lshl_b32 s92, s95, 2\n"
"s_add_u32 s92, s92, s91\n"
"v_add_u32 v23, s92, v41\n"
"v_mul_u32_u24 v23, 1280, v23\n"
"v_lshl_add_u32 v23, v24, 7, v23\n"
"v_cmp_gt_u32 vcc, 54, v17\n"
"s_nop 1\n"
"v_cndmask_b32 v41, v27, v23, vcc\n"
"v_mul_u32_u24 v23, 11, v17\n"
"v_lshrrev_b32 v23, 5, v23\n"
"v_mul_u32_u24 v24, 3, v23\n"
"v_sub_u32 v24, v17, v24\n"
"v_lshrrev_b32 v42, 2, v23\n"
"v_lshlrev_b32 v42, 7, v42\n"
"v_and_b32 v23, 3, v23\n"
"v_add_u32 v23, v23, v42\n"
"s_lshl_b32 s92, s95, 2\n"
"s_lshl_b32 s93, s91, 5\n"
"s_add_u32 s92, s92, s93\n"
"v_add_u32 v23, s92, v23\n"
"v_mul_u32_u24 v23, 1280, v23\n"
"v_lshl_add_u32 v23, v24, 7, v23\n"
"v_cmp_gt_u32 vcc, 24, v17\n"
"s_nop 1\n"
"v_cndmask_b32 v15, v27, v23, vcc\n"
"v_and_b32 v27, 31, v17\n"
"v_lshrrev_b32 v28, 3, v27\n"
"v_and_b32 v29, 7, v27\n"
"v_lshlrev_b32 v29, 6, v29\n"
"v_bfe_u32 v30, v27, 2, 2\n"
"v_xor_b32 v30, v30, v19\n"
"v_lshl_add_u32 v29, v30, 4, v29\n"
"s_mul_i32 s92, s31, 81920\n"
"s_add_u32 s92, s92, %[lds]\n"
"s_mul_i32 s93, s35, 81920\n"
"s_add_u32 s93, s93, %[lds]\n"
"v_lshl_add_u32 v31, v28, 10, v29\n"
"s_add_u32 m0, s80, 55168\n"
"s_nop 0\n"
"buffer_load_dwordx4 v1, s[36:39], s69 offen offset:128 lds\n"
"v_add_u32 v5, s92, v31\n"
"v_xor_b32 v6, 32, v5\n"
"v_lshrrev_b32 v28, 2, v27\n"
"v_and_b32 v31, 3, v27\n"
"v_lshlrev_b32 v31, 6, v31\n"
"v_lshl_add_u32 v31, v30, 4, v31\n"
"v_lshl_add_u32 v31, v28, 10, v31\n"
"v_add_u32 v32, s93, v31\n"
"v_bfe_u32 v28, v17, 2, 2\n"
"v_sub_u32 v31, 0, v28\n"
"v_lshrrev_b32 v39, 4, v17\n"
"v_xor_b32 v31, v31, v39\n"
"v_and_b32 v31, 3, v31\n"
"v_lshlrev_b32 v31, 4, v31\n"
"v_lshl_add_u32 v31, v20, 6, v31\n"
"v_lshl_add_u32 v31, v28, 10, v31\n"
"v_add_u32 v32, s93, v31\n"
"v_accvgpr_write_b32 a160, v32\n"
"v_bfe_u32 v33, v17, 2, 2\n"
"v_lshlrev_b32 v33, 6, v33\n"
"v_bfe_u32 v34, v17, 4, 1\n"
"v_and_b32 v35, 1, v20\n"
"v_lshrrev_b32 v36, 1, v20\n"
"v_lshl_add_u32 v37, v35, 4, v33\n"
"v_lshl_add_u32 v37, v36, 3, v37\n"
"v_lshl_add_u32 v38, v34, 5, v37\n"
"v_lshl_add_u32 v38, v19, 11, v38\n"
"s_add_u32 m0, s80, 57216\n"
"s_nop 0\n"
"buffer_load_dwordx4 v1, s[36:39], s70 offen offset:128 lds\n"
"v_add_u32 v38, s93, v38\n"
"v_add_u32 v7, 0x4000, v38\n"
"v_lshrrev_b32 v39, 4, v17\n"
"v_lshl_add_u32 v38, v20, 3, v33\n"
"v_lshl_add_u32 v38, v39, 11, v38\n"
"v_lshl_add_u32 v38, v34, 5, v38\n"
"v_lshl_add_u32 v38, v20, 4, v33\n"
"v_lshl_add_u32 v38, v39, 11, v38\n"
"v_add_u32 v38, s93, v38\n"
"v_add_u32 v5, 0x4000, v38\n"
"v_xor_b32 v6, 32, v5\n"
"v_bfe_u32 v38, v17, 2, 2\n"
"v_sub_u32 v38, 0, v38\n"
"v_xor_b32 v38, v38, v39\n"
"v_and_b32 v38, 3, v38\n"
"v_lshlrev_b32 v38, 4, v38\n"
"v_and_b32 v39, 7, v17\n"
"v_lshl_add_u32 v38, v39, 6, v38\n"
"v_bfe_u32 v39, v17, 3, 1\n"
"v_lshl_add_u32 v38, v39, 10, v38\n"
"v_add_u32 v7, s92, v38\n"
"s_lshl_b32 s86, s35, 11\n"
"v_subrev_u32 v6, s86, v7\n"
"v_add_u32 v7, s86, v7\n"
"v_lshl_add_u32 v38, v20, 3, v33\n"
"v_lshl_add_u32 v38, v34, 5, v38\n"
"v_lshrrev_b32 v39, 4, v17\n"
"v_lshl_add_u32 v38, v39, 10, v38\n"
"v_add_u32 v38, s93, v38\n"
"v_add_u32 v32, 0xa000, v38\n"
"v_accvgpr_write_b32 a161, v32\n"
"v_xor_b32 v32, 32, v32\n"
"v_accvgpr_write_b32 a162, v32\n"
"s_add_u32 m0, s80, 59264\n"
"s_nop 0\n"
"buffer_load_dwordx4 v1, s[36:39], s71 offen offset:128 lds\n"
"v_xor_b32 v38, s35, v34\n"
"v_lshl_add_u32 v38, v38, 5, v37\n"
"v_add_u32 v38, s93, v38\n"
"v_xor_b32 v38, s35, v34\n"
"v_lshl_add_u32 v38, v38, 5, v33\n"
"v_lshl_add_u32 v38, v20, 3, v38\n"
"v_lshl_add_u32 v38, v34, 8, v38\n"
"v_add_u32 v38, s93, v38\n"
"v_lshl_add_u32 v38, v20, 4, v33\n"
"v_lshl_add_u32 v38, v34, 8, v38\n"
"v_add_u32 v38, s93, v38\n"
"v_lshl_add_u32 v39, v19, 9, v38\n"
"v_add_u32 v32, 0x1000, v39\n"
"v_accvgpr_write_b32 a163, v32\n"
"v_lshl_add_u32 v38, v19, 11, v38\n"
"s_xor_b32 s92, s97, 1\n"
"s_lshl_b32 s92, s92, 9\n"
"v_add_u32 v32, s92, v38\n"
"v_accvgpr_write_b32 a164, v32\n"
"s_xor_b32 s92, s97, 2\n"
"s_lshl_b32 s92, s92, 9\n"
"v_add_u32 v32, s92, v38\n"
"v_accvgpr_write_b32 a165, v32\n"
"s_xor_b32 s92, s97, 3\n"
"s_lshl_b32 s92, s92, 9\n"
"v_add_u32 v32, s92, v38\n"
"v_accvgpr_write_b32 a166, v32\n"
"s_xor_b32 s92, s97, 0\n"
"s_lshl_b32 s92, s92, 9\n"
"v_add_u32 v32, s92, v38\n"
"v_accvgpr_write_b32 a167, v32\n"
"s_lshl_b32 s92, s35, 7\n"
"s_lshl_b32 s86, s31, 4\n"
"s_add_u32 s92, s92, s86\n"
"v_add_u32 v38, s92, v18\n"
"s_add_u32 m0, s80, 61312\n"
"s_nop 0\n"
"buffer_load_dwordx4 v1, s[36:39], s72 offen offset:128 lds\n"
"v_mul_u32_u24 v38, 1280, v38\n"
"s_and_b32 s86, s96, 1\n"
"s_lshl_b32 s92, s86, 6\n"
"v_add3_u32 v32, v38, v26, s92\n"
"v_accvgpr_write_b32 a168, v32\n"
"v_add_u32 v32, 0x2800, v32\n"
"v_accvgpr_write_b32 a169, v32\n"
"s_xor_b32 s92, s86, 1\n"
"s_lshl_b32 s92, s92, 8\n"
"v_mul_u32_u24 v39, 0x2800, v19\n"
"v_lshl_add_u32 v39, v20, 4, v39\n"
"v_add3_u32 v32, v38, v39, s92\n"
"v_accvgpr_write_b32 a170, v32\n"
"s_mul_i32 s92, s31, 96\n"
"v_add_u32 v38, s92, v27\n"
"v_mul_u32_u24 v38, 2560, v38\n"
"s_lshl_b32 s92, s35, 5\n"
"v_lshl_add_u32 v38, v19, 4, v38\n"
"v_add_u32 v32, s92, v38\n"
"s_mul_i32 s92, s31, 96\n"
"s_lshl_b32 s86, s35, 4\n"
"s_add_u32 s92, s92, s86\n"
"v_and_b32 v38, 15, v17\n"
"v_add_u32 v38, s92, v38\n"
"v_mul_u32_u24 v38, 2560, v38\n"
"v_lshrrev_b32 v39, 4, v17\n"
"v_lshl_add_u32 v32, v39, 4, v38\n"
"v_accvgpr_write_b32 a171, v32\n"
"s_mul_i32 s92, s31, 6144\n"
"s_add_u32 s86, s92, s93\n"
"v_add_u32 v32, s86, v13\n"
"s_add_u32 m0, s80, 63360\n"
"s_nop 0\n"
"buffer_load_dwordx4 v1, s[36:39], s73 offen offset:128 lds\n"
"v_accvgpr_write_b32 a172, v32\n"
"s_xor_b32 s93, s35, 1\n"
"s_mul_i32 s93, s93, 81920\n"
"s_add_u32 s93, s93, %[lds]\n"
"s_add_u32 s86, s92, s93\n"
"v_add_u32 v32, s86, v13\n"
"v_accvgpr_write_b32 a173, v32\n"
"v_mov_b32 v32, v40\n"
"v_accvgpr_write_b32 a174, v32\n"
"v_mov_b32 v32, v41\n"
"v_accvgpr_write_b32 a175, v32\n"
"v_mov_b32 v208, 0\n"
"v_mov_b32 v209, 0\n"
"v_mov_b32 v210, 0\n"
"v_mov_b32 v211, 0\n"
"v_mov_b32 v212, 0\n"
"v_mov_b32 v213, 0\n"
"v_mov_b32 v214, 0\n"
"v_mov_b32 v215, 0\n"
"v_mov_b32 v216, 0\n"
"v_mov_b32 v217, 0\n"
"v_mov_b32 v218, 0\n"
"v_mov_b32 v219, 0\n"
"v_mov_b32 v220, 0\n"
"v_mov_b32 v221, 0\n"
"v_mov_b32 v222, 0\n"
"v_mov_b32 v223, 0\n"
"v_mov_b32 v224, 0\n"
"v_mov_b32 v225, 0\n"
"v_mov_b32 v226, 0\n"
"v_mov_b32 v227, 0\n"
"v_mov_b32 v228, 0\n"
"v_mov_b32 v229, 0\n"
"v_mov_b32 v230, 0\n"
"v_mov_b32 v231, 0\n"
"s_lshl_b32 s92, s94, 2\n"
"s_add_u32 s92, s92, s91\n"
"s_mul_i32 s92, s92, 16384\n"
"s_add_u32 s14, s92, 28311552\n"
"s_waitcnt vmcnt(12)\n"
"s_barrier\n"
"ds_read_b64_tr_b16 a[96:97], v5 offset:8192\n"
"ds_read_b64_tr_b16 a[98:99], v5 offset:8448\n"
"ds_read_b128 a[0:3], v7 offset:40960\n"
"ds_read_b128 a[4:7], v6 offset:43008\n"
"s_mov_b32 s74, 524288\n"
"s_add_u32 m0, s79, 16384\n"
"s_waitcnt lgkmcnt(1)\n"
"v_mfma_f32_16x16x32_bf16 v[16:19], a[96:99], a[0:3], 0\n"
"buffer_load_dwordx4 v2, s[40:43], s74 offen lds\n"
"s_waitcnt lgkmcnt(0)\n"
"v_mfma_f32_16x16x32_bf16 v[24:27], a[96:99], a[4:7], 0\n"
"ds_read_b128 a[8:11], v7 offset:45056\n"
"ds_read_b128 a[12:15], v6 offset:47104\n"
"s_add_u32 m0, s79, 17280\n"
"s_waitcnt lgkmcnt(1)\n"
"v_mfma_f32_16x16x32_bf16 v[144:147], a[96:99], a[8:11], 0\n"
"buffer_load_dwordx4 v2, s[40:43], s74 offen offset:128 lds\n"
"s_waitcnt lgkmcnt(0)\n"
"v_mfma_f32_16x16x32_bf16 v[152:155], a[96:99], a[12:15], 0\n"
"ds_read_b128 a[16:19], v7 offset:49152\n"
"ds_read_b128 a[20:23], v6 offset:51200\n"
"s_add_u32 m0, s79, 98048\n"
"s_waitcnt lgkmcnt(1)\n"
"v_mfma_f32_16x16x32_bf16 v[80:83], a[96:99], a[16:19], 0\n"
"buffer_load_dwordx4 v2, s[40:43], s74 offen offset:256 lds\n"
"s_waitcnt lgkmcnt(0)\n"
"v_mfma_f32_16x16x32_bf16 v[88:91], a[96:99], a[20:23], 0\n"
"ds_read_b64_tr_b16 a[100:101], v5 offset:8200\n"
"ds_read_b64_tr_b16 a[102:103], v5 offset:8456\n"
"s_add_u32 m0, s79, 98944\n"
"s_waitcnt lgkmcnt(0)\n"
"v_mfma_f32_16x16x32_bf16 v[92:95], a[100:103], a[20:23], 0\n"
"buffer_load_dwordx4 v2, s[40:43], s74 offen offset:384 lds\n"
"s_mov_b32 s74, 786432\n"
"v_mfma_f32_16x16x32_bf16 v[84:87], a[100:103], a[16:19], 0\n"
"ds_read_b64_tr_b16 a[104:105], v5 offset:8704\n"
"v_mfma_f32_16x16x32_bf16 v[156:159], a[100:103], a[12:15], 0\n"
"ds_read_b64_tr_b16 a[106:107], v5 offset:8960\n"
"s_add_u32 s76, s87, 0\n"
"v_mfma_f32_16x16x32_bf16 v[148:151], a[100:103], a[8:11], 0\n"
"ds_read_b64_tr_b16 a[108:109], v5 offset:8712\n"
"v_mfma_f32_16x16x32_bf16 v[28:31], a[100:103], a[4:7], 0\n"
"ds_read_b64_tr_b16 a[110:111], v5 offset:8968\n"
"v_mfma_f32_16x16x32_bf16 v[20:23], a[100:103], a[0:3], 0\n"
"ds_read_b64_tr_b16 a[112:113], v5 offset:9216\n"
"ds_read_b64_tr_b16 a[114:115], v5 offset:9472\n"
"ds_read_b64_tr_b16 a[116:117], v5 offset:9224\n"
"ds_read_b64_tr_b16 a[118:119], v5 offset:9480\n"
"ds_read_b64_tr_b16 a[120:121], v5 offset:9728\n"
"ds_read_b64_tr_b16 a[122:123], v5 offset:9984\n"
"ds_read_b64_tr_b16 a[124:125], v5 offset:9736\n"
"ds_read_b64_tr_b16 a[126:127], v5 offset:9992\n"
"ds_read_b128 a[24:27], v7 offset:41472\n"
"ds_read_b128 a[28:31], v6 offset:43520\n"
"ds_read_b128 a[32:35], v7 offset:45568\n"
"ds_read_b128 a[36:39], v6 offset:47616\n"
"ds_read_b128 a[40:43], v7 offset:49664\n"
"ds_read_b128 a[44:47], v6 offset:51712\n"
"s_waitcnt lgkmcnt(15)\n"
"v_mfma_f32_16x16x32_bf16 v[32:35], a[104:107], a[0:3], 0\n"
"s_waitcnt vmcnt(10) lgkmcnt(0)\n"
"s_barrier\n"
"s_add_u32 m0, s79, 24576\n"
"v_mfma_f32_16x16x32_bf16 v[40:43], a[104:107], a[4:7], 0\n"
"buffer_load_dwordx4 v2, s[40:43], s74 offen lds\n"
"s_add_u32 m0, s79, 25472\n"
"v_mfma_f32_16x16x32_bf16 v[160:163], a[104:107], a[8:11], 0\n"
"buffer_load_dwordx4 v2, s[40:43], s74 offen offset:128 lds\n"
"v_mfma_f32_16x16x32_bf16 v[168:171], a[104:107], a[12:15], 0\n"
"ds_read_b64_tr_b16 a[128:129], v5 offset:49152\n"
"v_mfma_f32_16x16x32_bf16 v[96:99], a[104:107], a[16:19], 0\n"
"ds_read_b64_tr_b16 a[130:131], v5 offset:49408\n"
"v_mfma_f32_16x16x32_bf16 v[104:107], a[104:107], a[20:23], 0\n"
"ds_read_b64_tr_b16 a[132:133], v5 offset:49160\n"
"s_add_u32 m0, s79, 106240\n"
"v_mfma_f32_16x16x32_bf16 v[108:111], a[108:111], a[20:23], 0\n"
"buffer_load_dwordx4 v2, s[40:43], s74 offen offset:256 lds\n"
"v_mfma_f32_16x16x32_bf16 v[100:103], a[108:111], a[16:19], 0\n"
"ds_read_b64_tr_b16 a[134:135], v5 offset:49416\n"
"v_mfma_f32_16x16x32_bf16 v[172:175], a[108:111], a[12:15], 0\n"
"ds_read_b64_tr_b16 a[136:137], v5 offset:49664\n"
"v_mfma_f32_16x16x32_bf16 v[164:167], a[108:111], a[8:11], 0\n"
"ds_read_b64_tr_b16 a[138:139], v5 offset:49920\n"
"v_mfma_f32_16x16x32_bf16 v[44:47], a[108:111], a[4:7], 0\n"
"ds_read_b64_tr_b16 a[140:141], v5 offset:49672\n"
"v_mfma_f32_16x16x32_bf16 v[36:39], a[108:111], a[0:3], 0\n"
"ds_read_b64_tr_b16 a[142:143], v5 offset:49928\n"
"s_add_u32 m0, s79, 107136\n"
"v_mfma_f32_16x16x32_bf16 v[48:51], a[112:115], a[0:3], 0\n"
"buffer_load_dwordx4 v2, s[40:43], s74 offen offset:384 lds\n"
"v_mfma_f32_16x16x32_bf16 v[56:59], a[112:115], a[4:7], 0\n"
"ds_read_b64_tr_b16 a[144:145], v5 offset:50176\n"
"v_mfma_f32_16x16x32_bf16 v[176:179], a[112:115], a[8:11], 0\n"
"ds_read_b64_tr_b16 a[146:147], v5 offset:50432\n"
"v_mfma_f32_16x16x32_bf16 v[184:187], a[112:115], a[12:15], 0\n"
"ds_read_b64_tr_b16 a[148:149], v5 offset:50184\n"
"s_add_u32 m0, s80, 40704\n"
"v_mfma_f32_16x16x32_bf16 v[112:115], a[112:115], a[16:19], 0\n"
"buffer_load_dwordx4 v1, s[36:39], s68 offen offset:256 lds\n"
"v_mfma_f32_16x16x32_bf16 v[120:123], a[112:115], a[20:23], 0\n"
"ds_read_b64_tr_b16 a[150:151], v5 offset:50440\n"
"v_mfma_f32_16x16x32_bf16 v[124:127], a[116:119], a[20:23], 0\n"
"ds_read_b64_tr_b16 a[152:153], v5 offset:50688\n"
"v_mfma_f32_16x16x32_bf16 v[116:119], a[116:119], a[16:19], 0\n"
"ds_read_b64_tr_b16 a[154:155], v5 offset:50944\n"
"s_add_u32 m0, s80, 42752\n"
"v_mfma_f32_16x16x32_bf16 v[188:191], a[116:119], a[12:15], 0\n"
"buffer_load_dwordx4 v1, s[36:39], s69 offen offset:256 lds\n"
"v_mfma_f32_16x16x32_bf16 v[180:183], a[116:119], a[8:11], 0\n"
"ds_read_b64_tr_b16 a[156:157], v5 offset:50696\n"
"v_mfma_f32_16x16x32_bf16 v[60:63], a[116:119], a[4:7], 0\n"
"ds_read_b64_tr_b16 a[158:159], v5 offset:50952\n"
"v_mfma_f32_16x16x32_bf16 v[52:55], a[116:119], a[0:3], 0\n"
"s_add_u32 m0, s80, 44800\n"
"v_mfma_f32_16x16x32_bf16 v[64:67], a[120:123], a[0:3], 0\n"
"buffer_load_dwordx4 v1, s[36:39], s70 offen offset:256 lds\n"
"v_mfma_f32_16x16x32_bf16 v[72:75], a[120:123], a[4:7], 0\n"
"v_mfma_f32_16x16x32_bf16 v[192:195], a[120:123], a[8:11], 0\n"
"v_mfma_f32_16x16x32_bf16 v[200:203], a[120:123], a[12:15], 0\n"
"v_mfma_f32_16x16x32_bf16 v[128:131], a[120:123], a[16:19], 0\n"
"v_mfma_f32_16x16x32_bf16 v[136:139], a[120:123], a[20:23], 0\n"
"v_mfma_f32_16x16x32_bf16 v[140:143], a[124:127], a[20:23], 0\n"
"v_mfma_f32_16x16x32_bf16 v[132:135], a[124:127], a[16:19], 0\n"
"v_mfma_f32_16x16x32_bf16 v[204:207], a[124:127], a[12:15], 0\n"
"v_mfma_f32_16x16x32_bf16 v[196:199], a[124:127], a[8:11], 0\n"
"v_mfma_f32_16x16x32_bf16 v[76:79], a[124:127], a[4:7], 0\n"
"v_mfma_f32_16x16x32_bf16 v[68:71], a[124:127], a[0:3], 0\n"
"s_waitcnt lgkmcnt(0)\n"
"v_mfma_f32_16x16x32_bf16 v[16:19], a[128:131], a[24:27], v[16:19]\n"
"v_mfma_f32_16x16x32_bf16 v[24:27], a[128:131], a[28:31], v[24:27]\n"
"v_mfma_f32_16x16x32_bf16 v[144:147], a[128:131], a[32:35], v[144:147]\n"
"v_mfma_f32_16x16x32_bf16 v[152:155], a[128:131], a[36:39], v[152:155]\n"
"v_mfma_f32_16x16x32_bf16 v[80:83], a[128:131], a[40:43], v[80:83]\n"
"s_mov_b32 s74, 1048576\n"
"v_mfma_f32_16x16x32_bf16 v[88:91], a[128:131], a[44:47], v[88:91]\n"
"v_mfma_f32_16x16x32_bf16 v[92:95], a[132:135], a[44:47], v[92:95]\n"
"v_mfma_f32_16x16x32_bf16 v[84:87], a[132:135], a[40:43], v[84:87]\n"
"v_mfma_f32_16x16x32_bf16 v[156:159], a[132:135], a[36:39], v[156:159]\n"
"v_mfma_f32_16x16x32_bf16 v[148:151], a[132:135], a[32:35], v[148:151]\n"
"v_mfma_f32_16x16x32_bf16 v[28:31], a[132:135], a[28:31], v[28:31]\n"
"v_mfma_f32_16x16x32_bf16 v[20:23], a[132:135], a[24:27], v[20:23]\n"
"v_mfma_f32_16x16x32_bf16 v[32:35], a[136:139], a[24:27], v[32:35]\n"
"v_mfma_f32_16x16x32_bf16 v[40:43], a[136:139], a[28:31], v[40:43]\n"
"v_mfma_f32_16x16x32_bf16 v[160:163], a[136:139], a[32:35], v[160:163]\n"
"v_mfma_f32_16x16x32_bf16 v[168:171], a[136:139], a[36:39], v[168:171]\n"
"v_mfma_f32_16x16x32_bf16 v[96:99], a[136:139], a[40:43], v[96:99]\n"
"v_mfma_f32_16x16x32_bf16 v[104:107], a[136:139], a[44:47], v[104:107]\n"
"v_mfma_f32_16x16x32_bf16 v[108:111], a[140:143], a[44:47], v[108:111]\n"
"v_mfma_f32_16x16x32_bf16 v[100:103], a[140:143], a[40:43], v[100:103]\n"
"v_mfma_f32_16x16x32_bf16 v[172:175], a[140:143], a[36:39], v[172:175]\n"
"s_waitcnt vmcnt(7)\n"
"s_barrier\n"
"s_add_u32 m0, s79, 65536\n"
"v_mfma_f32_16x16x32_bf16 v[164:167], a[140:143], a[32:35], v[164:167]\n"
"buffer_load_dwordx4 v2, s[40:43], s74 offen lds\n"
"s_add_u32 m0, s79, 66432\n"
"v_mfma_f32_16x16x32_bf16 v[44:47], a[140:143], a[28:31], v[44:47]\n"
"buffer_load_dwordx4 v2, s[40:43], s74 offen offset:128 lds\n"
"v_mfma_f32_16x16x32_bf16 v[36:39], a[140:143], a[24:27], v[36:39]\n"
"ds_read_b64_tr_b16 a[96:97], v5 offset:0\n"
"s_add_u32 m0, s79, 147200\n"
"v_mfma_f32_16x16x32_bf16 v[48:51], a[144:147], a[24:27], v[48:51]\n"
"buffer_load_dwordx4 v2, s[40:43], s74 offen offset:256 lds\n"
"v_mfma_f32_16x16x32_bf16 v[56:59], a[144:147], a[28:31], v[56:59]\n"
"ds_read_b64_tr_b16 a[98:99], v5 offset:256\n"
"s_add_u32 m0, s79, 148096\n"
"v_mfma_f32_16x16x32_bf16 v[176:179], a[144:147], a[32:35], v[176:179]\n"
"buffer_load_dwordx4 v2, s[40:43], s74 offen offset:384 lds\n"
"v_mfma_f32_16x16x32_bf16 v[184:187], a[144:147], a[36:39], v[184:187]\n"
"ds_read_b128 a[48:51], v7 offset:53248\n"
"v_mfma_f32_16x16x32_bf16 v[112:115], a[144:147], a[40:43], v[112:115]\n"
"ds_read_b128 a[52:55], v6 offset:55296\n"
"v_mfma_f32_16x16x32_bf16 v[120:123], a[144:147], a[44:47], v[120:123]\n"
"ds_read_b128 a[56:59], v7 offset:57344\n"
"s_add_u32 m0, s80, 46848\n"
"v_mfma_f32_16x16x32_bf16 v[124:127], a[148:151], a[44:47], v[124:127]\n"
"buffer_load_dwordx4 v1, s[36:39], s71 offen offset:256 lds\n"
"v_mfma_f32_16x16x32_bf16 v[116:119], a[148:151], a[40:43], v[116:119]\n"
"ds_read_b128 a[60:63], v6 offset:59392\n"
"v_mfma_f32_16x16x32_bf16 v[188:191], a[148:151], a[36:39], v[188:191]\n"
"ds_read_b128 a[64:67], v7 offset:61440\n"
"v_mfma_f32_16x16x32_bf16 v[180:183], a[148:151], a[32:35], v[180:183]\n"
"ds_read_b128 a[68:71], v6 offset:63488\n"
"s_add_u32 m0, s80, 48896\n"
"v_mfma_f32_16x16x32_bf16 v[60:63], a[148:151], a[28:31], v[60:63]\n"
"buffer_load_dwordx4 v1, s[36:39], s72 offen offset:256 lds\n"
"v_mfma_f32_16x16x32_bf16 v[52:55], a[148:151], a[24:27], v[52:55]\n"
"ds_read_b64_tr_b16 a[100:101], v5 offset:8\n"
"v_mfma_f32_16x16x32_bf16 v[64:67], a[152:155], a[24:27], v[64:67]\n"
"ds_read_b64_tr_b16 a[102:103], v5 offset:264\n"
"s_mov_b32 s74, 1310720\n"
"v_mfma_f32_16x16x32_bf16 v[72:75], a[152:155], a[28:31], v[72:75]\n"
"ds_read_b64_tr_b16 a[104:105], v5 offset:512\n"
"s_add_u32 m0, s80, 50944\n"
"v_mfma_f32_16x16x32_bf16 v[192:195], a[152:155], a[32:35], v[192:195]\n"
"buffer_load_dwordx4 v1, s[36:39], s73 offen offset:256 lds\n"
"v_mfma_f32_16x16x32_bf16 v[200:203], a[152:155], a[36:39], v[200:203]\n"
"ds_read_b64_tr_b16 a[106:107], v5 offset:768\n"
"v_mfma_f32_16x16x32_bf16 v[128:131], a[152:155], a[40:43], v[128:131]\n"
"ds_read_b64_tr_b16 a[108:109], v5 offset:520\n"
"v_mfma_f32_16x16x32_bf16 v[136:139], a[152:155], a[44:47], v[136:139]\n"
"ds_read_b64_tr_b16 a[110:111], v5 offset:776\n"
"v_mfma_f32_16x16x32_bf16 v[140:143], a[156:159], a[44:47], v[140:143]\n"
"ds_read_b64_tr_b16 a[112:113], v5 offset:1024\n"
"v_mfma_f32_16x16x32_bf16 v[132:135], a[156:159], a[40:43], v[132:135]\n"
"ds_read_b64_tr_b16 a[114:115], v5 offset:1280\n"
"v_mfma_f32_16x16x32_bf16 v[204:207], a[156:159], a[36:39], v[204:207]\n"
"ds_read_b64_tr_b16 a[116:117], v5 offset:1032\n"
"v_mfma_f32_16x16x32_bf16 v[196:199], a[156:159], a[32:35], v[196:199]\n"
"ds_read_b64_tr_b16 a[118:119], v5 offset:1288\n"
"v_mfma_f32_16x16x32_bf16 v[76:79], a[156:159], a[28:31], v[76:79]\n"
"ds_read_b64_tr_b16 a[120:121], v5 offset:1536\n"
"v_mfma_f32_16x16x32_bf16 v[68:71], a[156:159], a[24:27], v[68:71]\n"
"ds_read_b64_tr_b16 a[122:123], v5 offset:1792\n"
"s_waitcnt lgkmcnt(6)\n"
"v_mfma_f32_16x16x32_bf16 v[16:19], a[96:99], a[48:51], v[16:19]\n"
"ds_read_b64_tr_b16 a[124:125], v5 offset:1544\n"
"v_mfma_f32_16x16x32_bf16 v[24:27], a[96:99], a[52:55], v[24:27]\n"
"ds_read_b64_tr_b16 a[126:127], v5 offset:1800\n"
"v_mfma_f32_16x16x32_bf16 v[144:147], a[96:99], a[56:59], v[144:147]\n"
"ds_read_b128 a[72:75], v7 offset:53760\n"
"v_mfma_f32_16x16x32_bf16 v[152:155], a[96:99], a[60:63], v[152:155]\n"
"ds_read_b128 a[76:79], v6 offset:55808\n"
"ds_read_b128 a[80:83], v7 offset:57856\n"
"ds_read_b128 a[84:87], v6 offset:59904\n"
"ds_read_b128 a[88:91], v7 offset:61952\n"
"ds_read_b128 a[92:95], v6 offset:64000\n"
"v_mfma_f32_16x16x32_bf16 v[80:83], a[96:99], a[64:67], v[80:83]\n"
"s_waitcnt vmcnt(10) lgkmcnt(0)\n"
"s_barrier\n"
"s_add_u32 m0, s79, 16384\n"
"v_mfma_f32_16x16x32_bf16 v[88:91], a[96:99], a[68:71], v[88:91]\n"
"buffer_load_dwordx4 v2, s[40:43], s74 offen lds\n"
"s_add_u32 m0, s79, 17280\n"
"v_mfma_f32_16x16x32_bf16 v[92:95], a[100:103], a[68:71], v[92:95]\n"
"buffer_load_dwordx4 v2, s[40:43], s74 offen offset:128 lds\n"
"v_mfma_f32_16x16x32_bf16 v[84:87], a[100:103], a[64:67], v[84:87]\n"
"ds_read_b64_tr_b16 a[128:129], v5 offset:8192\n"
"s_add_u32 m0, s79, 98048\n"
"v_mfma_f32_16x16x32_bf16 v[156:159], a[100:103], a[60:63], v[156:159]\n"
"buffer_load_dwordx4 v2, s[40:43], s74 offen offset:256 lds\n"
"v_mfma_f32_16x16x32_bf16 v[148:151], a[100:103], a[56:59], v[148:151]\n"
"ds_read_b64_tr_b16 a[130:131], v5 offset:8448\n"
"s_add_u32 m0, s79, 98944\n"
"v_mfma_f32_16x16x32_bf16 v[28:31], a[100:103], a[52:55], v[28:31]\n"
"buffer_load_dwordx4 v2, s[40:43], s74 offen offset:384 lds\n"
"v_mfma_f32_16x16x32_bf16 v[20:23], a[100:103], a[48:51], v[20:23]\n"
"ds_read_b64_tr_b16 a[132:133], v5 offset:8200\n"
"v_mfma_f32_16x16x32_bf16 v[32:35], a[104:107], a[48:51], v[32:35]\n"
"ds_read_b64_tr_b16 a[134:135], v5 offset:8456\n"
"v_mfma_f32_16x16x32_bf16 v[40:43], a[104:107], a[52:55], v[40:43]\n"
"ds_read_b64_tr_b16 a[136:137], v5 offset:8704\n"
"s_add_u32 m0, s80, 52864\n"
"v_mfma_f32_16x16x32_bf16 v[160:163], a[104:107], a[56:59], v[160:163]\n"
"buffer_load_dwordx4 v1, s[36:39], s68 offen offset:384 lds\n"
"v_mfma_f32_16x16x32_bf16 v[168:171], a[104:107], a[60:63], v[168:171]\n"
"ds_read_b64_tr_b16 a[138:139], v5 offset:8960\n"
"v_mfma_f32_16x16x32_bf16 v[96:99], a[104:107], a[64:67], v[96:99]\n"
"ds_read_b64_tr_b16 a[140:141], v5 offset:8712\n"
"v_mfma_f32_16x16x32_bf16 v[104:107], a[104:107], a[68:71], v[104:107]\n"
"ds_read_b64_tr_b16 a[142:143], v5 offset:8968\n"
"s_add_u32 m0, s80, 54912\n"
"v_mfma_f32_16x16x32_bf16 v[108:111], a[108:111], a[68:71], v[108:111]\n"
"buffer_load_dwordx4 v1, s[36:39], s69 offen offset:384 lds\n"
"v_mfma_f32_16x16x32_bf16 v[100:103], a[108:111], a[64:67], v[100:103]\n"
"ds_read_b64_tr_b16 a[144:145], v5 offset:9216\n"
"v_mfma_f32_16x16x32_bf16 v[172:175], a[108:111], a[60:63], v[172:175]\n"
"ds_read_b64_tr_b16 a[146:147], v5 offset:9472\n"
"v_mfma_f32_16x16x32_bf16 v[164:167], a[108:111], a[56:59], v[164:167]\n"
"ds_read_b64_tr_b16 a[148:149], v5 offset:9224\n"
"s_add_u32 m0, s80, 56960\n"
"v_mfma_f32_16x16x32_bf16 v[44:47], a[108:111], a[52:55], v[44:47]\n"
"buffer_load_dwordx4 v1, s[36:39], s70 offen offset:384 lds\n"
"v_mfma_f32_16x16x32_bf16 v[36:39], a[108:111], a[48:51], v[36:39]\n"
"ds_read_b64_tr_b16 a[150:151], v5 offset:9480\n"
"v_mfma_f32_16x16x32_bf16 v[48:51], a[112:115], a[48:51], v[48:51]\n"
"ds_read_b64_tr_b16 a[152:153], v5 offset:9728\n"
"v_mfma_f32_16x16x32_bf16 v[56:59], a[112:115], a[52:55], v[56:59]\n"
"ds_read_b64_tr_b16 a[154:155], v5 offset:9984\n"
"v_mfma_f32_16x16x32_bf16 v[176:179], a[112:115], a[56:59], v[176:179]\n"
"ds_read_b64_tr_b16 a[156:157], v5 offset:9736\n"
"v_mfma_f32_16x16x32_bf16 v[184:187], a[112:115], a[60:63], v[184:187]\n"
"ds_read_b64_tr_b16 a[158:159], v5 offset:9992\n"
"v_mfma_f32_16x16x32_bf16 v[112:115], a[112:115], a[64:67], v[112:115]\n"
"v_mfma_f32_16x16x32_bf16 v[120:123], a[112:115], a[68:71], v[120:123]\n"
"v_mfma_f32_16x16x32_bf16 v[124:127], a[116:119], a[68:71], v[124:127]\n"
"v_mfma_f32_16x16x32_bf16 v[116:119], a[116:119], a[64:67], v[116:119]\n"
"v_mfma_f32_16x16x32_bf16 v[188:191], a[116:119], a[60:63], v[188:191]\n"
"v_mfma_f32_16x16x32_bf16 v[180:183], a[116:119], a[56:59], v[180:183]\n"
"v_mfma_f32_16x16x32_bf16 v[60:63], a[116:119], a[52:55], v[60:63]\n"
"v_mfma_f32_16x16x32_bf16 v[52:55], a[116:119], a[48:51], v[52:55]\n"
"v_mfma_f32_16x16x32_bf16 v[64:67], a[120:123], a[48:51], v[64:67]\n"
"buffer_load_dword v11, v15, s[52:55], s82 offen\n"
"s_mov_b32 s74, 1572864\n"
"v_mfma_f32_16x16x32_bf16 v[72:75], a[120:123], a[52:55], v[72:75]\n"
"v_mfma_f32_16x16x32_bf16 v[192:195], a[120:123], a[56:59], v[192:195]\n"
"v_mfma_f32_16x16x32_bf16 v[200:203], a[120:123], a[60:63], v[200:203]\n"
"v_mfma_f32_16x16x32_bf16 v[128:131], a[120:123], a[64:67], v[128:131]\n"
"v_mfma_f32_16x16x32_bf16 v[136:139], a[120:123], a[68:71], v[136:139]\n"
"v_mfma_f32_16x16x32_bf16 v[140:143], a[124:127], a[68:71], v[140:143]\n"
"v_mfma_f32_16x16x32_bf16 v[132:135], a[124:127], a[64:67], v[132:135]\n"
"v_mfma_f32_16x16x32_bf16 v[204:207], a[124:127], a[60:63], v[204:207]\n"
"v_mfma_f32_16x16x32_bf16 v[196:199], a[124:127], a[56:59], v[196:199]\n"
"v_mfma_f32_16x16x32_bf16 v[76:79], a[124:127], a[52:55], v[76:79]\n"
"v_mfma_f32_16x16x32_bf16 v[68:71], a[124:127], a[48:51], v[68:71]\n"
"s_waitcnt lgkmcnt(0)\n"
"v_mfma_f32_16x16x32_bf16 v[16:19], a[128:131], a[72:75], v[16:19]\n"
"v_mfma_f32_16x16x32_bf16 v[24:27], a[128:131], a[76:79], v[24:27]\n"
"v_mfma_f32_16x16x32_bf16 v[144:147], a[128:131], a[80:83], v[144:147]\n"
"v_mfma_f32_16x16x32_bf16 v[152:155], a[128:131], a[84:87], v[152:155]\n"
"v_mfma_f32_16x16x32_bf16 v[80:83], a[128:131], a[88:91], v[80:83]\n"
"s_waitcnt vmcnt(8)\n"
"s_barrier\n"
"s_add_u32 m0, s79, 24576\n"
"v_mfma_f32_16x16x32_bf16 v[88:91], a[128:131], a[92:95], v[88:91]\n"
"buffer_load_dwordx4 v2, s[40:43], s74 offen lds\n"
"s_add_u32 m0, s79, 25472\n"
"v_mfma_f32_16x16x32_bf16 v[92:95], a[132:135], a[92:95], v[92:95]\n"
"buffer_load_dwordx4 v2, s[40:43], s74 offen offset:128 lds\n"
"v_add_u32 v10, 1, v12\n"
"v_mfma_f32_16x16x32_bf16 v[84:87], a[132:135], a[88:91], v[84:87]\n"
"ds_read_b64_tr_b16 a[96:97], v5 offset:49152\n"
"s_add_u32 m0, s79, 106240\n"
"v_mfma_f32_16x16x32_bf16 v[156:159], a[132:135], a[84:87], v[156:159]\n"
"buffer_load_dwordx4 v2, s[40:43], s74 offen offset:256 lds\n"
"v_mfma_f32_16x16x32_bf16 v[148:151], a[132:135], a[80:83], v[148:151]\n"
"ds_read_b64_tr_b16 a[98:99], v5 offset:49408\n"
"s_add_u32 m0, s79, 107136\n"
"v_mfma_f32_16x16x32_bf16 v[28:31], a[132:135], a[76:79], v[28:31]\n"
"buffer_load_dwordx4 v2, s[40:43], s74 offen offset:384 lds\n"
"v_mfma_f32_16x16x32_bf16 v[20:23], a[132:135], a[72:75], v[20:23]\n"
"ds_read_b128 a[0:3], v7 offset:40960\n"
"v_mfma_f32_16x16x32_bf16 v[32:35], a[136:139], a[72:75], v[32:35]\n"
"ds_read_b128 a[4:7], v6 offset:43008\n"
"v_mfma_f32_16x16x32_bf16 v[40:43], a[136:139], a[76:79], v[40:43]\n"
"ds_read_b128 a[8:11], v7 offset:45056\n"
"s_add_u32 m0, s80, 59008\n"
"v_mfma_f32_16x16x32_bf16 v[160:163], a[136:139], a[80:83], v[160:163]\n"
"buffer_load_dwordx4 v1, s[36:39], s71 offen offset:384 lds\n"
"v_mfma_f32_16x16x32_bf16 v[168:171], a[136:139], a[84:87], v[168:171]\n"
"ds_read_b128 a[12:15], v6 offset:47104\n"
"v_mfma_f32_16x16x32_bf16 v[96:99], a[136:139], a[88:91], v[96:99]\n"
"ds_read_b128 a[16:19], v7 offset:49152\n"
"v_mfma_f32_16x16x32_bf16 v[104:107], a[136:139], a[92:95], v[104:107]\n"
"ds_read_b128 a[20:23], v6 offset:51200\n"
"s_add_u32 m0, s80, 61056\n"
"v_mfma_f32_16x16x32_bf16 v[108:111], a[140:143], a[92:95], v[108:111]\n"
"buffer_load_dwordx4 v1, s[36:39], s72 offen offset:384 lds\n"
"v_mfma_f32_16x16x32_bf16 v[100:103], a[140:143], a[88:91], v[100:103]\n"
"ds_read_b64_tr_b16 a[100:101], v5 offset:49160\n"
"v_mfma_f32_16x16x32_bf16 v[172:175], a[140:143], a[84:87], v[172:175]\n"
"ds_read_b64_tr_b16 a[102:103], v5 offset:49416\n"
"v_mfma_f32_16x16x32_bf16 v[164:167], a[140:143], a[80:83], v[164:167]\n"
"ds_read_b64_tr_b16 a[104:105], v5 offset:49664\n"
"s_add_u32 m0, s80, 63104\n"
"v_mfma_f32_16x16x32_bf16 v[44:47], a[140:143], a[76:79], v[44:47]\n"
"buffer_load_dwordx4 v1, s[36:39], s73 offen offset:384 lds\n"
"v_mfma_f32_16x16x32_bf16 v[36:39], a[140:143], a[72:75], v[36:39]\n"
"ds_read_b64_tr_b16 a[106:107], v5 offset:49920\n"
"v_mfma_f32_16x16x32_bf16 v[48:51], a[144:147], a[72:75], v[48:51]\n"
"ds_read_b64_tr_b16 a[108:109], v5 offset:49672\n"
"v_mfma_f32_16x16x32_bf16 v[56:59], a[144:147], a[76:79], v[56:59]\n"
"ds_read_b64_tr_b16 a[110:111], v5 offset:49928\n"
"v_mfma_f32_16x16x32_bf16 v[176:179], a[144:147], a[80:83], v[176:179]\n"
"ds_read_b64_tr_b16 a[112:113], v5 offset:50176\n"
"v_mfma_f32_16x16x32_bf16 v[184:187], a[144:147], a[84:87], v[184:187]\n"
"ds_read_b64_tr_b16 a[114:115], v5 offset:50432\n"
"v_mfma_f32_16x16x32_bf16 v[112:115], a[144:147], a[88:91], v[112:115]\n"
"ds_read_b64_tr_b16 a[116:117], v5 offset:50184\n"
"v_mfma_f32_16x16x32_bf16 v[120:123], a[144:147], a[92:95], v[120:123]\n"
"ds_read_b64_tr_b16 a[118:119], v5 offset:50440\n"
"v_mfma_f32_16x16x32_bf16 v[124:127], a[148:151], a[92:95], v[124:127]\n"
"ds_read_b64_tr_b16 a[120:121], v5 offset:50688\n"
"v_mfma_f32_16x16x32_bf16 v[116:119], a[148:151], a[88:91], v[116:119]\n"
"ds_read_b64_tr_b16 a[122:123], v5 offset:50944\n"
"v_mfma_f32_16x16x32_bf16 v[188:191], a[148:151], a[84:87], v[188:191]\n"
"ds_read_b64_tr_b16 a[124:125], v5 offset:50696\n"
"v_mfma_f32_16x16x32_bf16 v[180:183], a[148:151], a[80:83], v[180:183]\n"
"ds_read_b64_tr_b16 a[126:127], v5 offset:50952\n"
"v_mfma_f32_16x16x32_bf16 v[60:63], a[148:151], a[76:79], v[60:63]\n"
"ds_read_b128 a[24:27], v7 offset:41472\n"
"v_mfma_f32_16x16x32_bf16 v[52:55], a[148:151], a[72:75], v[52:55]\n"
"ds_read_b128 a[28:31], v6 offset:43520\n"
"v_mfma_f32_16x16x32_bf16 v[64:67], a[152:155], a[72:75], v[64:67]\n"
"ds_read_b128 a[32:35], v7 offset:45568\n"
"s_mov_b32 s74, 1835008\n"
"v_mfma_f32_16x16x32_bf16 v[72:75], a[152:155], a[76:79], v[72:75]\n"
"ds_read_b128 a[36:39], v6 offset:47616\n"
"v_mfma_f32_16x16x32_bf16 v[192:195], a[152:155], a[80:83], v[192:195]\n"
"ds_read_b128 a[40:43], v7 offset:49664\n"
"v_mfma_f32_16x16x32_bf16 v[200:203], a[152:155], a[84:87], v[200:203]\n"
"ds_read_b128 a[44:47], v6 offset:51712\n"
"v_mfma_f32_16x16x32_bf16 v[128:131], a[152:155], a[88:91], v[128:131]\n"
"v_mfma_f32_16x16x32_bf16 v[136:139], a[152:155], a[92:95], v[136:139]\n"
"v_mfma_f32_16x16x32_bf16 v[140:143], a[156:159], a[92:95], v[140:143]\n"
"v_mfma_f32_16x16x32_bf16 v[132:135], a[156:159], a[88:91], v[132:135]\n"
"v_mfma_f32_16x16x32_bf16 v[204:207], a[156:159], a[84:87], v[204:207]\n"
"v_mfma_f32_16x16x32_bf16 v[196:199], a[156:159], a[80:83], v[196:199]\n"
"v_mfma_f32_16x16x32_bf16 v[76:79], a[156:159], a[76:79], v[76:79]\n"
"v_mfma_f32_16x16x32_bf16 v[68:71], a[156:159], a[72:75], v[68:71]\n"
"s_waitcnt lgkmcnt(0)\n"
"v_mfma_f32_16x16x32_bf16 v[16:19], a[96:99], a[0:3], v[16:19]\n"
"v_mfma_f32_16x16x32_bf16 v[24:27], a[96:99], a[4:7], v[24:27]\n"
"v_mfma_f32_16x16x32_bf16 v[144:147], a[96:99], a[8:11], v[144:147]\n"
"v_mfma_f32_16x16x32_bf16 v[152:155], a[96:99], a[12:15], v[152:155]\n"
"v_mfma_f32_16x16x32_bf16 v[80:83], a[96:99], a[16:19], v[80:83]\n"
"s_waitcnt vmcnt(11)\n"
"s_barrier\n"
"s_add_u32 m0, s79, 65536\n"
"v_mfma_f32_16x16x32_bf16 v[88:91], a[96:99], a[20:23], v[88:91]\n"
"buffer_load_dwordx4 v2, s[40:43], s74 offen lds\n"
"s_add_u32 m0, s79, 66432\n"
"v_mfma_f32_16x16x32_bf16 v[92:95], a[100:103], a[20:23], v[92:95]\n"
"buffer_load_dwordx4 v2, s[40:43], s74 offen offset:128 lds\n"
"v_mfma_f32_16x16x32_bf16 v[84:87], a[100:103], a[16:19], v[84:87]\n"
"ds_read_b64_tr_b16 a[128:129], v5 offset:0\n"
"s_add_u32 m0, s79, 147200\n"
"v_mfma_f32_16x16x32_bf16 v[156:159], a[100:103], a[12:15], v[156:159]\n"
"buffer_load_dwordx4 v2, s[40:43], s74 offen offset:256 lds\n"
"v_mfma_f32_16x16x32_bf16 v[148:151], a[100:103], a[8:11], v[148:151]\n"
"ds_read_b64_tr_b16 a[130:131], v5 offset:256\n"
"s_add_u32 m0, s79, 148096\n"
"v_mfma_f32_16x16x32_bf16 v[28:31], a[100:103], a[4:7], v[28:31]\n"
"buffer_load_dwordx4 v2, s[40:43], s74 offen offset:384 lds\n"
"v_mfma_f32_16x16x32_bf16 v[20:23], a[100:103], a[0:3], v[20:23]\n"
"ds_read_b64_tr_b16 a[132:133], v5 offset:8\n"
"v_mfma_f32_16x16x32_bf16 v[32:35], a[104:107], a[0:3], v[32:35]\n"
"ds_read_b64_tr_b16 a[134:135], v5 offset:264\n"
"v_mfma_f32_16x16x32_bf16 v[40:43], a[104:107], a[4:7], v[40:43]\n"
"ds_read_b64_tr_b16 a[136:137], v5 offset:512\n"
"s_add_u32 m0, s80, 40448\n"
"v_mfma_f32_16x16x32_bf16 v[160:163], a[104:107], a[8:11], v[160:163]\n"
"buffer_load_dwordx4 v1, s[36:39], s68 offen offset:512 lds\n"
"v_mfma_f32_16x16x32_bf16 v[168:171], a[104:107], a[12:15], v[168:171]\n"
"ds_read_b64_tr_b16 a[138:139], v5 offset:768\n"
"v_mfma_f32_16x16x32_bf16 v[96:99], a[104:107], a[16:19], v[96:99]\n"
"ds_read_b64_tr_b16 a[140:141], v5 offset:520\n"
"v_mfma_f32_16x16x32_bf16 v[104:107], a[104:107], a[20:23], v[104:107]\n"
"ds_read_b64_tr_b16 a[142:143], v5 offset:776\n"
"s_add_u32 m0, s80, 42496\n"
"v_mfma_f32_16x16x32_bf16 v[108:111], a[108:111], a[20:23], v[108:111]\n"
"buffer_load_dwordx4 v1, s[36:39], s69 offen offset:512 lds\n"
"v_mfma_f32_16x16x32_bf16 v[100:103], a[108:111], a[16:19], v[100:103]\n"
"ds_read_b64_tr_b16 a[144:145], v5 offset:1024\n"
"v_mfma_f32_16x16x32_bf16 v[172:175], a[108:111], a[12:15], v[172:175]\n"
"ds_read_b64_tr_b16 a[146:147], v5 offset:1280\n"
"v_mfma_f32_16x16x32_bf16 v[164:167], a[108:111], a[8:11], v[164:167]\n"
"ds_read_b64_tr_b16 a[148:149], v5 offset:1032\n"
"s_add_u32 m0, s80, 44544\n"
"v_mfma_f32_16x16x32_bf16 v[44:47], a[108:111], a[4:7], v[44:47]\n"
"buffer_load_dwordx4 v1, s[36:39], s70 offen offset:512 lds\n"
"v_mfma_f32_16x16x32_bf16 v[36:39], a[108:111], a[0:3], v[36:39]\n"
"ds_read_b64_tr_b16 a[150:151], v5 offset:1288\n"
"v_mfma_f32_16x16x32_bf16 v[48:51], a[112:115], a[0:3], v[48:51]\n"
"ds_read_b64_tr_b16 a[152:153], v5 offset:1536\n"
"v_mfma_f32_16x16x32_bf16 v[56:59], a[112:115], a[4:7], v[56:59]\n"
"ds_read_b64_tr_b16 a[154:155], v5 offset:1792\n"
"v_mfma_f32_16x16x32_bf16 v[176:179], a[112:115], a[8:11], v[176:179]\n"
"ds_read_b64_tr_b16 a[156:157], v5 offset:1544\n"
"v_mfma_f32_16x16x32_bf16 v[184:187], a[112:115], a[12:15], v[184:187]\n"
"ds_read_b64_tr_b16 a[158:159], v5 offset:1800\n"
"v_mfma_f32_16x16x32_bf16 v[112:115], a[112:115], a[16:19], v[112:115]\n"
"v_mfma_f32_16x16x32_bf16 v[120:123], a[112:115], a[20:23], v[120:123]\n"
"v_mfma_f32_16x16x32_bf16 v[124:127], a[116:119], a[20:23], v[124:127]\n"
"v_mfma_f32_16x16x32_bf16 v[116:119], a[116:119], a[16:19], v[116:119]\n"
"v_mfma_f32_16x16x32_bf16 v[188:191], a[116:119], a[12:15], v[188:191]\n"
"v_mfma_f32_16x16x32_bf16 v[180:183], a[116:119], a[8:11], v[180:183]\n"
"v_mfma_f32_16x16x32_bf16 v[60:63], a[116:119], a[4:7], v[60:63]\n"
"v_mfma_f32_16x16x32_bf16 v[52:55], a[116:119], a[0:3], v[52:55]\n"
"v_mfma_f32_16x16x32_bf16 v[64:67], a[120:123], a[0:3], v[64:67]\n"
"s_mov_b32 s74, 2097152\n"
"v_mfma_f32_16x16x32_bf16 v[72:75], a[120:123], a[4:7], v[72:75]\n"
"v_mfma_f32_16x16x32_bf16 v[192:195], a[120:123], a[8:11], v[192:195]\n"
"v_mfma_f32_16x16x32_bf16 v[200:203], a[120:123], a[12:15], v[200:203]\n"
"v_mfma_f32_16x16x32_bf16 v[128:131], a[120:123], a[16:19], v[128:131]\n"
"v_mfma_f32_16x16x32_bf16 v[136:139], a[120:123], a[20:23], v[136:139]\n"
"v_mfma_f32_16x16x32_bf16 v[140:143], a[124:127], a[20:23], v[140:143]\n"
"v_mfma_f32_16x16x32_bf16 v[132:135], a[124:127], a[16:19], v[132:135]\n"
"v_mfma_f32_16x16x32_bf16 v[204:207], a[124:127], a[12:15], v[204:207]\n"
"v_mfma_f32_16x16x32_bf16 v[196:199], a[124:127], a[8:11], v[196:199]\n"
"v_mfma_f32_16x16x32_bf16 v[76:79], a[124:127], a[4:7], v[76:79]\n"
"v_mfma_f32_16x16x32_bf16 v[68:71], a[124:127], a[0:3], v[68:71]\n"
"s_waitcnt lgkmcnt(0)\n"
"v_mfma_f32_16x16x32_bf16 v[16:19], a[128:131], a[24:27], v[16:19]\n"
"v_mfma_f32_16x16x32_bf16 v[24:27], a[128:131], a[28:31], v[24:27]\n"
"v_mfma_f32_16x16x32_bf16 v[144:147], a[128:131], a[32:35], v[144:147]\n"
"v_mfma_f32_16x16x32_bf16 v[152:155], a[128:131], a[36:39], v[152:155]\n"
"v_mfma_f32_16x16x32_bf16 v[80:83], a[128:131], a[40:43], v[80:83]\n"
"s_waitcnt vmcnt(7)\n"
"s_barrier\n"
"s_add_u32 m0, s79, 16384\n"
"v_mfma_f32_16x16x32_bf16 v[88:91], a[128:131], a[44:47], v[88:91]\n"
"buffer_load_dwordx4 v2, s[40:43], s74 offen lds\n"
"s_add_u32 m0, s79, 17280\n"
"v_mfma_f32_16x16x32_bf16 v[92:95], a[132:135], a[44:47], v[92:95]\n"
"buffer_load_dwordx4 v2, s[40:43], s74 offen offset:128 lds\n"
"v_mfma_f32_16x16x32_bf16 v[84:87], a[132:135], a[40:43], v[84:87]\n"
"ds_read_b64_tr_b16 a[96:97], v5 offset:8192\n"
"s_add_u32 m0, s79, 98048\n"
"v_mfma_f32_16x16x32_bf16 v[156:159], a[132:135], a[36:39], v[156:159]\n"
"buffer_load_dwordx4 v2, s[40:43], s74 offen offset:256 lds\n"
"v_mfma_f32_16x16x32_bf16 v[148:151], a[132:135], a[32:35], v[148:151]\n"
"ds_read_b64_tr_b16 a[98:99], v5 offset:8448\n"
"s_add_u32 m0, s79, 98944\n"
"v_mfma_f32_16x16x32_bf16 v[28:31], a[132:135], a[28:31], v[28:31]\n"
"buffer_load_dwordx4 v2, s[40:43], s74 offen offset:384 lds\n"
"v_mfma_f32_16x16x32_bf16 v[20:23], a[132:135], a[24:27], v[20:23]\n"
"ds_read_b128 a[48:51], v7 offset:53248\n"
"v_mfma_f32_16x16x32_bf16 v[32:35], a[136:139], a[24:27], v[32:35]\n"
"ds_read_b128 a[52:55], v6 offset:55296\n"
"v_mfma_f32_16x16x32_bf16 v[40:43], a[136:139], a[28:31], v[40:43]\n"
"ds_read_b128 a[56:59], v7 offset:57344\n"
"s_add_u32 m0, s80, 46592\n"
"v_mfma_f32_16x16x32_bf16 v[160:163], a[136:139], a[32:35], v[160:163]\n"
"buffer_load_dwordx4 v1, s[36:39], s71 offen offset:512 lds\n"
"v_mfma_f32_16x16x32_bf16 v[168:171], a[136:139], a[36:39], v[168:171]\n"
"ds_read_b128 a[60:63], v6 offset:59392\n"
"v_mfma_f32_16x16x32_bf16 v[96:99], a[136:139], a[40:43], v[96:99]\n"
"ds_read_b128 a[64:67], v7 offset:61440\n"
"v_mfma_f32_16x16x32_bf16 v[104:107], a[136:139], a[44:47], v[104:107]\n"
"ds_read_b128 a[68:71], v6 offset:63488\n"
"s_add_u32 m0, s80, 48640\n"
"v_mfma_f32_16x16x32_bf16 v[108:111], a[140:143], a[44:47], v[108:111]\n"
"buffer_load_dwordx4 v1, s[36:39], s72 offen offset:512 lds\n"
"v_mfma_f32_16x16x32_bf16 v[100:103], a[140:143], a[40:43], v[100:103]\n"
"ds_read_b64_tr_b16 a[100:101], v5 offset:8200\n"
"v_mfma_f32_16x16x32_bf16 v[172:175], a[140:143], a[36:39], v[172:175]\n"
"ds_read_b64_tr_b16 a[102:103], v5 offset:8456\n"
"v_mfma_f32_16x16x32_bf16 v[164:167], a[140:143], a[32:35], v[164:167]\n"
"ds_read_b64_tr_b16 a[104:105], v5 offset:8704\n"
"s_add_u32 m0, s80, 50688\n"
"v_mfma_f32_16x16x32_bf16 v[44:47], a[140:143], a[28:31], v[44:47]\n"
"buffer_load_dwordx4 v1, s[36:39], s73 offen offset:512 lds\n"
"v_mfma_f32_16x16x32_bf16 v[36:39], a[140:143], a[24:27], v[36:39]\n"
"ds_read_b64_tr_b16 a[106:107], v5 offset:8960\n"
"v_mfma_f32_16x16x32_bf16 v[48:51], a[144:147], a[24:27], v[48:51]\n"
"ds_read_b64_tr_b16 a[108:109], v5 offset:8712\n"
"v_mfma_f32_16x16x32_bf16 v[56:59], a[144:147], a[28:31], v[56:59]\n"
"ds_read_b64_tr_b16 a[110:111], v5 offset:8968\n"
"v_mfma_f32_16x16x32_bf16 v[176:179], a[144:147], a[32:35], v[176:179]\n"
"ds_read_b64_tr_b16 a[112:113], v5 offset:9216\n"
"v_mfma_f32_16x16x32_bf16 v[184:187], a[144:147], a[36:39], v[184:187]\n"
"ds_read_b64_tr_b16 a[114:115], v5 offset:9472\n"
"v_mfma_f32_16x16x32_bf16 v[112:115], a[144:147], a[40:43], v[112:115]\n"
"ds_read_b64_tr_b16 a[116:117], v5 offset:9224\n"
"v_mfma_f32_16x16x32_bf16 v[120:123], a[144:147], a[44:47], v[120:123]\n"
"ds_read_b64_tr_b16 a[118:119], v5 offset:9480\n"
"v_mfma_f32_16x16x32_bf16 v[124:127], a[148:151], a[44:47], v[124:127]\n"
"ds_read_b64_tr_b16 a[120:121], v5 offset:9728\n"
"v_mfma_f32_16x16x32_bf16 v[116:119], a[148:151], a[40:43], v[116:119]\n"
"ds_read_b64_tr_b16 a[122:123], v5 offset:9984\n"
"v_mfma_f32_16x16x32_bf16 v[188:191], a[148:151], a[36:39], v[188:191]\n"
"ds_read_b64_tr_b16 a[124:125], v5 offset:9736\n"
"v_mfma_f32_16x16x32_bf16 v[180:183], a[148:151], a[32:35], v[180:183]\n"
"ds_read_b64_tr_b16 a[126:127], v5 offset:9992\n"
"v_mfma_f32_16x16x32_bf16 v[60:63], a[148:151], a[28:31], v[60:63]\n"
"ds_read_b128 a[72:75], v7 offset:53760\n"
"v_mfma_f32_16x16x32_bf16 v[52:55], a[148:151], a[24:27], v[52:55]\n"
"ds_read_b128 a[76:79], v6 offset:55808\n"
"v_mfma_f32_16x16x32_bf16 v[64:67], a[152:155], a[24:27], v[64:67]\n"
"buffer_load_dword v11, v15, s[52:55], s83 offen\n"
"s_mov_b32 s74, 2359296\n"
"v_mfma_f32_16x16x32_bf16 v[72:75], a[152:155], a[28:31], v[72:75]\n"
"ds_read_b128 a[80:83], v7 offset:57856\n"
"v_mfma_f32_16x16x32_bf16 v[192:195], a[152:155], a[32:35], v[192:195]\n"
"ds_read_b128 a[84:87], v6 offset:59904\n"
"v_mfma_f32_16x16x32_bf16 v[200:203], a[152:155], a[36:39], v[200:203]\n"
"ds_read_b128 a[88:91], v7 offset:61952\n"
"v_mfma_f32_16x16x32_bf16 v[128:131], a[152:155], a[40:43], v[128:131]\n"
"ds_read_b128 a[92:95], v6 offset:64000\n"
"v_mfma_f32_16x16x32_bf16 v[136:139], a[152:155], a[44:47], v[136:139]\n"
"v_mfma_f32_16x16x32_bf16 v[140:143], a[156:159], a[44:47], v[140:143]\n"
"v_mfma_f32_16x16x32_bf16 v[132:135], a[156:159], a[40:43], v[132:135]\n"
"v_mfma_f32_16x16x32_bf16 v[204:207], a[156:159], a[36:39], v[204:207]\n"
"v_mfma_f32_16x16x32_bf16 v[196:199], a[156:159], a[32:35], v[196:199]\n"
"v_mfma_f32_16x16x32_bf16 v[76:79], a[156:159], a[28:31], v[76:79]\n"
"v_mfma_f32_16x16x32_bf16 v[68:71], a[156:159], a[24:27], v[68:71]\n"
"s_waitcnt lgkmcnt(0)\n"
"v_mfma_f32_16x16x32_bf16 v[16:19], a[96:99], a[48:51], v[16:19]\n"
"v_mfma_f32_16x16x32_bf16 v[24:27], a[96:99], a[52:55], v[24:27]\n"
"v_mfma_f32_16x16x32_bf16 v[144:147], a[96:99], a[56:59], v[144:147]\n"
"v_mfma_f32_16x16x32_bf16 v[152:155], a[96:99], a[60:63], v[152:155]\n"
"v_mfma_f32_16x16x32_bf16 v[80:83], a[96:99], a[64:67], v[80:83]\n"
"s_waitcnt vmcnt(11)\n"
"s_barrier\n"
"s_add_u32 m0, s79, 24576\n"
"v_mfma_f32_16x16x32_bf16 v[88:91], a[96:99], a[68:71], v[88:91]\n"
"buffer_load_dwordx4 v2, s[40:43], s74 offen lds\n"
"s_add_u32 m0, s79, 25472\n"
"v_mfma_f32_16x16x32_bf16 v[92:95], a[100:103], a[68:71], v[92:95]\n"
"buffer_load_dwordx4 v2, s[40:43], s74 offen offset:128 lds\n"
"v_mfma_f32_16x16x32_bf16 v[84:87], a[100:103], a[64:67], v[84:87]\n"
"ds_read_b64_tr_b16 a[128:129], v5 offset:49152\n"
"s_add_u32 m0, s79, 106240\n"
"v_mfma_f32_16x16x32_bf16 v[156:159], a[100:103], a[60:63], v[156:159]\n"
"buffer_load_dwordx4 v2, s[40:43], s74 offen offset:256 lds\n"
"v_mfma_f32_16x16x32_bf16 v[148:151], a[100:103], a[56:59], v[148:151]\n"
"ds_read_b64_tr_b16 a[130:131], v5 offset:49408\n"
"s_add_u32 m0, s79, 107136\n"
"v_mfma_f32_16x16x32_bf16 v[28:31], a[100:103], a[52:55], v[28:31]\n"
"buffer_load_dwordx4 v2, s[40:43], s74 offen offset:384 lds\n"
"v_mfma_f32_16x16x32_bf16 v[20:23], a[100:103], a[48:51], v[20:23]\n"
"ds_read_b64_tr_b16 a[132:133], v5 offset:49160\n"
"v_mfma_f32_16x16x32_bf16 v[32:35], a[104:107], a[48:51], v[32:35]\n"
"ds_read_b64_tr_b16 a[134:135], v5 offset:49416\n"
"v_mfma_f32_16x16x32_bf16 v[40:43], a[104:107], a[52:55], v[40:43]\n"
"ds_read_b64_tr_b16 a[136:137], v5 offset:49664\n"
"s_add_u32 m0, s80, 52608\n"
"v_mfma_f32_16x16x32_bf16 v[160:163], a[104:107], a[56:59], v[160:163]\n"
"buffer_load_dwordx4 v1, s[36:39], s68 offen offset:640 lds\n"
"v_mfma_f32_16x16x32_bf16 v[168:171], a[104:107], a[60:63], v[168:171]\n"
"ds_read_b64_tr_b16 a[138:139], v5 offset:49920\n"
"v_mfma_f32_16x16x32_bf16 v[96:99], a[104:107], a[64:67], v[96:99]\n"
"ds_read_b64_tr_b16 a[140:141], v5 offset:49672\n"
"v_mfma_f32_16x16x32_bf16 v[104:107], a[104:107], a[68:71], v[104:107]\n"
"ds_read_b64_tr_b16 a[142:143], v5 offset:49928\n"
"s_add_u32 m0, s80, 54656\n"
"v_mfma_f32_16x16x32_bf16 v[108:111], a[108:111], a[68:71], v[108:111]\n"
"buffer_load_dwordx4 v1, s[36:39], s69 offen offset:640 lds\n"
"v_mfma_f32_16x16x32_bf16 v[100:103], a[108:111], a[64:67], v[100:103]\n"
"ds_read_b64_tr_b16 a[144:145], v5 offset:50176\n"
"v_mfma_f32_16x16x32_bf16 v[172:175], a[108:111], a[60:63], v[172:175]\n"
"ds_read_b64_tr_b16 a[146:147], v5 offset:50432\n"
"v_mfma_f32_16x16x32_bf16 v[164:167], a[108:111], a[56:59], v[164:167]\n"
"ds_read_b64_tr_b16 a[148:149], v5 offset:50184\n"
"s_add_u32 m0, s80, 56704\n"
"v_mfma_f32_16x16x32_bf16 v[44:47], a[108:111], a[52:55], v[44:47]\n"
"buffer_load_dwordx4 v1, s[36:39], s70 offen offset:640 lds\n"
"v_mfma_f32_16x16x32_bf16 v[36:39], a[108:111], a[48:51], v[36:39]\n"
"ds_read_b64_tr_b16 a[150:151], v5 offset:50440\n"
"v_mfma_f32_16x16x32_bf16 v[48:51], a[112:115], a[48:51], v[48:51]\n"
"ds_read_b64_tr_b16 a[152:153], v5 offset:50688\n"
"v_mfma_f32_16x16x32_bf16 v[56:59], a[112:115], a[52:55], v[56:59]\n"
"ds_read_b64_tr_b16 a[154:155], v5 offset:50944\n"
"v_mfma_f32_16x16x32_bf16 v[176:179], a[112:115], a[56:59], v[176:179]\n"
"ds_read_b64_tr_b16 a[156:157], v5 offset:50696\n"
"v_mfma_f32_16x16x32_bf16 v[184:187], a[112:115], a[60:63], v[184:187]\n"
"ds_read_b64_tr_b16 a[158:159], v5 offset:50952\n"
"v_mfma_f32_16x16x32_bf16 v[112:115], a[112:115], a[64:67], v[112:115]\n"
"v_mfma_f32_16x16x32_bf16 v[120:123], a[112:115], a[68:71], v[120:123]\n"
"v_mfma_f32_16x16x32_bf16 v[124:127], a[116:119], a[68:71], v[124:127]\n"
"v_mfma_f32_16x16x32_bf16 v[116:119], a[116:119], a[64:67], v[116:119]\n"
"v_mfma_f32_16x16x32_bf16 v[188:191], a[116:119], a[60:63], v[188:191]\n"
"v_mfma_f32_16x16x32_bf16 v[180:183], a[116:119], a[56:59], v[180:183]\n"
"v_mfma_f32_16x16x32_bf16 v[60:63], a[116:119], a[52:55], v[60:63]\n"
"v_mfma_f32_16x16x32_bf16 v[52:55], a[116:119], a[48:51], v[52:55]\n"
"v_mfma_f32_16x16x32_bf16 v[64:67], a[120:123], a[48:51], v[64:67]\n"
"s_mov_b32 s74, 2621440\n"
"v_mfma_f32_16x16x32_bf16 v[72:75], a[120:123], a[52:55], v[72:75]\n"
"v_mfma_f32_16x16x32_bf16 v[192:195], a[120:123], a[56:59], v[192:195]\n"
"v_mfma_f32_16x16x32_bf16 v[200:203], a[120:123], a[60:63], v[200:203]\n"
"v_mfma_f32_16x16x32_bf16 v[128:131], a[120:123], a[64:67], v[128:131]\n"
"v_mfma_f32_16x16x32_bf16 v[136:139], a[120:123], a[68:71], v[136:139]\n"
"v_mfma_f32_16x16x32_bf16 v[140:143], a[124:127], a[68:71], v[140:143]\n"
"v_mfma_f32_16x16x32_bf16 v[132:135], a[124:127], a[64:67], v[132:135]\n"
"v_mfma_f32_16x16x32_bf16 v[204:207], a[124:127], a[60:63], v[204:207]\n"
"v_mfma_f32_16x16x32_bf16 v[196:199], a[124:127], a[56:59], v[196:199]\n"
"v_mfma_f32_16x16x32_bf16 v[76:79], a[124:127], a[52:55], v[76:79]\n"
"v_mfma_f32_16x16x32_bf16 v[68:71], a[124:127], a[48:51], v[68:71]\n"
"s_waitcnt lgkmcnt(0)\n"
"v_mfma_f32_16x16x32_bf16 v[16:19], a[128:131], a[72:75], v[16:19]\n"
"v_mfma_f32_16x16x32_bf16 v[24:27], a[128:131], a[76:79], v[24:27]\n"
"v_mfma_f32_16x16x32_bf16 v[144:147], a[128:131], a[80:83], v[144:147]\n"
"v_mfma_f32_16x16x32_bf16 v[152:155], a[128:131], a[84:87], v[152:155]\n"
"v_mfma_f32_16x16x32_bf16 v[80:83], a[128:131], a[88:91], v[80:83]\n"
"s_waitcnt vmcnt(8)\n"
"s_barrier\n"
"s_add_u32 m0, s79, 65536\n"
"v_mfma_f32_16x16x32_bf16 v[88:91], a[128:131], a[92:95], v[88:91]\n"
"buffer_load_dwordx4 v2, s[40:43], s74 offen lds\n"
"s_add_u32 m0, s79, 66432\n"
"v_mfma_f32_16x16x32_bf16 v[92:95], a[132:135], a[92:95], v[92:95]\n"
"buffer_load_dwordx4 v2, s[40:43], s74 offen offset:128 lds\n"
"v_mfma_f32_16x16x32_bf16 v[84:87], a[132:135], a[88:91], v[84:87]\n"
"ds_read_b64_tr_b16 a[96:97], v5 offset:0\n"
"s_add_u32 m0, s79, 147200\n"
"v_mfma_f32_16x16x32_bf16 v[156:159], a[132:135], a[84:87], v[156:159]\n"
"buffer_load_dwordx4 v2, s[40:43], s74 offen offset:256 lds\n"
"v_mfma_f32_16x16x32_bf16 v[148:151], a[132:135], a[80:83], v[148:151]\n"
"ds_read_b64_tr_b16 a[98:99], v5 offset:256\n"
"s_add_u32 m0, s79, 148096\n"
"v_mfma_f32_16x16x32_bf16 v[28:31], a[132:135], a[76:79], v[28:31]\n"
"buffer_load_dwordx4 v2, s[40:43], s74 offen offset:384 lds\n"
"v_mfma_f32_16x16x32_bf16 v[20:23], a[132:135], a[72:75], v[20:23]\n"
"ds_read_b128 a[0:3], v7 offset:40960\n"
"v_mfma_f32_16x16x32_bf16 v[32:35], a[136:139], a[72:75], v[32:35]\n"
"ds_read_b128 a[4:7], v6 offset:43008\n"
"v_mfma_f32_16x16x32_bf16 v[40:43], a[136:139], a[76:79], v[40:43]\n"
"ds_read_b128 a[8:11], v7 offset:45056\n"
"s_add_u32 m0, s80, 58752\n"
"v_mfma_f32_16x16x32_bf16 v[160:163], a[136:139], a[80:83], v[160:163]\n"
"buffer_load_dwordx4 v1, s[36:39], s71 offen offset:640 lds\n"
"v_mfma_f32_16x16x32_bf16 v[168:171], a[136:139], a[84:87], v[168:171]\n"
"ds_read_b128 a[12:15], v6 offset:47104\n"
"v_mfma_f32_16x16x32_bf16 v[96:99], a[136:139], a[88:91], v[96:99]\n"
"ds_read_b128 a[16:19], v7 offset:49152\n"
"v_mfma_f32_16x16x32_bf16 v[104:107], a[136:139], a[92:95], v[104:107]\n"
"ds_read_b128 a[20:23], v6 offset:51200\n"
"s_add_u32 m0, s80, 60800\n"
"v_mfma_f32_16x16x32_bf16 v[108:111], a[140:143], a[92:95], v[108:111]\n"
"buffer_load_dwordx4 v1, s[36:39], s72 offen offset:640 lds\n"
"v_mfma_f32_16x16x32_bf16 v[100:103], a[140:143], a[88:91], v[100:103]\n"
"ds_read_b64_tr_b16 a[100:101], v5 offset:8\n"
"v_mfma_f32_16x16x32_bf16 v[172:175], a[140:143], a[84:87], v[172:175]\n"
"ds_read_b64_tr_b16 a[102:103], v5 offset:264\n"
"v_mfma_f32_16x16x32_bf16 v[164:167], a[140:143], a[80:83], v[164:167]\n"
"ds_read_b64_tr_b16 a[104:105], v5 offset:512\n"
"s_add_u32 m0, s80, 62848\n"
"v_mfma_f32_16x16x32_bf16 v[44:47], a[140:143], a[76:79], v[44:47]\n"
"buffer_load_dwordx4 v1, s[36:39], s73 offen offset:640 lds\n"
"v_mfma_f32_16x16x32_bf16 v[36:39], a[140:143], a[72:75], v[36:39]\n"
"ds_read_b64_tr_b16 a[106:107], v5 offset:768\n"
"v_mfma_f32_16x16x32_bf16 v[48:51], a[144:147], a[72:75], v[48:51]\n"
"ds_read_b64_tr_b16 a[108:109], v5 offset:520\n"
"v_mfma_f32_16x16x32_bf16 v[56:59], a[144:147], a[76:79], v[56:59]\n"
"ds_read_b64_tr_b16 a[110:111], v5 offset:776\n"
"v_mfma_f32_16x16x32_bf16 v[176:179], a[144:147], a[80:83], v[176:179]\n"
"ds_read_b64_tr_b16 a[112:113], v5 offset:1024\n"
"v_mfma_f32_16x16x32_bf16 v[184:187], a[144:147], a[84:87], v[184:187]\n"
"ds_read_b64_tr_b16 a[114:115], v5 offset:1280\n"
"v_mfma_f32_16x16x32_bf16 v[112:115], a[144:147], a[88:91], v[112:115]\n"
"ds_read_b64_tr_b16 a[116:117], v5 offset:1032\n"
"v_mfma_f32_16x16x32_bf16 v[120:123], a[144:147], a[92:95], v[120:123]\n"
"ds_read_b64_tr_b16 a[118:119], v5 offset:1288\n"
"v_mfma_f32_16x16x32_bf16 v[124:127], a[148:151], a[92:95], v[124:127]\n"
"ds_read_b64_tr_b16 a[120:121], v5 offset:1536\n"
"v_mfma_f32_16x16x32_bf16 v[116:119], a[148:151], a[88:91], v[116:119]\n"
"ds_read_b64_tr_b16 a[122:123], v5 offset:1792\n"
"v_mfma_f32_16x16x32_bf16 v[188:191], a[148:151], a[84:87], v[188:191]\n"
"ds_read_b64_tr_b16 a[124:125], v5 offset:1544\n"
"v_mfma_f32_16x16x32_bf16 v[180:183], a[148:151], a[80:83], v[180:183]\n"
"ds_read_b64_tr_b16 a[126:127], v5 offset:1800\n"
"v_mfma_f32_16x16x32_bf16 v[60:63], a[148:151], a[76:79], v[60:63]\n"
"ds_read_b128 a[24:27], v7 offset:41472\n"
"v_mfma_f32_16x16x32_bf16 v[52:55], a[148:151], a[72:75], v[52:55]\n"
"ds_read_b128 a[28:31], v6 offset:43520\n"
"v_mfma_f32_16x16x32_bf16 v[64:67], a[152:155], a[72:75], v[64:67]\n"
"ds_read_b128 a[32:35], v7 offset:45568\n"
"s_mov_b32 s74, 2883584\n"
"v_mfma_f32_16x16x32_bf16 v[72:75], a[152:155], a[76:79], v[72:75]\n"
"ds_read_b128 a[36:39], v6 offset:47616\n"
"v_mfma_f32_16x16x32_bf16 v[192:195], a[152:155], a[80:83], v[192:195]\n"
"ds_read_b128 a[40:43], v7 offset:49664\n"
"v_mfma_f32_16x16x32_bf16 v[200:203], a[152:155], a[84:87], v[200:203]\n"
"ds_read_b128 a[44:47], v6 offset:51712\n"
"v_mfma_f32_16x16x32_bf16 v[128:131], a[152:155], a[88:91], v[128:131]\n"
"v_mfma_f32_16x16x32_bf16 v[136:139], a[152:155], a[92:95], v[136:139]\n"
"v_mfma_f32_16x16x32_bf16 v[140:143], a[156:159], a[92:95], v[140:143]\n"
"v_mfma_f32_16x16x32_bf16 v[132:135], a[156:159], a[88:91], v[132:135]\n"
"v_mfma_f32_16x16x32_bf16 v[204:207], a[156:159], a[84:87], v[204:207]\n"
"v_mfma_f32_16x16x32_bf16 v[196:199], a[156:159], a[80:83], v[196:199]\n"
"v_mfma_f32_16x16x32_bf16 v[76:79], a[156:159], a[76:79], v[76:79]\n"
"v_mfma_f32_16x16x32_bf16 v[68:71], a[156:159], a[72:75], v[68:71]\n"
"s_waitcnt lgkmcnt(0)\n"
"v_mfma_f32_16x16x32_bf16 v[16:19], a[96:99], a[0:3], v[16:19]\n"
"v_mfma_f32_16x16x32_bf16 v[24:27], a[96:99], a[4:7], v[24:27]\n"
"v_mfma_f32_16x16x32_bf16 v[144:147], a[96:99], a[8:11], v[144:147]\n"
"v_mfma_f32_16x16x32_bf16 v[152:155], a[96:99], a[12:15], v[152:155]\n"
"v_mfma_f32_16x16x32_bf16 v[80:83], a[96:99], a[16:19], v[80:83]\n"
"s_waitcnt vmcnt(10)\n"
"s_barrier\n"
"s_add_u32 m0, s79, 16384\n"
"v_mfma_f32_16x16x32_bf16 v[88:91], a[96:99], a[20:23], v[88:91]\n"
"buffer_load_dwordx4 v2, s[40:43], s74 offen lds\n"
"s_add_u32 m0, s79, 17280\n"
"v_mfma_f32_16x16x32_bf16 v[92:95], a[100:103], a[20:23], v[92:95]\n"
"buffer_load_dwordx4 v2, s[40:43], s74 offen offset:128 lds\n"
"v_mfma_f32_16x16x32_bf16 v[84:87], a[100:103], a[16:19], v[84:87]\n"
"ds_read_b64_tr_b16 a[128:129], v5 offset:8192\n"
"s_add_u32 m0, s79, 98048\n"
"v_mfma_f32_16x16x32_bf16 v[156:159], a[100:103], a[12:15], v[156:159]\n"
"buffer_load_dwordx4 v2, s[40:43], s74 offen offset:256 lds\n"
"v_mfma_f32_16x16x32_bf16 v[148:151], a[100:103], a[8:11], v[148:151]\n"
"ds_read_b64_tr_b16 a[130:131], v5 offset:8448\n"
"s_add_u32 m0, s79, 98944\n"
"v_mfma_f32_16x16x32_bf16 v[28:31], a[100:103], a[4:7], v[28:31]\n"
"buffer_load_dwordx4 v2, s[40:43], s74 offen offset:384 lds\n"
"v_mfma_f32_16x16x32_bf16 v[20:23], a[100:103], a[0:3], v[20:23]\n"
"ds_read_b64_tr_b16 a[132:133], v5 offset:8200\n"
"v_mfma_f32_16x16x32_bf16 v[32:35], a[104:107], a[0:3], v[32:35]\n"
"ds_read_b64_tr_b16 a[134:135], v5 offset:8456\n"
"v_mfma_f32_16x16x32_bf16 v[40:43], a[104:107], a[4:7], v[40:43]\n"
"ds_read_b64_tr_b16 a[136:137], v5 offset:8704\n"
"s_add_u32 m0, s80, 40192\n"
"v_mfma_f32_16x16x32_bf16 v[160:163], a[104:107], a[8:11], v[160:163]\n"
"buffer_load_dwordx4 v1, s[36:39], s68 offen offset:768 lds\n"
"v_mfma_f32_16x16x32_bf16 v[168:171], a[104:107], a[12:15], v[168:171]\n"
"ds_read_b64_tr_b16 a[138:139], v5 offset:8960\n"
"v_mfma_f32_16x16x32_bf16 v[96:99], a[104:107], a[16:19], v[96:99]\n"
"ds_read_b64_tr_b16 a[140:141], v5 offset:8712\n"
"v_mfma_f32_16x16x32_bf16 v[104:107], a[104:107], a[20:23], v[104:107]\n"
"ds_read_b64_tr_b16 a[142:143], v5 offset:8968\n"
"s_add_u32 m0, s80, 42240\n"
"v_mfma_f32_16x16x32_bf16 v[108:111], a[108:111], a[20:23], v[108:111]\n"
"buffer_load_dwordx4 v1, s[36:39], s69 offen offset:768 lds\n"
"v_mfma_f32_16x16x32_bf16 v[100:103], a[108:111], a[16:19], v[100:103]\n"
"ds_read_b64_tr_b16 a[144:145], v5 offset:9216\n"
"v_mfma_f32_16x16x32_bf16 v[172:175], a[108:111], a[12:15], v[172:175]\n"
"ds_read_b64_tr_b16 a[146:147], v5 offset:9472\n"
"v_mfma_f32_16x16x32_bf16 v[164:167], a[108:111], a[8:11], v[164:167]\n"
"ds_read_b64_tr_b16 a[148:149], v5 offset:9224\n"
"s_add_u32 m0, s80, 44288\n"
"v_mfma_f32_16x16x32_bf16 v[44:47], a[108:111], a[4:7], v[44:47]\n"
"buffer_load_dwordx4 v1, s[36:39], s70 offen offset:768 lds\n"
"v_mfma_f32_16x16x32_bf16 v[36:39], a[108:111], a[0:3], v[36:39]\n"
"ds_read_b64_tr_b16 a[150:151], v5 offset:9480\n"
"v_mfma_f32_16x16x32_bf16 v[48:51], a[112:115], a[0:3], v[48:51]\n"
"ds_read_b64_tr_b16 a[152:153], v5 offset:9728\n"
"v_mfma_f32_16x16x32_bf16 v[56:59], a[112:115], a[4:7], v[56:59]\n"
"ds_read_b64_tr_b16 a[154:155], v5 offset:9984\n"
"v_mfma_f32_16x16x32_bf16 v[176:179], a[112:115], a[8:11], v[176:179]\n"
"ds_read_b64_tr_b16 a[156:157], v5 offset:9736\n"
"v_mfma_f32_16x16x32_bf16 v[184:187], a[112:115], a[12:15], v[184:187]\n"
"ds_read_b64_tr_b16 a[158:159], v5 offset:9992\n"
"v_mfma_f32_16x16x32_bf16 v[112:115], a[112:115], a[16:19], v[112:115]\n"
"v_mfma_f32_16x16x32_bf16 v[120:123], a[112:115], a[20:23], v[120:123]\n"
"v_mfma_f32_16x16x32_bf16 v[124:127], a[116:119], a[20:23], v[124:127]\n"
"v_mfma_f32_16x16x32_bf16 v[116:119], a[116:119], a[16:19], v[116:119]\n"
"v_mfma_f32_16x16x32_bf16 v[188:191], a[116:119], a[12:15], v[188:191]\n"
"v_mfma_f32_16x16x32_bf16 v[180:183], a[116:119], a[8:11], v[180:183]\n"
"v_mfma_f32_16x16x32_bf16 v[60:63], a[116:119], a[4:7], v[60:63]\n"
"v_mfma_f32_16x16x32_bf16 v[52:55], a[116:119], a[0:3], v[52:55]\n"
"v_mfma_f32_16x16x32_bf16 v[64:67], a[120:123], a[0:3], v[64:67]\n"
"buffer_load_dword v11, v15, s[52:55], s84 offen\n"
"s_mov_b32 s74, 3145728\n"
"v_mfma_f32_16x16x32_bf16 v[72:75], a[120:123], a[4:7], v[72:75]\n"
"v_mfma_f32_16x16x32_bf16 v[192:195], a[120:123], a[8:11], v[192:195]\n"
"v_mfma_f32_16x16x32_bf16 v[200:203], a[120:123], a[12:15], v[200:203]\n"
"v_mfma_f32_16x16x32_bf16 v[128:131], a[120:123], a[16:19], v[128:131]\n"
"v_mfma_f32_16x16x32_bf16 v[136:139], a[120:123], a[20:23], v[136:139]\n"
"v_mfma_f32_16x16x32_bf16 v[140:143], a[124:127], a[20:23], v[140:143]\n"
"v_mfma_f32_16x16x32_bf16 v[132:135], a[124:127], a[16:19], v[132:135]\n"
"v_mfma_f32_16x16x32_bf16 v[204:207], a[124:127], a[12:15], v[204:207]\n"
"v_mfma_f32_16x16x32_bf16 v[196:199], a[124:127], a[8:11], v[196:199]\n"
"v_mfma_f32_16x16x32_bf16 v[76:79], a[124:127], a[4:7], v[76:79]\n"
"v_mfma_f32_16x16x32_bf16 v[68:71], a[124:127], a[0:3], v[68:71]\n"
"s_waitcnt lgkmcnt(0)\n"
"v_mfma_f32_16x16x32_bf16 v[16:19], a[128:131], a[24:27], v[16:19]\n"
"v_mfma_f32_16x16x32_bf16 v[24:27], a[128:131], a[28:31], v[24:27]\n"
"v_mfma_f32_16x16x32_bf16 v[144:147], a[128:131], a[32:35], v[144:147]\n"
"v_mfma_f32_16x16x32_bf16 v[152:155], a[128:131], a[36:39], v[152:155]\n"
"v_mfma_f32_16x16x32_bf16 v[80:83], a[128:131], a[40:43], v[80:83]\n"
"s_waitcnt vmcnt(8)\n"
"s_barrier\n"
"s_add_u32 m0, s79, 24576\n"
"v_mfma_f32_16x16x32_bf16 v[88:91], a[128:131], a[44:47], v[88:91]\n"
"buffer_load_dwordx4 v2, s[40:43], s74 offen lds\n"
"s_add_u32 m0, s79, 25472\n"
"v_mfma_f32_16x16x32_bf16 v[92:95], a[132:135], a[44:47], v[92:95]\n"
"buffer_load_dwordx4 v2, s[40:43], s74 offen offset:128 lds\n"
"v_mfma_f32_16x16x32_bf16 v[84:87], a[132:135], a[40:43], v[84:87]\n"
"ds_read_b64_tr_b16 a[96:97], v5 offset:49152\n"
"s_add_u32 m0, s79, 106240\n"
"v_mfma_f32_16x16x32_bf16 v[156:159], a[132:135], a[36:39], v[156:159]\n"
"buffer_load_dwordx4 v2, s[40:43], s74 offen offset:256 lds\n"
"v_mfma_f32_16x16x32_bf16 v[148:151], a[132:135], a[32:35], v[148:151]\n"
"ds_read_b64_tr_b16 a[98:99], v5 offset:49408\n"
"s_add_u32 m0, s79, 107136\n"
"v_mfma_f32_16x16x32_bf16 v[28:31], a[132:135], a[28:31], v[28:31]\n"
"buffer_load_dwordx4 v2, s[40:43], s74 offen offset:384 lds\n"
"v_mfma_f32_16x16x32_bf16 v[20:23], a[132:135], a[24:27], v[20:23]\n"
"ds_read_b128 a[48:51], v7 offset:53248\n"
"v_mfma_f32_16x16x32_bf16 v[32:35], a[136:139], a[24:27], v[32:35]\n"
"ds_read_b128 a[52:55], v6 offset:55296\n"
"v_mfma_f32_16x16x32_bf16 v[40:43], a[136:139], a[28:31], v[40:43]\n"
"ds_read_b128 a[56:59], v7 offset:57344\n"
"s_add_u32 m0, s80, 46336\n"
"v_mfma_f32_16x16x32_bf16 v[160:163], a[136:139], a[32:35], v[160:163]\n"
"buffer_load_dwordx4 v1, s[36:39], s71 offen offset:768 lds\n"
"v_mfma_f32_16x16x32_bf16 v[168:171], a[136:139], a[36:39], v[168:171]\n"
"ds_read_b128 a[60:63], v6 offset:59392\n"
"v_mfma_f32_16x16x32_bf16 v[96:99], a[136:139], a[40:43], v[96:99]\n"
"ds_read_b128 a[64:67], v7 offset:61440\n"
"v_mfma_f32_16x16x32_bf16 v[104:107], a[136:139], a[44:47], v[104:107]\n"
"ds_read_b128 a[68:71], v6 offset:63488\n"
"s_add_u32 m0, s80, 48384\n"
"v_mfma_f32_16x16x32_bf16 v[108:111], a[140:143], a[44:47], v[108:111]\n"
"buffer_load_dwordx4 v1, s[36:39], s72 offen offset:768 lds\n"
"v_mfma_f32_16x16x32_bf16 v[100:103], a[140:143], a[40:43], v[100:103]\n"
"ds_read_b64_tr_b16 a[100:101], v5 offset:49160\n"
"v_mfma_f32_16x16x32_bf16 v[172:175], a[140:143], a[36:39], v[172:175]\n"
"ds_read_b64_tr_b16 a[102:103], v5 offset:49416\n"
"v_mfma_f32_16x16x32_bf16 v[164:167], a[140:143], a[32:35], v[164:167]\n"
"ds_read_b64_tr_b16 a[104:105], v5 offset:49664\n"
"s_add_u32 m0, s80, 50432\n"
"v_mfma_f32_16x16x32_bf16 v[44:47], a[140:143], a[28:31], v[44:47]\n"
"buffer_load_dwordx4 v1, s[36:39], s73 offen offset:768 lds\n"
"v_mfma_f32_16x16x32_bf16 v[36:39], a[140:143], a[24:27], v[36:39]\n"
"ds_read_b64_tr_b16 a[106:107], v5 offset:49920\n"
"v_mfma_f32_16x16x32_bf16 v[48:51], a[144:147], a[24:27], v[48:51]\n"
"ds_read_b64_tr_b16 a[108:109], v5 offset:49672\n"
"v_mfma_f32_16x16x32_bf16 v[56:59], a[144:147], a[28:31], v[56:59]\n"
"ds_read_b64_tr_b16 a[110:111], v5 offset:49928\n"
"v_mfma_f32_16x16x32_bf16 v[176:179], a[144:147], a[32:35], v[176:179]\n"
"ds_read_b64_tr_b16 a[112:113], v5 offset:50176\n"
"v_mfma_f32_16x16x32_bf16 v[184:187], a[144:147], a[36:39], v[184:187]\n"
"ds_read_b64_tr_b16 a[114:115], v5 offset:50432\n"
"v_mfma_f32_16x16x32_bf16 v[112:115], a[144:147], a[40:43], v[112:115]\n"
"ds_read_b64_tr_b16 a[116:117], v5 offset:50184\n"
"v_mfma_f32_16x16x32_bf16 v[120:123], a[144:147], a[44:47], v[120:123]\n"
"ds_read_b64_tr_b16 a[118:119], v5 offset:50440\n"
"v_mfma_f32_16x16x32_bf16 v[124:127], a[148:151], a[44:47], v[124:127]\n"
"ds_read_b64_tr_b16 a[120:121], v5 offset:50688\n"
"v_mfma_f32_16x16x32_bf16 v[116:119], a[148:151], a[40:43], v[116:119]\n"
"ds_read_b64_tr_b16 a[122:123], v5 offset:50944\n"
"v_mfma_f32_16x16x32_bf16 v[188:191], a[148:151], a[36:39], v[188:191]\n"
"ds_read_b64_tr_b16 a[124:125], v5 offset:50696\n"
"v_mfma_f32_16x16x32_bf16 v[180:183], a[148:151], a[32:35], v[180:183]\n"
"ds_read_b64_tr_b16 a[126:127], v5 offset:50952\n"
"v_mfma_f32_16x16x32_bf16 v[60:63], a[148:151], a[28:31], v[60:63]\n"
"ds_read_b128 a[72:75], v7 offset:53760\n"
"v_mfma_f32_16x16x32_bf16 v[52:55], a[148:151], a[24:27], v[52:55]\n"
"ds_read_b128 a[76:79], v6 offset:55808\n"
"v_mfma_f32_16x16x32_bf16 v[64:67], a[152:155], a[24:27], v[64:67]\n"
"ds_read_b128 a[80:83], v7 offset:57856\n"
"s_mov_b32 s74, 3407872\n"
"v_mfma_f32_16x16x32_bf16 v[72:75], a[152:155], a[28:31], v[72:75]\n"
"ds_read_b128 a[84:87], v6 offset:59904\n"
"v_mfma_f32_16x16x32_bf16 v[192:195], a[152:155], a[32:35], v[192:195]\n"
"ds_read_b128 a[88:91], v7 offset:61952\n"
"v_mfma_f32_16x16x32_bf16 v[200:203], a[152:155], a[36:39], v[200:203]\n"
"ds_read_b128 a[92:95], v6 offset:64000\n"
"v_mfma_f32_16x16x32_bf16 v[128:131], a[152:155], a[40:43], v[128:131]\n"
"v_mfma_f32_16x16x32_bf16 v[136:139], a[152:155], a[44:47], v[136:139]\n"
"v_mfma_f32_16x16x32_bf16 v[140:143], a[156:159], a[44:47], v[140:143]\n"
"v_mfma_f32_16x16x32_bf16 v[132:135], a[156:159], a[40:43], v[132:135]\n"
"v_mfma_f32_16x16x32_bf16 v[204:207], a[156:159], a[36:39], v[204:207]\n"
"v_mfma_f32_16x16x32_bf16 v[196:199], a[156:159], a[32:35], v[196:199]\n"
"v_mfma_f32_16x16x32_bf16 v[76:79], a[156:159], a[28:31], v[76:79]\n"
"v_mfma_f32_16x16x32_bf16 v[68:71], a[156:159], a[24:27], v[68:71]\n"
"s_waitcnt lgkmcnt(0)\n"
"v_mfma_f32_16x16x32_bf16 v[16:19], a[96:99], a[48:51], v[16:19]\n"
"v_mfma_f32_16x16x32_bf16 v[24:27], a[96:99], a[52:55], v[24:27]\n"
"v_mfma_f32_16x16x32_bf16 v[144:147], a[96:99], a[56:59], v[144:147]\n"
"v_mfma_f32_16x16x32_bf16 v[152:155], a[96:99], a[60:63], v[152:155]\n"
"v_mfma_f32_16x16x32_bf16 v[80:83], a[96:99], a[64:67], v[80:83]\n"
"s_waitcnt vmcnt(11)\n"
"s_barrier\n"
"s_add_u32 m0, s79, 65536\n"
"v_mfma_f32_16x16x32_bf16 v[88:91], a[96:99], a[68:71], v[88:91]\n"
"buffer_load_dwordx4 v2, s[40:43], s74 offen lds\n"
"s_add_u32 m0, s79, 66432\n"
"v_mfma_f32_16x16x32_bf16 v[92:95], a[100:103], a[68:71], v[92:95]\n"
"buffer_load_dwordx4 v2, s[40:43], s74 offen offset:128 lds\n"
"v_mfma_f32_16x16x32_bf16 v[84:87], a[100:103], a[64:67], v[84:87]\n"
"ds_read_b64_tr_b16 a[128:129], v5 offset:0\n"
"s_add_u32 m0, s79, 147200\n"
"v_mfma_f32_16x16x32_bf16 v[156:159], a[100:103], a[60:63], v[156:159]\n"
"buffer_load_dwordx4 v2, s[40:43], s74 offen offset:256 lds\n"
"v_mfma_f32_16x16x32_bf16 v[148:151], a[100:103], a[56:59], v[148:151]\n"
"ds_read_b64_tr_b16 a[130:131], v5 offset:256\n"
"s_add_u32 m0, s79, 148096\n"
"v_mfma_f32_16x16x32_bf16 v[28:31], a[100:103], a[52:55], v[28:31]\n"
"buffer_load_dwordx4 v2, s[40:43], s74 offen offset:384 lds\n"
"v_mfma_f32_16x16x32_bf16 v[20:23], a[100:103], a[48:51], v[20:23]\n"
"ds_read_b64_tr_b16 a[132:133], v5 offset:8\n"
"v_mfma_f32_16x16x32_bf16 v[32:35], a[104:107], a[48:51], v[32:35]\n"
"ds_read_b64_tr_b16 a[134:135], v5 offset:264\n"
"v_mfma_f32_16x16x32_bf16 v[40:43], a[104:107], a[52:55], v[40:43]\n"
"ds_read_b64_tr_b16 a[136:137], v5 offset:512\n"
"s_add_u32 m0, s80, 52352\n"
"v_mfma_f32_16x16x32_bf16 v[160:163], a[104:107], a[56:59], v[160:163]\n"
"buffer_load_dwordx4 v1, s[36:39], s68 offen offset:896 lds\n"
"v_mfma_f32_16x16x32_bf16 v[168:171], a[104:107], a[60:63], v[168:171]\n"
"ds_read_b64_tr_b16 a[138:139], v5 offset:768\n"
"v_mfma_f32_16x16x32_bf16 v[96:99], a[104:107], a[64:67], v[96:99]\n"
"ds_read_b64_tr_b16 a[140:141], v5 offset:520\n"
"v_mfma_f32_16x16x32_bf16 v[104:107], a[104:107], a[68:71], v[104:107]\n"
"ds_read_b64_tr_b16 a[142:143], v5 offset:776\n"
"s_add_u32 m0, s80, 54400\n"
"v_mfma_f32_16x16x32_bf16 v[108:111], a[108:111], a[68:71], v[108:111]\n"
"buffer_load_dwordx4 v1, s[36:39], s69 offen offset:896 lds\n"
"v_mfma_f32_16x16x32_bf16 v[100:103], a[108:111], a[64:67], v[100:103]\n"
"ds_read_b64_tr_b16 a[144:145], v5 offset:1024\n"
"v_mfma_f32_16x16x32_bf16 v[172:175], a[108:111], a[60:63], v[172:175]\n"
"ds_read_b64_tr_b16 a[146:147], v5 offset:1280\n"
"v_mfma_f32_16x16x32_bf16 v[164:167], a[108:111], a[56:59], v[164:167]\n"
"ds_read_b64_tr_b16 a[148:149], v5 offset:1032\n"
"s_add_u32 m0, s80, 56448\n"
"v_mfma_f32_16x16x32_bf16 v[44:47], a[108:111], a[52:55], v[44:47]\n"
"buffer_load_dwordx4 v1, s[36:39], s70 offen offset:896 lds\n"
"v_mfma_f32_16x16x32_bf16 v[36:39], a[108:111], a[48:51], v[36:39]\n"
"ds_read_b64_tr_b16 a[150:151], v5 offset:1288\n"
"v_mfma_f32_16x16x32_bf16 v[48:51], a[112:115], a[48:51], v[48:51]\n"
"ds_read_b64_tr_b16 a[152:153], v5 offset:1536\n"
"v_mfma_f32_16x16x32_bf16 v[56:59], a[112:115], a[52:55], v[56:59]\n"
"ds_read_b64_tr_b16 a[154:155], v5 offset:1792\n"
"v_mfma_f32_16x16x32_bf16 v[176:179], a[112:115], a[56:59], v[176:179]\n"
"ds_read_b64_tr_b16 a[156:157], v5 offset:1544\n"
"v_mfma_f32_16x16x32_bf16 v[184:187], a[112:115], a[60:63], v[184:187]\n"
"ds_read_b64_tr_b16 a[158:159], v5 offset:1800\n"
"v_mfma_f32_16x16x32_bf16 v[112:115], a[112:115], a[64:67], v[112:115]\n"
"v_mfma_f32_16x16x32_bf16 v[120:123], a[112:115], a[68:71], v[120:123]\n"
"v_mfma_f32_16x16x32_bf16 v[124:127], a[116:119], a[68:71], v[124:127]\n"
"v_mfma_f32_16x16x32_bf16 v[116:119], a[116:119], a[64:67], v[116:119]\n"
"v_mfma_f32_16x16x32_bf16 v[188:191], a[116:119], a[60:63], v[188:191]\n"
"v_mfma_f32_16x16x32_bf16 v[180:183], a[116:119], a[56:59], v[180:183]\n"
"v_mfma_f32_16x16x32_bf16 v[60:63], a[116:119], a[52:55], v[60:63]\n"
"v_mfma_f32_16x16x32_bf16 v[52:55], a[116:119], a[48:51], v[52:55]\n"
"v_mfma_f32_16x16x32_bf16 v[64:67], a[120:123], a[48:51], v[64:67]\n"
"s_mov_b32 s74, 3670016\n"
"v_mfma_f32_16x16x32_bf16 v[72:75], a[120:123], a[52:55], v[72:75]\n"
"v_mfma_f32_16x16x32_bf16 v[192:195], a[120:123], a[56:59], v[192:195]\n"
"v_mfma_f32_16x16x32_bf16 v[200:203], a[120:123], a[60:63], v[200:203]\n"
"v_mfma_f32_16x16x32_bf16 v[128:131], a[120:123], a[64:67], v[128:131]\n"
"v_mfma_f32_16x16x32_bf16 v[136:139], a[120:123], a[68:71], v[136:139]\n"
"v_mfma_f32_16x16x32_bf16 v[140:143], a[124:127], a[68:71], v[140:143]\n"
"v_mfma_f32_16x16x32_bf16 v[132:135], a[124:127], a[64:67], v[132:135]\n"
"v_mfma_f32_16x16x32_bf16 v[204:207], a[124:127], a[60:63], v[204:207]\n"
"v_mfma_f32_16x16x32_bf16 v[196:199], a[124:127], a[56:59], v[196:199]\n"
"v_mfma_f32_16x16x32_bf16 v[76:79], a[124:127], a[52:55], v[76:79]\n"
"v_mfma_f32_16x16x32_bf16 v[68:71], a[124:127], a[48:51], v[68:71]\n"
"s_waitcnt lgkmcnt(0)\n"
"v_mfma_f32_16x16x32_bf16 v[16:19], a[128:131], a[72:75], v[16:19]\n"
"v_mfma_f32_16x16x32_bf16 v[24:27], a[128:131], a[76:79], v[24:27]\n"
"v_mfma_f32_16x16x32_bf16 v[144:147], a[128:131], a[80:83], v[144:147]\n"
"v_mfma_f32_16x16x32_bf16 v[152:155], a[128:131], a[84:87], v[152:155]\n"
"v_mfma_f32_16x16x32_bf16 v[80:83], a[128:131], a[88:91], v[80:83]\n"
"s_waitcnt vmcnt(7)\n"
"s_barrier\n"
"s_add_u32 m0, s79, 16384\n"
"v_mfma_f32_16x16x32_bf16 v[88:91], a[128:131], a[92:95], v[88:91]\n"
"buffer_load_dwordx4 v2, s[40:43], s74 offen lds\n"
"s_add_u32 m0, s79, 17280\n"
"v_mfma_f32_16x16x32_bf16 v[92:95], a[132:135], a[92:95], v[92:95]\n"
"buffer_load_dwordx4 v2, s[40:43], s74 offen offset:128 lds\n"
"v_mfma_f32_16x16x32_bf16 v[84:87], a[132:135], a[88:91], v[84:87]\n"
"ds_read_b64_tr_b16 a[96:97], v5 offset:8192\n"
"s_add_u32 m0, s79, 98048\n"
"v_mfma_f32_16x16x32_bf16 v[156:159], a[132:135], a[84:87], v[156:159]\n"
"buffer_load_dwordx4 v2, s[40:43], s74 offen offset:256 lds\n"
"v_mfma_f32_16x16x32_bf16 v[148:151], a[132:135], a[80:83], v[148:151]\n"
"ds_read_b64_tr_b16 a[98:99], v5 offset:8448\n"
"s_add_u32 m0, s79, 98944\n"
"v_mfma_f32_16x16x32_bf16 v[28:31], a[132:135], a[76:79], v[28:31]\n"
"buffer_load_dwordx4 v2, s[40:43], s74 offen offset:384 lds\n"
"v_mfma_f32_16x16x32_bf16 v[20:23], a[132:135], a[72:75], v[20:23]\n"
"ds_read_b128 a[0:3], v7 offset:40960\n"
"v_mfma_f32_16x16x32_bf16 v[32:35], a[136:139], a[72:75], v[32:35]\n"
"ds_read_b128 a[4:7], v6 offset:43008\n"
"v_mfma_f32_16x16x32_bf16 v[40:43], a[136:139], a[76:79], v[40:43]\n"
"ds_read_b128 a[8:11], v7 offset:45056\n"
"s_add_u32 m0, s80, 58496\n"
"v_mfma_f32_16x16x32_bf16 v[160:163], a[136:139], a[80:83], v[160:163]\n"
"buffer_load_dwordx4 v1, s[36:39], s71 offen offset:896 lds\n"
"v_mfma_f32_16x16x32_bf16 v[168:171], a[136:139], a[84:87], v[168:171]\n"
"ds_read_b128 a[12:15], v6 offset:47104\n"
"v_mfma_f32_16x16x32_bf16 v[96:99], a[136:139], a[88:91], v[96:99]\n"
"ds_read_b128 a[16:19], v7 offset:49152\n"
"v_mfma_f32_16x16x32_bf16 v[104:107], a[136:139], a[92:95], v[104:107]\n"
"ds_read_b128 a[20:23], v6 offset:51200\n"
"s_add_u32 m0, s80, 60544\n"
"v_mfma_f32_16x16x32_bf16 v[108:111], a[140:143], a[92:95], v[108:111]\n"
"buffer_load_dwordx4 v1, s[36:39], s72 offen offset:896 lds\n"
"v_mfma_f32_16x16x32_bf16 v[100:103], a[140:143], a[88:91], v[100:103]\n"
"ds_read_b64_tr_b16 a[100:101], v5 offset:8200\n"
"v_mfma_f32_16x16x32_bf16 v[172:175], a[140:143], a[84:87], v[172:175]\n"
"ds_read_b64_tr_b16 a[102:103], v5 offset:8456\n"
"v_mfma_f32_16x16x32_bf16 v[164:167], a[140:143], a[80:83], v[164:167]\n"
"ds_read_b64_tr_b16 a[104:105], v5 offset:8704\n"
"s_add_u32 m0, s80, 62592\n"
"v_mfma_f32_16x16x32_bf16 v[44:47], a[140:143], a[76:79], v[44:47]\n"
"buffer_load_dwordx4 v1, s[36:39], s73 offen offset:896 lds\n"
"v_mfma_f32_16x16x32_bf16 v[36:39], a[140:143], a[72:75], v[36:39]\n"
"ds_read_b64_tr_b16 a[106:107], v5 offset:8960\n"
"v_mfma_f32_16x16x32_bf16 v[48:51], a[144:147], a[72:75], v[48:51]\n"
"ds_read_b64_tr_b16 a[108:109], v5 offset:8712\n"
"v_mfma_f32_16x16x32_bf16 v[56:59], a[144:147], a[76:79], v[56:59]\n"
"ds_read_b64_tr_b16 a[110:111], v5 offset:8968\n"
"v_mfma_f32_16x16x32_bf16 v[176:179], a[144:147], a[80:83], v[176:179]\n"
"ds_read_b64_tr_b16 a[112:113], v5 offset:9216\n"
"v_mfma_f32_16x16x32_bf16 v[184:187], a[144:147], a[84:87], v[184:187]\n"
"ds_read_b64_tr_b16 a[114:115], v5 offset:9472\n"
"v_mfma_f32_16x16x32_bf16 v[112:115], a[144:147], a[88:91], v[112:115]\n"
"ds_read_b64_tr_b16 a[116:117], v5 offset:9224\n"
"v_mfma_f32_16x16x32_bf16 v[120:123], a[144:147], a[92:95], v[120:123]\n"
"ds_read_b64_tr_b16 a[118:119], v5 offset:9480\n"
"v_mfma_f32_16x16x32_bf16 v[124:127], a[148:151], a[92:95], v[124:127]\n"
"ds_read_b64_tr_b16 a[120:121], v5 offset:9728\n"
"v_mfma_f32_16x16x32_bf16 v[116:119], a[148:151], a[88:91], v[116:119]\n"
"ds_read_b64_tr_b16 a[122:123], v5 offset:9984\n"
"v_mfma_f32_16x16x32_bf16 v[188:191], a[148:151], a[84:87], v[188:191]\n"
"ds_read_b64_tr_b16 a[124:125], v5 offset:9736\n"
"v_mfma_f32_16x16x32_bf16 v[180:183], a[148:151], a[80:83], v[180:183]\n"
"ds_read_b64_tr_b16 a[126:127], v5 offset:9992\n"
"v_mfma_f32_16x16x32_bf16 v[60:63], a[148:151], a[76:79], v[60:63]\n"
"ds_read_b128 a[24:27], v7 offset:41472\n"
"v_mfma_f32_16x16x32_bf16 v[52:55], a[148:151], a[72:75], v[52:55]\n"
"ds_read_b128 a[28:31], v6 offset:43520\n"
"v_mfma_f32_16x16x32_bf16 v[64:67], a[152:155], a[72:75], v[64:67]\n"
"buffer_load_dword v11, v15, s[52:55], s85 offen\n"
"s_mov_b32 s74, 3932160\n"
"v_mfma_f32_16x16x32_bf16 v[72:75], a[152:155], a[76:79], v[72:75]\n"
"ds_read_b128 a[32:35], v7 offset:45568\n"
"v_mfma_f32_16x16x32_bf16 v[192:195], a[152:155], a[80:83], v[192:195]\n"
"ds_read_b128 a[36:39], v6 offset:47616\n"
"v_mfma_f32_16x16x32_bf16 v[200:203], a[152:155], a[84:87], v[200:203]\n"
"ds_read_b128 a[40:43], v7 offset:49664\n"
"v_mfma_f32_16x16x32_bf16 v[128:131], a[152:155], a[88:91], v[128:131]\n"
"ds_read_b128 a[44:47], v6 offset:51712\n"
"v_mfma_f32_16x16x32_bf16 v[136:139], a[152:155], a[92:95], v[136:139]\n"
"v_mfma_f32_16x16x32_bf16 v[140:143], a[156:159], a[92:95], v[140:143]\n"
"v_mfma_f32_16x16x32_bf16 v[132:135], a[156:159], a[88:91], v[132:135]\n"
"v_mfma_f32_16x16x32_bf16 v[204:207], a[156:159], a[84:87], v[204:207]\n"
"v_mfma_f32_16x16x32_bf16 v[196:199], a[156:159], a[80:83], v[196:199]\n"
"v_mfma_f32_16x16x32_bf16 v[76:79], a[156:159], a[76:79], v[76:79]\n"
"v_mfma_f32_16x16x32_bf16 v[68:71], a[156:159], a[72:75], v[68:71]\n"
"s_waitcnt lgkmcnt(0)\n"
"v_mfma_f32_16x16x32_bf16 v[16:19], a[96:99], a[0:3], v[16:19]\n"
"v_mfma_f32_16x16x32_bf16 v[24:27], a[96:99], a[4:7], v[24:27]\n"
"v_mfma_f32_16x16x32_bf16 v[144:147], a[96:99], a[8:11], v[144:147]\n"
"v_mfma_f32_16x16x32_bf16 v[152:155], a[96:99], a[12:15], v[152:155]\n"
"v_mfma_f32_16x16x32_bf16 v[80:83], a[96:99], a[16:19], v[80:83]\n"
"s_waitcnt vmcnt(11)\n"
"s_barrier\n"
"s_add_u32 m0, s79, 24576\n"
"v_mfma_f32_16x16x32_bf16 v[88:91], a[96:99], a[20:23], v[88:91]\n"
"buffer_load_dwordx4 v2, s[40:43], s74 offen lds\n"
"s_add_u32 m0, s79, 25472\n"
"v_mfma_f32_16x16x32_bf16 v[92:95], a[100:103], a[20:23], v[92:95]\n"
"buffer_load_dwordx4 v2, s[40:43], s74 offen offset:128 lds\n"
"v_mfma_f32_16x16x32_bf16 v[84:87], a[100:103], a[16:19], v[84:87]\n"
"ds_read_b64_tr_b16 a[128:129], v5 offset:49152\n"
"s_add_u32 m0, s79, 106240\n"
"v_mfma_f32_16x16x32_bf16 v[156:159], a[100:103], a[12:15], v[156:159]\n"
"buffer_load_dwordx4 v2, s[40:43], s74 offen offset:256 lds\n"
"v_mfma_f32_16x16x32_bf16 v[148:151], a[100:103], a[8:11], v[148:151]\n"
"ds_read_b64_tr_b16 a[130:131], v5 offset:49408\n"
"s_add_u32 m0, s79, 107136\n"
"v_mfma_f32_16x16x32_bf16 v[28:31], a[100:103], a[4:7], v[28:31]\n"
"buffer_load_dwordx4 v2, s[40:43], s74 offen offset:384 lds\n"
"v_mfma_f32_16x16x32_bf16 v[20:23], a[100:103], a[0:3], v[20:23]\n"
"ds_read_b64_tr_b16 a[132:133], v5 offset:49160\n"
"v_mfma_f32_16x16x32_bf16 v[32:35], a[104:107], a[0:3], v[32:35]\n"
"ds_read_b64_tr_b16 a[134:135], v5 offset:49416\n"
"v_mfma_f32_16x16x32_bf16 v[40:43], a[104:107], a[4:7], v[40:43]\n"
"ds_read_b64_tr_b16 a[136:137], v5 offset:49664\n"
"s_add_u32 m0, s80, 39936\n"
"v_mfma_f32_16x16x32_bf16 v[160:163], a[104:107], a[8:11], v[160:163]\n"
"buffer_load_dwordx4 v1, s[36:39], s68 offen offset:1024 lds\n"
"v_mfma_f32_16x16x32_bf16 v[168:171], a[104:107], a[12:15], v[168:171]\n"
"ds_read_b64_tr_b16 a[138:139], v5 offset:49920\n"
"v_mfma_f32_16x16x32_bf16 v[96:99], a[104:107], a[16:19], v[96:99]\n"
"ds_read_b64_tr_b16 a[140:141], v5 offset:49672\n"
"v_mfma_f32_16x16x32_bf16 v[104:107], a[104:107], a[20:23], v[104:107]\n"
"ds_read_b64_tr_b16 a[142:143], v5 offset:49928\n"
"s_add_u32 m0, s80, 41984\n"
"v_mfma_f32_16x16x32_bf16 v[108:111], a[108:111], a[20:23], v[108:111]\n"
"buffer_load_dwordx4 v1, s[36:39], s69 offen offset:1024 lds\n"
"v_mfma_f32_16x16x32_bf16 v[100:103], a[108:111], a[16:19], v[100:103]\n"
"ds_read_b64_tr_b16 a[144:145], v5 offset:50176\n"
"v_mfma_f32_16x16x32_bf16 v[172:175], a[108:111], a[12:15], v[172:175]\n"
"ds_read_b64_tr_b16 a[146:147], v5 offset:50432\n"
"v_mfma_f32_16x16x32_bf16 v[164:167], a[108:111], a[8:11], v[164:167]\n"
"ds_read_b64_tr_b16 a[148:149], v5 offset:50184\n"
"s_add_u32 m0, s80, 44032\n"
"v_mfma_f32_16x16x32_bf16 v[44:47], a[108:111], a[4:7], v[44:47]\n"
"buffer_load_dwordx4 v1, s[36:39], s70 offen offset:1024 lds\n"
"v_mfma_f32_16x16x32_bf16 v[36:39], a[108:111], a[0:3], v[36:39]\n"
"ds_read_b64_tr_b16 a[150:151], v5 offset:50440\n"
"v_mfma_f32_16x16x32_bf16 v[48:51], a[112:115], a[0:3], v[48:51]\n"
"ds_read_b64_tr_b16 a[152:153], v5 offset:50688\n"
"v_mfma_f32_16x16x32_bf16 v[56:59], a[112:115], a[4:7], v[56:59]\n"
"ds_read_b64_tr_b16 a[154:155], v5 offset:50944\n"
"v_mfma_f32_16x16x32_bf16 v[176:179], a[112:115], a[8:11], v[176:179]\n"
"ds_read_b64_tr_b16 a[156:157], v5 offset:50696\n"
"v_mfma_f32_16x16x32_bf16 v[184:187], a[112:115], a[12:15], v[184:187]\n"
"ds_read_b64_tr_b16 a[158:159], v5 offset:50952\n"
"v_mfma_f32_16x16x32_bf16 v[112:115], a[112:115], a[16:19], v[112:115]\n"
"v_mfma_f32_16x16x32_bf16 v[120:123], a[112:115], a[20:23], v[120:123]\n"
"v_mfma_f32_16x16x32_bf16 v[124:127], a[116:119], a[20:23], v[124:127]\n"
"v_mfma_f32_16x16x32_bf16 v[116:119], a[116:119], a[16:19], v[116:119]\n"
"v_mfma_f32_16x16x32_bf16 v[188:191], a[116:119], a[12:15], v[188:191]\n"
"v_mfma_f32_16x16x32_bf16 v[180:183], a[116:119], a[8:11], v[180:183]\n"
"v_mfma_f32_16x16x32_bf16 v[60:63], a[116:119], a[4:7], v[60:63]\n"
"v_mfma_f32_16x16x32_bf16 v[52:55], a[116:119], a[0:3], v[52:55]\n"
"v_mfma_f32_16x16x32_bf16 v[64:67], a[120:123], a[0:3], v[64:67]\n"
"s_mov_b32 s74, 4194304\n"
"v_mfma_f32_16x16x32_bf16 v[72:75], a[120:123], a[4:7], v[72:75]\n"
"v_mfma_f32_16x16x32_bf16 v[192:195], a[120:123], a[8:11], v[192:195]\n"
"v_mfma_f32_16x16x32_bf16 v[200:203], a[120:123], a[12:15], v[200:203]\n"
"v_mfma_f32_16x16x32_bf16 v[128:131], a[120:123], a[16:19], v[128:131]\n"
"v_mfma_f32_16x16x32_bf16 v[136:139], a[120:123], a[20:23], v[136:139]\n"
"v_mfma_f32_16x16x32_bf16 v[140:143], a[124:127], a[20:23], v[140:143]\n"
"v_mfma_f32_16x16x32_bf16 v[132:135], a[124:127], a[16:19], v[132:135]\n"
"v_mfma_f32_16x16x32_bf16 v[204:207], a[124:127], a[12:15], v[204:207]\n"
"v_mfma_f32_16x16x32_bf16 v[196:199], a[124:127], a[8:11], v[196:199]\n"
"v_mfma_f32_16x16x32_bf16 v[76:79], a[124:127], a[4:7], v[76:79]\n"
"v_mfma_f32_16x16x32_bf16 v[68:71], a[124:127], a[0:3], v[68:71]\n"
"s_waitcnt lgkmcnt(0)\n"
"v_mfma_f32_16x16x32_bf16 v[16:19], a[128:131], a[24:27], v[16:19]\n"
"v_mfma_f32_16x16x32_bf16 v[24:27], a[128:131], a[28:31], v[24:27]\n"
"v_mfma_f32_16x16x32_bf16 v[144:147], a[128:131], a[32:35], v[144:147]\n"
"v_mfma_f32_16x16x32_bf16 v[152:155], a[128:131], a[36:39], v[152:155]\n"
"v_mfma_f32_16x16x32_bf16 v[80:83], a[128:131], a[40:43], v[80:83]\n"
"s_waitcnt vmcnt(8)\n"
"s_barrier\n"
"s_add_u32 m0, s79, 65536\n"
"v_mfma_f32_16x16x32_bf16 v[88:91], a[128:131], a[44:47], v[88:91]\n"
"buffer_load_dwordx4 v2, s[40:43], s74 offen lds\n"
"s_add_u32 m0, s79, 66432\n"
"v_mfma_f32_16x16x32_bf16 v[92:95], a[132:135], a[44:47], v[92:95]\n"
"buffer_load_dwordx4 v2, s[40:43], s74 offen offset:128 lds\n"
"v_mfma_f32_16x16x32_bf16 v[84:87], a[132:135], a[40:43], v[84:87]\n"
"ds_read_b64_tr_b16 a[96:97], v5 offset:0\n"
"s_add_u32 m0, s79, 147200\n"
"v_mfma_f32_16x16x32_bf16 v[156:159], a[132:135], a[36:39], v[156:159]\n"
"buffer_load_dwordx4 v2, s[40:43], s74 offen offset:256 lds\n"
"v_mfma_f32_16x16x32_bf16 v[148:151], a[132:135], a[32:35], v[148:151]\n"
"ds_read_b64_tr_b16 a[98:99], v5 offset:256\n"
"s_add_u32 m0, s79, 148096\n"
"v_mfma_f32_16x16x32_bf16 v[28:31], a[132:135], a[28:31], v[28:31]\n"
"buffer_load_dwordx4 v2, s[40:43], s74 offen offset:384 lds\n"
"v_mfma_f32_16x16x32_bf16 v[20:23], a[132:135], a[24:27], v[20:23]\n"
"ds_read_b128 a[48:51], v7 offset:53248\n"
"v_mfma_f32_16x16x32_bf16 v[32:35], a[136:139], a[24:27], v[32:35]\n"
"ds_read_b128 a[52:55], v6 offset:55296\n"
"v_mfma_f32_16x16x32_bf16 v[40:43], a[136:139], a[28:31], v[40:43]\n"
"ds_read_b128 a[56:59], v7 offset:57344\n"
"s_add_u32 m0, s80, 46080\n"
"v_mfma_f32_16x16x32_bf16 v[160:163], a[136:139], a[32:35], v[160:163]\n"
"buffer_load_dwordx4 v1, s[36:39], s71 offen offset:1024 lds\n"
"v_mfma_f32_16x16x32_bf16 v[168:171], a[136:139], a[36:39], v[168:171]\n"
"ds_read_b128 a[60:63], v6 offset:59392\n"
"v_mfma_f32_16x16x32_bf16 v[96:99], a[136:139], a[40:43], v[96:99]\n"
"ds_read_b128 a[64:67], v7 offset:61440\n"
"v_mfma_f32_16x16x32_bf16 v[104:107], a[136:139], a[44:47], v[104:107]\n"
"ds_read_b128 a[68:71], v6 offset:63488\n"
"s_add_u32 m0, s80, 48128\n"
"v_mfma_f32_16x16x32_bf16 v[108:111], a[140:143], a[44:47], v[108:111]\n"
"buffer_load_dwordx4 v1, s[36:39], s72 offen offset:1024 lds\n"
"v_mfma_f32_16x16x32_bf16 v[100:103], a[140:143], a[40:43], v[100:103]\n"
"ds_read_b64_tr_b16 a[100:101], v5 offset:8\n"
"v_mfma_f32_16x16x32_bf16 v[172:175], a[140:143], a[36:39], v[172:175]\n"
"ds_read_b64_tr_b16 a[102:103], v5 offset:264\n"
"v_mfma_f32_16x16x32_bf16 v[164:167], a[140:143], a[32:35], v[164:167]\n"
"ds_read_b64_tr_b16 a[104:105], v5 offset:512\n"
"s_add_u32 m0, s80, 50176\n"
"v_mfma_f32_16x16x32_bf16 v[44:47], a[140:143], a[28:31], v[44:47]\n"
"buffer_load_dwordx4 v1, s[36:39], s73 offen offset:1024 lds\n"
"v_mfma_f32_16x16x32_bf16 v[36:39], a[140:143], a[24:27], v[36:39]\n"
"ds_read_b64_tr_b16 a[106:107], v5 offset:768\n"
"v_mfma_f32_16x16x32_bf16 v[48:51], a[144:147], a[24:27], v[48:51]\n"
"ds_read_b64_tr_b16 a[108:109], v5 offset:520\n"
"v_mfma_f32_16x16x32_bf16 v[56:59], a[144:147], a[28:31], v[56:59]\n"
"ds_read_b64_tr_b16 a[110:111], v5 offset:776\n"
"v_mfma_f32_16x16x32_bf16 v[176:179], a[144:147], a[32:35], v[176:179]\n"
"ds_read_b64_tr_b16 a[112:113], v5 offset:1024\n"
"v_mfma_f32_16x16x32_bf16 v[184:187], a[144:147], a[36:39], v[184:187]\n"
"ds_read_b64_tr_b16 a[114:115], v5 offset:1280\n"
"v_mfma_f32_16x16x32_bf16 v[112:115], a[144:147], a[40:43], v[112:115]\n"
"ds_read_b64_tr_b16 a[116:117], v5 offset:1032\n"
"v_mfma_f32_16x16x32_bf16 v[120:123], a[144:147], a[44:47], v[120:123]\n"
"ds_read_b64_tr_b16 a[118:119], v5 offset:1288\n"
"v_mfma_f32_16x16x32_bf16 v[124:127], a[148:151], a[44:47], v[124:127]\n"
"ds_read_b64_tr_b16 a[120:121], v5 offset:1536\n"
"v_mfma_f32_16x16x32_bf16 v[116:119], a[148:151], a[40:43], v[116:119]\n"
"ds_read_b64_tr_b16 a[122:123], v5 offset:1792\n"
"v_mfma_f32_16x16x32_bf16 v[188:191], a[148:151], a[36:39], v[188:191]\n"
"ds_read_b64_tr_b16 a[124:125], v5 offset:1544\n"
"v_mfma_f32_16x16x32_bf16 v[180:183], a[148:151], a[32:35], v[180:183]\n"
"ds_read_b64_tr_b16 a[126:127], v5 offset:1800\n"
"v_mfma_f32_16x16x32_bf16 v[60:63], a[148:151], a[28:31], v[60:63]\n"
"ds_read_b128 a[72:75], v7 offset:53760\n"
"v_mfma_f32_16x16x32_bf16 v[52:55], a[148:151], a[24:27], v[52:55]\n"
"ds_read_b128 a[76:79], v6 offset:55808\n"
"v_mfma_f32_16x16x32_bf16 v[64:67], a[152:155], a[24:27], v[64:67]\n"
"ds_read_b128 a[80:83], v7 offset:57856\n"
"s_mov_b32 s74, 4456448\n"
"v_mfma_f32_16x16x32_bf16 v[72:75], a[152:155], a[28:31], v[72:75]\n"
"ds_read_b128 a[84:87], v6 offset:59904\n"
"v_mfma_f32_16x16x32_bf16 v[192:195], a[152:155], a[32:35], v[192:195]\n"
"ds_read_b128 a[88:91], v7 offset:61952\n"
"v_mfma_f32_16x16x32_bf16 v[200:203], a[152:155], a[36:39], v[200:203]\n"
"ds_read_b128 a[92:95], v6 offset:64000\n"
"v_mfma_f32_16x16x32_bf16 v[128:131], a[152:155], a[40:43], v[128:131]\n"
"v_mfma_f32_16x16x32_bf16 v[136:139], a[152:155], a[44:47], v[136:139]\n"
"v_mfma_f32_16x16x32_bf16 v[140:143], a[156:159], a[44:47], v[140:143]\n"
"v_mfma_f32_16x16x32_bf16 v[132:135], a[156:159], a[40:43], v[132:135]\n"
"v_mfma_f32_16x16x32_bf16 v[204:207], a[156:159], a[36:39], v[204:207]\n"
"v_mfma_f32_16x16x32_bf16 v[196:199], a[156:159], a[32:35], v[196:199]\n"
"v_mfma_f32_16x16x32_bf16 v[76:79], a[156:159], a[28:31], v[76:79]\n"
"v_mfma_f32_16x16x32_bf16 v[68:71], a[156:159], a[24:27], v[68:71]\n"
"s_waitcnt lgkmcnt(0)\n"
"v_mfma_f32_16x16x32_bf16 v[16:19], a[96:99], a[48:51], v[16:19]\n"
"v_mfma_f32_16x16x32_bf16 v[24:27], a[96:99], a[52:55], v[24:27]\n"
"v_mfma_f32_16x16x32_bf16 v[144:147], a[96:99], a[56:59], v[144:147]\n"
"v_mfma_f32_16x16x32_bf16 v[152:155], a[96:99], a[60:63], v[152:155]\n"
"v_mfma_f32_16x16x32_bf16 v[80:83], a[96:99], a[64:67], v[80:83]\n"
"s_waitcnt vmcnt(10)\n"
"s_barrier\n"
"s_add_u32 m0, s79, 16384\n"
"v_mfma_f32_16x16x32_bf16 v[88:91], a[96:99], a[68:71], v[88:91]\n"
"buffer_load_dwordx4 v2, s[40:43], s74 offen lds\n"
"s_add_u32 m0, s79, 17280\n"
"v_mfma_f32_16x16x32_bf16 v[92:95], a[100:103], a[68:71], v[92:95]\n"
"buffer_load_dwordx4 v2, s[40:43], s74 offen offset:128 lds\n"
"v_mfma_f32_16x16x32_bf16 v[84:87], a[100:103], a[64:67], v[84:87]\n"
"ds_read_b64_tr_b16 a[128:129], v5 offset:8192\n"
"v_mfma_f32_16x16x32_bf16 v[156:159], a[100:103], a[60:63], v[156:159]\n"
"ds_read_b64_tr_b16 a[130:131], v5 offset:8448\n"
"s_add_u32 m0, s79, 32768\n"
"v_mfma_f32_16x16x32_bf16 v[148:151], a[100:103], a[56:59], v[148:151]\n"
"buffer_load_dwordx4 v3, s[44:47], s75 offen nt lds\n"
"s_add_u32 m0, s79, 98048\n"
"v_mfma_f32_16x16x32_bf16 v[28:31], a[100:103], a[52:55], v[28:31]\n"
"buffer_load_dwordx4 v2, s[40:43], s74 offen offset:256 lds\n"
"v_mfma_f32_16x16x32_bf16 v[20:23], a[100:103], a[48:51], v[20:23]\n"
"ds_read_b64_tr_b16 a[132:133], v5 offset:8200\n"
"v_mfma_f32_16x16x32_bf16 v[32:35], a[104:107], a[48:51], v[32:35]\n"
"ds_read_b64_tr_b16 a[134:135], v5 offset:8456\n"
"v_mfma_f32_16x16x32_bf16 v[40:43], a[104:107], a[52:55], v[40:43]\n"
"ds_read_b64_tr_b16 a[136:137], v5 offset:8704\n"
"s_add_u32 m0, s79, 98944\n"
"v_mfma_f32_16x16x32_bf16 v[160:163], a[104:107], a[56:59], v[160:163]\n"
"buffer_load_dwordx4 v2, s[40:43], s74 offen offset:384 lds\n"
"v_mfma_f32_16x16x32_bf16 v[168:171], a[104:107], a[60:63], v[168:171]\n"
"ds_read_b64_tr_b16 a[138:139], v5 offset:8960\n"
"v_mfma_f32_16x16x32_bf16 v[96:99], a[104:107], a[64:67], v[96:99]\n"
"ds_read_b64_tr_b16 a[140:141], v5 offset:8712\n"
"v_mfma_f32_16x16x32_bf16 v[104:107], a[104:107], a[68:71], v[104:107]\n"
"ds_read_b64_tr_b16 a[142:143], v5 offset:8968\n"
"s_add_u32 m0, s80, 52096\n"
"v_mfma_f32_16x16x32_bf16 v[108:111], a[108:111], a[68:71], v[108:111]\n"
"buffer_load_dwordx4 v1, s[36:39], s68 offen offset:1152 lds\n"
"v_mfma_f32_16x16x32_bf16 v[100:103], a[108:111], a[64:67], v[100:103]\n"
"ds_read_b64_tr_b16 a[144:145], v5 offset:9216\n"
"v_mfma_f32_16x16x32_bf16 v[172:175], a[108:111], a[60:63], v[172:175]\n"
"ds_read_b64_tr_b16 a[146:147], v5 offset:9472\n"
"v_mfma_f32_16x16x32_bf16 v[164:167], a[108:111], a[56:59], v[164:167]\n"
"ds_read_b64_tr_b16 a[148:149], v5 offset:9224\n"
"s_add_u32 m0, s80, 54144\n"
"v_mfma_f32_16x16x32_bf16 v[44:47], a[108:111], a[52:55], v[44:47]\n"
"buffer_load_dwordx4 v1, s[36:39], s69 offen offset:1152 lds\n"
"v_mfma_f32_16x16x32_bf16 v[36:39], a[108:111], a[48:51], v[36:39]\n"
"ds_read_b64_tr_b16 a[150:151], v5 offset:9480\n"
"v_mfma_f32_16x16x32_bf16 v[48:51], a[112:115], a[48:51], v[48:51]\n"
"ds_read_b64_tr_b16 a[152:153], v5 offset:9728\n"
"v_mfma_f32_16x16x32_bf16 v[56:59], a[112:115], a[52:55], v[56:59]\n"
"ds_read_b64_tr_b16 a[154:155], v5 offset:9984\n"
"v_mfma_f32_16x16x32_bf16 v[176:179], a[112:115], a[56:59], v[176:179]\n"
"ds_read_b64_tr_b16 a[156:157], v5 offset:9736\n"
"s_add_u32 m0, s79, 32768\n"
"v_mfma_f32_16x16x32_bf16 v[184:187], a[112:115], a[60:63], v[184:187]\n"
"buffer_load_dwordx4 v14, s[44:47], s75 offen offset:1024 nt lds\n"
"v_mfma_f32_16x16x32_bf16 v[112:115], a[112:115], a[64:67], v[112:115]\n"
"ds_read_b64_tr_b16 a[158:159], v5 offset:9992\n"
"v_mfma_f32_16x16x32_bf16 v[120:123], a[112:115], a[68:71], v[120:123]\n"
"v_mfma_f32_16x16x32_bf16 v[124:127], a[116:119], a[68:71], v[124:127]\n"
"v_mfma_f32_16x16x32_bf16 v[116:119], a[116:119], a[64:67], v[116:119]\n"
"s_add_u32 m0, s80, 56192\n"
"v_mfma_f32_16x16x32_bf16 v[188:191], a[116:119], a[60:63], v[188:191]\n"
"buffer_load_dwordx4 v1, s[36:39], s70 offen offset:1152 lds\n"
"v_mfma_f32_16x16x32_bf16 v[180:183], a[116:119], a[56:59], v[180:183]\n"
"v_mfma_f32_16x16x32_bf16 v[60:63], a[116:119], a[52:55], v[60:63]\n"
"v_mfma_f32_16x16x32_bf16 v[52:55], a[116:119], a[48:51], v[52:55]\n"
"v_mfma_f32_16x16x32_bf16 v[64:67], a[120:123], a[48:51], v[64:67]\n"
"s_mov_b32 s74, 4718592\n"
"v_mfma_f32_16x16x32_bf16 v[72:75], a[120:123], a[52:55], v[72:75]\n"
"v_mfma_f32_16x16x32_bf16 v[192:195], a[120:123], a[56:59], v[192:195]\n"
"v_mfma_f32_16x16x32_bf16 v[200:203], a[120:123], a[60:63], v[200:203]\n"
"v_mfma_f32_16x16x32_bf16 v[128:131], a[120:123], a[64:67], v[128:131]\n"
"v_mfma_f32_16x16x32_bf16 v[136:139], a[120:123], a[68:71], v[136:139]\n"
"s_add_u32 m0, s79, 114688\n"
"v_mfma_f32_16x16x32_bf16 v[140:143], a[124:127], a[68:71], v[140:143]\n"
"buffer_load_dwordx4 v3, s[20:23], s75 offen nt lds\n"
"v_mfma_f32_16x16x32_bf16 v[132:135], a[124:127], a[64:67], v[132:135]\n"
"v_mfma_f32_16x16x32_bf16 v[204:207], a[124:127], a[60:63], v[204:207]\n"
"v_mfma_f32_16x16x32_bf16 v[196:199], a[124:127], a[56:59], v[196:199]\n"
"v_mfma_f32_16x16x32_bf16 v[76:79], a[124:127], a[52:55], v[76:79]\n"
"v_mfma_f32_16x16x32_bf16 v[68:71], a[124:127], a[48:51], v[68:71]\n"
"s_waitcnt lgkmcnt(0)\n"
"v_mfma_f32_16x16x32_bf16 v[16:19], a[128:131], a[72:75], v[16:19]\n"
"v_mfma_f32_16x16x32_bf16 v[24:27], a[128:131], a[76:79], v[24:27]\n"
"v_mfma_f32_16x16x32_bf16 v[144:147], a[128:131], a[80:83], v[144:147]\n"
"v_mfma_f32_16x16x32_bf16 v[152:155], a[128:131], a[84:87], v[152:155]\n"
"v_mfma_f32_16x16x32_bf16 v[80:83], a[128:131], a[88:91], v[80:83]\n"
"s_waitcnt vmcnt(10)\n"
"s_barrier\n"
"s_add_u32 m0, s79, 24576\n"
"v_mfma_f32_16x16x32_bf16 v[88:91], a[128:131], a[92:95], v[88:91]\n"
"buffer_load_dwordx4 v2, s[40:43], s74 offen lds\n"
"s_add_u32 m0, s79, 25472\n"
"v_mfma_f32_16x16x32_bf16 v[92:95], a[132:135], a[92:95], v[92:95]\n"
"buffer_load_dwordx4 v2, s[40:43], s74 offen offset:128 lds\n"
"v_mfma_f32_16x16x32_bf16 v[84:87], a[132:135], a[88:91], v[84:87]\n"
"ds_read_b64_tr_b16 a[96:97], v5 offset:49152\n"
"s_add_u32 m0, s79, 106240\n"
"v_mfma_f32_16x16x32_bf16 v[156:159], a[132:135], a[84:87], v[156:159]\n"
"buffer_load_dwordx4 v2, s[40:43], s74 offen offset:256 lds\n"
"v_mfma_f32_16x16x32_bf16 v[148:151], a[132:135], a[80:83], v[148:151]\n"
"ds_read_b64_tr_b16 a[98:99], v5 offset:49408\n"
"v_mfma_f32_16x16x32_bf16 v[28:31], a[132:135], a[76:79], v[28:31]\n"
"ds_read_b128 a[0:3], v7 offset:40960\n"
"s_add_u32 m0, s79, 114688\n"
"v_mfma_f32_16x16x32_bf16 v[20:23], a[132:135], a[72:75], v[20:23]\n"
"buffer_load_dwordx4 v14, s[20:23], s75 offen offset:1024 nt lds\n"
"v_mfma_f32_16x16x32_bf16 v[32:35], a[136:139], a[72:75], v[32:35]\n"
"ds_read_b128 a[4:7], v6 offset:43008\n"
"v_mfma_f32_16x16x32_bf16 v[40:43], a[136:139], a[76:79], v[40:43]\n"
"ds_read_b128 a[8:11], v7 offset:45056\n"
"s_add_u32 m0, s79, 107136\n"
"v_mfma_f32_16x16x32_bf16 v[160:163], a[136:139], a[80:83], v[160:163]\n"
"buffer_load_dwordx4 v2, s[40:43], s74 offen offset:384 lds\n"
"v_mfma_f32_16x16x32_bf16 v[168:171], a[136:139], a[84:87], v[168:171]\n"
"ds_read_b128 a[12:15], v6 offset:47104\n"
"v_mfma_f32_16x16x32_bf16 v[96:99], a[136:139], a[88:91], v[96:99]\n"
"ds_read_b128 a[16:19], v7 offset:49152\n"
"v_mfma_f32_16x16x32_bf16 v[104:107], a[136:139], a[92:95], v[104:107]\n"
"ds_read_b128 a[20:23], v6 offset:51200\n"
"s_add_u32 s75, s75, 8192\n"
"s_and_b32 s75, s75, 0x1ffff\n"
"s_add_u32 m0, s80, 58240\n"
"v_mfma_f32_16x16x32_bf16 v[108:111], a[140:143], a[92:95], v[108:111]\n"
"buffer_load_dwordx4 v1, s[36:39], s71 offen offset:1152 lds\n"
"v_mfma_f32_16x16x32_bf16 v[100:103], a[140:143], a[88:91], v[100:103]\n"
"ds_read_b64_tr_b16 a[100:101], v5 offset:49160\n"
"v_mfma_f32_16x16x32_bf16 v[172:175], a[140:143], a[84:87], v[172:175]\n"
"ds_read_b64_tr_b16 a[102:103], v5 offset:49416\n"
"v_mfma_f32_16x16x32_bf16 v[164:167], a[140:143], a[80:83], v[164:167]\n"
"ds_read_b64_tr_b16 a[104:105], v5 offset:49664\n"
"s_add_u32 m0, s80, 60288\n"
"v_mfma_f32_16x16x32_bf16 v[44:47], a[140:143], a[76:79], v[44:47]\n"
"buffer_load_dwordx4 v1, s[36:39], s72 offen offset:1152 lds\n"
"v_mfma_f32_16x16x32_bf16 v[36:39], a[140:143], a[72:75], v[36:39]\n"
"ds_read_b64_tr_b16 a[106:107], v5 offset:49920\n"
"v_mfma_f32_16x16x32_bf16 v[48:51], a[144:147], a[72:75], v[48:51]\n"
"ds_read_b64_tr_b16 a[108:109], v5 offset:49672\n"
"v_mfma_f32_16x16x32_bf16 v[56:59], a[144:147], a[76:79], v[56:59]\n"
"ds_read_b64_tr_b16 a[110:111], v5 offset:49928\n"
"s_add_u32 m0, s80, 62336\n"
"v_mfma_f32_16x16x32_bf16 v[176:179], a[144:147], a[80:83], v[176:179]\n"
"buffer_load_dwordx4 v1, s[36:39], s73 offen offset:1152 lds\n"
"v_mfma_f32_16x16x32_bf16 v[184:187], a[144:147], a[84:87], v[184:187]\n"
"ds_read_b64_tr_b16 a[112:113], v5 offset:50176\n"
"v_mfma_f32_16x16x32_bf16 v[112:115], a[144:147], a[88:91], v[112:115]\n"
"ds_read_b64_tr_b16 a[114:115], v5 offset:50432\n"
"v_mfma_f32_16x16x32_bf16 v[120:123], a[144:147], a[92:95], v[120:123]\n"
"ds_read_b64_tr_b16 a[116:117], v5 offset:50184\n"
"v_mfma_f32_16x16x32_bf16 v[124:127], a[148:151], a[92:95], v[124:127]\n"
"ds_read_b64_tr_b16 a[118:119], v5 offset:50440\n"
"v_mfma_f32_16x16x32_bf16 v[116:119], a[148:151], a[88:91], v[116:119]\n"
"ds_read_b64_tr_b16 a[120:121], v5 offset:50688\n"
"v_mfma_f32_16x16x32_bf16 v[188:191], a[148:151], a[84:87], v[188:191]\n"
"ds_read_b64_tr_b16 a[122:123], v5 offset:50944\n"
"s_add_u32 m0, s79, 0\n"
"v_mfma_f32_16x16x32_bf16 v[180:183], a[148:151], a[80:83], v[180:183]\n"
"buffer_load_dwordx4 v3, s[44:47], s75 offen nt lds\n"
"v_mfma_f32_16x16x32_bf16 v[60:63], a[148:151], a[76:79], v[60:63]\n"
"ds_read_b64_tr_b16 a[124:125], v5 offset:50696\n"
"v_mfma_f32_16x16x32_bf16 v[52:55], a[148:151], a[72:75], v[52:55]\n"
"ds_read_b64_tr_b16 a[126:127], v5 offset:50952\n"
"v_mfma_f32_16x16x32_bf16 v[64:67], a[152:155], a[72:75], v[64:67]\n"
"ds_read_b128 a[24:27], v7 offset:41472\n"
"s_mov_b32 s74, 4980736\n"
"v_mfma_f32_16x16x32_bf16 v[72:75], a[152:155], a[76:79], v[72:75]\n"
"ds_read_b128 a[28:31], v6 offset:43520\n"
"v_mfma_f32_16x16x32_bf16 v[192:195], a[152:155], a[80:83], v[192:195]\n"
"ds_read_b128 a[32:35], v7 offset:45568\n"
"v_mfma_f32_16x16x32_bf16 v[200:203], a[152:155], a[84:87], v[200:203]\n"
"ds_read_b128 a[36:39], v6 offset:47616\n"
"v_mfma_f32_16x16x32_bf16 v[128:131], a[152:155], a[88:91], v[128:131]\n"
"ds_read_b128 a[40:43], v7 offset:49664\n"
"v_mfma_f32_16x16x32_bf16 v[136:139], a[152:155], a[92:95], v[136:139]\n"
"ds_read_b128 a[44:47], v6 offset:51712\n"
"v_mfma_f32_16x16x32_bf16 v[140:143], a[156:159], a[92:95], v[140:143]\n"
"v_mfma_f32_16x16x32_bf16 v[132:135], a[156:159], a[88:91], v[132:135]\n"
"s_add_u32 m0, s79, 0\n"
"v_mfma_f32_16x16x32_bf16 v[204:207], a[156:159], a[84:87], v[204:207]\n"
"buffer_load_dwordx4 v14, s[44:47], s75 offen offset:1024 nt lds\n"
"v_mfma_f32_16x16x32_bf16 v[196:199], a[156:159], a[80:83], v[196:199]\n"
"v_mfma_f32_16x16x32_bf16 v[76:79], a[156:159], a[76:79], v[76:79]\n"
"v_mfma_f32_16x16x32_bf16 v[68:71], a[156:159], a[72:75], v[68:71]\n"
"s_waitcnt lgkmcnt(0)\n"
"v_mfma_f32_16x16x32_bf16 v[16:19], a[96:99], a[0:3], v[16:19]\n"
"v_mfma_f32_16x16x32_bf16 v[24:27], a[96:99], a[4:7], v[24:27]\n"
"v_mfma_f32_16x16x32_bf16 v[144:147], a[96:99], a[8:11], v[144:147]\n"
"v_mfma_f32_16x16x32_bf16 v[152:155], a[96:99], a[12:15], v[152:155]\n"
"v_mfma_f32_16x16x32_bf16 v[80:83], a[96:99], a[16:19], v[80:83]\n"
"s_waitcnt vmcnt(15)\n"
"s_barrier\n"
"s_add_u32 m0, s79, 65536\n"
"v_mfma_f32_16x16x32_bf16 v[88:91], a[96:99], a[20:23], v[88:91]\n"
"buffer_load_dwordx4 v2, s[40:43], s74 offen lds\n"
"s_add_u32 m0, s79, 66432\n"
"v_mfma_f32_16x16x32_bf16 v[92:95], a[100:103], a[20:23], v[92:95]\n"
"buffer_load_dwordx4 v2, s[40:43], s74 offen offset:128 lds\n"
"v_mfma_f32_16x16x32_bf16 v[84:87], a[100:103], a[16:19], v[84:87]\n"
"ds_read_b64_tr_b16 a[128:129], v5 offset:0\n"
"s_add_u32 m0, s79, 147200\n"
"v_mfma_f32_16x16x32_bf16 v[156:159], a[100:103], a[12:15], v[156:159]\n"
"buffer_load_dwordx4 v2, s[40:43], s74 offen offset:256 lds\n"
"v_mfma_f32_16x16x32_bf16 v[148:151], a[100:103], a[8:11], v[148:151]\n"
"ds_read_b64_tr_b16 a[130:131], v5 offset:256\n"
"s_add_u32 m0, s79, 148096\n"
"v_mfma_f32_16x16x32_bf16 v[28:31], a[100:103], a[4:7], v[28:31]\n"
"buffer_load_dwordx4 v2, s[40:43], s74 offen offset:384 lds\n"
"v_mfma_f32_16x16x32_bf16 v[20:23], a[100:103], a[0:3], v[20:23]\n"
"ds_read_b64_tr_b16 a[132:133], v5 offset:8\n"
"v_mfma_f32_16x16x32_bf16 v[32:35], a[104:107], a[0:3], v[32:35]\n"
"ds_read_b64_tr_b16 a[134:135], v5 offset:264\n"
"v_mfma_f32_16x16x32_bf16 v[40:43], a[104:107], a[4:7], v[40:43]\n"
"ds_read_b64_tr_b16 a[136:137], v5 offset:512\n"
"v_mfma_f32_16x16x32_bf16 v[160:163], a[104:107], a[8:11], v[160:163]\n"
"ds_read_b64_tr_b16 a[138:139], v5 offset:768\n"
"s_add_u32 m0, s79, 81920\n"
"v_mfma_f32_16x16x32_bf16 v[168:171], a[104:107], a[12:15], v[168:171]\n"
"buffer_load_dwordx4 v3, s[20:23], s75 offen nt lds\n"
"v_mfma_f32_16x16x32_bf16 v[96:99], a[104:107], a[16:19], v[96:99]\n"
"ds_read_b64_tr_b16 a[140:141], v5 offset:520\n"
"v_mfma_f32_16x16x32_bf16 v[104:107], a[104:107], a[20:23], v[104:107]\n"
"ds_read_b64_tr_b16 a[142:143], v5 offset:776\n"
"v_mfma_f32_16x16x32_bf16 v[108:111], a[108:111], a[20:23], v[108:111]\n"
"ds_read_b64_tr_b16 a[144:145], v5 offset:1024\n"
"v_mfma_f32_16x16x32_bf16 v[100:103], a[108:111], a[16:19], v[100:103]\n"
"ds_read_b64_tr_b16 a[146:147], v5 offset:1280\n"
"v_mfma_f32_16x16x32_bf16 v[172:175], a[108:111], a[12:15], v[172:175]\n"
"ds_read_b64_tr_b16 a[148:149], v5 offset:1032\n"
"v_mfma_f32_16x16x32_bf16 v[164:167], a[108:111], a[8:11], v[164:167]\n"
"ds_read_b64_tr_b16 a[150:151], v5 offset:1288\n"
"v_mfma_f32_16x16x32_bf16 v[44:47], a[108:111], a[4:7], v[44:47]\n"
"ds_read_b64_tr_b16 a[152:153], v5 offset:1536\n"
"v_mfma_f32_16x16x32_bf16 v[36:39], a[108:111], a[0:3], v[36:39]\n"
"ds_read_b64_tr_b16 a[154:155], v5 offset:1792\n"
"v_mfma_f32_16x16x32_bf16 v[48:51], a[112:115], a[0:3], v[48:51]\n"
"ds_read_b64_tr_b16 a[156:157], v5 offset:1544\n"
"v_mfma_f32_16x16x32_bf16 v[56:59], a[112:115], a[4:7], v[56:59]\n"
"ds_read_b64_tr_b16 a[158:159], v5 offset:1800\n"
"v_mfma_f32_16x16x32_bf16 v[176:179], a[112:115], a[8:11], v[176:179]\n"
"v_mfma_f32_16x16x32_bf16 v[184:187], a[112:115], a[12:15], v[184:187]\n"
"v_mfma_f32_16x16x32_bf16 v[112:115], a[112:115], a[16:19], v[112:115]\n"
"v_mfma_f32_16x16x32_bf16 v[120:123], a[112:115], a[20:23], v[120:123]\n"
"s_add_u32 m0, s79, 81920\n"
"v_mfma_f32_16x16x32_bf16 v[124:127], a[116:119], a[20:23], v[124:127]\n"
"buffer_load_dwordx4 v14, s[20:23], s75 offen offset:1024 nt lds\n"
"v_mfma_f32_16x16x32_bf16 v[116:119], a[116:119], a[16:19], v[116:119]\n"
"v_mfma_f32_16x16x32_bf16 v[188:191], a[116:119], a[12:15], v[188:191]\n"
"v_mfma_f32_16x16x32_bf16 v[180:183], a[116:119], a[8:11], v[180:183]\n"
"v_mfma_f32_16x16x32_bf16 v[60:63], a[116:119], a[4:7], v[60:63]\n"
"v_mfma_f32_16x16x32_bf16 v[52:55], a[116:119], a[0:3], v[52:55]\n"
"v_mfma_f32_16x16x32_bf16 v[64:67], a[120:123], a[0:3], v[64:67]\n"
"v_mfma_f32_16x16x32_bf16 v[72:75], a[120:123], a[4:7], v[72:75]\n"
"v_mfma_f32_16x16x32_bf16 v[192:195], a[120:123], a[8:11], v[192:195]\n"
"v_mfma_f32_16x16x32_bf16 v[200:203], a[120:123], a[12:15], v[200:203]\n"
"v_mfma_f32_16x16x32_bf16 v[128:131], a[120:123], a[16:19], v[128:131]\n"
"v_mfma_f32_16x16x32_bf16 v[136:139], a[120:123], a[20:23], v[136:139]\n"
"v_mfma_f32_16x16x32_bf16 v[140:143], a[124:127], a[20:23], v[140:143]\n"
"v_mfma_f32_16x16x32_bf16 v[132:135], a[124:127], a[16:19], v[132:135]\n"
"v_mfma_f32_16x16x32_bf16 v[204:207], a[124:127], a[12:15], v[204:207]\n"
"v_mfma_f32_16x16x32_bf16 v[196:199], a[124:127], a[8:11], v[196:199]\n"
"v_mfma_f32_16x16x32_bf16 v[76:79], a[124:127], a[4:7], v[76:79]\n"
"v_mfma_f32_16x16x32_bf16 v[68:71], a[124:127], a[0:3], v[68:71]\n"
"s_add_u32 m0, s79, 73728\n"
"s_waitcnt lgkmcnt(0)\n"
"v_mfma_f32_16x16x32_bf16 v[16:19], a[128:131], a[24:27], v[16:19]\n"
"buffer_load_dwordx4 v4, s[48:51], s77 offen nt lds\n"
"v_mfma_f32_16x16x32_bf16 v[24:27], a[128:131], a[28:31], v[24:27]\n"
"v_mfma_f32_16x16x32_bf16 v[144:147], a[128:131], a[32:35], v[144:147]\n"
"v_mfma_f32_16x16x32_bf16 v[152:155], a[128:131], a[36:39], v[152:155]\n"
"v_mfma_f32_16x16x32_bf16 v[80:83], a[128:131], a[40:43], v[80:83]\n"
"s_waitcnt vmcnt(9)\n"
"s_barrier\n"
"v_mfma_f32_16x16x32_bf16 v[88:91], a[128:131], a[44:47], v[88:91]\n"
"ds_read_b64_tr_b16 a[96:97], v5 offset:8192\n"
"ds_read_b64_tr_b16 a[98:99], v5 offset:8448\n"
"v_mfma_f32_16x16x32_bf16 v[92:95], a[132:135], a[44:47], v[92:95]\n"
"ds_read_b128 a[48:51], v7 offset:53248\n"
"v_mfma_f32_16x16x32_bf16 v[84:87], a[132:135], a[40:43], v[84:87]\n"
"ds_read_b128 a[52:55], v6 offset:55296\n"
"v_mfma_f32_16x16x32_bf16 v[156:159], a[132:135], a[36:39], v[156:159]\n"
"ds_read_b128 a[56:59], v7 offset:57344\n"
"v_mfma_f32_16x16x32_bf16 v[148:151], a[132:135], a[32:35], v[148:151]\n"
"ds_read_b128 a[60:63], v6 offset:59392\n"
"v_mfma_f32_16x16x32_bf16 v[28:31], a[132:135], a[28:31], v[28:31]\n"
"ds_read_b128 a[64:67], v7 offset:61440\n"
"v_mfma_f32_16x16x32_bf16 v[20:23], a[132:135], a[24:27], v[20:23]\n"
"ds_read_b128 a[68:71], v6 offset:63488\n"
"v_mfma_f32_16x16x32_bf16 v[32:35], a[136:139], a[24:27], v[32:35]\n"
"ds_read_b64_tr_b16 a[100:101], v5 offset:8200\n"
"v_mfma_f32_16x16x32_bf16 v[40:43], a[136:139], a[28:31], v[40:43]\n"
"ds_read_b64_tr_b16 a[102:103], v5 offset:8456\n"
"v_mfma_f32_16x16x32_bf16 v[160:163], a[136:139], a[32:35], v[160:163]\n"
"ds_read_b64_tr_b16 a[104:105], v5 offset:8704\n"
"v_mfma_f32_16x16x32_bf16 v[168:171], a[136:139], a[36:39], v[168:171]\n"
"ds_read_b64_tr_b16 a[106:107], v5 offset:8960\n"
"v_mfma_f32_16x16x32_bf16 v[96:99], a[136:139], a[40:43], v[96:99]\n"
"ds_read_b64_tr_b16 a[108:109], v5 offset:8712\n"
"s_add_u32 m0, s79, 73728\n"
"v_mfma_f32_16x16x32_bf16 v[104:107], a[136:139], a[44:47], v[104:107]\n"
"buffer_load_dwordx4 v232, s[48:51], s77 offen offset:1024 nt lds\n"
"v_mfma_f32_16x16x32_bf16 v[108:111], a[140:143], a[44:47], v[108:111]\n"
"ds_read_b64_tr_b16 a[110:111], v5 offset:8968\n"
"v_mfma_f32_16x16x32_bf16 v[100:103], a[140:143], a[40:43], v[100:103]\n"
"ds_read_b64_tr_b16 a[112:113], v5 offset:9216\n"
"v_mfma_f32_16x16x32_bf16 v[172:175], a[140:143], a[36:39], v[172:175]\n"
"ds_read_b64_tr_b16 a[114:115], v5 offset:9472\n"
"v_mfma_f32_16x16x32_bf16 v[164:167], a[140:143], a[32:35], v[164:167]\n"
"ds_read_b64_tr_b16 a[116:117], v5 offset:9224\n"
"v_mfma_f32_16x16x32_bf16 v[44:47], a[140:143], a[28:31], v[44:47]\n"
"ds_read_b64_tr_b16 a[118:119], v5 offset:9480\n"
"v_mfma_f32_16x16x32_bf16 v[36:39], a[140:143], a[24:27], v[36:39]\n"
"ds_read_b64_tr_b16 a[120:121], v5 offset:9728\n"
"v_mfma_f32_16x16x32_bf16 v[48:51], a[144:147], a[24:27], v[48:51]\n"
"ds_read_b64_tr_b16 a[122:123], v5 offset:9984\n"
"v_mfma_f32_16x16x32_bf16 v[56:59], a[144:147], a[28:31], v[56:59]\n"
"ds_read_b64_tr_b16 a[124:125], v5 offset:9736\n"
"v_mfma_f32_16x16x32_bf16 v[176:179], a[144:147], a[32:35], v[176:179]\n"
"ds_read_b64_tr_b16 a[126:127], v5 offset:9992\n"
"v_mfma_f32_16x16x32_bf16 v[184:187], a[144:147], a[36:39], v[184:187]\n"
"ds_read_b128 a[72:75], v7 offset:53760\n"
"v_mfma_f32_16x16x32_bf16 v[112:115], a[144:147], a[40:43], v[112:115]\n"
"ds_read_b128 a[76:79], v6 offset:55808\n"
"v_mfma_f32_16x16x32_bf16 v[120:123], a[144:147], a[44:47], v[120:123]\n"
"ds_read_b128 a[80:83], v7 offset:57856\n"
"v_mfma_f32_16x16x32_bf16 v[124:127], a[148:151], a[44:47], v[124:127]\n"
"ds_read_b128 a[84:87], v6 offset:59904\n"
"v_mfma_f32_16x16x32_bf16 v[116:119], a[148:151], a[40:43], v[116:119]\n"
"ds_read_b128 a[88:91], v7 offset:61952\n"
"v_accvgpr_read_b32 v7, a162\n"
"v_mfma_f32_16x16x32_bf16 v[188:191], a[148:151], a[36:39], v[188:191]\n"
"ds_read_b128 a[92:95], v6 offset:64000\n"
"v_accvgpr_read_b32 v6, a161\n"
"v_mfma_f32_16x16x32_bf16 v[180:183], a[148:151], a[32:35], v[180:183]\n"
"s_add_u32 m0, s79, 155648\n"
"v_mfma_f32_16x16x32_bf16 v[60:63], a[148:151], a[28:31], v[60:63]\n"
"buffer_load_dwordx4 v4, s[24:27], s77 offen nt lds\n"
"v_mfma_f32_16x16x32_bf16 v[52:55], a[148:151], a[24:27], v[52:55]\n"
"v_mfma_f32_16x16x32_bf16 v[64:67], a[152:155], a[24:27], v[64:67]\n"
"v_mfma_f32_16x16x32_bf16 v[72:75], a[152:155], a[28:31], v[72:75]\n"
"v_mfma_f32_16x16x32_bf16 v[192:195], a[152:155], a[32:35], v[192:195]\n"
"v_mfma_f32_16x16x32_bf16 v[200:203], a[152:155], a[36:39], v[200:203]\n"
"v_mfma_f32_16x16x32_bf16 v[128:131], a[152:155], a[40:43], v[128:131]\n"
"v_mfma_f32_16x16x32_bf16 v[136:139], a[152:155], a[44:47], v[136:139]\n"
"v_mfma_f32_16x16x32_bf16 v[140:143], a[156:159], a[44:47], v[140:143]\n"
"v_mfma_f32_16x16x32_bf16 v[132:135], a[156:159], a[40:43], v[132:135]\n"
"v_mfma_f32_16x16x32_bf16 v[204:207], a[156:159], a[36:39], v[204:207]\n"
"v_mfma_f32_16x16x32_bf16 v[196:199], a[156:159], a[32:35], v[196:199]\n"
"v_mfma_f32_16x16x32_bf16 v[76:79], a[156:159], a[28:31], v[76:79]\n"
"v_mfma_f32_16x16x32_bf16 v[68:71], a[156:159], a[24:27], v[68:71]\n"
"s_waitcnt lgkmcnt(0)\n"
"v_mfma_f32_16x16x32_bf16 v[16:19], a[96:99], a[48:51], v[16:19]\n"
"s_waitcnt vmcnt(5)\n"
"s_barrier\n"
"v_mfma_f32_16x16x32_bf16 v[24:27], a[96:99], a[52:55], v[24:27]\n"
"ds_read_b64_tr_b16 a[128:129], v5 offset:49152\n"
"ds_read_b64_tr_b16 a[130:131], v5 offset:49408\n"
"v_mfma_f32_16x16x32_bf16 v[20:23], a[100:103], a[48:51], v[20:23]\n"
"ds_read_b64_tr_b16 a[132:133], v5 offset:49160\n"
"s_add_u32 m0, s79, 155648\n"
"v_mfma_f32_16x16x32_bf16 v[28:31], a[100:103], a[52:55], v[28:31]\n"
"buffer_load_dwordx4 v232, s[24:27], s77 offen offset:1024 nt lds\n"
"v_mfma_f32_16x16x32_bf16 v[32:35], a[104:107], a[48:51], v[32:35]\n"
"ds_read_b64_tr_b16 a[134:135], v5 offset:49416\n"
"v_mfma_f32_16x16x32_bf16 v[40:43], a[104:107], a[52:55], v[40:43]\n"
"ds_read_b64_tr_b16 a[136:137], v5 offset:49664\n"
"v_mfma_f32_16x16x32_bf16 v[36:39], a[108:111], a[48:51], v[36:39]\n"
"ds_read_b64_tr_b16 a[138:139], v5 offset:49920\n"
"v_mfma_f32_16x16x32_bf16 v[44:47], a[108:111], a[52:55], v[44:47]\n"
"ds_read_b64_tr_b16 a[140:141], v5 offset:49672\n"
"v_mfma_f32_16x16x32_bf16 v[48:51], a[112:115], a[48:51], v[48:51]\n"
"ds_read_b64_tr_b16 a[142:143], v5 offset:49928\n"
"v_mfma_f32_16x16x32_bf16 v[56:59], a[112:115], a[52:55], v[56:59]\n"
"ds_read_b64_tr_b16 a[144:145], v5 offset:50176\n"
"s_add_u32 s75, s75, 8192\n"
"s_and_b32 s75, s75, 0x1ffff\n"
"v_mfma_f32_16x16x32_bf16 v[52:55], a[116:119], a[48:51], v[52:55]\n"
"ds_read_b64_tr_b16 a[146:147], v5 offset:50432\n"
"v_mfma_f32_16x16x32_bf16 v[60:63], a[116:119], a[52:55], v[60:63]\n"
"ds_read_b64_tr_b16 a[148:149], v5 offset:50184\n"
"v_mfma_f32_16x16x32_bf16 v[64:67], a[120:123], a[48:51], v[64:67]\n"
"ds_read_b64_tr_b16 a[150:151], v5 offset:50440\n"
"v_mfma_f32_16x16x32_bf16 v[72:75], a[120:123], a[52:55], v[72:75]\n"
"ds_read_b64_tr_b16 a[152:153], v5 offset:50688\n"
"v_mfma_f32_16x16x32_bf16 v[68:71], a[124:127], a[48:51], v[68:71]\n"
"ds_read_b64_tr_b16 a[154:155], v5 offset:50944\n"
"v_mfma_f32_16x16x32_bf16 v[76:79], a[124:127], a[52:55], v[76:79]\n"
"ds_read_b64_tr_b16 a[156:157], v5 offset:50696\n"
"v_mfma_f32_16x16x32_bf16 v[144:147], a[96:99], a[56:59], v[144:147]\n"
"ds_read_b64_tr_b16 a[158:159], v5 offset:50952\n"
"v_accvgpr_read_b32 v5, a160\n"
"v_mfma_f32_16x16x32_bf16 v[152:155], a[96:99], a[60:63], v[152:155]\n"
"v_mfma_f32_16x16x32_bf16 v[148:151], a[100:103], a[56:59], v[148:151]\n"
"v_mfma_f32_16x16x32_bf16 v[156:159], a[100:103], a[60:63], v[156:159]\n"
"s_add_u32 m0, s79, 8192\n"
"v_mfma_f32_16x16x32_bf16 v[160:163], a[104:107], a[56:59], v[160:163]\n"
"buffer_load_dwordx4 v3, s[44:47], s75 offen nt lds\n"
"v_mfma_f32_16x16x32_bf16 v[168:171], a[104:107], a[60:63], v[168:171]\n"
"v_mfma_f32_16x16x32_bf16 v[164:167], a[108:111], a[56:59], v[164:167]\n"
"v_mfma_f32_16x16x32_bf16 v[172:175], a[108:111], a[60:63], v[172:175]\n"
"s_waitcnt lgkmcnt(0)\n"
"v_mfma_f32_16x16x32_bf16 v[16:19], a[128:131], a[72:75], v[16:19]\n"
"v_mfma_f32_16x16x32_bf16 v[24:27], a[128:131], a[76:79], v[24:27]\n"
"v_mfma_f32_16x16x32_bf16 v[20:23], a[132:135], a[72:75], v[20:23]\n"
"v_mfma_f32_16x16x32_bf16 v[28:31], a[132:135], a[76:79], v[28:31]\n"
"v_mfma_f32_16x16x32_bf16 v[32:35], a[136:139], a[72:75], v[32:35]\n"
"s_barrier\n"
"v_mfma_f32_16x16x32_bf16 v[40:43], a[136:139], a[76:79], v[40:43]\n"
"buffer_store_dword v10, v9, s[56:59], 0 offen offset:256 sc1\n"
"v_mfma_f32_16x16x32_bf16 v[36:39], a[140:143], a[72:75], v[36:39]\n"
"v_mul_f32 v16, s34, v16\n"
"v_mul_f32 v17, s34, v17\n"
"v_mfma_f32_16x16x32_bf16 v[44:47], a[140:143], a[76:79], v[44:47]\n"
"v_cvt_pk_bf16_f32 v240, v16, v17\n"
"v_accvgpr_write_b32 a192, v240\n"
"v_mfma_f32_16x16x32_bf16 v[48:51], a[144:147], a[72:75], v[48:51]\n"
"v_mul_f32 v18, s34, v18\n"
"v_mul_f32 v19, s34, v19\n"
"v_mfma_f32_16x16x32_bf16 v[56:59], a[144:147], a[76:79], v[56:59]\n"
"v_cvt_pk_bf16_f32 v241, v18, v19\n"
"v_accvgpr_write_b32 a193, v241\n"
"v_mfma_f32_16x16x32_bf16 v[52:55], a[148:151], a[72:75], v[52:55]\n"
"v_mul_f32 v20, s34, v20\n"
"v_mul_f32 v21, s34, v21\n"
"v_mfma_f32_16x16x32_bf16 v[60:63], a[148:151], a[76:79], v[60:63]\n"
"v_cvt_pk_bf16_f32 v242, v20, v21\n"
"v_accvgpr_write_b32 a194, v242\n"
"s_add_u32 m0, s79, 8192\n"
"v_mfma_f32_16x16x32_bf16 v[64:67], a[152:155], a[72:75], v[64:67]\n"
"buffer_load_dwordx4 v14, s[44:47], s75 offen offset:1024 nt lds\n"
"v_mul_f32 v22, s34, v22\n"
"v_mfma_f32_16x16x32_bf16 v[72:75], a[152:155], a[76:79], v[72:75]\n"
"v_mul_f32 v23, s34, v23\n"
"v_mfma_f32_16x16x32_bf16 v[68:71], a[156:159], a[72:75], v[68:71]\n"
"v_cvt_pk_bf16_f32 v243, v22, v23\n"
"v_accvgpr_write_b32 a195, v243\n"
"v_mfma_f32_16x16x32_bf16 v[76:79], a[156:159], a[76:79], v[76:79]\n"
"v_mul_f32 v24, s34, v24\n"
"v_mul_f32 v25, s34, v25\n"
"v_mfma_f32_16x16x32_bf16 v[176:179], a[112:115], a[56:59], v[176:179]\n"
"v_cvt_pk_bf16_f32 v244, v24, v25\n"
"v_accvgpr_write_b32 a196, v244\n"
"v_mfma_f32_16x16x32_bf16 v[184:187], a[112:115], a[60:63], v[184:187]\n"
"v_mul_f32 v26, s34, v26\n"
"v_mul_f32 v27, s34, v27\n"
"v_mfma_f32_16x16x32_bf16 v[180:183], a[116:119], a[56:59], v[180:183]\n"
"v_cvt_pk_bf16_f32 v245, v26, v27\n"
"v_accvgpr_write_b32 a197, v245\n"
"v_mfma_f32_16x16x32_bf16 v[188:191], a[116:119], a[60:63], v[188:191]\n"
"v_mul_f32 v28, s34, v28\n"
"v_mul_f32 v29, s34, v29\n"
"v_mfma_f32_16x16x32_bf16 v[192:195], a[120:123], a[56:59], v[192:195]\n"
"v_cvt_pk_bf16_f32 v246, v28, v29\n"
"v_accvgpr_write_b32 a198, v246\n"
"v_mfma_f32_16x16x32_bf16 v[200:203], a[120:123], a[60:63], v[200:203]\n"
"v_mul_f32 v30, s34, v30\n"
"v_mul_f32 v31, s34, v31\n"
"v_mfma_f32_16x16x32_bf16 v[196:199], a[124:127], a[56:59], v[196:199]\n"
"v_cvt_pk_bf16_f32 v247, v30, v31\n"
"v_accvgpr_write_b32 a199, v247\n"
"v_mfma_f32_16x16x32_bf16 v[204:207], a[124:127], a[60:63], v[204:207]\n"
"v_mul_f32 v32, s34, v32\n"
"v_mul_f32 v33, s34, v33\n"
"v_mfma_f32_16x16x32_bf16 v[196:199], a[156:159], a[80:83], v[196:199]\n"
"v_cvt_pk_bf16_f32 v248, v32, v33\n"
"v_accvgpr_write_b32 a200, v248\n"
"v_mfma_f32_16x16x32_bf16 v[204:207], a[156:159], a[84:87], v[204:207]\n"
"v_mul_f32 v34, s34, v34\n"
"v_mul_f32 v35, s34, v35\n"
"v_mfma_f32_16x16x32_bf16 v[192:195], a[152:155], a[80:83], v[192:195]\n"
"v_cvt_pk_bf16_f32 v249, v34, v35\n"
"v_accvgpr_write_b32 a201, v249\n"
"v_mfma_f32_16x16x32_bf16 v[200:203], a[152:155], a[84:87], v[200:203]\n"
"v_mul_f32 v36, s34, v36\n"
"v_mul_f32 v37, s34, v37\n"
"v_mfma_f32_16x16x32_bf16 v[180:183], a[148:151], a[80:83], v[180:183]\n"
"v_cvt_pk_bf16_f32 v250, v36, v37\n"
"v_accvgpr_write_b32 a202, v250\n"
"v_mfma_f32_16x16x32_bf16 v[188:191], a[148:151], a[84:87], v[188:191]\n"
"v_mul_f32 v38, s34, v38\n"
"v_mul_f32 v39, s34, v39\n"
"s_add_u32 m0, s79, 90112\n"
"v_mfma_f32_16x16x32_bf16 v[176:179], a[144:147], a[80:83], v[176:179]\n"
"buffer_load_dwordx4 v3, s[20:23], s75 offen nt lds\n"
"v_cvt_pk_bf16_f32 v251, v38, v39\n"
"v_mfma_f32_16x16x32_bf16 v[184:187], a[144:147], a[84:87], v[184:187]\n"
"v_accvgpr_write_b32 a203, v251\n"
"v_mfma_f32_16x16x32_bf16 v[164:167], a[140:143], a[80:83], v[164:167]\n"
"v_mul_f32 v40, s34, v40\n"
"v_mul_f32 v41, s34, v41\n"
"v_mfma_f32_16x16x32_bf16 v[172:175], a[140:143], a[84:87], v[172:175]\n"
"v_cvt_pk_bf16_f32 v252, v40, v41\n"
"v_accvgpr_write_b32 a204, v252\n"
"v_mfma_f32_16x16x32_bf16 v[160:163], a[136:139], a[80:83], v[160:163]\n"
"v_mul_f32 v42, s34, v42\n"
"v_mul_f32 v43, s34, v43\n"
"v_mfma_f32_16x16x32_bf16 v[168:171], a[136:139], a[84:87], v[168:171]\n"
"v_cvt_pk_bf16_f32 v253, v42, v43\n"
"v_accvgpr_write_b32 a205, v253\n"
"v_mfma_f32_16x16x32_bf16 v[148:151], a[132:135], a[80:83], v[148:151]\n"
"v_mul_f32 v44, s34, v44\n"
"v_mul_f32 v45, s34, v45\n"
"v_mfma_f32_16x16x32_bf16 v[156:159], a[132:135], a[84:87], v[156:159]\n"
"v_cvt_pk_bf16_f32 v254, v44, v45\n"
"v_accvgpr_write_b32 a206, v254\n"
"v_mfma_f32_16x16x32_bf16 v[144:147], a[128:131], a[80:83], v[144:147]\n"
"v_mul_f32 v46, s34, v46\n"
"v_mul_f32 v47, s34, v47\n"
"v_mfma_f32_16x16x32_bf16 v[152:155], a[128:131], a[84:87], v[152:155]\n"
"v_cvt_pk_bf16_f32 v255, v46, v47\n"
"v_accvgpr_write_b32 a207, v255\n"
"v_mfma_f32_16x16x32_bf16 v[80:83], a[96:99], a[64:67], v[80:83]\n"
"v_mul_f32 v48, s34, v48\n"
"v_mul_f32 v49, s34, v49\n"
"v_mfma_f32_16x16x32_bf16 v[88:91], a[96:99], a[68:71], v[88:91]\n"
"v_cvt_pk_bf16_f32 v240, v48, v49\n"
"v_accvgpr_write_b32 a208, v240\n"
"v_mfma_f32_16x16x32_bf16 v[84:87], a[100:103], a[64:67], v[84:87]\n"
"v_mul_f32 v50, s34, v50\n"
"v_mul_f32 v51, s34, v51\n"
"v_mfma_f32_16x16x32_bf16 v[92:95], a[100:103], a[68:71], v[92:95]\n"
"v_cvt_pk_bf16_f32 v241, v50, v51\n"
"v_accvgpr_write_b32 a209, v241\n"
"v_mfma_f32_16x16x32_bf16 v[96:99], a[104:107], a[64:67], v[96:99]\n"
"v_mul_f32 v52, s34, v52\n"
"v_mul_f32 v53, s34, v53\n"
"v_mfma_f32_16x16x32_bf16 v[104:107], a[104:107], a[68:71], v[104:107]\n"
"v_cvt_pk_bf16_f32 v242, v52, v53\n"
"v_accvgpr_write_b32 a210, v242\n"
"v_mfma_f32_16x16x32_bf16 v[100:103], a[108:111], a[64:67], v[100:103]\n"
"v_mul_f32 v54, s34, v54\n"
"v_mul_f32 v55, s34, v55\n"
"v_mfma_f32_16x16x32_bf16 v[108:111], a[108:111], a[68:71], v[108:111]\n"
"v_cvt_pk_bf16_f32 v243, v54, v55\n"
"v_accvgpr_write_b32 a211, v243\n"
"v_mfma_f32_16x16x32_bf16 v[112:115], a[112:115], a[64:67], v[112:115]\n"
"s_barrier\n"
"s_add_u32 m0, s79, 90112\n"
"v_mfma_f32_16x16x32_bf16 v[120:123], a[112:115], a[68:71], v[120:123]\n"
"buffer_load_dwordx4 v14, s[20:23], s75 offen offset:1024 nt lds\n"
"v_mfma_f32_16x16x32_bf16 v[116:119], a[116:119], a[64:67], v[116:119]\n"
"ds_read_b128 a[224:227], v5 offset:32768\n"
"v_mfma_f32_16x16x32_bf16 v[124:127], a[116:119], a[68:71], v[124:127]\n"
"ds_read_b128 a[228:231], v5 offset:36864\n"
"s_add_u32 s77, s77, 8192\n"
"s_and_b32 s77, s77, 0x1ffff\n"
"v_mfma_f32_16x16x32_bf16 v[128:131], a[120:123], a[64:67], v[128:131]\n"
"ds_read_b128 a[232:235], v5 offset:33024\n"
"v_mul_f32 v56, s34, v56\n"
"v_mfma_f32_16x16x32_bf16 v[136:139], a[120:123], a[68:71], v[136:139]\n"
"ds_read_b128 a[236:239], v5 offset:37120\n"
"v_mul_f32 v57, s34, v57\n"
"v_mfma_f32_16x16x32_bf16 v[132:135], a[124:127], a[64:67], v[132:135]\n"
"ds_read_b128 a[240:243], v5 offset:33280\n"
"v_cvt_pk_bf16_f32 v244, v56, v57\n"
"v_mfma_f32_16x16x32_bf16 v[140:143], a[124:127], a[68:71], v[140:143]\n"
"ds_read_b128 a[244:247], v5 offset:37376\n"
"v_accvgpr_write_b32 a212, v244\n"
"v_mfma_f32_16x16x32_bf16 v[80:83], a[128:131], a[88:91], v[80:83]\n"
"ds_read_b128 a[248:251], v5 offset:33536\n"
"v_mul_f32 v58, s34, v58\n"
"v_mfma_f32_16x16x32_bf16 v[88:91], a[128:131], a[92:95], v[88:91]\n"
"ds_read_b128 a[252:255], v5 offset:37632\n"
"v_mul_f32 v59, s34, v59\n"
"v_mfma_f32_16x16x32_bf16 v[84:87], a[132:135], a[88:91], v[84:87]\n"
"v_cvt_pk_bf16_f32 v245, v58, v59\n"
"v_accvgpr_write_b32 a213, v245\n"
"v_mfma_f32_16x16x32_bf16 v[92:95], a[132:135], a[92:95], v[92:95]\n"
"v_mul_f32 v60, s34, v60\n"
"v_mul_f32 v61, s34, v61\n"
"s_add_u32 m0, s79, 40960\n"
"v_mfma_f32_16x16x32_bf16 v[96:99], a[136:139], a[88:91], v[96:99]\n"
"s_waitcnt vmcnt(5)\n"
"buffer_load_dwordx4 v4, s[48:51], s77 offen nt lds\n"
"v_mfma_f32_16x16x32_bf16 v[104:107], a[136:139], a[92:95], v[104:107]\n"
"v_cvt_pk_bf16_f32 v246, v60, v61\n"
"v_mfma_f32_16x16x32_bf16 v[100:103], a[140:143], a[88:91], v[100:103]\n"
"v_accvgpr_write_b32 a214, v246\n"
"v_mul_f32 v62, s34, v62\n"
"v_mfma_f32_16x16x32_bf16 v[108:111], a[140:143], a[92:95], v[108:111]\n"
"v_mul_f32 v63, s34, v63\n"
"v_cvt_pk_bf16_f32 v247, v62, v63\n"
"v_mfma_f32_16x16x32_bf16 v[112:115], a[144:147], a[88:91], v[112:115]\n"
"v_accvgpr_write_b32 a215, v247\n"
"v_mul_f32 v64, s34, v64\n"
"v_mfma_f32_16x16x32_bf16 v[120:123], a[144:147], a[92:95], v[120:123]\n"
"v_mul_f32 v65, s34, v65\n"
"v_cvt_pk_bf16_f32 v248, v64, v65\n"
"v_mfma_f32_16x16x32_bf16 v[116:119], a[148:151], a[88:91], v[116:119]\n"
"v_accvgpr_write_b32 a216, v248\n"
"v_mul_f32 v66, s34, v66\n"
"v_mfma_f32_16x16x32_bf16 v[124:127], a[148:151], a[92:95], v[124:127]\n"
"v_mul_f32 v67, s34, v67\n"
"v_cvt_pk_bf16_f32 v249, v66, v67\n"
"v_mfma_f32_16x16x32_bf16 v[128:131], a[152:155], a[88:91], v[128:131]\n"
"v_accvgpr_write_b32 a217, v249\n"
"v_mul_f32 v68, s34, v68\n"
"v_mfma_f32_16x16x32_bf16 v[136:139], a[152:155], a[92:95], v[136:139]\n"
"v_mul_f32 v69, s34, v69\n"
"v_cvt_pk_bf16_f32 v250, v68, v69\n"
"v_mfma_f32_16x16x32_bf16 v[132:135], a[156:159], a[88:91], v[132:135]\n"
"v_accvgpr_write_b32 a218, v250\n"
"v_mul_f32 v70, s34, v70\n"
"v_mfma_f32_16x16x32_bf16 v[140:143], a[156:159], a[92:95], v[140:143]\n"
"v_mul_f32 v71, s34, v71\n"
"s_add_u32 m0, s79, 40960\n"
"s_waitcnt lgkmcnt(0)\n"
"v_mfma_f32_16x16x32_bf16 v[16:19], a[224:227], a[192:195], 0\n"
"s_waitcnt vmcnt(3)\n"
"buffer_load_dwordx4 v232, s[48:51], s77 offen offset:1024 nt lds\n"
"v_mfma_f32_16x16x32_bf16 v[24:27], a[224:227], a[196:199], 0\n"
"v_cvt_pk_bf16_f32 v251, v70, v71\n"
"v_mfma_f32_16x16x32_bf16 v[20:23], a[228:231], a[192:195], 0\n"
"v_accvgpr_write_b32 a219, v251\n"
"v_mul_f32 v72, s34, v72\n"
"v_mfma_f32_16x16x32_bf16 v[28:31], a[228:231], a[196:199], 0\n"
"v_mul_f32 v73, s34, v73\n"
"v_cvt_pk_bf16_f32 v252, v72, v73\n"
"v_accvgpr_write_b32 a220, v252\n"
"v_mfma_f32_16x16x32_bf16 v[16:19], a[232:235], a[200:203], v[16:19]\n"
"v_mul_f32 v74, s34, v74\n"
"v_mul_f32 v75, s34, v75\n"
"v_mfma_f32_16x16x32_bf16 v[24:27], a[232:235], a[204:207], v[24:27]\n"
"v_cvt_pk_bf16_f32 v253, v74, v75\n"
"v_accvgpr_write_b32 a221, v253\n"
"v_mfma_f32_16x16x32_bf16 v[20:23], a[236:239], a[200:203], v[20:23]\n"
"v_mul_f32 v76, s34, v76\n"
"v_mul_f32 v77, s34, v77\n"
"v_mfma_f32_16x16x32_bf16 v[28:31], a[236:239], a[204:207], v[28:31]\n"
"v_cvt_pk_bf16_f32 v254, v76, v77\n"
"v_accvgpr_write_b32 a222, v254\n"
"v_mul_f32 v78, s34, v78\n"
"v_mfma_f32_16x16x32_bf16 v[16:19], a[240:243], a[208:211], v[16:19]\n"
"v_mul_f32 v79, s34, v79\n"
"v_cvt_pk_bf16_f32 v255, v78, v79\n"
"v_mfma_f32_16x16x32_bf16 v[24:27], a[240:243], a[212:215], v[24:27]\n"
"v_accvgpr_write_b32 a223, v255\n"
"v_mul_f32 v206, s34, v206\n"
"v_mfma_f32_16x16x32_bf16 v[20:23], a[244:247], a[208:211], v[20:23]\n"
"v_mul_f32 v207, s34, v207\n"
"v_cvt_pk_bf16_f32 v207, v206, v207\n"
"v_mfma_f32_16x16x32_bf16 v[28:31], a[244:247], a[212:215], v[28:31]\n"
"v_mul_f32 v204, s34, v204\n"
"v_mul_f32 v205, s34, v205\n"
"v_cvt_pk_bf16_f32 v206, v204, v205\n"
"s_add_u32 m0, s79, 122880\n"
"v_mfma_f32_16x16x32_bf16 v[16:19], a[248:251], a[216:219], v[16:19]\n"
"buffer_load_dwordx4 v4, s[24:27], s77 offen nt lds\n"
"v_mfma_f32_16x16x32_bf16 v[24:27], a[248:251], a[220:223], v[24:27]\n"
"v_mul_f32 v202, s34, v202\n"
"v_mfma_f32_16x16x32_bf16 v[20:23], a[252:255], a[216:219], v[20:23]\n"
"v_mul_f32 v203, s34, v203\n"
"v_cvt_pk_bf16_f32 v205, v202, v203\n"
"v_mul_f32 v200, s34, v200\n"
"v_mul_f32 v201, s34, v201\n"
"v_cvt_pk_bf16_f32 v204, v200, v201\n"
"v_mul_f32 v198, s34, v198\n"
"v_mul_f32 v199, s34, v199\n"
"v_cvt_pk_bf16_f32 v203, v198, v199\n"
"v_mfma_f32_16x16x32_bf16 v[28:31], a[252:255], a[220:223], v[28:31]\n"
"v_mul_f32 v196, s34, v196\n"
"v_mul_f32 v197, s34, v197\n"
"v_cvt_pk_bf16_f32 v202, v196, v197\n"
"v_mul_f32 v194, s34, v194\n"
"v_mul_f32 v195, s34, v195\n"
"v_cvt_pk_bf16_f32 v201, v194, v195\n"
"v_mul_f32 v192, s34, v192\n"
"v_mul_f32 v193, s34, v193\n"
"v_cvt_pk_bf16_f32 v200, v192, v193\n"
"s_nop 1\n"
"v_mfma_f32_16x16x32_bf16 v[36:39], a[252:255], v[200:203], 0\n"
"v_mul_f32 v190, s34, v190\n"
"v_mul_f32 v191, s34, v191\n"
"v_mfma_f32_16x16x32_bf16 v[44:47], a[252:255], v[204:207], 0\n"
"v_cvt_pk_bf16_f32 v199, v190, v191\n"
"v_mul_f32 v188, s34, v188\n"
"v_mfma_f32_16x16x32_bf16 v[32:35], a[248:251], v[200:203], 0\n"
"v_mul_f32 v189, s34, v189\n"
"v_cvt_pk_bf16_f32 v198, v188, v189\n"
"v_mul_f32 v186, s34, v186\n"
"v_mul_f32 v187, s34, v187\n"
"v_cvt_pk_bf16_f32 v197, v186, v187\n"
"v_mul_f32 v184, s34, v184\n"
"v_mul_f32 v185, s34, v185\n"
"v_cvt_pk_bf16_f32 v196, v184, v185\n"
"v_mul_f32 v182, s34, v182\n"
"v_mul_f32 v183, s34, v183\n"
"v_mfma_f32_16x16x32_bf16 v[40:43], a[248:251], v[204:207], 0\n"
"v_cvt_pk_bf16_f32 v195, v182, v183\n"
"v_mul_f32 v180, s34, v180\n"
"v_mul_f32 v181, s34, v181\n"
"v_cvt_pk_bf16_f32 v194, v180, v181\n"
"v_mul_f32 v178, s34, v178\n"
"v_mul_f32 v179, s34, v179\n"
"v_cvt_pk_bf16_f32 v193, v178, v179\n"
"v_mul_f32 v176, s34, v176\n"
"v_mul_f32 v177, s34, v177\n"
"v_cvt_pk_bf16_f32 v192, v176, v177\n"
"s_nop 1\n"
"v_mfma_f32_16x16x32_bf16 v[36:39], a[244:247], v[192:195], v[36:39]\n"
"v_mul_f32 v174, s34, v174\n"
"v_mul_f32 v175, s34, v175\n"
"v_mfma_f32_16x16x32_bf16 v[44:47], a[244:247], v[196:199], v[44:47]\n"
"v_cvt_pk_bf16_f32 v191, v174, v175\n"
"v_mul_f32 v172, s34, v172\n"
"v_mfma_f32_16x16x32_bf16 v[32:35], a[240:243], v[192:195], v[32:35]\n"
"v_mul_f32 v173, s34, v173\n"
"v_cvt_pk_bf16_f32 v190, v172, v173\n"
"v_mul_f32 v170, s34, v170\n"
"v_mul_f32 v171, s34, v171\n"
"v_cvt_pk_bf16_f32 v189, v170, v171\n"
"v_mul_f32 v168, s34, v168\n"
"v_mul_f32 v169, s34, v169\n"
"v_cvt_pk_bf16_f32 v188, v168, v169\n"
"v_mul_f32 v166, s34, v166\n"
"v_mul_f32 v167, s34, v167\n"
"v_mfma_f32_16x16x32_bf16 v[40:43], a[240:243], v[196:199], v[40:43]\n"
"v_cvt_pk_bf16_f32 v187, v166, v167\n"
"v_mul_f32 v164, s34, v164\n"
"v_mul_f32 v165, s34, v165\n"
"v_cvt_pk_bf16_f32 v186, v164, v165\n"
"v_mul_f32 v162, s34, v162\n"
"v_mul_f32 v163, s34, v163\n"
"v_cvt_pk_bf16_f32 v185, v162, v163\n"
"v_mul_f32 v160, s34, v160\n"
"v_mul_f32 v161, s34, v161\n"
"v_cvt_pk_bf16_f32 v184, v160, v161\n"
"s_nop 1\n"
"v_mfma_f32_16x16x32_bf16 v[36:39], a[236:239], v[184:187], v[36:39]\n"
"v_mul_f32 v158, s34, v158\n"
"v_mul_f32 v159, s34, v159\n"
"v_mfma_f32_16x16x32_bf16 v[44:47], a[236:239], v[188:191], v[44:47]\n"
"v_cvt_pk_bf16_f32 v183, v158, v159\n"
"v_mul_f32 v156, s34, v156\n"
"s_add_u32 m0, s79, 122880\n"
"v_mfma_f32_16x16x32_bf16 v[32:35], a[232:235], v[184:187], v[32:35]\n"
"s_waitcnt vmcnt(4)\n"
"buffer_load_dwordx4 v232, s[24:27], s77 offen offset:1024 nt lds\n"
"v_mul_f32 v157, s34, v157\n"
"v_cvt_pk_bf16_f32 v182, v156, v157\n"
"v_mul_f32 v154, s34, v154\n"
"v_mul_f32 v155, s34, v155\n"
"v_cvt_pk_bf16_f32 v181, v154, v155\n"
"v_mul_f32 v152, s34, v152\n"
"v_mul_f32 v153, s34, v153\n"
"v_cvt_pk_bf16_f32 v180, v152, v153\n"
"v_mul_f32 v150, s34, v150\n"
"v_mul_f32 v151, s34, v151\n"
"v_mfma_f32_16x16x32_bf16 v[40:43], a[232:235], v[188:191], v[40:43]\n"
"v_cvt_pk_bf16_f32 v179, v150, v151\n"
"v_mul_f32 v148, s34, v148\n"
"v_mul_f32 v149, s34, v149\n"
"v_cvt_pk_bf16_f32 v178, v148, v149\n"
"v_mul_f32 v146, s34, v146\n"
"v_mul_f32 v147, s34, v147\n"
"v_cvt_pk_bf16_f32 v177, v146, v147\n"
"v_mul_f32 v144, s34, v144\n"
"v_mul_f32 v145, s34, v145\n"
"v_cvt_pk_bf16_f32 v176, v144, v145\n"
"s_add_u32 s75, s75, 8192\n"
"s_and_b32 s75, s75, 0x1ffff\n"
"v_mfma_f32_16x16x32_bf16 v[36:39], a[228:231], v[176:179], v[36:39]\n"
"s_barrier\n"
"v_mfma_f32_16x16x32_bf16 v[44:47], a[228:231], v[180:183], v[44:47]\n"
"ds_read_b128 v[240:243], v5 offset:4096\n"
"ds_read_b128 v[244:247], v5 offset:4352\n"
"v_mfma_f32_16x16x32_bf16 v[32:35], a[224:227], v[176:179], v[32:35]\n"
"ds_read_b128 v[248:251], v5 offset:4608\n"
"v_mul_f32 v80, s34, v80\n"
"v_mul_f32 v81, s34, v81\n"
"v_cvt_pk_bf16_f32 v144, v80, v81\n"
"v_mul_f32 v82, s34, v82\n"
"v_mul_f32 v83, s34, v83\n"
"v_cvt_pk_bf16_f32 v145, v82, v83\n"
"v_mul_f32 v84, s34, v84\n"
"v_mul_f32 v85, s34, v85\n"
"v_cvt_pk_bf16_f32 v146, v84, v85\n"
"v_mul_f32 v86, s34, v86\n"
"v_mul_f32 v87, s34, v87\n"
"v_cvt_pk_bf16_f32 v147, v86, v87\n"
"v_mfma_f32_16x16x32_bf16 v[40:43], a[224:227], v[180:183], v[40:43]\n"
"ds_read_b128 v[252:255], v5 offset:4864\n"
"v_mul_f32 v88, s34, v88\n"
"v_mul_f32 v89, s34, v89\n"
"v_cvt_pk_bf16_f32 v148, v88, v89\n"
"v_mul_f32 v90, s34, v90\n"
"v_mul_f32 v91, s34, v91\n"
"v_cvt_pk_bf16_f32 v149, v90, v91\n"
"v_mul_f32 v92, s34, v92\n"
"v_mul_f32 v93, s34, v93\n"
"v_cvt_pk_bf16_f32 v150, v92, v93\n"
"v_mul_f32 v94, s34, v94\n"
"v_mul_f32 v95, s34, v95\n"
"v_cvt_pk_bf16_f32 v151, v94, v95\n"
"v_mfma_f32_16x16x32_bf16 v[48:51], a[224:227], v[144:147], 0\n"
"v_mul_f32 v96, s34, v96\n"
"v_mul_f32 v97, s34, v97\n"
"v_mfma_f32_16x16x32_bf16 v[56:59], a[224:227], v[148:151], 0\n"
"ds_read_b128 a[224:227], v5 offset:0\n"
"v_cvt_pk_bf16_f32 v152, v96, v97\n"
"v_mfma_f32_16x16x32_bf16 v[52:55], a[228:231], v[144:147], 0\n"
"v_mul_f32 v98, s34, v98\n"
"v_mul_f32 v99, s34, v99\n"
"v_cvt_pk_bf16_f32 v153, v98, v99\n"
"v_mul_f32 v100, s34, v100\n"
"v_mul_f32 v101, s34, v101\n"
"v_cvt_pk_bf16_f32 v154, v100, v101\n"
"v_mul_f32 v102, s34, v102\n"
"v_mul_f32 v103, s34, v103\n"
"v_cvt_pk_bf16_f32 v155, v102, v103\n"
"v_mul_f32 v104, s34, v104\n"
"v_mul_f32 v105, s34, v105\n"
"v_mfma_f32_16x16x32_bf16 v[60:63], a[228:231], v[148:151], 0\n"
"ds_read_b128 a[228:231], v5 offset:256\n"
"v_cvt_pk_bf16_f32 v156, v104, v105\n"
"v_mul_f32 v106, s34, v106\n"
"v_mul_f32 v107, s34, v107\n"
"v_cvt_pk_bf16_f32 v157, v106, v107\n"
"v_mul_f32 v108, s34, v108\n"
"v_mul_f32 v109, s34, v109\n"
"v_cvt_pk_bf16_f32 v158, v108, v109\n"
"v_mul_f32 v110, s34, v110\n"
"v_mul_f32 v111, s34, v111\n"
"v_cvt_pk_bf16_f32 v159, v110, v111\n"
"v_mfma_f32_16x16x32_bf16 v[48:51], a[232:235], v[152:155], v[48:51]\n"
"v_mul_f32 v112, s34, v112\n"
"v_mul_f32 v113, s34, v113\n"
"v_mfma_f32_16x16x32_bf16 v[56:59], a[232:235], v[156:159], v[56:59]\n"
"ds_read_b128 a[232:235], v5 offset:512\n"
"v_cvt_pk_bf16_f32 v160, v112, v113\n"
"v_mfma_f32_16x16x32_bf16 v[52:55], a[236:239], v[152:155], v[52:55]\n"
"v_mul_f32 v114, s34, v114\n"
"v_mul_f32 v115, s34, v115\n"
"v_cvt_pk_bf16_f32 v161, v114, v115\n"
"v_mul_f32 v116, s34, v116\n"
"v_mul_f32 v117, s34, v117\n"
"v_cvt_pk_bf16_f32 v162, v116, v117\n"
"v_mul_f32 v118, s34, v118\n"
"v_mul_f32 v119, s34, v119\n"
"v_cvt_pk_bf16_f32 v163, v118, v119\n"
"v_mul_f32 v120, s34, v120\n"
"v_mul_f32 v121, s34, v121\n"
"v_cvt_pk_bf16_f32 v164, v120, v121\n"
"v_mfma_f32_16x16x32_bf16 v[60:63], a[236:239], v[156:159], v[60:63]\n"
"ds_read_b128 a[236:239], v5 offset:768\n"
"s_add_u32 m0, s79, 16384\n"
"s_waitcnt vmcnt(4)\n"
"buffer_load_dwordx4 v3, s[44:47], s75 offen nt lds\n"
"v_mul_f32 v122, s34, v122\n"
"v_mul_f32 v123, s34, v123\n"
"v_cvt_pk_bf16_f32 v165, v122, v123\n"
"v_mul_f32 v124, s34, v124\n"
"v_mul_f32 v125, s34, v125\n"
"v_cvt_pk_bf16_f32 v166, v124, v125\n"
"v_mul_f32 v126, s34, v126\n"
"v_mul_f32 v127, s34, v127\n"
"v_cvt_pk_bf16_f32 v167, v126, v127\n"
"v_mfma_f32_16x16x32_bf16 v[48:51], a[240:243], v[160:163], v[48:51]\n"
"v_mul_f32 v128, s34, v128\n"
"v_mul_f32 v129, s34, v129\n"
"v_mfma_f32_16x16x32_bf16 v[56:59], a[240:243], v[164:167], v[56:59]\n"
"v_cvt_pk_bf16_f32 v168, v128, v129\n"
"v_mul_f32 v130, s34, v130\n"
"v_mfma_f32_16x16x32_bf16 v[52:55], a[244:247], v[160:163], v[52:55]\n"
"v_mul_f32 v131, s34, v131\n"
"v_cvt_pk_bf16_f32 v169, v130, v131\n"
"v_mul_f32 v132, s34, v132\n"
"v_mul_f32 v133, s34, v133\n"
"v_cvt_pk_bf16_f32 v170, v132, v133\n"
"v_mul_f32 v134, s34, v134\n"
"v_mul_f32 v135, s34, v135\n"
"v_cvt_pk_bf16_f32 v171, v134, v135\n"
"v_mul_f32 v136, s34, v136\n"
"v_mul_f32 v137, s34, v137\n"
"v_mfma_f32_16x16x32_bf16 v[60:63], a[244:247], v[164:167], v[60:63]\n"
"v_cvt_pk_bf16_f32 v172, v136, v137\n"
"v_mul_f32 v138, s34, v138\n"
"v_mul_f32 v139, s34, v139\n"
"v_cvt_pk_bf16_f32 v173, v138, v139\n"
"v_mul_f32 v140, s34, v140\n"
"v_mul_f32 v141, s34, v141\n"
"v_cvt_pk_bf16_f32 v174, v140, v141\n"
"v_mul_f32 v142, s34, v142\n"
"v_mul_f32 v143, s34, v143\n"
"v_cvt_pk_bf16_f32 v175, v142, v143\n"
"v_mfma_f32_16x16x32_bf16 v[48:51], a[248:251], v[168:171], v[48:51]\n"
"v_exp_f32 v16, v16\n"
"v_mfma_f32_16x16x32_bf16 v[56:59], a[248:251], v[172:175], v[56:59]\n"
"v_exp_f32 v17, v17\n"
"v_mfma_f32_16x16x32_bf16 v[52:55], a[252:255], v[168:171], v[52:55]\n"
"v_exp_f32 v24, v24\n"
"v_mfma_f32_16x16x32_bf16 v[60:63], a[252:255], v[172:175], v[60:63]\n"
"v_exp_f32 v25, v25\n"
"s_waitcnt lgkmcnt(0)\n"
"v_mfma_f32_16x16x32_bf16 v[64:67], a[224:227], a[192:195], 0\n"
"v_exp_f32 v18, v18\n"
"v_mfma_f32_16x16x32_bf16 v[64:67], a[228:231], a[200:203], v[64:67]\n"
"v_exp_f32 v19, v19\n"
"s_add_u32 m0, s79, 16384\n"
"v_mfma_f32_16x16x32_bf16 v[64:67], a[232:235], a[208:211], v[64:67]\n"
"s_waitcnt vmcnt(3)\n"
"buffer_load_dwordx4 v14, s[44:47], s75 offen offset:1024 nt lds\n"
"v_mfma_f32_16x16x32_bf16 v[64:67], a[236:239], a[216:219], v[64:67]\n"
"v_exp_f32 v26, v26\n"
"v_mfma_f32_16x16x32_bf16 v[72:75], a[224:227], a[196:199], 0\n"
"v_exp_f32 v27, v27\n"
"v_mfma_f32_16x16x32_bf16 v[72:75], a[228:231], a[204:207], v[72:75]\n"
"v_exp_f32 v20, v20\n"
"v_mfma_f32_16x16x32_bf16 v[72:75], a[232:235], a[212:215], v[72:75]\n"
"v_exp_f32 v21, v21\n"
"v_mfma_f32_16x16x32_bf16 v[72:75], a[236:239], a[220:223], v[72:75]\n"
"v_exp_f32 v28, v28\n"
"v_accvgpr_read_b32 v120, a168\n"
"v_mfma_f32_16x16x32_bf16 v[80:83], a[224:227], v[176:179], 0\n"
"v_exp_f32 v29, v29\n"
"v_mfma_f32_16x16x32_bf16 v[80:83], a[228:231], v[184:187], v[80:83]\n"
"v_exp_f32 v22, v22\n"
"v_mfma_f32_16x16x32_bf16 v[80:83], a[232:235], v[192:195], v[80:83]\n"
"v_exp_f32 v23, v23\n"
"v_mfma_f32_16x16x32_bf16 v[80:83], a[236:239], v[200:203], v[80:83]\n"
"v_exp_f32 v30, v30\n"
"v_accvgpr_read_b32 v121, a169\n"
"v_mfma_f32_16x16x32_bf16 v[88:91], a[224:227], v[180:183], 0\n"
"v_exp_f32 v31, v31\n"
"v_mfma_f32_16x16x32_bf16 v[88:91], a[228:231], v[188:191], v[88:91]\n"
"v_accvgpr_read_b32 v122, a170\n"
"v_accvgpr_read_b32 v128, a171\n"
"s_add_u32 m0, s79, 98304\n"
"v_mfma_f32_16x16x32_bf16 v[88:91], a[232:235], v[196:199], v[88:91]\n"
"buffer_load_dwordx4 v3, s[20:23], s75 offen nt lds\n"
"v_mfma_f32_16x16x32_bf16 v[88:91], a[236:239], v[204:207], v[88:91]\n"
"v_accvgpr_read_b32 v129, a172\n"
"v_accvgpr_read_b32 v130, a173\n"
"v_mfma_f32_16x16x32_bf16 v[96:99], a[224:227], v[144:147], 0\n"
"v_accvgpr_read_b32 v142, a175\n"
"v_accvgpr_read_b32 v123, a163\n"
"v_mfma_f32_16x16x32_bf16 v[96:99], a[228:231], v[152:155], v[96:99]\n"
"v_accvgpr_read_b32 v124, a164\n"
"v_accvgpr_read_b32 v125, a165\n"
"v_mfma_f32_16x16x32_bf16 v[96:99], a[232:235], v[160:163], v[96:99]\n"
"v_accvgpr_read_b32 v126, a166\n"
"v_accvgpr_read_b32 v127, a167\n"
"v_mfma_f32_16x16x32_bf16 v[96:99], a[236:239], v[168:171], v[96:99]\n"
"v_add_f32 v233, v16, v17\n"
"v_cvt_pk_bf16_f32 v16, v16, v17\n"
"v_add_f32 v234, v24, v25\n"
"v_mfma_f32_16x16x32_bf16 v[104:107], a[224:227], v[148:151], 0\n"
"v_exp_f32 v32, v32\n"
"v_mfma_f32_16x16x32_bf16 v[104:107], a[228:231], v[156:159], v[104:107]\n"
"v_cvt_pk_bf16_f32 v24, v24, v25\n"
"v_add_f32 v233, v233, v18\n"
"v_mfma_f32_16x16x32_bf16 v[104:107], a[232:235], v[164:167], v[104:107]\n"
"v_exp_f32 v33, v33\n"
"v_mfma_f32_16x16x32_bf16 v[104:107], a[236:239], v[172:175], v[104:107]\n"
"v_exp_f32 v40, v40\n"
"v_add_f32 v233, v233, v19\n"
"v_mfma_f32_16x16x32_bf16 v[68:71], v[240:243], a[192:195], 0\n"
"v_exp_f32 v41, v41\n"
"v_mfma_f32_16x16x32_bf16 v[68:71], v[244:247], a[200:203], v[68:71]\n"
"v_exp_f32 v48, v48\n"
"v_mfma_f32_16x16x32_bf16 v[68:71], v[248:251], a[208:211], v[68:71]\n"
"v_exp_f32 v49, v49\n"
"v_mfma_f32_16x16x32_bf16 v[68:71], v[252:255], a[216:219], v[68:71]\n"
"v_exp_f32 v56, v56\n"
"v_cvt_pk_bf16_f32 v17, v18, v19\n"
"v_mfma_f32_16x16x32_bf16 v[76:79], v[240:243], a[196:199], 0\n"
"s_barrier\n"
"v_mfma_f32_16x16x32_bf16 v[76:79], v[244:247], a[204:207], v[76:79]\n"
"ds_read_b64_tr_b16 a[240:241], v6 offset:32768\n"
"ds_read_b64_tr_b16 a[242:243], v6 offset:36864\n"
"s_add_u32 m0, s79, 98304\n"
"v_mfma_f32_16x16x32_bf16 v[76:79], v[248:251], a[212:215], v[76:79]\n"
"s_waitcnt vmcnt(4)\n"
"buffer_load_dwordx4 v14, s[20:23], s75 offen offset:1024 nt lds\n"
"v_mfma_f32_16x16x32_bf16 v[76:79], v[252:255], a[220:223], v[76:79]\n"
"s_add_u32 s77, s77, 8192\n"
"s_and_b32 s77, s77, 0x1ffff\n"
"v_mfma_f32_16x16x32_bf16 v[84:87], v[240:243], v[176:179], 0\n"
"ds_read_b64_tr_b16 a[244:245], v7 offset:32768\n"
"v_mfma_f32_16x16x32_bf16 v[84:87], v[244:247], v[184:187], v[84:87]\n"
"ds_read_b64_tr_b16 a[246:247], v7 offset:36864\n"
"v_exp_f32 v57, v57\n"
"v_mfma_f32_16x16x32_bf16 v[84:87], v[248:251], v[192:195], v[84:87]\n"
"ds_read_b64_tr_b16 a[248:249], v6 offset:33024\n"
"v_add_f32 v234, v234, v26\n"
"v_mfma_f32_16x16x32_bf16 v[84:87], v[252:255], v[200:203], v[84:87]\n"
"ds_read_b64_tr_b16 a[250:251], v6 offset:37120\n"
"v_add_f32 v234, v234, v27\n"
"v_cvt_pk_bf16_f32 v25, v26, v27\n"
"v_mfma_f32_16x16x32_bf16 v[92:95], v[240:243], v[180:183], 0\n"
"ds_read_b64_tr_b16 a[252:253], v7 offset:33024\n"
"v_mfma_f32_16x16x32_bf16 v[92:95], v[244:247], v[188:191], v[92:95]\n"
"ds_read_b64_tr_b16 a[254:255], v7 offset:37120\n"
"v_exp_f32 v34, v34\n"
"v_mfma_f32_16x16x32_bf16 v[92:95], v[248:251], v[196:199], v[92:95]\n"
"v_exp_f32 v35, v35\n"
"v_mfma_f32_16x16x32_bf16 v[92:95], v[252:255], v[204:207], v[92:95]\n"
"v_exp_f32 v42, v42\n"
"v_cvt_pk_bf16_f32 v18, v20, v21\n"
"s_add_u32 m0, s79, 49152\n"
"v_mfma_f32_16x16x32_bf16 v[100:103], v[240:243], v[144:147], 0\n"
"s_waitcnt vmcnt(3)\n"
"buffer_load_dwordx4 v4, s[48:51], s77 offen nt lds\n"
"v_mfma_f32_16x16x32_bf16 v[100:103], v[244:247], v[152:155], v[100:103]\n"
"v_cvt_pk_bf16_f32 v26, v28, v29\n"
"v_mfma_f32_16x16x32_bf16 v[100:103], v[248:251], v[160:163], v[100:103]\n"
"v_exp_f32 v43, v43\n"
"v_mfma_f32_16x16x32_bf16 v[100:103], v[252:255], v[168:171], v[100:103]\n"
"v_exp_f32 v50, v50\n"
"v_cvt_pk_bf16_f32 v19, v22, v23\n"
"v_mfma_f32_16x16x32_bf16 v[108:111], v[240:243], v[148:151], 0\n"
"v_exp_f32 v51, v51\n"
"v_exp_f32 v58, v58\n"
"v_exp_f32 v59, v59\n"
"v_exp_f32 v36, v36\n"
"v_exp_f32 v37, v37\n"
"v_exp_f32 v44, v44\n"
"v_exp_f32 v45, v45\n"
"v_exp_f32 v52, v52\n"
"v_exp_f32 v53, v53\n"
"v_mfma_f32_16x16x32_bf16 v[108:111], v[244:247], v[156:159], v[108:111]\n"
"v_exp_f32 v60, v60\n"
"v_exp_f32 v61, v61\n"
"v_exp_f32 v38, v38\n"
"v_exp_f32 v39, v39\n"
"v_exp_f32 v46, v46\n"
"v_exp_f32 v47, v47\n"
"v_exp_f32 v54, v54\n"
"v_exp_f32 v55, v55\n"
"v_exp_f32 v62, v62\n"
"v_exp_f32 v63, v63\n"
"v_mfma_f32_16x16x32_bf16 v[108:111], v[248:251], v[164:167], v[108:111]\n"
"v_add_f32 v235, v32, v33\n"
"v_cvt_pk_bf16_f32 v32, v32, v33\n"
"v_add_f32 v236, v40, v41\n"
"v_cvt_pk_bf16_f32 v40, v40, v41\n"
"v_add_f32 v237, v48, v49\n"
"v_cvt_pk_bf16_f32 v48, v48, v49\n"
"v_add_f32 v238, v56, v57\n"
"v_cvt_pk_bf16_f32 v56, v56, v57\n"
"v_add_f32 v235, v235, v34\n"
"v_add_f32 v235, v235, v35\n"
"v_cvt_pk_bf16_f32 v33, v34, v35\n"
"v_add_f32 v236, v236, v42\n"
"v_add_f32 v236, v236, v43\n"
"v_cvt_pk_bf16_f32 v41, v42, v43\n"
"v_add_f32 v237, v237, v50\n"
"v_add_f32 v237, v237, v51\n"
"v_cvt_pk_bf16_f32 v49, v50, v51\n"
"v_add_f32 v238, v238, v58\n"
"v_add_f32 v238, v238, v59\n"
"v_cvt_pk_bf16_f32 v57, v58, v59\n"
"v_add_f32 v233, v233, v20\n"
"v_add_f32 v233, v233, v21\n"
"v_add_f32 v234, v234, v28\n"
"v_add_f32 v234, v234, v29\n"
"v_add_f32 v235, v235, v36\n"
"v_add_f32 v235, v235, v37\n"
"v_cvt_pk_bf16_f32 v34, v36, v37\n"
"v_add_f32 v236, v236, v44\n"
"v_add_f32 v236, v236, v45\n"
"v_cvt_pk_bf16_f32 v42, v44, v45\n"
"v_add_f32 v237, v237, v52\n"
"v_add_f32 v237, v237, v53\n"
"v_mfma_f32_16x16x32_bf16 v[108:111], v[252:255], v[172:175], v[108:111]\n"
"v_cvt_pk_bf16_f32 v50, v52, v53\n"
"v_add_f32 v238, v238, v60\n"
"v_add_f32 v238, v238, v61\n"
"v_cvt_pk_bf16_f32 v58, v60, v61\n"
"v_add_f32 v233, v233, v22\n"
"v_add_f32 v233, v233, v23\n"
"v_add_f32 v234, v234, v30\n"
"v_add_f32 v234, v234, v31\n"
"v_cvt_pk_bf16_f32 v27, v30, v31\n"
"v_add_f32 v235, v235, v38\n"
"v_add_f32 v235, v235, v39\n"
"v_cvt_pk_bf16_f32 v35, v38, v39\n"
"v_add_f32 v236, v236, v46\n"
"v_add_f32 v236, v236, v47\n"
"v_cvt_pk_bf16_f32 v43, v46, v47\n"
"v_add_f32 v237, v237, v54\n"
"v_add_f32 v237, v237, v55\n"
"v_cvt_pk_bf16_f32 v51, v54, v55\n"
"v_add_f32 v238, v238, v62\n"
"v_add_f32 v238, v238, v63\n"
"v_cvt_pk_bf16_f32 v59, v62, v63\n"
"v_max3_f32 v118, v233, v234, v235\n"
"v_max3_f32 v118, v118, v236, v237\n"
"v_max_f32 v118, v118, v238\n"
"v_cmp_nge_f32 vcc, 0x47800000, v118\n"
"s_nop 0\n"
"s_mov_b64 s[92:93], vcc\n"
"v_min3_f32 v119, v233, v234, v235\n"
"v_min3_f32 v119, v119, v236, v237\n"
"v_min_f32 v119, v119, v238\n"
"v_cmp_nle_f32 vcc, 0x37800000, v119\n"
"s_nop 0\n"
"s_or_b64 vcc, vcc, s[92:93]\n"
"s_cbranch_vccnz Ltr0_%=\n"
"Lrt0_%=:\n"
"s_waitcnt lgkmcnt(0)\n"
"v_mfma_f32_16x16x32_bf16 a[0:3], a[240:243], v[16:19], 0\n"
"v_exp_f32 v64, v64\n"
"v_mfma_f32_16x16x32_bf16 a[4:7], a[240:243], v[24:27], 0\n"
"v_exp_f32 v65, v65\n"
"v_mfma_f32_16x16x32_bf16 a[64:67], a[240:243], v[32:35], 0\n"
"v_exp_f32 v72, v72\n"
"v_mfma_f32_16x16x32_bf16 a[68:71], a[240:243], v[40:43], 0\n"
"v_exp_f32 v73, v73\n"
"v_mov_b32 v112, v233\n"
"s_add_u32 m0, s79, 49152\n"
"v_mfma_f32_16x16x32_bf16 a[128:131], a[240:243], v[48:51], 0\n"
"buffer_load_dwordx4 v232, s[48:51], s77 offen offset:1024 nt lds\n"
"v_mfma_f32_16x16x32_bf16 a[132:135], a[240:243], v[56:59], 0\n"
"v_mfma_f32_16x16x32_bf16 a[136:139], a[244:247], v[48:51], 0\n"
"ds_read_b64_tr_b16 a[240:241], v6 offset:33280\n"
"v_mov_b32 v113, v234\n"
"v_mfma_f32_16x16x32_bf16 a[140:143], a[244:247], v[56:59], 0\n"
"ds_read_b64_tr_b16 a[242:243], v6 offset:37376\n"
"v_exp_f32 v66, v66\n"
"v_mfma_f32_16x16x32_bf16 a[72:75], a[244:247], v[32:35], 0\n"
"v_exp_f32 v67, v67\n"
"v_mfma_f32_16x16x32_bf16 a[76:79], a[244:247], v[40:43], 0\n"
"v_exp_f32 v74, v74\n"
"v_mfma_f32_16x16x32_bf16 a[8:11], a[244:247], v[16:19], 0\n"
"v_exp_f32 v75, v75\n"
"v_mfma_f32_16x16x32_bf16 a[12:15], a[244:247], v[24:27], 0\n"
"ds_read_b64_tr_b16 a[244:245], v7 offset:33280\n"
"ds_read_b64_tr_b16 a[246:247], v7 offset:37376\n"
"v_add_f32 v233, v64, v65\n"
"v_mfma_f32_16x16x32_bf16 a[16:19], a[248:251], v[16:19], 0\n"
"v_exp_f32 v68, v68\n"
"v_mfma_f32_16x16x32_bf16 a[20:23], a[248:251], v[24:27], 0\n"
"v_exp_f32 v69, v69\n"
"v_mfma_f32_16x16x32_bf16 a[80:83], a[248:251], v[32:35], 0\n"
"v_exp_f32 v76, v76\n"
"v_mfma_f32_16x16x32_bf16 a[84:87], a[248:251], v[40:43], 0\n"
"v_exp_f32 v77, v77\n"
"v_cvt_pk_bf16_f32 v64, v64, v65\n"
"v_mfma_f32_16x16x32_bf16 a[144:147], a[248:251], v[48:51], 0\n"
"v_mfma_f32_16x16x32_bf16 a[148:151], a[248:251], v[56:59], 0\n"
"ds_read_b64_tr_b16 a[248:249], v6 offset:33536\n"
"s_add_u32 m0, s79, 131072\n"
"s_waitcnt vmcnt(4)\n"
"buffer_load_dwordx4 v4, s[24:27], s77 offen nt lds\n"
"v_mfma_f32_16x16x32_bf16 a[152:155], a[252:255], v[48:51], 0\n"
"ds_read_b64_tr_b16 a[250:251], v6 offset:37632\n"
"v_mfma_f32_16x16x32_bf16 a[156:159], a[252:255], v[56:59], 0\n"
"v_exp_f32 v70, v70\n"
"v_exp_f32 v71, v71\n"
"v_mfma_f32_16x16x32_bf16 a[88:91], a[252:255], v[32:35], 0\n"
"v_exp_f32 v78, v78\n"
"v_mfma_f32_16x16x32_bf16 a[92:95], a[252:255], v[40:43], 0\n"
"v_exp_f32 v79, v79\n"
"v_mfma_f32_16x16x32_bf16 a[24:27], a[252:255], v[16:19], 0\n"
"v_add_f32 v234, v72, v73\n"
"v_cvt_pk_bf16_f32 v72, v72, v73\n"
"v_mfma_f32_16x16x32_bf16 a[28:31], a[252:255], v[24:27], 0\n"
"ds_read_b64_tr_b16 a[252:253], v7 offset:33536\n"
"ds_read_b64_tr_b16 a[254:255], v7 offset:37632\n"
"s_waitcnt lgkmcnt(3)\n"
"v_mfma_f32_16x16x32_bf16 a[32:35], a[240:243], v[16:19], 0\n"
"v_exp_f32 v80, v80\n"
"v_mfma_f32_16x16x32_bf16 a[36:39], a[240:243], v[24:27], 0\n"
"v_exp_f32 v81, v81\n"
"v_mfma_f32_16x16x32_bf16 a[96:99], a[240:243], v[32:35], 0\n"
"v_exp_f32 v88, v88\n"
"v_mfma_f32_16x16x32_bf16 a[100:103], a[240:243], v[40:43], 0\n"
"v_exp_f32 v89, v89\n"
"v_add_f32 v233, v233, v66\n"
"v_mfma_f32_16x16x32_bf16 a[160:163], a[240:243], v[48:51], 0\n"
"s_barrier\n"
"v_mfma_f32_16x16x32_bf16 a[164:167], a[240:243], v[56:59], 0\n"
"ds_read_b128 a[224:227], v5 offset:8192\n"
"ds_read_b128 a[228:231], v5 offset:8448\n"
"v_mfma_f32_16x16x32_bf16 a[168:171], a[244:247], v[48:51], 0\n"
"ds_read_b128 a[232:235], v5 offset:8704\n"
"v_add_f32 v233, v233, v67\n"
"s_add_u32 m0, s79, 131072\n"
"v_mfma_f32_16x16x32_bf16 a[172:175], a[244:247], v[56:59], 0\n"
"s_waitcnt vmcnt(3)\n"
"buffer_load_dwordx4 v232, s[24:27], s77 offen offset:1024 nt lds\n"
"v_mfma_f32_16x16x32_bf16 a[104:107], a[244:247], v[32:35], 0\n"
"ds_read_b128 a[236:239], v5 offset:8960\n"
"v_mfma_f32_16x16x32_bf16 a[108:111], a[244:247], v[40:43], 0\n"
"ds_read_b128 v[240:243], v5 offset:12288\n"
"s_add_u32 s75, s75, 8192\n"
"s_and_b32 s75, s75, 0x1ffff\n"
"v_mfma_f32_16x16x32_bf16 a[40:43], a[244:247], v[16:19], 0\n"
"ds_read_b128 v[244:247], v5 offset:12544\n"
"v_mfma_f32_16x16x32_bf16 a[44:47], a[244:247], v[24:27], 0\n"
"ds_read_b128 v[248:251], v5 offset:12800\n"
"v_exp_f32 v96, v96\n"
"s_waitcnt lgkmcnt(5)\n"
"v_mfma_f32_16x16x32_bf16 a[48:51], a[248:251], v[16:19], 0\n"
"ds_read_b128 v[252:255], v5 offset:13056\n"
"v_cvt_pk_bf16_f32 v65, v66, v67\n"
"v_mfma_f32_16x16x32_bf16 a[52:55], a[248:251], v[24:27], 0\n"
"v_exp_f32 v97, v97\n"
"v_mfma_f32_16x16x32_bf16 a[112:115], a[248:251], v[32:35], 0\n"
"v_exp_f32 v104, v104\n"
"v_mfma_f32_16x16x32_bf16 a[116:119], a[248:251], v[40:43], 0\n"
"v_exp_f32 v105, v105\n"
"v_add_f32 v234, v234, v74\n"
"v_mfma_f32_16x16x32_bf16 a[176:179], a[248:251], v[48:51], 0\n"
"v_exp_f32 v82, v82\n"
"v_mfma_f32_16x16x32_bf16 a[180:183], a[248:251], v[56:59], 0\n"
"v_add_f32 v234, v234, v75\n"
"v_cvt_pk_bf16_f32 v73, v74, v75\n"
"s_add_u32 m0, s79, 24576\n"
"v_mfma_f32_16x16x32_bf16 a[184:187], a[252:255], v[48:51], 0\n"
"buffer_load_dwordx4 v3, s[44:47], s75 offen nt lds\n"
"v_mfma_f32_16x16x32_bf16 a[188:191], a[252:255], v[56:59], 0\n"
"v_exp_f32 v83, v83\n"
"v_mfma_f32_16x16x32_bf16 a[120:123], a[252:255], v[32:35], 0\n"
"v_exp_f32 v90, v90\n"
"v_mfma_f32_16x16x32_bf16 a[124:127], a[252:255], v[40:43], 0\n"
"v_exp_f32 v91, v91\n"
"v_mfma_f32_16x16x32_bf16 a[56:59], a[252:255], v[16:19], 0\n"
"v_exp_f32 v98, v98\n"
"v_mfma_f32_16x16x32_bf16 a[60:63], a[252:255], v[24:27], 0\n"
"v_exp_f32 v99, v99\n"
"v_cvt_pk_bf16_f32 v66, v68, v69\n"
"v_mfma_f32_16x16x32_bf16 v[16:19], a[224:227], a[192:195], v[208:211]\n"
"v_exp_f32 v106, v106\n"
"v_mfma_f32_16x16x32_bf16 v[16:19], a[228:231], a[200:203], v[16:19]\n"
"v_cvt_pk_bf16_f32 v74, v76, v77\n"
"s_waitcnt lgkmcnt(0)\n"
"v_mfma_f32_16x16x32_bf16 v[16:19], a[232:235], a[208:211], v[16:19]\n"
"v_exp_f32 v107, v107\n"
"v_mfma_f32_16x16x32_bf16 v[16:19], a[236:239], a[216:219], v[16:19]\n"
"v_exp_f32 v84, v84\n"
"v_cvt_pk_bf16_f32 v67, v70, v71\n"
"v_mfma_f32_16x16x32_bf16 v[24:27], a[224:227], a[196:199], v[212:215]\n"
"v_exp_f32 v85, v85\n"
"v_mfma_f32_16x16x32_bf16 v[24:27], a[228:231], a[204:207], v[24:27]\n"
"v_exp_f32 v92, v92\n"
"s_add_u32 m0, s79, 24576\n"
"v_mfma_f32_16x16x32_bf16 v[24:27], a[232:235], a[212:215], v[24:27]\n"
"s_waitcnt vmcnt(4)\n"
"buffer_load_dwordx4 v14, s[44:47], s75 offen offset:1024 nt lds\n"
"v_mfma_f32_16x16x32_bf16 v[24:27], a[236:239], a[220:223], v[24:27]\n"
"v_exp_f32 v93, v93\n"
"v_mfma_f32_16x16x32_bf16 v[32:35], a[224:227], v[176:179], v[216:219]\n"
"v_exp_f32 v100, v100\n"
"v_mfma_f32_16x16x32_bf16 v[32:35], a[228:231], v[184:187], v[32:35]\n"
"v_exp_f32 v101, v101\n"
"v_mfma_f32_16x16x32_bf16 v[32:35], a[232:235], v[192:195], v[32:35]\n"
"v_exp_f32 v108, v108\n"
"v_mfma_f32_16x16x32_bf16 v[32:35], a[236:239], v[200:203], v[32:35]\n"
"v_exp_f32 v109, v109\n"
"v_cvt_pk_bf16_f32 v75, v78, v79\n"
"v_mfma_f32_16x16x32_bf16 v[40:43], a[224:227], v[180:183], v[220:223]\n"
"v_exp_f32 v86, v86\n"
"v_mfma_f32_16x16x32_bf16 v[40:43], a[228:231], v[188:191], v[40:43]\n"
"v_exp_f32 v87, v87\n"
"v_mfma_f32_16x16x32_bf16 v[40:43], a[232:235], v[196:199], v[40:43]\n"
"v_exp_f32 v94, v94\n"
"v_mfma_f32_16x16x32_bf16 v[40:43], a[236:239], v[204:207], v[40:43]\n"
"v_exp_f32 v95, v95\n"
"v_mov_b32 v114, v235\n"
"v_mfma_f32_16x16x32_bf16 v[48:51], a[224:227], v[144:147], v[224:227]\n"
"v_exp_f32 v102, v102\n"
"v_mfma_f32_16x16x32_bf16 v[48:51], a[228:231], v[152:155], v[48:51]\n"
"v_exp_f32 v103, v103\n"
"v_mfma_f32_16x16x32_bf16 v[48:51], a[232:235], v[160:163], v[48:51]\n"
"v_exp_f32 v110, v110\n"
"v_mfma_f32_16x16x32_bf16 v[48:51], a[236:239], v[168:171], v[48:51]\n"
"v_exp_f32 v111, v111\n"
"v_mov_b32 v115, v236\n"
"s_add_u32 m0, s79, 106496\n"
"v_mfma_f32_16x16x32_bf16 v[56:59], a[224:227], v[148:151], v[228:231]\n"
"s_waitcnt vmcnt(4)\n"
"buffer_load_dwordx4 v3, s[20:23], s75 offen nt lds\n"
"v_mfma_f32_16x16x32_bf16 v[56:59], a[228:231], v[156:159], v[56:59]\n"
"v_mov_b32 v116, v237\n"
"v_mfma_f32_16x16x32_bf16 v[56:59], a[232:235], v[164:167], v[56:59]\n"
"v_mov_b32 v117, v238\n"
"v_add_f32 v235, v80, v81\n"
"v_mfma_f32_16x16x32_bf16 v[56:59], a[236:239], v[172:175], v[56:59]\n"
"v_cvt_pk_bf16_f32 v80, v80, v81\n"
"v_add_f32 v236, v88, v89\n"
"v_cvt_pk_bf16_f32 v88, v88, v89\n"
"v_mfma_f32_16x16x32_bf16 v[20:23], v[240:243], a[192:195], v[208:211]\n"
"v_add_f32 v237, v96, v97\n"
"v_cvt_pk_bf16_f32 v96, v96, v97\n"
"v_mfma_f32_16x16x32_bf16 v[20:23], v[244:247], a[200:203], v[20:23]\n"
"v_add_f32 v238, v104, v105\n"
"v_cvt_pk_bf16_f32 v104, v104, v105\n"
"v_mfma_f32_16x16x32_bf16 v[20:23], v[248:251], a[208:211], v[20:23]\n"
"v_add_f32 v235, v235, v82\n"
"v_add_f32 v235, v235, v83\n"
"v_mfma_f32_16x16x32_bf16 v[20:23], v[252:255], a[216:219], v[20:23]\n"
"v_cvt_pk_bf16_f32 v81, v82, v83\n"
"v_add_f32 v236, v236, v90\n"
"v_add_f32 v236, v236, v91\n"
"v_mfma_f32_16x16x32_bf16 v[28:31], v[240:243], a[196:199], v[212:215]\n"
"s_barrier\n"
"v_mfma_f32_16x16x32_bf16 v[28:31], v[244:247], a[204:207], v[28:31]\n"
"ds_read_b64_tr_b16 a[240:241], v6 offset:0\n"
"ds_read_b64_tr_b16 a[242:243], v6 offset:4096\n"
"v_mfma_f32_16x16x32_bf16 v[28:31], v[248:251], a[212:215], v[28:31]\n"
"ds_read_b64_tr_b16 a[244:245], v7 offset:0\n"
"v_cvt_pk_bf16_f32 v89, v90, v91\n"
"v_mfma_f32_16x16x32_bf16 v[28:31], v[252:255], a[220:223], v[28:31]\n"
"ds_read_b64_tr_b16 a[246:247], v7 offset:4096\n"
"v_add_f32 v237, v237, v98\n"
"v_add_f32 v237, v237, v99\n"
"v_mfma_f32_16x16x32_bf16 v[36:39], v[240:243], v[176:179], v[216:219]\n"
"ds_read_b64_tr_b16 a[248:249], v6 offset:256\n"
"s_add_u32 m0, s79, 106496\n"
"v_mfma_f32_16x16x32_bf16 v[36:39], v[244:247], v[184:187], v[36:39]\n"
"s_waitcnt vmcnt(3)\n"
"buffer_load_dwordx4 v14, s[20:23], s75 offen offset:1024 nt lds\n"
"v_mfma_f32_16x16x32_bf16 v[36:39], v[248:251], v[192:195], v[36:39]\n"
"ds_read_b64_tr_b16 a[250:251], v6 offset:4352\n"
"v_cvt_pk_bf16_f32 v97, v98, v99\n"
"v_mfma_f32_16x16x32_bf16 v[36:39], v[252:255], v[200:203], v[36:39]\n"
"ds_read_b64_tr_b16 a[252:253], v7 offset:256\n"
"s_add_u32 s77, s77, 8192\n"
"s_and_b32 s77, s77, 0x1ffff\n"
"v_mfma_f32_16x16x32_bf16 v[44:47], v[240:243], v[180:183], v[220:223]\n"
"ds_read_b64_tr_b16 a[254:255], v7 offset:4352\n"
"v_add_f32 v238, v238, v106\n"
"v_mfma_f32_16x16x32_bf16 v[44:47], v[244:247], v[188:191], v[44:47]\n"
"v_add_f32 v238, v238, v107\n"
"v_cvt_pk_bf16_f32 v105, v106, v107\n"
"v_mfma_f32_16x16x32_bf16 v[44:47], v[248:251], v[196:199], v[44:47]\n"
"v_add_f32 v233, v233, v68\n"
"v_add_f32 v233, v233, v69\n"
"v_mfma_f32_16x16x32_bf16 v[44:47], v[252:255], v[204:207], v[44:47]\n"
"v_add_f32 v234, v234, v76\n"
"v_add_f32 v234, v234, v77\n"
"v_add_f32 v235, v235, v84\n"
"v_mfma_f32_16x16x32_bf16 v[52:55], v[240:243], v[144:147], v[224:227]\n"
"v_add_f32 v235, v235, v85\n"
"v_cvt_pk_bf16_f32 v82, v84, v85\n"
"v_mfma_f32_16x16x32_bf16 v[52:55], v[244:247], v[152:155], v[52:55]\n"
"v_add_f32 v236, v236, v92\n"
"v_add_f32 v236, v236, v93\n"
"v_mfma_f32_16x16x32_bf16 v[52:55], v[248:251], v[160:163], v[52:55]\n"
"v_cvt_pk_bf16_f32 v90, v92, v93\n"
"v_add_f32 v237, v237, v100\n"
"v_mfma_f32_16x16x32_bf16 v[52:55], v[252:255], v[168:171], v[52:55]\n"
"v_add_f32 v237, v237, v101\n"
"v_cvt_pk_bf16_f32 v98, v100, v101\n"
"v_add_f32 v238, v238, v108\n"
"s_add_u32 m0, s79, 57344\n"
"v_mfma_f32_16x16x32_bf16 v[60:63], v[240:243], v[148:151], v[228:231]\n"
"buffer_load_dwordx4 v4, s[48:51], s77 offen nt lds\n"
"v_mfma_f32_16x16x32_bf16 v[60:63], v[244:247], v[156:159], v[60:63]\n"
"v_add_f32 v238, v238, v109\n"
"v_mfma_f32_16x16x32_bf16 v[60:63], v[248:251], v[164:167], v[60:63]\n"
"v_cvt_pk_bf16_f32 v106, v108, v109\n"
"v_add_f32 v233, v233, v70\n"
"v_add_f32 v233, v233, v71\n"
"v_add_f32 v234, v234, v78\n"
"v_add_f32 v234, v234, v79\n"
"v_add_f32 v235, v235, v86\n"
"v_add_f32 v235, v235, v87\n"
"v_cvt_pk_bf16_f32 v83, v86, v87\n"
"v_add_f32 v236, v236, v94\n"
"v_add_f32 v236, v236, v95\n"
"v_cvt_pk_bf16_f32 v91, v94, v95\n"
"v_mfma_f32_16x16x32_bf16 v[60:63], v[252:255], v[172:175], v[60:63]\n"
"v_add_f32 v237, v237, v102\n"
"v_add_f32 v237, v237, v103\n"
"v_cvt_pk_bf16_f32 v99, v102, v103\n"
"v_add_f32 v238, v238, v110\n"
"v_add_f32 v238, v238, v111\n"
"v_cvt_pk_bf16_f32 v107, v110, v111\n"
"v_max3_f32 v118, v233, v234, v235\n"
"v_max3_f32 v118, v118, v236, v237\n"
"v_max_f32 v118, v118, v238\n"
"v_cmp_nge_f32 vcc, 0x47800000, v118\n"
"s_nop 0\n"
"s_cbranch_vccnz Ltr1_%=\n"
"Lrt1_%=:\n"
"s_waitcnt lgkmcnt(0)\n"
"v_mfma_f32_16x16x32_bf16 a[0:3], a[240:243], v[64:67], a[0:3]\n"
"v_exp_f32 v16, v16\n"
"v_mfma_f32_16x16x32_bf16 a[4:7], a[240:243], v[72:75], a[4:7]\n"
"v_exp_f32 v17, v17\n"
"v_mfma_f32_16x16x32_bf16 a[64:67], a[240:243], v[80:83], a[64:67]\n"
"v_exp_f32 v24, v24\n"
"v_mfma_f32_16x16x32_bf16 a[68:71], a[240:243], v[88:91], a[68:71]\n"
"v_exp_f32 v25, v25\n"
"v_add_f32 v112, v112, v233\n"
"v_mfma_f32_16x16x32_bf16 a[128:131], a[240:243], v[96:99], a[128:131]\n"
"v_exp_f32 v18, v18\n"
"v_mfma_f32_16x16x32_bf16 a[132:135], a[240:243], v[104:107], a[132:135]\n"
"ds_read_b64_tr_b16 a[240:241], v6 offset:512\n"
"ds_read_b64_tr_b16 a[242:243], v6 offset:4608\n"
"v_mfma_f32_16x16x32_bf16 a[136:139], a[244:247], v[96:99], a[136:139]\n"
"v_exp_f32 v19, v19\n"
"v_mfma_f32_16x16x32_bf16 a[140:143], a[244:247], v[104:107], a[140:143]\n"
"v_exp_f32 v26, v26\n"
"v_add_f32 v113, v113, v234\n"
"v_mfma_f32_16x16x32_bf16 a[72:75], a[244:247], v[80:83], a[72:75]\n"
"v_exp_f32 v27, v27\n"
"v_mfma_f32_16x16x32_bf16 a[76:79], a[244:247], v[88:91], a[76:79]\n"
"v_exp_f32 v20, v20\n"
"v_mfma_f32_16x16x32_bf16 a[8:11], a[244:247], v[64:67], a[8:11]\n"
"v_add_f32 v233, v16, v17\n"
"v_mfma_f32_16x16x32_bf16 a[12:15], a[244:247], v[72:75], a[12:15]\n"
"ds_read_b64_tr_b16 a[244:245], v7 offset:512\n"
"s_add_u32 m0, s79, 57344\n"
"s_waitcnt vmcnt(4)\n"
"buffer_load_dwordx4 v232, s[48:51], s77 offen offset:1024 nt lds\n"
"v_mfma_f32_16x16x32_bf16 a[16:19], a[248:251], v[64:67], a[16:19]\n"
"ds_read_b64_tr_b16 a[246:247], v7 offset:4608\n"
"v_cvt_pk_bf16_f32 v16, v16, v17\n"
"v_mfma_f32_16x16x32_bf16 a[20:23], a[248:251], v[72:75], a[20:23]\n"
"v_exp_f32 v21, v21\n"
"v_mfma_f32_16x16x32_bf16 a[80:83], a[248:251], v[80:83], a[80:83]\n"
"v_exp_f32 v28, v28\n"
"v_mfma_f32_16x16x32_bf16 a[84:87], a[248:251], v[88:91], a[84:87]\n"
"v_exp_f32 v29, v29\n"
"v_add_f32 v234, v24, v25\n"
"v_mfma_f32_16x16x32_bf16 a[144:147], a[248:251], v[96:99], a[144:147]\n"
"v_exp_f32 v22, v22\n"
"v_mfma_f32_16x16x32_bf16 a[148:151], a[248:251], v[104:107], a[148:151]\n"
"ds_read_b64_tr_b16 a[248:249], v6 offset:768\n"
"ds_read_b64_tr_b16 a[250:251], v6 offset:4864\n"
"v_mfma_f32_16x16x32_bf16 a[152:155], a[252:255], v[96:99], a[152:155]\n"
"v_exp_f32 v23, v23\n"
"v_mfma_f32_16x16x32_bf16 a[156:159], a[252:255], v[104:107], a[156:159]\n"
"v_exp_f32 v30, v30\n"
"v_cvt_pk_bf16_f32 v24, v24, v25\n"
"v_mfma_f32_16x16x32_bf16 a[88:91], a[252:255], v[80:83], a[88:91]\n"
"v_exp_f32 v32, v32\n"
"v_mfma_f32_16x16x32_bf16 a[92:95], a[252:255], v[88:91], a[92:95]\n"
"v_exp_f32 v31, v31\n"
"v_mfma_f32_16x16x32_bf16 a[24:27], a[252:255], v[64:67], a[24:27]\n"
"v_mfma_f32_16x16x32_bf16 a[28:31], a[252:255], v[72:75], a[28:31]\n"
"ds_read_b64_tr_b16 a[252:253], v7 offset:768\n"
"s_add_u32 m0, s79, 139264\n"
"s_waitcnt vmcnt(3)\n"
"buffer_load_dwordx4 v4, s[24:27], s77 offen nt lds\n"
"s_waitcnt lgkmcnt(1)\n"
"v_mfma_f32_16x16x32_bf16 a[32:35], a[240:243], v[64:67], a[32:35]\n"
"ds_read_b64_tr_b16 a[254:255], v7 offset:4864\n"
"v_add_f32 v233, v233, v18\n"
"v_mfma_f32_16x16x32_bf16 a[36:39], a[240:243], v[72:75], a[36:39]\n"
"v_exp_f32 v33, v33\n"
"v_mfma_f32_16x16x32_bf16 a[96:99], a[240:243], v[80:83], a[96:99]\n"
"v_exp_f32 v40, v40\n"
"v_mfma_f32_16x16x32_bf16 a[100:103], a[240:243], v[88:91], a[100:103]\n"
"v_exp_f32 v41, v41\n"
"v_add_f32 v233, v233, v19\n"
"v_mfma_f32_16x16x32_bf16 a[160:163], a[240:243], v[96:99], a[160:163]\n"
"s_barrier\n"
"v_mfma_f32_16x16x32_bf16 a[164:167], a[240:243], v[104:107], a[164:167]\n"
"ds_read_b128 a[224:227], v5 offset:16384\n"
"ds_read_b128 a[228:231], v5 offset:16640\n"
"v_mfma_f32_16x16x32_bf16 a[168:171], a[244:247], v[96:99], a[168:171]\n"
"ds_read_b128 a[232:235], v5 offset:16896\n"
"v_cvt_pk_bf16_f32 v17, v18, v19\n"
"v_mfma_f32_16x16x32_bf16 a[172:175], a[244:247], v[104:107], a[172:175]\n"
"ds_read_b128 a[236:239], v5 offset:17152\n"
"v_exp_f32 v48, v48\n"
"v_mfma_f32_16x16x32_bf16 a[104:107], a[244:247], v[80:83], a[104:107]\n"
"ds_read_b128 v[240:243], v5 offset:20480\n"
"v_mfma_f32_16x16x32_bf16 a[108:111], a[244:247], v[88:91], a[108:111]\n"
"ds_read_b128 v[244:247], v5 offset:20736\n"
"v_exp_f32 v49, v49\n"
"s_add_u32 m0, s79, 139264\n"
"v_mfma_f32_16x16x32_bf16 a[40:43], a[244:247], v[64:67], a[40:43]\n"
"buffer_load_dwordx4 v232, s[24:27], s77 offen offset:1024 nt lds\n"
"v_mfma_f32_16x16x32_bf16 a[44:47], a[244:247], v[72:75], a[44:47]\n"
"s_add_u32 s75, s75, 8192\n"
"s_and_b32 s75, s75, 0x1ffff\n"
"v_mfma_f32_16x16x32_bf16 a[48:51], a[248:251], v[64:67], a[48:51]\n"
"ds_read_b128 v[248:251], v5 offset:20992\n"
"v_mfma_f32_16x16x32_bf16 a[52:55], a[248:251], v[72:75], a[52:55]\n"
"ds_read_b128 v[252:255], v5 offset:21248\n"
"v_exp_f32 v56, v56\n"
"v_mfma_f32_16x16x32_bf16 a[112:115], a[248:251], v[80:83], a[112:115]\n"
"v_exp_f32 v57, v57\n"
"v_mfma_f32_16x16x32_bf16 a[116:119], a[248:251], v[88:91], a[116:119]\n"
"v_add_f32 v234, v234, v26\n"
"v_add_f32 v234, v234, v27\n"
"v_cvt_pk_bf16_f32 v25, v26, v27\n"
"v_mfma_f32_16x16x32_bf16 a[176:179], a[248:251], v[96:99], a[176:179]\n"
"v_exp_f32 v34, v34\n"
"v_mfma_f32_16x16x32_bf16 a[180:183], a[248:251], v[104:107], a[180:183]\n"
"v_cvt_pk_bf16_f32 v18, v20, v21\n"
"s_waitcnt lgkmcnt(2)\n"
"v_mfma_f32_16x16x32_bf16 a[184:187], a[252:255], v[96:99], a[184:187]\n"
"v_exp_f32 v35, v35\n"
"v_mfma_f32_16x16x32_bf16 a[188:191], a[252:255], v[104:107], a[188:191]\n"
"v_exp_f32 v42, v42\n"
"v_cvt_pk_bf16_f32 v26, v28, v29\n"
"v_mfma_f32_16x16x32_bf16 a[120:123], a[252:255], v[80:83], a[120:123]\n"
"v_exp_f32 v43, v43\n"
"v_mfma_f32_16x16x32_bf16 a[124:127], a[252:255], v[88:91], a[124:127]\n"
"v_exp_f32 v50, v50\n"
"s_add_u32 m0, s79, 32768\n"
"v_mfma_f32_16x16x32_bf16 a[56:59], a[252:255], v[64:67], a[56:59]\n"
"s_waitcnt vmcnt(4)\n"
"buffer_load_dwordx4 v3, s[44:47], s75 offen nt lds\n"
"v_mfma_f32_16x16x32_bf16 a[60:63], a[252:255], v[72:75], a[60:63]\n"
"v_exp_f32 v51, v51\n"
"v_mfma_f32_16x16x32_bf16 v[64:67], a[224:227], a[192:195], v[208:211]\n"
"v_exp_f32 v58, v58\n"
"v_mfma_f32_16x16x32_bf16 v[64:67], a[228:231], a[200:203], v[64:67]\n"
"v_exp_f32 v59, v59\n"
"v_mfma_f32_16x16x32_bf16 v[64:67], a[232:235], a[208:211], v[64:67]\n"
"v_exp_f32 v36, v36\n"
"v_mfma_f32_16x16x32_bf16 v[64:67], a[236:239], a[216:219], v[64:67]\n"
"v_exp_f32 v37, v37\n"
"v_cvt_pk_bf16_f32 v19, v22, v23\n"
"v_mfma_f32_16x16x32_bf16 v[72:75], a[224:227], a[196:199], v[212:215]\n"
"v_exp_f32 v44, v44\n"
"v_mfma_f32_16x16x32_bf16 v[72:75], a[228:231], a[204:207], v[72:75]\n"
"v_exp_f32 v45, v45\n"
"v_mfma_f32_16x16x32_bf16 v[72:75], a[232:235], a[212:215], v[72:75]\n"
"v_exp_f32 v52, v52\n"
"v_mfma_f32_16x16x32_bf16 v[72:75], a[236:239], a[220:223], v[72:75]\n"
"v_exp_f32 v53, v53\n"
"v_cvt_pk_bf16_f32 v27, v30, v31\n"
"v_mfma_f32_16x16x32_bf16 v[80:83], a[224:227], v[176:179], v[216:219]\n"
"v_exp_f32 v60, v60\n"
"v_mfma_f32_16x16x32_bf16 v[80:83], a[228:231], v[184:187], v[80:83]\n"
"v_exp_f32 v61, v61\n"
"v_mfma_f32_16x16x32_bf16 v[80:83], a[232:235], v[192:195], v[80:83]\n"
"v_exp_f32 v38, v38\n"
"v_mfma_f32_16x16x32_bf16 v[80:83], a[236:239], v[200:203], v[80:83]\n"
"v_exp_f32 v39, v39\n"
"v_add_f32 v114, v114, v235\n"
"s_add_u32 m0, s79, 32768\n"
"v_mfma_f32_16x16x32_bf16 v[88:91], a[224:227], v[180:183], v[220:223]\n"
"s_waitcnt vmcnt(3)\n"
"buffer_load_dwordx4 v14, s[44:47], s75 offen offset:1024 nt lds\n"
"v_mfma_f32_16x16x32_bf16 v[88:91], a[228:231], v[188:191], v[88:91]\n"
"v_add_f32 v115, v115, v236\n"
"v_mfma_f32_16x16x32_bf16 v[88:91], a[232:235], v[196:199], v[88:91]\n"
"v_exp_f32 v46, v46\n"
"v_mfma_f32_16x16x32_bf16 v[88:91], a[236:239], v[204:207], v[88:91]\n"
"v_exp_f32 v47, v47\n"
"v_add_f32 v116, v116, v237\n"
"v_mfma_f32_16x16x32_bf16 v[96:99], a[224:227], v[144:147], v[224:227]\n"
"v_exp_f32 v54, v54\n"
"v_mfma_f32_16x16x32_bf16 v[96:99], a[228:231], v[152:155], v[96:99]\n"
"v_exp_f32 v55, v55\n"
"v_mfma_f32_16x16x32_bf16 v[96:99], a[232:235], v[160:163], v[96:99]\n"
"v_exp_f32 v62, v62\n"
"v_mfma_f32_16x16x32_bf16 v[96:99], a[236:239], v[168:171], v[96:99]\n"
"v_exp_f32 v63, v63\n"
"v_add_f32 v117, v117, v238\n"
"v_mfma_f32_16x16x32_bf16 v[104:107], a[224:227], v[148:151], v[228:231]\n"
"v_add_f32 v235, v32, v33\n"
"v_cvt_pk_bf16_f32 v32, v32, v33\n"
"v_mfma_f32_16x16x32_bf16 v[104:107], a[228:231], v[156:159], v[104:107]\n"
"v_add_f32 v236, v40, v41\n"
"v_cvt_pk_bf16_f32 v40, v40, v41\n"
"v_mfma_f32_16x16x32_bf16 v[104:107], a[232:235], v[164:167], v[104:107]\n"
"v_add_f32 v237, v48, v49\n"
"v_cvt_pk_bf16_f32 v48, v48, v49\n"
"v_mfma_f32_16x16x32_bf16 v[104:107], a[236:239], v[172:175], v[104:107]\n"
"v_add_f32 v238, v56, v57\n"
"v_cvt_pk_bf16_f32 v56, v56, v57\n"
"v_add_f32 v235, v235, v34\n"
"s_add_u32 m0, s79, 114688\n"
"v_mfma_f32_16x16x32_bf16 v[68:71], v[240:243], a[192:195], v[208:211]\n"
"buffer_load_dwordx4 v3, s[20:23], s75 offen nt lds\n"
"v_mfma_f32_16x16x32_bf16 v[68:71], v[244:247], a[200:203], v[68:71]\n"
"s_waitcnt lgkmcnt(0)\n"
"v_mfma_f32_16x16x32_bf16 v[68:71], v[248:251], a[208:211], v[68:71]\n"
"v_add_f32 v235, v235, v35\n"
"v_cvt_pk_bf16_f32 v33, v34, v35\n"
"v_mfma_f32_16x16x32_bf16 v[68:71], v[252:255], a[216:219], v[68:71]\n"
"v_add_f32 v236, v236, v42\n"
"v_add_f32 v236, v236, v43\n"
"v_cvt_pk_bf16_f32 v41, v42, v43\n"
"v_mfma_f32_16x16x32_bf16 v[76:79], v[240:243], a[196:199], v[212:215]\n"
"s_barrier\n"
"v_mfma_f32_16x16x32_bf16 v[76:79], v[244:247], a[204:207], v[76:79]\n"
"ds_read_b64_tr_b16 a[240:241], v6 offset:8192\n"
"ds_read_b64_tr_b16 a[242:243], v6 offset:12288\n"
"v_mfma_f32_16x16x32_bf16 v[76:79], v[248:251], a[212:215], v[76:79]\n"
"ds_read_b64_tr_b16 a[244:245], v7 offset:8192\n"
"v_add_f32 v237, v237, v50\n"
"v_mfma_f32_16x16x32_bf16 v[76:79], v[252:255], a[220:223], v[76:79]\n"
"ds_read_b64_tr_b16 a[246:247], v7 offset:12288\n"
"v_add_f32 v237, v237, v51\n"
"v_cvt_pk_bf16_f32 v49, v50, v51\n"
"v_mfma_f32_16x16x32_bf16 v[84:87], v[240:243], v[176:179], v[216:219]\n"
"ds_read_b64_tr_b16 a[248:249], v6 offset:8448\n"
"v_add_f32 v238, v238, v58\n"
"v_mfma_f32_16x16x32_bf16 v[84:87], v[244:247], v[184:187], v[84:87]\n"
"ds_read_b64_tr_b16 a[250:251], v6 offset:12544\n"
"v_add_f32 v238, v238, v59\n"
"v_mfma_f32_16x16x32_bf16 v[84:87], v[248:251], v[192:195], v[84:87]\n"
"ds_read_b64_tr_b16 a[252:253], v7 offset:8448\n"
"v_cvt_pk_bf16_f32 v57, v58, v59\n"
"v_mfma_f32_16x16x32_bf16 v[84:87], v[252:255], v[200:203], v[84:87]\n"
"ds_read_b64_tr_b16 a[254:255], v7 offset:12544\n"
"v_add_f32 v233, v233, v20\n"
"v_add_f32 v233, v233, v21\n"
"s_add_u32 m0, s79, 114688\n"
"v_mfma_f32_16x16x32_bf16 v[92:95], v[240:243], v[180:183], v[220:223]\n"
"s_waitcnt vmcnt(4)\n"
"buffer_load_dwordx4 v14, s[20:23], s75 offen offset:1024 nt lds\n"
"v_mfma_f32_16x16x32_bf16 v[92:95], v[244:247], v[188:191], v[92:95]\n"
"v_add_f32 v234, v234, v28\n"
"v_mfma_f32_16x16x32_bf16 v[92:95], v[248:251], v[196:199], v[92:95]\n"
"v_add_f32 v234, v234, v29\n"
"v_add_f32 v235, v235, v36\n"
"v_mfma_f32_16x16x32_bf16 v[92:95], v[252:255], v[204:207], v[92:95]\n"
"v_add_f32 v235, v235, v37\n"
"s_add_u32 s77, s77, 8192\n"
"s_and_b32 s77, s77, 0x1ffff\n"
"v_mfma_f32_16x16x32_bf16 v[100:103], v[240:243], v[144:147], v[224:227]\n"
"v_cvt_pk_bf16_f32 v34, v36, v37\n"
"v_add_f32 v236, v236, v44\n"
"v_mfma_f32_16x16x32_bf16 v[100:103], v[244:247], v[152:155], v[100:103]\n"
"v_add_f32 v236, v236, v45\n"
"v_cvt_pk_bf16_f32 v42, v44, v45\n"
"v_mfma_f32_16x16x32_bf16 v[100:103], v[248:251], v[160:163], v[100:103]\n"
"v_add_f32 v237, v237, v52\n"
"v_add_f32 v237, v237, v53\n"
"v_mfma_f32_16x16x32_bf16 v[100:103], v[252:255], v[168:171], v[100:103]\n"
"v_cvt_pk_bf16_f32 v50, v52, v53\n"
"v_add_f32 v238, v238, v60\n"
"v_add_f32 v238, v238, v61\n"
"v_mfma_f32_16x16x32_bf16 v[108:111], v[240:243], v[148:151], v[228:231]\n"
"v_cvt_pk_bf16_f32 v58, v60, v61\n"
"v_add_f32 v233, v233, v22\n"
"v_mfma_f32_16x16x32_bf16 v[108:111], v[244:247], v[156:159], v[108:111]\n"
"v_add_f32 v233, v233, v23\n"
"v_add_f32 v234, v234, v30\n"
"v_mfma_f32_16x16x32_bf16 v[108:111], v[248:251], v[164:167], v[108:111]\n"
"v_add_f32 v234, v234, v31\n"
"v_add_f32 v235, v235, v38\n"
"v_add_f32 v235, v235, v39\n"
"v_cvt_pk_bf16_f32 v35, v38, v39\n"
"v_add_f32 v236, v236, v46\n"
"v_add_f32 v236, v236, v47\n"
"v_cvt_pk_bf16_f32 v43, v46, v47\n"
"v_add_f32 v237, v237, v54\n"
"v_add_f32 v237, v237, v55\n"
"v_mfma_f32_16x16x32_bf16 v[108:111], v[252:255], v[172:175], v[108:111]\n"
"v_cvt_pk_bf16_f32 v51, v54, v55\n"
"v_add_f32 v238, v238, v62\n"
"v_add_f32 v238, v238, v63\n"
"v_cvt_pk_bf16_f32 v59, v62, v63\n"
"v_max3_f32 v118, v233, v234, v235\n"
"v_max3_f32 v118, v118, v236, v237\n"
"v_max_f32 v118, v118, v238\n"
"v_cmp_nge_f32 vcc, 0x47800000, v118\n"
"s_nop 0\n"
"s_cbranch_vccnz Ltr2_%=\n"
"Lrt2_%=:\n"
"s_waitcnt lgkmcnt(0)\n"
"v_mfma_f32_16x16x32_bf16 a[0:3], a[240:243], v[16:19], a[0:3]\n"
"v_exp_f32 v64, v64\n"
"v_mfma_f32_16x16x32_bf16 a[4:7], a[240:243], v[24:27], a[4:7]\n"
"v_exp_f32 v65, v65\n"
"s_add_u32 m0, s79, 65536\n"
"v_mfma_f32_16x16x32_bf16 a[64:67], a[240:243], v[32:35], a[64:67]\n"
"s_waitcnt vmcnt(4)\n"
"buffer_load_dwordx4 v4, s[48:51], s77 offen nt lds\n"
"v_mfma_f32_16x16x32_bf16 a[68:71], a[240:243], v[40:43], a[68:71]\n"
"v_exp_f32 v72, v72\n"
"v_mfma_f32_16x16x32_bf16 a[128:131], a[240:243], v[48:51], a[128:131]\n"
"v_exp_f32 v73, v73\n"
"v_mfma_f32_16x16x32_bf16 a[132:135], a[240:243], v[56:59], a[132:135]\n"
"ds_read_b64_tr_b16 a[240:241], v6 offset:8704\n"
"ds_read_b64_tr_b16 a[242:243], v6 offset:12800\n"
"v_mfma_f32_16x16x32_bf16 a[136:139], a[244:247], v[48:51], a[136:139]\n"
"v_exp_f32 v66, v66\n"
"v_mfma_f32_16x16x32_bf16 a[140:143], a[244:247], v[56:59], a[140:143]\n"
"v_exp_f32 v67, v67\n"
"v_add_f32 v112, v112, v233\n"
"v_mfma_f32_16x16x32_bf16 a[72:75], a[244:247], v[32:35], a[72:75]\n"
"v_exp_f32 v74, v74\n"
"v_mfma_f32_16x16x32_bf16 a[76:79], a[244:247], v[40:43], a[76:79]\n"
"v_exp_f32 v75, v75\n"
"v_mfma_f32_16x16x32_bf16 a[8:11], a[244:247], v[16:19], a[8:11]\n"
"v_exp_f32 v68, v68\n"
"v_mfma_f32_16x16x32_bf16 a[12:15], a[244:247], v[24:27], a[12:15]\n"
"ds_read_b64_tr_b16 a[244:245], v7 offset:8704\n"
"ds_read_b64_tr_b16 a[246:247], v7 offset:12800\n"
"v_add_f32 v113, v113, v234\n"
"v_mfma_f32_16x16x32_bf16 a[16:19], a[248:251], v[16:19], a[16:19]\n"
"v_exp_f32 v69, v69\n"
"v_mfma_f32_16x16x32_bf16 a[20:23], a[248:251], v[24:27], a[20:23]\n"
"v_exp_f32 v76, v76\n"
"s_add_u32 m0, s79, 65536\n"
"v_mfma_f32_16x16x32_bf16 a[80:83], a[248:251], v[32:35], a[80:83]\n"
"s_waitcnt vmcnt(3)\n"
"buffer_load_dwordx4 v232, s[48:51], s77 offen offset:1024 nt lds\n"
"v_mfma_f32_16x16x32_bf16 a[84:87], a[248:251], v[40:43], a[84:87]\n"
"v_exp_f32 v77, v77\n"
"v_mfma_f32_16x16x32_bf16 a[144:147], a[248:251], v[48:51], a[144:147]\n"
"v_exp_f32 v70, v70\n"
"v_mfma_f32_16x16x32_bf16 a[148:151], a[248:251], v[56:59], a[148:151]\n"
"ds_read_b64_tr_b16 a[248:249], v6 offset:8960\n"
"ds_read_b64_tr_b16 a[250:251], v6 offset:13056\n"
"v_mfma_f32_16x16x32_bf16 a[152:155], a[252:255], v[48:51], a[152:155]\n"
"v_exp_f32 v71, v71\n"
"v_mfma_f32_16x16x32_bf16 a[156:159], a[252:255], v[56:59], a[156:159]\n"
"v_exp_f32 v78, v78\n"
"v_add_f32 v233, v64, v65\n"
"v_mfma_f32_16x16x32_bf16 a[88:91], a[252:255], v[32:35], a[88:91]\n"
"v_exp_f32 v79, v79\n"
"v_mfma_f32_16x16x32_bf16 a[92:95], a[252:255], v[40:43], a[92:95]\n"
"v_cvt_pk_bf16_f32 v64, v64, v65\n"
"v_add_f32 v234, v72, v73\n"
"v_mfma_f32_16x16x32_bf16 a[24:27], a[252:255], v[16:19], a[24:27]\n"
"v_cvt_pk_bf16_f32 v72, v72, v73\n"
"v_add_f32 v233, v233, v66\n"
"v_mfma_f32_16x16x32_bf16 a[28:31], a[252:255], v[24:27], a[28:31]\n"
"ds_read_b64_tr_b16 a[252:253], v7 offset:8960\n"
"ds_read_b64_tr_b16 a[254:255], v7 offset:13056\n"
"s_waitcnt lgkmcnt(2)\n"
"v_mfma_f32_16x16x32_bf16 a[32:35], a[240:243], v[16:19], a[32:35]\n"
"v_exp_f32 v80, v80\n"
"v_mfma_f32_16x16x32_bf16 a[36:39], a[240:243], v[24:27], a[36:39]\n"
"v_exp_f32 v81, v81\n"
"s_add_u32 m0, s79, 147456\n"
"v_mfma_f32_16x16x32_bf16 a[96:99], a[240:243], v[32:35], a[96:99]\n"
"buffer_load_dwordx4 v4, s[24:27], s77 offen nt lds\n"
"v_mfma_f32_16x16x32_bf16 a[100:103], a[240:243], v[40:43], a[100:103]\n"
"v_exp_f32 v88, v88\n"
"v_mfma_f32_16x16x32_bf16 a[160:163], a[240:243], v[48:51], a[160:163]\n"
"s_barrier\n"
"v_mfma_f32_16x16x32_bf16 a[164:167], a[240:243], v[56:59], a[164:167]\n"
"ds_read_b128 a[224:227], v5 offset:24576\n"
"ds_read_b128 a[228:231], v5 offset:24832\n"
"v_mfma_f32_16x16x32_bf16 a[168:171], a[244:247], v[48:51], a[168:171]\n"
"ds_read_b128 a[232:235], v5 offset:25088\n"
"v_add_f32 v233, v233, v67\n"
"v_mfma_f32_16x16x32_bf16 a[172:175], a[244:247], v[56:59], a[172:175]\n"
"ds_read_b128 a[236:239], v5 offset:25344\n"
"v_exp_f32 v89, v89\n"
"v_mfma_f32_16x16x32_bf16 a[104:107], a[244:247], v[32:35], a[104:107]\n"
"ds_read_b128 v[240:243], v5 offset:28672\n"
"v_mfma_f32_16x16x32_bf16 a[108:111], a[244:247], v[40:43], a[108:111]\n"
"ds_read_b128 v[244:247], v5 offset:28928\n"
"v_exp_f32 v96, v96\n"
"v_mfma_f32_16x16x32_bf16 a[40:43], a[244:247], v[16:19], a[40:43]\n"
"ds_read_b128 v[248:251], v5 offset:29184\n"
"v_cvt_pk_bf16_f32 v65, v66, v67\n"
"v_mfma_f32_16x16x32_bf16 a[44:47], a[244:247], v[24:27], a[44:47]\n"
"ds_read_b128 v[252:255], v5 offset:29440\n"
"v_exp_f32 v97, v97\n"
"v_mfma_f32_16x16x32_bf16 a[48:51], a[248:251], v[16:19], a[48:51]\n"
"v_exp_f32 v104, v104\n"
"v_mfma_f32_16x16x32_bf16 a[52:55], a[248:251], v[24:27], a[52:55]\n"
"v_exp_f32 v105, v105\n"
"v_mfma_f32_16x16x32_bf16 a[112:115], a[248:251], v[32:35], a[112:115]\n"
"v_exp_f32 v82, v82\n"
"v_mfma_f32_16x16x32_bf16 a[116:119], a[248:251], v[40:43], a[116:119]\n"
"v_add_f32 v234, v234, v74\n"
"v_add_f32 v234, v234, v75\n"
"v_cvt_pk_bf16_f32 v73, v74, v75\n"
"s_add_u32 m0, s79, 147456\n"
"v_mfma_f32_16x16x32_bf16 a[176:179], a[248:251], v[48:51], a[176:179]\n"
"s_waitcnt vmcnt(4)\n"
"buffer_load_dwordx4 v232, s[24:27], s77 offen offset:1024 nt lds\n"
"v_mfma_f32_16x16x32_bf16 a[180:183], a[248:251], v[56:59], a[180:183]\n"
"s_waitcnt lgkmcnt(0)\n"
"v_mfma_f32_16x16x32_bf16 a[184:187], a[252:255], v[48:51], a[184:187]\n"
"v_exp_f32 v83, v83\n"
"v_mfma_f32_16x16x32_bf16 a[188:191], a[252:255], v[56:59], a[188:191]\n"
"v_cvt_pk_bf16_f32 v66, v68, v69\n"
"s_add_u32 s75, s75, 8192\n"
"s_and_b32 s75, s75, 0x1ffff\n"
"v_mfma_f32_16x16x32_bf16 a[120:123], a[252:255], v[32:35], a[120:123]\n"
"v_exp_f32 v90, v90\n"
"v_mfma_f32_16x16x32_bf16 a[124:127], a[252:255], v[40:43], a[124:127]\n"
"v_exp_f32 v91, v91\n"
"v_mfma_f32_16x16x32_bf16 a[56:59], a[252:255], v[16:19], a[56:59]\n"
"v_exp_f32 v98, v98\n"
"v_mfma_f32_16x16x32_bf16 a[60:63], a[252:255], v[24:27], a[60:63]\n"
"v_exp_f32 v99, v99\n"
"v_cvt_pk_bf16_f32 v74, v76, v77\n"
"v_mfma_f32_16x16x32_bf16 v[16:19], a[224:227], a[192:195], v[208:211]\n"
"v_exp_f32 v106, v106\n"
"v_mfma_f32_16x16x32_bf16 v[16:19], a[228:231], a[200:203], v[16:19]\n"
"v_exp_f32 v107, v107\n"
"v_mfma_f32_16x16x32_bf16 v[16:19], a[232:235], a[208:211], v[16:19]\n"
"v_exp_f32 v84, v84\n"
"v_mfma_f32_16x16x32_bf16 v[16:19], a[236:239], a[216:219], v[16:19]\n"
"v_exp_f32 v85, v85\n"
"v_cvt_pk_bf16_f32 v67, v70, v71\n"
"s_add_u32 m0, s79, 0\n"
"v_mfma_f32_16x16x32_bf16 v[24:27], a[224:227], a[196:199], v[212:215]\n"
"s_waitcnt vmcnt(3)\n"
"buffer_load_dwordx4 v3, s[44:47], s75 offen nt lds\n"
"v_mfma_f32_16x16x32_bf16 v[24:27], a[228:231], a[204:207], v[24:27]\n"
"v_cvt_pk_bf16_f32 v75, v78, v79\n"
"v_mfma_f32_16x16x32_bf16 v[24:27], a[232:235], a[212:215], v[24:27]\n"
"v_exp_f32 v92, v92\n"
"v_mfma_f32_16x16x32_bf16 v[24:27], a[236:239], a[220:223], v[24:27]\n"
"v_exp_f32 v93, v93\n"
"v_add_f32 v114, v114, v235\n"
"v_mfma_f32_16x16x32_bf16 v[32:35], a[224:227], v[176:179], v[216:219]\n"
"v_exp_f32 v100, v100\n"
"v_mfma_f32_16x16x32_bf16 v[32:35], a[228:231], v[184:187], v[32:35]\n"
"v_exp_f32 v101, v101\n"
"v_mfma_f32_16x16x32_bf16 v[32:35], a[232:235], v[192:195], v[32:35]\n"
"v_exp_f32 v108, v108\n"
"v_mfma_f32_16x16x32_bf16 v[32:35], a[236:239], v[200:203], v[32:35]\n"
"v_exp_f32 v109, v109\n"
"v_add_f32 v115, v115, v236\n"
"v_mfma_f32_16x16x32_bf16 v[40:43], a[224:227], v[180:183], v[220:223]\n"
"v_exp_f32 v86, v86\n"
"v_mfma_f32_16x16x32_bf16 v[40:43], a[228:231], v[188:191], v[40:43]\n"
"v_exp_f32 v87, v87\n"
"v_mfma_f32_16x16x32_bf16 v[40:43], a[232:235], v[196:199], v[40:43]\n"
"v_exp_f32 v94, v94\n"
"v_mfma_f32_16x16x32_bf16 v[40:43], a[236:239], v[204:207], v[40:43]\n"
"v_exp_f32 v95, v95\n"
"v_add_f32 v116, v116, v237\n"
"s_add_u32 m0, s79, 0\n"
"v_mfma_f32_16x16x32_bf16 v[48:51], a[224:227], v[144:147], v[224:227]\n"
"buffer_load_dwordx4 v14, s[44:47], s75 offen offset:1024 nt lds\n"
"v_mfma_f32_16x16x32_bf16 v[48:51], a[228:231], v[152:155], v[48:51]\n"
"v_add_f32 v117, v117, v238\n"
"v_mfma_f32_16x16x32_bf16 v[48:51], a[232:235], v[160:163], v[48:51]\n"
"v_exp_f32 v102, v102\n"
"v_mfma_f32_16x16x32_bf16 v[48:51], a[236:239], v[168:171], v[48:51]\n"
"v_exp_f32 v103, v103\n"
"v_add_f32 v235, v80, v81\n"
"v_mfma_f32_16x16x32_bf16 v[56:59], a[224:227], v[148:151], v[228:231]\n"
"v_exp_f32 v110, v110\n"
"v_mfma_f32_16x16x32_bf16 v[56:59], a[228:231], v[156:159], v[56:59]\n"
"v_exp_f32 v111, v111\n"
"v_mfma_f32_16x16x32_bf16 v[56:59], a[232:235], v[164:167], v[56:59]\n"
"v_cvt_pk_bf16_f32 v80, v80, v81\n"
"v_add_f32 v236, v88, v89\n"
"v_mfma_f32_16x16x32_bf16 v[56:59], a[236:239], v[172:175], v[56:59]\n"
"v_cvt_pk_bf16_f32 v88, v88, v89\n"
"v_add_f32 v237, v96, v97\n"
"v_cvt_pk_bf16_f32 v96, v96, v97\n"
"v_mfma_f32_16x16x32_bf16 v[20:23], v[240:243], a[192:195], v[208:211]\n"
"v_add_f32 v238, v104, v105\n"
"v_cvt_pk_bf16_f32 v104, v104, v105\n"
"v_mfma_f32_16x16x32_bf16 v[20:23], v[244:247], a[200:203], v[20:23]\n"
"v_add_f32 v235, v235, v82\n"
"v_add_f32 v235, v235, v83\n"
"v_mfma_f32_16x16x32_bf16 v[20:23], v[248:251], a[208:211], v[20:23]\n"
"v_cvt_pk_bf16_f32 v81, v82, v83\n"
"v_add_f32 v236, v236, v90\n"
"v_mfma_f32_16x16x32_bf16 v[20:23], v[252:255], a[216:219], v[20:23]\n"
"v_add_f32 v236, v236, v91\n"
"v_cvt_pk_bf16_f32 v89, v90, v91\n"
"v_add_f32 v237, v237, v98\n"
"v_mfma_f32_16x16x32_bf16 v[28:31], v[240:243], a[196:199], v[212:215]\n"
"s_barrier\n"
"v_mfma_f32_16x16x32_bf16 v[28:31], v[244:247], a[204:207], v[28:31]\n"
"ds_read_b64_tr_b16 a[240:241], v6 offset:16384\n"
"ds_read_b64_tr_b16 a[242:243], v6 offset:20480\n"
"v_mfma_f32_16x16x32_bf16 v[28:31], v[248:251], a[212:215], v[28:31]\n"
"ds_read_b64_tr_b16 a[244:245], v7 offset:16384\n"
"v_add_f32 v237, v237, v99\n"
"s_add_u32 m0, s79, 81920\n"
"v_mfma_f32_16x16x32_bf16 v[28:31], v[252:255], a[220:223], v[28:31]\n"
"s_waitcnt vmcnt(4)\n"
"buffer_load_dwordx4 v3, s[20:23], s75 offen nt lds\n"
"v_mfma_f32_16x16x32_bf16 v[36:39], v[240:243], v[176:179], v[216:219]\n"
"ds_read_b64_tr_b16 a[246:247], v7 offset:20480\n"
"v_cvt_pk_bf16_f32 v97, v98, v99\n"
"v_mfma_f32_16x16x32_bf16 v[36:39], v[244:247], v[184:187], v[36:39]\n"
"ds_read_b64_tr_b16 a[248:249], v6 offset:16640\n"
"v_add_f32 v238, v238, v106\n"
"v_mfma_f32_16x16x32_bf16 v[36:39], v[248:251], v[192:195], v[36:39]\n"
"ds_read_b64_tr_b16 a[250:251], v6 offset:20736\n"
"v_add_f32 v238, v238, v107\n"
"v_mfma_f32_16x16x32_bf16 v[36:39], v[252:255], v[200:203], v[36:39]\n"
"ds_read_b64_tr_b16 a[252:253], v7 offset:16640\n"
"v_cvt_pk_bf16_f32 v105, v106, v107\n"
"v_add_f32 v233, v233, v68\n"
"v_mfma_f32_16x16x32_bf16 v[44:47], v[240:243], v[180:183], v[220:223]\n"
"ds_read_b64_tr_b16 a[254:255], v7 offset:20736\n"
"v_add_f32 v233, v233, v69\n"
"v_mfma_f32_16x16x32_bf16 v[44:47], v[244:247], v[188:191], v[44:47]\n"
"v_add_f32 v234, v234, v76\n"
"v_add_f32 v234, v234, v77\n"
"v_mfma_f32_16x16x32_bf16 v[44:47], v[248:251], v[196:199], v[44:47]\n"
"v_add_f32 v235, v235, v84\n"
"v_add_f32 v235, v235, v85\n"
"v_mfma_f32_16x16x32_bf16 v[44:47], v[252:255], v[204:207], v[44:47]\n"
"v_cvt_pk_bf16_f32 v82, v84, v85\n"
"v_add_f32 v236, v236, v92\n"
"v_add_f32 v236, v236, v93\n"
"v_mfma_f32_16x16x32_bf16 v[52:55], v[240:243], v[144:147], v[224:227]\n"
"v_cvt_pk_bf16_f32 v90, v92, v93\n"
"v_add_f32 v237, v237, v100\n"
"v_mfma_f32_16x16x32_bf16 v[52:55], v[244:247], v[152:155], v[52:55]\n"
"v_add_f32 v237, v237, v101\n"
"v_cvt_pk_bf16_f32 v98, v100, v101\n"
"s_add_u32 m0, s79, 81920\n"
"v_mfma_f32_16x16x32_bf16 v[52:55], v[248:251], v[160:163], v[52:55]\n"
"s_waitcnt vmcnt(3)\n"
"buffer_load_dwordx4 v14, s[20:23], s75 offen offset:1024 nt lds\n"
"v_mfma_f32_16x16x32_bf16 v[52:55], v[252:255], v[168:171], v[52:55]\n"
"s_add_u32 s77, s77, 8192\n"
"s_and_b32 s77, s77, 0x1ffff\n"
"v_mfma_f32_16x16x32_bf16 v[60:63], v[240:243], v[148:151], v[228:231]\n"
"v_add_f32 v238, v238, v108\n"
"v_add_f32 v238, v238, v109\n"
"v_mfma_f32_16x16x32_bf16 v[60:63], v[244:247], v[156:159], v[60:63]\n"
"v_cvt_pk_bf16_f32 v106, v108, v109\n"
"v_add_f32 v233, v233, v70\n"
"v_mfma_f32_16x16x32_bf16 v[60:63], v[248:251], v[164:167], v[60:63]\n"
"v_add_f32 v233, v233, v71\n"
"v_add_f32 v234, v234, v78\n"
"v_add_f32 v234, v234, v79\n"
"v_add_f32 v235, v235, v86\n"
"v_add_f32 v235, v235, v87\n"
"v_cvt_pk_bf16_f32 v83, v86, v87\n"
"v_add_f32 v236, v236, v94\n"
"v_add_f32 v236, v236, v95\n"
"v_cvt_pk_bf16_f32 v91, v94, v95\n"
"v_add_f32 v237, v237, v102\n"
"v_mfma_f32_16x16x32_bf16 v[60:63], v[252:255], v[172:175], v[60:63]\n"
"v_add_f32 v237, v237, v103\n"
"v_cvt_pk_bf16_f32 v99, v102, v103\n"
"v_add_f32 v238, v238, v110\n"
"v_add_f32 v238, v238, v111\n"
"v_cvt_pk_bf16_f32 v107, v110, v111\n"
"v_max3_f32 v118, v233, v234, v235\n"
"v_max3_f32 v118, v118, v236, v237\n"
"v_max_f32 v118, v118, v238\n"
"v_cmp_nge_f32 vcc, 0x47800000, v118\n"
"s_nop 0\n"
"s_cbranch_vccnz Ltr3_%=\n"
"Lrt3_%=:\n"
"s_waitcnt lgkmcnt(0)\n"
"v_mfma_f32_16x16x32_bf16 a[0:3], a[240:243], v[64:67], a[0:3]\n"
"v_exp_f32 v16, v16\n"
"v_mfma_f32_16x16x32_bf16 a[4:7], a[240:243], v[72:75], a[4:7]\n"
"v_exp_f32 v17, v17\n"
"v_mfma_f32_16x16x32_bf16 a[64:67], a[240:243], v[80:83], a[64:67]\n"
"v_exp_f32 v24, v24\n"
"v_mfma_f32_16x16x32_bf16 a[68:71], a[240:243], v[88:91], a[68:71]\n"
"v_exp_f32 v25, v25\n"
"v_add_f32 v112, v112, v233\n"
"v_mfma_f32_16x16x32_bf16 a[128:131], a[240:243], v[96:99], a[128:131]\n"
"v_exp_f32 v18, v18\n"
"v_mfma_f32_16x16x32_bf16 a[132:135], a[240:243], v[104:107], a[132:135]\n"
"ds_read_b64_tr_b16 a[240:241], v6 offset:16896\n"
"ds_read_b64_tr_b16 a[242:243], v6 offset:20992\n"
"s_add_u32 m0, s79, 73728\n"
"v_mfma_f32_16x16x32_bf16 a[136:139], a[244:247], v[96:99], a[136:139]\n"
"buffer_load_dwordx4 v4, s[48:51], s77 offen nt lds\n"
"v_mfma_f32_16x16x32_bf16 a[140:143], a[244:247], v[104:107], a[140:143]\n"
"v_exp_f32 v19, v19\n"
"v_mfma_f32_16x16x32_bf16 a[72:75], a[244:247], v[80:83], a[72:75]\n"
"v_exp_f32 v26, v26\n"
"v_mfma_f32_16x16x32_bf16 a[76:79], a[244:247], v[88:91], a[76:79]\n"
"v_exp_f32 v27, v27\n"
"v_mfma_f32_16x16x32_bf16 a[8:11], a[244:247], v[64:67], a[8:11]\n"
"v_exp_f32 v20, v20\n"
"v_mfma_f32_16x16x32_bf16 a[12:15], a[244:247], v[72:75], a[12:15]\n"
"ds_read_b64_tr_b16 a[244:245], v7 offset:16896\n"
"ds_read_b64_tr_b16 a[246:247], v7 offset:20992\n"
"v_add_f32 v113, v113, v234\n"
"v_mfma_f32_16x16x32_bf16 a[16:19], a[248:251], v[64:67], a[16:19]\n"
"v_exp_f32 v21, v21\n"
"v_mfma_f32_16x16x32_bf16 a[20:23], a[248:251], v[72:75], a[20:23]\n"
"v_exp_f32 v28, v28\n"
"v_mfma_f32_16x16x32_bf16 a[80:83], a[248:251], v[80:83], a[80:83]\n"
"v_exp_f32 v29, v29\n"
"v_mfma_f32_16x16x32_bf16 a[84:87], a[248:251], v[88:91], a[84:87]\n"
"v_exp_f32 v22, v22\n"
"v_add_f32 v233, v16, v17\n"
"v_mfma_f32_16x16x32_bf16 a[144:147], a[248:251], v[96:99], a[144:147]\n"
"v_exp_f32 v23, v23\n"
"v_mfma_f32_16x16x32_bf16 a[148:151], a[248:251], v[104:107], a[148:151]\n"
"ds_read_b64_tr_b16 a[248:249], v6 offset:17152\n"
"ds_read_b64_tr_b16 a[250:251], v6 offset:21248\n"
"s_add_u32 m0, s79, 73728\n"
"v_mfma_f32_16x16x32_bf16 a[152:155], a[252:255], v[96:99], a[152:155]\n"
"s_waitcnt vmcnt(4)\n"
"buffer_load_dwordx4 v232, s[48:51], s77 offen offset:1024 nt lds\n"
"v_mfma_f32_16x16x32_bf16 a[156:159], a[252:255], v[104:107], a[156:159]\n"
"v_exp_f32 v30, v30\n"
"v_mfma_f32_16x16x32_bf16 a[88:91], a[252:255], v[80:83], a[88:91]\n"
"v_exp_f32 v31, v31\n"
"v_mfma_f32_16x16x32_bf16 a[92:95], a[252:255], v[88:91], a[92:95]\n"
"v_cvt_pk_bf16_f32 v16, v16, v17\n"
"v_add_f32 v234, v24, v25\n"
"v_mfma_f32_16x16x32_bf16 a[24:27], a[252:255], v[64:67], a[24:27]\n"
"v_cvt_pk_bf16_f32 v24, v24, v25\n"
"v_add_f32 v233, v233, v18\n"
"v_mfma_f32_16x16x32_bf16 a[28:31], a[252:255], v[72:75], a[28:31]\n"
"ds_read_b64_tr_b16 a[252:253], v7 offset:17152\n"
"ds_read_b64_tr_b16 a[254:255], v7 offset:21248\n"
"s_waitcnt lgkmcnt(2)\n"
"v_mfma_f32_16x16x32_bf16 a[32:35], a[240:243], v[64:67], a[32:35]\n"
"v_exp_f32 v32, v32\n"
"v_mfma_f32_16x16x32_bf16 a[36:39], a[240:243], v[72:75], a[36:39]\n"
"v_exp_f32 v33, v33\n"
"v_mfma_f32_16x16x32_bf16 a[96:99], a[240:243], v[80:83], a[96:99]\n"
"v_exp_f32 v40, v40\n"
"v_mfma_f32_16x16x32_bf16 a[100:103], a[240:243], v[88:91], a[100:103]\n"
"v_exp_f32 v41, v41\n"
"v_add_f32 v233, v233, v19\n"
"v_mfma_f32_16x16x32_bf16 a[160:163], a[240:243], v[96:99], a[160:163]\n"
"s_barrier\n"
"v_mfma_f32_16x16x32_bf16 a[164:167], a[240:243], v[104:107], a[164:167]\n"
"ds_read_b128 a[224:227], v5 offset:32768\n"
"ds_read_b128 a[228:231], v5 offset:33024\n"
"v_mfma_f32_16x16x32_bf16 a[168:171], a[244:247], v[96:99], a[168:171]\n"
"ds_read_b128 a[232:235], v5 offset:33280\n"
"v_cvt_pk_bf16_f32 v17, v18, v19\n"
"v_mfma_f32_16x16x32_bf16 a[172:175], a[244:247], v[104:107], a[172:175]\n"
"ds_read_b128 a[236:239], v5 offset:33536\n"
"v_exp_f32 v48, v48\n"
"v_mfma_f32_16x16x32_bf16 a[104:107], a[244:247], v[80:83], a[104:107]\n"
"ds_read_b128 v[240:243], v5 offset:36864\n"
"s_add_u32 m0, s79, 155648\n"
"v_mfma_f32_16x16x32_bf16 a[108:111], a[244:247], v[88:91], a[108:111]\n"
"s_waitcnt vmcnt(4)\n"
"buffer_load_dwordx4 v4, s[24:27], s77 offen nt lds\n"
"v_mfma_f32_16x16x32_bf16 a[40:43], a[244:247], v[64:67], a[40:43]\n"
"ds_read_b128 v[244:247], v5 offset:37120\n"
"v_add_f32 v234, v234, v26\n"
"v_mfma_f32_16x16x32_bf16 a[44:47], a[244:247], v[72:75], a[44:47]\n"
"ds_read_b128 v[248:251], v5 offset:37376\n"
"v_exp_f32 v49, v49\n"
"v_mfma_f32_16x16x32_bf16 a[48:51], a[248:251], v[64:67], a[48:51]\n"
"ds_read_b128 v[252:255], v5 offset:37632\n"
"v_add_f32 v234, v234, v27\n"
"v_mfma_f32_16x16x32_bf16 a[52:55], a[248:251], v[72:75], a[52:55]\n"
"v_exp_f32 v56, v56\n"
"v_mfma_f32_16x16x32_bf16 a[112:115], a[248:251], v[80:83], a[112:115]\n"
"v_exp_f32 v57, v57\n"
"v_mfma_f32_16x16x32_bf16 a[116:119], a[248:251], v[88:91], a[116:119]\n"
"v_exp_f32 v34, v34\n"
"v_cvt_pk_bf16_f32 v25, v26, v27\n"
"v_mfma_f32_16x16x32_bf16 a[176:179], a[248:251], v[96:99], a[176:179]\n"
"v_exp_f32 v35, v35\n"
"v_mfma_f32_16x16x32_bf16 a[180:183], a[248:251], v[104:107], a[180:183]\n"
"v_cvt_pk_bf16_f32 v18, v20, v21\n"
"s_waitcnt lgkmcnt(1)\n"
"v_mfma_f32_16x16x32_bf16 a[184:187], a[252:255], v[96:99], a[184:187]\n"
"v_exp_f32 v42, v42\n"
"v_mfma_f32_16x16x32_bf16 a[188:191], a[252:255], v[104:107], a[188:191]\n"
"v_exp_f32 v43, v43\n"
"v_cvt_pk_bf16_f32 v26, v28, v29\n"
"s_add_u32 m0, s79, 155648\n"
"v_mfma_f32_16x16x32_bf16 a[120:123], a[252:255], v[80:83], a[120:123]\n"
"s_waitcnt vmcnt(3)\n"
"buffer_load_dwordx4 v232, s[24:27], s77 offen offset:1024 nt lds\n"
"v_mfma_f32_16x16x32_bf16 a[124:127], a[252:255], v[88:91], a[124:127]\n"
"v_cvt_pk_bf16_f32 v19, v22, v23\n"
"v_mfma_f32_16x16x32_bf16 a[56:59], a[252:255], v[64:67], a[56:59]\n"
"v_exp_f32 v50, v50\n"
"v_mfma_f32_16x16x32_bf16 a[60:63], a[252:255], v[72:75], a[60:63]\n"
"v_cvt_pk_bf16_f32 v27, v30, v31\n"
"s_add_u32 s75, s75, 8192\n"
"s_and_b32 s75, s75, 0x1ffff\n"
"v_mfma_f32_16x16x32_bf16 v[64:67], a[224:227], a[192:195], v[208:211]\n"
"v_exp_f32 v51, v51\n"
"v_mfma_f32_16x16x32_bf16 v[64:67], a[228:231], a[200:203], v[64:67]\n"
"v_exp_f32 v58, v58\n"
"v_mfma_f32_16x16x32_bf16 v[64:67], a[232:235], a[208:211], v[64:67]\n"
"v_exp_f32 v59, v59\n"
"v_mfma_f32_16x16x32_bf16 v[64:67], a[236:239], a[216:219], v[64:67]\n"
"v_exp_f32 v36, v36\n"
"v_add_f32 v114, v114, v235\n"
"v_mfma_f32_16x16x32_bf16 v[72:75], a[224:227], a[196:199], v[212:215]\n"
"v_exp_f32 v37, v37\n"
"v_mfma_f32_16x16x32_bf16 v[72:75], a[228:231], a[204:207], v[72:75]\n"
"v_exp_f32 v44, v44\n"
"v_mfma_f32_16x16x32_bf16 v[72:75], a[232:235], a[212:215], v[72:75]\n"
"v_exp_f32 v45, v45\n"
"v_mfma_f32_16x16x32_bf16 v[72:75], a[236:239], a[220:223], v[72:75]\n"
"v_exp_f32 v52, v52\n"
"v_add_f32 v115, v115, v236\n"
"s_add_u32 m0, s79, 8192\n"
"v_mfma_f32_16x16x32_bf16 v[80:83], a[224:227], v[176:179], v[216:219]\n"
"buffer_load_dwordx4 v3, s[44:47], s75 offen nt lds\n"
"v_mfma_f32_16x16x32_bf16 v[80:83], a[228:231], v[184:187], v[80:83]\n"
"v_add_f32 v116, v116, v237\n"
"v_mfma_f32_16x16x32_bf16 v[80:83], a[232:235], v[192:195], v[80:83]\n"
"v_exp_f32 v53, v53\n"
"v_mfma_f32_16x16x32_bf16 v[80:83], a[236:239], v[200:203], v[80:83]\n"
"v_exp_f32 v60, v60\n"
"v_add_f32 v117, v117, v238\n"
"v_mfma_f32_16x16x32_bf16 v[88:91], a[224:227], v[180:183], v[220:223]\n"
"v_exp_f32 v61, v61\n"
"v_mfma_f32_16x16x32_bf16 v[88:91], a[228:231], v[188:191], v[88:91]\n"
"v_exp_f32 v38, v38\n"
"v_mfma_f32_16x16x32_bf16 v[88:91], a[232:235], v[196:199], v[88:91]\n"
"v_exp_f32 v39, v39\n"
"v_mfma_f32_16x16x32_bf16 v[88:91], a[236:239], v[204:207], v[88:91]\n"
"v_exp_f32 v46, v46\n"
"v_add_f32 v235, v32, v33\n"
"v_mfma_f32_16x16x32_bf16 v[96:99], a[224:227], v[144:147], v[224:227]\n"
"v_exp_f32 v47, v47\n"
"v_mfma_f32_16x16x32_bf16 v[96:99], a[228:231], v[152:155], v[96:99]\n"
"v_exp_f32 v54, v54\n"
"v_mfma_f32_16x16x32_bf16 v[96:99], a[232:235], v[160:163], v[96:99]\n"
"v_exp_f32 v55, v55\n"
"v_mfma_f32_16x16x32_bf16 v[96:99], a[236:239], v[168:171], v[96:99]\n"
"v_exp_f32 v62, v62\n"
"v_cvt_pk_bf16_f32 v32, v32, v33\n"
"v_mfma_f32_16x16x32_bf16 v[104:107], a[224:227], v[148:151], v[228:231]\n"
"v_exp_f32 v63, v63\n"
"v_mfma_f32_16x16x32_bf16 v[104:107], a[228:231], v[156:159], v[104:107]\n"
"v_add_f32 v236, v40, v41\n"
"v_cvt_pk_bf16_f32 v40, v40, v41\n"
"s_add_u32 m0, s79, 8192\n"
"v_mfma_f32_16x16x32_bf16 v[104:107], a[232:235], v[164:167], v[104:107]\n"
"s_waitcnt vmcnt(4)\n"
"buffer_load_dwordx4 v14, s[44:47], s75 offen offset:1024 nt lds\n"
"v_mfma_f32_16x16x32_bf16 v[104:107], a[236:239], v[172:175], v[104:107]\n"
"v_add_f32 v237, v48, v49\n"
"v_cvt_pk_bf16_f32 v48, v48, v49\n"
"v_mfma_f32_16x16x32_bf16 v[68:71], v[240:243], a[192:195], v[208:211]\n"
"v_add_f32 v238, v56, v57\n"
"v_cvt_pk_bf16_f32 v56, v56, v57\n"
"v_mfma_f32_16x16x32_bf16 v[68:71], v[244:247], a[200:203], v[68:71]\n"
"v_add_f32 v235, v235, v34\n"
"v_mfma_f32_16x16x32_bf16 v[68:71], v[248:251], a[208:211], v[68:71]\n"
"v_add_f32 v235, v235, v35\n"
"v_cvt_pk_bf16_f32 v33, v34, v35\n"
"s_waitcnt lgkmcnt(0)\n"
"v_mfma_f32_16x16x32_bf16 v[68:71], v[252:255], a[216:219], v[68:71]\n"
"v_add_f32 v236, v236, v42\n"
"v_add_f32 v236, v236, v43\n"
"v_cvt_pk_bf16_f32 v41, v42, v43\n"
"v_mfma_f32_16x16x32_bf16 v[76:79], v[240:243], a[196:199], v[212:215]\n"
"s_barrier\n"
"v_mfma_f32_16x16x32_bf16 v[76:79], v[244:247], a[204:207], v[76:79]\n"
"ds_read_b64_tr_b16 a[240:241], v6 offset:24576\n"
"ds_read_b64_tr_b16 a[242:243], v6 offset:28672\n"
"v_mfma_f32_16x16x32_bf16 v[76:79], v[248:251], a[212:215], v[76:79]\n"
"ds_read_b64_tr_b16 a[244:245], v7 offset:24576\n"
"v_add_f32 v237, v237, v50\n"
"v_mfma_f32_16x16x32_bf16 v[76:79], v[252:255], a[220:223], v[76:79]\n"
"ds_read_b64_tr_b16 a[246:247], v7 offset:28672\n"
"v_add_f32 v237, v237, v51\n"
"v_cvt_pk_bf16_f32 v49, v50, v51\n"
"v_mfma_f32_16x16x32_bf16 v[84:87], v[240:243], v[176:179], v[216:219]\n"
"ds_read_b64_tr_b16 a[248:249], v6 offset:24832\n"
"v_add_f32 v238, v238, v58\n"
"v_mfma_f32_16x16x32_bf16 v[84:87], v[244:247], v[184:187], v[84:87]\n"
"ds_read_b64_tr_b16 a[250:251], v6 offset:28928\n"
"v_add_f32 v238, v238, v59\n"
"v_mfma_f32_16x16x32_bf16 v[84:87], v[248:251], v[192:195], v[84:87]\n"
"ds_read_b64_tr_b16 a[252:253], v7 offset:24832\n"
"v_cvt_pk_bf16_f32 v57, v58, v59\n"
"s_add_u32 m0, s79, 90112\n"
"v_mfma_f32_16x16x32_bf16 v[84:87], v[252:255], v[200:203], v[84:87]\n"
"s_waitcnt vmcnt(3)\n"
"buffer_load_dwordx4 v3, s[20:23], s75 offen nt lds\n"
"v_mfma_f32_16x16x32_bf16 v[92:95], v[240:243], v[180:183], v[220:223]\n"
"ds_read_b64_tr_b16 a[254:255], v7 offset:28928\n"
"v_add_f32 v233, v233, v20\n"
"v_mfma_f32_16x16x32_bf16 v[92:95], v[244:247], v[188:191], v[92:95]\n"
"v_add_f32 v233, v233, v21\n"
"v_add_f32 v234, v234, v28\n"
"v_mfma_f32_16x16x32_bf16 v[92:95], v[248:251], v[196:199], v[92:95]\n"
"v_add_f32 v234, v234, v29\n"
"v_add_f32 v235, v235, v36\n"
"v_mfma_f32_16x16x32_bf16 v[92:95], v[252:255], v[204:207], v[92:95]\n"
"v_add_f32 v235, v235, v37\n"
"v_cvt_pk_bf16_f32 v34, v36, v37\n"
"v_add_f32 v236, v236, v44\n"
"v_mfma_f32_16x16x32_bf16 v[100:103], v[240:243], v[144:147], v[224:227]\n"
"v_add_f32 v236, v236, v45\n"
"v_cvt_pk_bf16_f32 v42, v44, v45\n"
"v_mfma_f32_16x16x32_bf16 v[100:103], v[244:247], v[152:155], v[100:103]\n"
"v_add_f32 v237, v237, v52\n"
"v_add_f32 v237, v237, v53\n"
"v_mfma_f32_16x16x32_bf16 v[100:103], v[248:251], v[160:163], v[100:103]\n"
"v_cvt_pk_bf16_f32 v50, v52, v53\n"
"v_add_f32 v238, v238, v60\n"
"v_mfma_f32_16x16x32_bf16 v[100:103], v[252:255], v[168:171], v[100:103]\n"
"v_add_f32 v238, v238, v61\n"
"v_cvt_pk_bf16_f32 v58, v60, v61\n"
"v_add_f32 v233, v233, v22\n"
"v_mfma_f32_16x16x32_bf16 v[108:111], v[240:243], v[148:151], v[228:231]\n"
"v_add_f32 v233, v233, v23\n"
"v_add_f32 v234, v234, v30\n"
"v_mfma_f32_16x16x32_bf16 v[108:111], v[244:247], v[156:159], v[108:111]\n"
"v_add_f32 v234, v234, v31\n"
"v_add_f32 v235, v235, v38\n"
"v_mfma_f32_16x16x32_bf16 v[108:111], v[248:251], v[164:167], v[108:111]\n"
"v_add_f32 v235, v235, v39\n"
"v_cvt_pk_bf16_f32 v35, v38, v39\n"
"v_add_f32 v236, v236, v46\n"
"v_add_f32 v236, v236, v47\n"
"v_cvt_pk_bf16_f32 v43, v46, v47\n"
"v_add_f32 v237, v237, v54\n"
"v_add_f32 v237, v237, v55\n"
"v_cvt_pk_bf16_f32 v51, v54, v55\n"
"v_mfma_f32_16x16x32_bf16 v[108:111], v[252:255], v[172:175], v[108:111]\n"
"v_add_f32 v238, v238, v62\n"
"v_add_f32 v238, v238, v63\n"
"v_cvt_pk_bf16_f32 v59, v62, v63\n"
"v_max3_f32 v118, v233, v234, v235\n"
"v_max3_f32 v118, v118, v236, v237\n"
"v_max_f32 v118, v118, v238\n"
"v_cmp_nge_f32 vcc, 0x47800000, v118\n"
"s_nop 0\n"
"s_cbranch_vccnz Ltr4_%=\n"
"Lrt4_%=:\n"
"s_add_u32 m0, s79, 90112\n"
"s_waitcnt lgkmcnt(0)\n"
"v_mfma_f32_16x16x32_bf16 a[0:3], a[240:243], v[16:19], a[0:3]\n"
"buffer_load_dwordx4 v14, s[20:23], s75 offen offset:1024 nt lds\n"
"v_mfma_f32_16x16x32_bf16 a[4:7], a[240:243], v[24:27], a[4:7]\n"
"v_add_f32 v112, v112, v233\n"
"v_mfma_f32_16x16x32_bf16 a[64:67], a[240:243], v[32:35], a[64:67]\n"
"v_exp_f32 v64, v64\n"
"v_mfma_f32_16x16x32_bf16 a[68:71], a[240:243], v[40:43], a[68:71]\n"
"v_add_f32 v113, v113, v234\n"
"s_add_u32 s77, s77, 8192\n"
"s_and_b32 s77, s77, 0x1ffff\n"
"v_mfma_f32_16x16x32_bf16 a[128:131], a[240:243], v[48:51], a[128:131]\n"
"v_exp_f32 v65, v65\n"
"v_mfma_f32_16x16x32_bf16 a[132:135], a[240:243], v[56:59], a[132:135]\n"
"ds_read_b64_tr_b16 a[240:241], v6 offset:25088\n"
"ds_read_b64_tr_b16 a[242:243], v6 offset:29184\n"
"v_mfma_f32_16x16x32_bf16 a[136:139], a[244:247], v[48:51], a[136:139]\n"
"v_exp_f32 v72, v72\n"
"v_mfma_f32_16x16x32_bf16 a[140:143], a[244:247], v[56:59], a[140:143]\n"
"v_exp_f32 v73, v73\n"
"v_add_f32 v233, v64, v65\n"
"v_mfma_f32_16x16x32_bf16 a[72:75], a[244:247], v[32:35], a[72:75]\n"
"v_exp_f32 v66, v66\n"
"v_mfma_f32_16x16x32_bf16 a[76:79], a[244:247], v[40:43], a[76:79]\n"
"v_exp_f32 v67, v67\n"
"v_mfma_f32_16x16x32_bf16 a[8:11], a[244:247], v[16:19], a[8:11]\n"
"v_cvt_pk_bf16_f32 v64, v64, v65\n"
"v_mfma_f32_16x16x32_bf16 a[12:15], a[244:247], v[24:27], a[12:15]\n"
"ds_read_b64_tr_b16 a[244:245], v7 offset:25088\n"
"s_add_u32 m0, s79, 40960\n"
"s_waitcnt vmcnt(4)\n"
"buffer_load_dwordx4 v4, s[48:51], s77 offen nt lds\n"
"v_mfma_f32_16x16x32_bf16 a[16:19], a[248:251], v[16:19], a[16:19]\n"
"ds_read_b64_tr_b16 a[246:247], v7 offset:29184\n"
"v_add_f32 v234, v72, v73\n"
"v_mfma_f32_16x16x32_bf16 a[20:23], a[248:251], v[24:27], a[20:23]\n"
"v_exp_f32 v74, v74\n"
"v_mfma_f32_16x16x32_bf16 a[80:83], a[248:251], v[32:35], a[80:83]\n"
"v_exp_f32 v75, v75\n"
"v_mfma_f32_16x16x32_bf16 a[84:87], a[248:251], v[40:43], a[84:87]\n"
"v_exp_f32 v68, v68\n"
"v_cvt_pk_bf16_f32 v72, v72, v73\n"
"v_mfma_f32_16x16x32_bf16 a[144:147], a[248:251], v[48:51], a[144:147]\n"
"v_exp_f32 v69, v69\n"
"v_mfma_f32_16x16x32_bf16 a[148:151], a[248:251], v[56:59], a[148:151]\n"
"ds_read_b64_tr_b16 a[248:249], v6 offset:25344\n"
"ds_read_b64_tr_b16 a[250:251], v6 offset:29440\n"
"v_mfma_f32_16x16x32_bf16 a[152:155], a[252:255], v[48:51], a[152:155]\n"
"v_exp_f32 v76, v76\n"
"v_mfma_f32_16x16x32_bf16 a[156:159], a[252:255], v[56:59], a[156:159]\n"
"v_exp_f32 v77, v77\n"
"v_add_f32 v233, v233, v66\n"
"v_mfma_f32_16x16x32_bf16 a[88:91], a[252:255], v[32:35], a[88:91]\n"
"v_exp_f32 v70, v70\n"
"v_mfma_f32_16x16x32_bf16 a[92:95], a[252:255], v[40:43], a[92:95]\n"
"v_exp_f32 v71, v71\n"
"v_mfma_f32_16x16x32_bf16 a[24:27], a[252:255], v[16:19], a[24:27]\n"
"v_exp_f32 v78, v78\n"
"v_mfma_f32_16x16x32_bf16 a[28:31], a[252:255], v[24:27], a[28:31]\n"
"ds_read_b64_tr_b16 a[252:253], v7 offset:25344\n"
"ds_read_b64_tr_b16 a[254:255], v7 offset:29440\n"
"s_add_u32 m0, s79, 40960\n"
"s_waitcnt lgkmcnt(2)\n"
"v_mfma_f32_16x16x32_bf16 a[32:35], a[240:243], v[16:19], a[32:35]\n"
"s_waitcnt vmcnt(3)\n"
"buffer_load_dwordx4 v232, s[48:51], s77 offen offset:1024 nt lds\n"
"v_mfma_f32_16x16x32_bf16 a[36:39], a[240:243], v[24:27], a[36:39]\n"
"v_add_f32 v233, v233, v67\n"
"v_mfma_f32_16x16x32_bf16 a[96:99], a[240:243], v[32:35], a[96:99]\n"
"v_exp_f32 v80, v80\n"
"v_mfma_f32_16x16x32_bf16 a[100:103], a[240:243], v[40:43], a[100:103]\n"
"v_exp_f32 v79, v79\n"
"v_cvt_pk_bf16_f32 v65, v66, v67\n"
"v_mfma_f32_16x16x32_bf16 a[160:163], a[240:243], v[48:51], a[160:163]\n"
"s_barrier\n"
"v_mfma_f32_16x16x32_bf16 a[164:167], a[240:243], v[56:59], a[164:167]\n"
"ds_read_b128 a[224:227], v5 offset:0\n"
"ds_read_b128 a[228:231], v5 offset:256\n"
"v_mfma_f32_16x16x32_bf16 a[168:171], a[244:247], v[48:51], a[168:171]\n"
"ds_read_b128 a[232:235], v5 offset:512\n"
"v_add_f32 v234, v234, v74\n"
"v_mfma_f32_16x16x32_bf16 a[172:175], a[244:247], v[56:59], a[172:175]\n"
"ds_read_b128 a[236:239], v5 offset:768\n"
"v_exp_f32 v81, v81\n"
"v_mfma_f32_16x16x32_bf16 a[104:107], a[244:247], v[32:35], a[104:107]\n"
"ds_read_b128 v[240:243], v5 offset:4096\n"
"v_mfma_f32_16x16x32_bf16 a[108:111], a[244:247], v[40:43], a[108:111]\n"
"ds_read_b128 v[244:247], v5 offset:4352\n"
"v_exp_f32 v88, v88\n"
"v_mfma_f32_16x16x32_bf16 a[40:43], a[244:247], v[16:19], a[40:43]\n"
"ds_read_b128 v[248:251], v5 offset:4608\n"
"v_add_f32 v234, v234, v75\n"
"v_mfma_f32_16x16x32_bf16 a[44:47], a[244:247], v[24:27], a[44:47]\n"
"ds_read_b128 v[252:255], v5 offset:4864\n"
"v_exp_f32 v89, v89\n"
"s_add_u32 m0, s79, 122880\n"
"v_mfma_f32_16x16x32_bf16 a[48:51], a[248:251], v[16:19], a[48:51]\n"
"buffer_load_dwordx4 v4, s[24:27], s77 offen nt lds\n"
"v_mfma_f32_16x16x32_bf16 a[52:55], a[248:251], v[24:27], a[52:55]\n"
"v_cvt_pk_bf16_f32 v73, v74, v75\n"
"v_mfma_f32_16x16x32_bf16 a[112:115], a[248:251], v[32:35], a[112:115]\n"
"v_exp_f32 v96, v96\n"
"v_mfma_f32_16x16x32_bf16 a[116:119], a[248:251], v[40:43], a[116:119]\n"
"v_exp_f32 v97, v97\n"
"v_cvt_pk_bf16_f32 v66, v68, v69\n"
"v_mfma_f32_16x16x32_bf16 a[176:179], a[248:251], v[48:51], a[176:179]\n"
"v_exp_f32 v104, v104\n"
"v_mfma_f32_16x16x32_bf16 a[180:183], a[248:251], v[56:59], a[180:183]\n"
"v_cvt_pk_bf16_f32 v74, v76, v77\n"
"s_waitcnt lgkmcnt(0)\n"
"v_mfma_f32_16x16x32_bf16 a[184:187], a[252:255], v[48:51], a[184:187]\n"
"v_exp_f32 v105, v105\n"
"v_mfma_f32_16x16x32_bf16 a[188:191], a[252:255], v[56:59], a[188:191]\n"
"v_exp_f32 v82, v82\n"
"v_cvt_pk_bf16_f32 v67, v70, v71\n"
"v_mfma_f32_16x16x32_bf16 a[120:123], a[252:255], v[32:35], a[120:123]\n"
"v_exp_f32 v83, v83\n"
"v_mfma_f32_16x16x32_bf16 a[124:127], a[252:255], v[40:43], a[124:127]\n"
"v_exp_f32 v90, v90\n"
"v_mfma_f32_16x16x32_bf16 a[56:59], a[252:255], v[16:19], a[56:59]\n"
"v_exp_f32 v91, v91\n"
"v_mfma_f32_16x16x32_bf16 a[60:63], a[252:255], v[24:27], a[60:63]\n"
"v_exp_f32 v98, v98\n"
"v_cvt_pk_bf16_f32 v75, v78, v79\n"
"s_add_u32 m0, s79, 122880\n"
"v_mfma_f32_16x16x32_bf16 v[16:19], a[224:227], a[192:195], v[208:211]\n"
"s_waitcnt vmcnt(4)\n"
"buffer_load_dwordx4 v232, s[24:27], s77 offen offset:1024 nt lds\n"
"v_mfma_f32_16x16x32_bf16 v[16:19], a[228:231], a[200:203], v[16:19]\n"
"v_add_f32 v114, v114, v235\n"
"v_mfma_f32_16x16x32_bf16 v[16:19], a[232:235], a[208:211], v[16:19]\n"
"v_exp_f32 v99, v99\n"
"v_mfma_f32_16x16x32_bf16 v[16:19], a[236:239], a[216:219], v[16:19]\n"
"v_add_f32 v115, v115, v236\n"
"s_add_u32 s75, s75, 8192\n"
"s_and_b32 s75, s75, 0x1ffff\n"
"v_mfma_f32_16x16x32_bf16 v[24:27], a[224:227], a[196:199], v[212:215]\n"
"v_exp_f32 v106, v106\n"
"v_mfma_f32_16x16x32_bf16 v[24:27], a[228:231], a[204:207], v[24:27]\n"
"v_exp_f32 v107, v107\n"
"v_mfma_f32_16x16x32_bf16 v[24:27], a[232:235], a[212:215], v[24:27]\n"
"v_exp_f32 v84, v84\n"
"v_mfma_f32_16x16x32_bf16 v[24:27], a[236:239], a[220:223], v[24:27]\n"
"v_exp_f32 v85, v85\n"
"v_add_f32 v116, v116, v237\n"
"v_mfma_f32_16x16x32_bf16 v[32:35], a[224:227], v[176:179], v[216:219]\n"
"v_exp_f32 v92, v92\n"
"v_mfma_f32_16x16x32_bf16 v[32:35], a[228:231], v[184:187], v[32:35]\n"
"v_exp_f32 v93, v93\n"
"v_mfma_f32_16x16x32_bf16 v[32:35], a[232:235], v[192:195], v[32:35]\n"
"v_exp_f32 v100, v100\n"
"v_mfma_f32_16x16x32_bf16 v[32:35], a[236:239], v[200:203], v[32:35]\n"
"v_exp_f32 v101, v101\n"
"v_add_f32 v117, v117, v238\n"
"v_mfma_f32_16x16x32_bf16 v[40:43], a[224:227], v[180:183], v[220:223]\n"
"v_exp_f32 v108, v108\n"
"v_mfma_f32_16x16x32_bf16 v[40:43], a[228:231], v[188:191], v[40:43]\n"
"v_exp_f32 v109, v109\n"
"s_add_u32 m0, s79, 16384\n"
"v_mfma_f32_16x16x32_bf16 v[40:43], a[232:235], v[196:199], v[40:43]\n"
"s_waitcnt vmcnt(4)\n"
"buffer_load_dwordx4 v3, s[44:47], s75 offen nt lds\n"
"v_mfma_f32_16x16x32_bf16 v[40:43], a[236:239], v[204:207], v[40:43]\n"
"v_exp_f32 v86, v86\n"
"v_mfma_f32_16x16x32_bf16 v[48:51], a[224:227], v[144:147], v[224:227]\n"
"v_exp_f32 v87, v87\n"
"v_mfma_f32_16x16x32_bf16 v[48:51], a[228:231], v[152:155], v[48:51]\n"
"v_exp_f32 v94, v94\n"
"v_mfma_f32_16x16x32_bf16 v[48:51], a[232:235], v[160:163], v[48:51]\n"
"v_exp_f32 v95, v95\n"
"v_mfma_f32_16x16x32_bf16 v[48:51], a[236:239], v[168:171], v[48:51]\n"
"v_exp_f32 v102, v102\n"
"v_add_f32 v235, v80, v81\n"
"v_mfma_f32_16x16x32_bf16 v[56:59], a[224:227], v[148:151], v[228:231]\n"
"v_exp_f32 v103, v103\n"
"v_mfma_f32_16x16x32_bf16 v[56:59], a[228:231], v[156:159], v[56:59]\n"
"v_exp_f32 v110, v110\n"
"v_mfma_f32_16x16x32_bf16 v[56:59], a[232:235], v[164:167], v[56:59]\n"
"v_exp_f32 v111, v111\n"
"v_mfma_f32_16x16x32_bf16 v[56:59], a[236:239], v[172:175], v[56:59]\n"
"v_cvt_pk_bf16_f32 v80, v80, v81\n"
"v_add_f32 v236, v88, v89\n"
"v_cvt_pk_bf16_f32 v88, v88, v89\n"
"v_mfma_f32_16x16x32_bf16 v[20:23], v[240:243], a[192:195], v[208:211]\n"
"v_add_f32 v237, v96, v97\n"
"v_cvt_pk_bf16_f32 v96, v96, v97\n"
"v_mfma_f32_16x16x32_bf16 v[20:23], v[244:247], a[200:203], v[20:23]\n"
"v_add_f32 v238, v104, v105\n"
"v_cvt_pk_bf16_f32 v104, v104, v105\n"
"s_add_u32 m0, s79, 16384\n"
"v_mfma_f32_16x16x32_bf16 v[20:23], v[248:251], a[208:211], v[20:23]\n"
"s_waitcnt vmcnt(3)\n"
"buffer_load_dwordx4 v14, s[44:47], s75 offen offset:1024 nt lds\n"
"v_mfma_f32_16x16x32_bf16 v[20:23], v[252:255], a[216:219], v[20:23]\n"
"v_add_f32 v235, v235, v82\n"
"v_add_f32 v235, v235, v83\n"
"v_mfma_f32_16x16x32_bf16 v[28:31], v[240:243], a[196:199], v[212:215]\n"
"s_barrier\n"
"v_mfma_f32_16x16x32_bf16 v[28:31], v[244:247], a[204:207], v[28:31]\n"
"ds_read_b64_tr_b16 a[240:241], v6 offset:32768\n"
"ds_read_b64_tr_b16 a[242:243], v6 offset:36864\n"
"v_mfma_f32_16x16x32_bf16 v[28:31], v[248:251], a[212:215], v[28:31]\n"
"ds_read_b64_tr_b16 a[244:245], v7 offset:32768\n"
"v_cvt_pk_bf16_f32 v81, v82, v83\n"
"v_mfma_f32_16x16x32_bf16 v[28:31], v[252:255], a[220:223], v[28:31]\n"
"ds_read_b64_tr_b16 a[246:247], v7 offset:36864\n"
"v_add_f32 v236, v236, v90\n"
"v_add_f32 v236, v236, v91\n"
"v_mfma_f32_16x16x32_bf16 v[36:39], v[240:243], v[176:179], v[216:219]\n"
"ds_read_b64_tr_b16 a[248:249], v6 offset:33024\n"
"v_cvt_pk_bf16_f32 v89, v90, v91\n"
"v_mfma_f32_16x16x32_bf16 v[36:39], v[244:247], v[184:187], v[36:39]\n"
"ds_read_b64_tr_b16 a[250:251], v6 offset:37120\n"
"v_add_f32 v237, v237, v98\n"
"v_mfma_f32_16x16x32_bf16 v[36:39], v[248:251], v[192:195], v[36:39]\n"
"ds_read_b64_tr_b16 a[252:253], v7 offset:33024\n"
"v_add_f32 v237, v237, v99\n"
"v_mfma_f32_16x16x32_bf16 v[36:39], v[252:255], v[200:203], v[36:39]\n"
"ds_read_b64_tr_b16 a[254:255], v7 offset:37120\n"
"v_cvt_pk_bf16_f32 v97, v98, v99\n"
"v_add_f32 v238, v238, v106\n"
"v_mfma_f32_16x16x32_bf16 v[44:47], v[240:243], v[180:183], v[220:223]\n"
"v_add_f32 v238, v238, v107\n"
"v_cvt_pk_bf16_f32 v105, v106, v107\n"
"v_mfma_f32_16x16x32_bf16 v[44:47], v[244:247], v[188:191], v[44:47]\n"
"v_add_f32 v233, v233, v68\n"
"v_add_f32 v233, v233, v69\n"
"s_add_u32 m0, s79, 98304\n"
"v_mfma_f32_16x16x32_bf16 v[44:47], v[248:251], v[196:199], v[44:47]\n"
"buffer_load_dwordx4 v3, s[20:23], s75 offen nt lds\n"
"v_mfma_f32_16x16x32_bf16 v[44:47], v[252:255], v[204:207], v[44:47]\n"
"v_add_f32 v234, v234, v76\n"
"v_add_f32 v234, v234, v77\n"
"v_mfma_f32_16x16x32_bf16 v[52:55], v[240:243], v[144:147], v[224:227]\n"
"v_add_f32 v235, v235, v84\n"
"v_add_f32 v235, v235, v85\n"
"v_mfma_f32_16x16x32_bf16 v[52:55], v[244:247], v[152:155], v[52:55]\n"
"v_cvt_pk_bf16_f32 v82, v84, v85\n"
"v_add_f32 v236, v236, v92\n"
"v_mfma_f32_16x16x32_bf16 v[52:55], v[248:251], v[160:163], v[52:55]\n"
"v_add_f32 v236, v236, v93\n"
"v_cvt_pk_bf16_f32 v90, v92, v93\n"
"v_mfma_f32_16x16x32_bf16 v[52:55], v[252:255], v[168:171], v[52:55]\n"
"v_add_f32 v237, v237, v100\n"
"v_add_f32 v237, v237, v101\n"
"v_cvt_pk_bf16_f32 v98, v100, v101\n"
"v_mfma_f32_16x16x32_bf16 v[60:63], v[240:243], v[148:151], v[228:231]\n"
"v_add_f32 v238, v238, v108\n"
"v_add_f32 v238, v238, v109\n"
"v_mfma_f32_16x16x32_bf16 v[60:63], v[244:247], v[156:159], v[60:63]\n"
"v_cvt_pk_bf16_f32 v106, v108, v109\n"
"v_add_f32 v233, v233, v70\n"
"v_mfma_f32_16x16x32_bf16 v[60:63], v[248:251], v[164:167], v[60:63]\n"
"v_add_f32 v233, v233, v71\n"
"v_add_f32 v234, v234, v78\n"
"v_add_f32 v234, v234, v79\n"
"v_add_f32 v235, v235, v86\n"
"v_add_f32 v235, v235, v87\n"
"v_cvt_pk_bf16_f32 v83, v86, v87\n"
"v_add_f32 v236, v236, v94\n"
"v_add_f32 v236, v236, v95\n"
"v_cvt_pk_bf16_f32 v91, v94, v95\n"
"v_add_f32 v237, v237, v102\n"
"v_mfma_f32_16x16x32_bf16 v[60:63], v[252:255], v[172:175], v[60:63]\n"
"v_add_f32 v237, v237, v103\n"
"v_cvt_pk_bf16_f32 v99, v102, v103\n"
"v_add_f32 v238, v238, v110\n"
"v_add_f32 v238, v238, v111\n"
"v_cvt_pk_bf16_f32 v107, v110, v111\n"
"v_max3_f32 v118, v233, v234, v235\n"
"v_max3_f32 v118, v118, v236, v237\n"
"v_max_f32 v118, v118, v238\n"
"v_cmp_nge_f32 vcc, 0x47800000, v118\n"
"s_nop 0\n"
"s_cbranch_vccnz Ltr5_%=\n"
"Lrt5_%=:\n"
"s_waitcnt lgkmcnt(0)\n"
"v_mfma_f32_16x16x32_bf16 a[0:3], a[240:243], v[64:67], a[0:3]\n"
"v_exp_f32 v16, v16\n"
"v_mfma_f32_16x16x32_bf16 a[4:7], a[240:243], v[72:75], a[4:7]\n"
"v_exp_f32 v17, v17\n"
"v_mfma_f32_16x16x32_bf16 a[64:67], a[240:243], v[80:83], a[64:67]\n"
"v_exp_f32 v24, v24\n"
"v_mfma_f32_16x16x32_bf16 a[68:71], a[240:243], v[88:91], a[68:71]\n"
"v_exp_f32 v25, v25\n"
"v_add_f32 v112, v112, v233\n"
"v_mfma_f32_16x16x32_bf16 a[128:131], a[240:243], v[96:99], a[128:131]\n"
"v_mfma_f32_16x16x32_bf16 a[132:135], a[240:243], v[104:107], a[132:135]\n"
"ds_read_b64_tr_b16 a[240:241], v6 offset:33280\n"
"s_add_u32 m0, s79, 98304\n"
"s_waitcnt vmcnt(4)\n"
"buffer_load_dwordx4 v14, s[20:23], s75 offen offset:1024 nt lds\n"
"v_mfma_f32_16x16x32_bf16 a[136:139], a[244:247], v[96:99], a[136:139]\n"
"ds_read_b64_tr_b16 a[242:243], v6 offset:37376\n"
"v_mfma_f32_16x16x32_bf16 a[140:143], a[244:247], v[104:107], a[140:143]\n"
"v_exp_f32 v18, v18\n"
"s_add_u32 s77, s77, 8192\n"
"s_and_b32 s77, s77, 0x1ffff\n"
"v_mfma_f32_16x16x32_bf16 a[72:75], a[244:247], v[80:83], a[72:75]\n"
"v_exp_f32 v19, v19\n"
"v_mfma_f32_16x16x32_bf16 a[76:79], a[244:247], v[88:91], a[76:79]\n"
"v_exp_f32 v26, v26\n"
"v_mfma_f32_16x16x32_bf16 a[8:11], a[244:247], v[64:67], a[8:11]\n"
"v_exp_f32 v27, v27\n"
"v_mfma_f32_16x16x32_bf16 a[12:15], a[244:247], v[72:75], a[12:15]\n"
"ds_read_b64_tr_b16 a[244:245], v7 offset:33280\n"
"ds_read_b64_tr_b16 a[246:247], v7 offset:37376\n"
"v_add_f32 v113, v113, v234\n"
"v_mfma_f32_16x16x32_bf16 a[16:19], a[248:251], v[64:67], a[16:19]\n"
"v_exp_f32 v20, v20\n"
"v_mfma_f32_16x16x32_bf16 a[20:23], a[248:251], v[72:75], a[20:23]\n"
"v_exp_f32 v21, v21\n"
"v_mfma_f32_16x16x32_bf16 a[80:83], a[248:251], v[80:83], a[80:83]\n"
"v_exp_f32 v28, v28\n"
"v_mfma_f32_16x16x32_bf16 a[84:87], a[248:251], v[88:91], a[84:87]\n"
"v_exp_f32 v29, v29\n"
"v_add_f32 v233, v16, v17\n"
"v_mfma_f32_16x16x32_bf16 a[144:147], a[248:251], v[96:99], a[144:147]\n"
"v_mfma_f32_16x16x32_bf16 a[148:151], a[248:251], v[104:107], a[148:151]\n"
"ds_read_b64_tr_b16 a[248:249], v6 offset:33536\n"
"s_add_u32 m0, s79, 49152\n"
"s_waitcnt vmcnt(3)\n"
"buffer_load_dwordx4 v4, s[48:51], s77 offen nt lds\n"
"v_mfma_f32_16x16x32_bf16 a[152:155], a[252:255], v[96:99], a[152:155]\n"
"ds_read_b64_tr_b16 a[250:251], v6 offset:37632\n"
"v_mfma_f32_16x16x32_bf16 a[156:159], a[252:255], v[104:107], a[156:159]\n"
"v_exp_f32 v22, v22\n"
"v_exp_f32 v23, v23\n"
"v_mfma_f32_16x16x32_bf16 a[88:91], a[252:255], v[80:83], a[88:91]\n"
"v_exp_f32 v30, v30\n"
"v_mfma_f32_16x16x32_bf16 a[92:95], a[252:255], v[88:91], a[92:95]\n"
"v_exp_f32 v31, v31\n"
"v_mfma_f32_16x16x32_bf16 a[24:27], a[252:255], v[64:67], a[24:27]\n"
"v_cvt_pk_bf16_f32 v16, v16, v17\n"
"v_add_f32 v234, v24, v25\n"
"v_mfma_f32_16x16x32_bf16 a[28:31], a[252:255], v[72:75], a[28:31]\n"
"ds_read_b64_tr_b16 a[252:253], v7 offset:33536\n"
"ds_read_b64_tr_b16 a[254:255], v7 offset:37632\n"
"s_waitcnt lgkmcnt(3)\n"
"v_mfma_f32_16x16x32_bf16 a[32:35], a[240:243], v[64:67], a[32:35]\n"
"v_exp_f32 v32, v32\n"
"v_mfma_f32_16x16x32_bf16 a[36:39], a[240:243], v[72:75], a[36:39]\n"
"v_cvt_pk_bf16_f32 v24, v24, v25\n"
"v_add_f32 v233, v233, v18\n"
"v_mfma_f32_16x16x32_bf16 a[96:99], a[240:243], v[80:83], a[96:99]\n"
"v_exp_f32 v33, v33\n"
"v_mfma_f32_16x16x32_bf16 a[100:103], a[240:243], v[88:91], a[100:103]\n"
"v_exp_f32 v40, v40\n"
"v_add_f32 v233, v233, v19\n"
"v_mfma_f32_16x16x32_bf16 a[160:163], a[240:243], v[96:99], a[160:163]\n"
"s_barrier\n"
"v_mfma_f32_16x16x32_bf16 a[164:167], a[240:243], v[104:107], a[164:167]\n"
"ds_read_b128 a[224:227], v5 offset:8192\n"
"ds_read_b128 a[228:231], v5 offset:8448\n"
"v_mfma_f32_16x16x32_bf16 a[168:171], a[244:247], v[96:99], a[168:171]\n"
"ds_read_b128 a[232:235], v5 offset:8704\n"
"v_cvt_pk_bf16_f32 v17, v18, v19\n"
"s_add_u32 m0, s79, 49152\n"
"v_mfma_f32_16x16x32_bf16 a[172:175], a[244:247], v[104:107], a[172:175]\n"
"buffer_load_dwordx4 v232, s[48:51], s77 offen offset:1024 nt lds\n"
"v_mfma_f32_16x16x32_bf16 a[104:107], a[244:247], v[80:83], a[104:107]\n"
"ds_read_b128 a[236:239], v5 offset:8960\n"
"v_mfma_f32_16x16x32_bf16 a[108:111], a[244:247], v[88:91], a[108:111]\n"
"ds_read_b128 v[240:243], v5 offset:12288\n"
"v_exp_f32 v41, v41\n"
"v_mfma_f32_16x16x32_bf16 a[40:43], a[244:247], v[64:67], a[40:43]\n"
"ds_read_b128 v[244:247], v5 offset:12544\n"
"v_mfma_f32_16x16x32_bf16 a[44:47], a[244:247], v[72:75], a[44:47]\n"
"ds_read_b128 v[248:251], v5 offset:12800\n"
"v_exp_f32 v48, v48\n"
"s_waitcnt lgkmcnt(5)\n"
"v_mfma_f32_16x16x32_bf16 a[48:51], a[248:251], v[64:67], a[48:51]\n"
"ds_read_b128 v[252:255], v5 offset:13056\n"
"v_add_f32 v234, v234, v26\n"
"v_mfma_f32_16x16x32_bf16 a[52:55], a[248:251], v[72:75], a[52:55]\n"
"v_exp_f32 v49, v49\n"
"v_mfma_f32_16x16x32_bf16 a[112:115], a[248:251], v[80:83], a[112:115]\n"
"v_exp_f32 v56, v56\n"
"v_mfma_f32_16x16x32_bf16 a[116:119], a[248:251], v[88:91], a[116:119]\n"
"v_exp_f32 v57, v57\n"
"v_add_f32 v234, v234, v27\n"
"s_add_u32 m0, s79, 131072\n"
"v_mfma_f32_16x16x32_bf16 a[176:179], a[248:251], v[96:99], a[176:179]\n"
"s_waitcnt vmcnt(4)\n"
"buffer_load_dwordx4 v4, s[24:27], s77 offen nt lds\n"
"v_mfma_f32_16x16x32_bf16 a[180:183], a[248:251], v[104:107], a[180:183]\n"
"v_cvt_pk_bf16_f32 v25, v26, v27\n"
"v_mfma_f32_16x16x32_bf16 a[184:187], a[252:255], v[96:99], a[184:187]\n"
"v_exp_f32 v34, v34\n"
"v_mfma_f32_16x16x32_bf16 a[188:191], a[252:255], v[104:107], a[188:191]\n"
"v_exp_f32 v35, v35\n"
"v_cvt_pk_bf16_f32 v18, v20, v21\n"
"v_mfma_f32_16x16x32_bf16 a[120:123], a[252:255], v[80:83], a[120:123]\n"
"v_exp_f32 v42, v42\n"
"v_mfma_f32_16x16x32_bf16 a[124:127], a[252:255], v[88:91], a[124:127]\n"
"v_exp_f32 v43, v43\n"
"v_mfma_f32_16x16x32_bf16 a[56:59], a[252:255], v[64:67], a[56:59]\n"
"v_exp_f32 v50, v50\n"
"v_mfma_f32_16x16x32_bf16 a[60:63], a[252:255], v[72:75], a[60:63]\n"
"v_exp_f32 v51, v51\n"
"v_cvt_pk_bf16_f32 v26, v28, v29\n"
"v_mfma_f32_16x16x32_bf16 v[64:67], a[224:227], a[192:195], v[208:211]\n"
"v_exp_f32 v58, v58\n"
"v_mfma_f32_16x16x32_bf16 v[64:67], a[228:231], a[200:203], v[64:67]\n"
"v_cvt_pk_bf16_f32 v19, v22, v23\n"
"s_waitcnt lgkmcnt(0)\n"
"v_mfma_f32_16x16x32_bf16 v[64:67], a[232:235], a[208:211], v[64:67]\n"
"v_exp_f32 v59, v59\n"
"v_mfma_f32_16x16x32_bf16 v[64:67], a[236:239], a[216:219], v[64:67]\n"
"v_exp_f32 v36, v36\n"
"v_cvt_pk_bf16_f32 v27, v30, v31\n"
"v_mfma_f32_16x16x32_bf16 v[72:75], a[224:227], a[196:199], v[212:215]\n"
"v_exp_f32 v37, v37\n"
"v_mfma_f32_16x16x32_bf16 v[72:75], a[228:231], a[204:207], v[72:75]\n"
"v_exp_f32 v44, v44\n"
"s_add_u32 m0, s79, 131072\n"
"v_mfma_f32_16x16x32_bf16 v[72:75], a[232:235], a[212:215], v[72:75]\n"
"s_waitcnt vmcnt(3)\n"
"buffer_load_dwordx4 v232, s[24:27], s77 offen offset:1024 nt lds\n"
"v_mfma_f32_16x16x32_bf16 v[72:75], a[236:239], a[220:223], v[72:75]\n"
"s_add_u32 s75, s75, 8192\n"
"s_and_b32 s75, s75, 0x1ffff\n"
"v_mfma_f32_16x16x32_bf16 v[80:83], a[224:227], v[176:179], v[216:219]\n"
"v_exp_f32 v45, v45\n"
"v_mfma_f32_16x16x32_bf16 v[80:83], a[228:231], v[184:187], v[80:83]\n"
"v_exp_f32 v52, v52\n"
"v_mfma_f32_16x16x32_bf16 v[80:83], a[232:235], v[192:195], v[80:83]\n"
"v_exp_f32 v53, v53\n"
"v_mfma_f32_16x16x32_bf16 v[80:83], a[236:239], v[200:203], v[80:83]\n"
"v_exp_f32 v60, v60\n"
"v_add_f32 v114, v114, v235\n"
"v_mfma_f32_16x16x32_bf16 v[88:91], a[224:227], v[180:183], v[220:223]\n"
"v_exp_f32 v61, v61\n"
"v_mfma_f32_16x16x32_bf16 v[88:91], a[228:231], v[188:191], v[88:91]\n"
"v_exp_f32 v38, v38\n"
"v_mfma_f32_16x16x32_bf16 v[88:91], a[232:235], v[196:199], v[88:91]\n"
"v_exp_f32 v39, v39\n"
"v_mfma_f32_16x16x32_bf16 v[88:91], a[236:239], v[204:207], v[88:91]\n"
"v_exp_f32 v46, v46\n"
"v_add_f32 v115, v115, v236\n"
"v_mfma_f32_16x16x32_bf16 v[96:99], a[224:227], v[144:147], v[224:227]\n"
"v_exp_f32 v47, v47\n"
"v_mfma_f32_16x16x32_bf16 v[96:99], a[228:231], v[152:155], v[96:99]\n"
"v_exp_f32 v54, v54\n"
"s_add_u32 m0, s79, 24576\n"
"v_mfma_f32_16x16x32_bf16 v[96:99], a[232:235], v[160:163], v[96:99]\n"
"buffer_load_dwordx4 v3, s[44:47], s75 offen nt lds\n"
"v_mfma_f32_16x16x32_bf16 v[96:99], a[236:239], v[168:171], v[96:99]\n"
"v_exp_f32 v55, v55\n"
"v_mfma_f32_16x16x32_bf16 v[104:107], a[224:227], v[148:151], v[228:231]\n"
"v_exp_f32 v62, v62\n"
"v_mfma_f32_16x16x32_bf16 v[104:107], a[228:231], v[156:159], v[104:107]\n"
"v_exp_f32 v63, v63\n"
"v_mfma_f32_16x16x32_bf16 v[104:107], a[232:235], v[164:167], v[104:107]\n"
"v_add_f32 v116, v116, v237\n"
"v_add_f32 v117, v117, v238\n"
"v_mfma_f32_16x16x32_bf16 v[104:107], a[236:239], v[172:175], v[104:107]\n"
"v_add_f32 v235, v32, v33\n"
"v_cvt_pk_bf16_f32 v32, v32, v33\n"
"v_add_f32 v236, v40, v41\n"
"v_mfma_f32_16x16x32_bf16 v[68:71], v[240:243], a[192:195], v[208:211]\n"
"v_cvt_pk_bf16_f32 v40, v40, v41\n"
"v_add_f32 v237, v48, v49\n"
"v_mfma_f32_16x16x32_bf16 v[68:71], v[244:247], a[200:203], v[68:71]\n"
"v_cvt_pk_bf16_f32 v48, v48, v49\n"
"v_add_f32 v238, v56, v57\n"
"v_mfma_f32_16x16x32_bf16 v[68:71], v[248:251], a[208:211], v[68:71]\n"
"v_cvt_pk_bf16_f32 v56, v56, v57\n"
"v_add_f32 v235, v235, v34\n"
"v_mfma_f32_16x16x32_bf16 v[68:71], v[252:255], a[216:219], v[68:71]\n"
"v_add_f32 v235, v235, v35\n"
"v_cvt_pk_bf16_f32 v33, v34, v35\n"
"v_add_f32 v236, v236, v42\n"
"v_mfma_f32_16x16x32_bf16 v[76:79], v[240:243], a[196:199], v[212:215]\n"
"s_barrier\n"
"v_mfma_f32_16x16x32_bf16 v[76:79], v[244:247], a[204:207], v[76:79]\n"
"ds_read_b64_tr_b16 a[240:241], v6 offset:0\n"
"ds_read_b64_tr_b16 a[242:243], v6 offset:4096\n"
"v_mfma_f32_16x16x32_bf16 v[76:79], v[248:251], a[212:215], v[76:79]\n"
"ds_read_b64_tr_b16 a[244:245], v7 offset:0\n"
"v_add_f32 v236, v236, v43\n"
"s_add_u32 m0, s79, 24576\n"
"v_mfma_f32_16x16x32_bf16 v[76:79], v[252:255], a[220:223], v[76:79]\n"
"s_waitcnt vmcnt(4)\n"
"buffer_load_dwordx4 v14, s[44:47], s75 offen offset:1024 nt lds\n"
"v_mfma_f32_16x16x32_bf16 v[84:87], v[240:243], v[176:179], v[216:219]\n"
"ds_read_b64_tr_b16 a[246:247], v7 offset:4096\n"
"v_cvt_pk_bf16_f32 v41, v42, v43\n"
"v_mfma_f32_16x16x32_bf16 v[84:87], v[244:247], v[184:187], v[84:87]\n"
"ds_read_b64_tr_b16 a[248:249], v6 offset:256\n"
"v_add_f32 v237, v237, v50\n"
"v_mfma_f32_16x16x32_bf16 v[84:87], v[248:251], v[192:195], v[84:87]\n"
"ds_read_b64_tr_b16 a[250:251], v6 offset:4352\n"
"v_add_f32 v237, v237, v51\n"
"v_mfma_f32_16x16x32_bf16 v[84:87], v[252:255], v[200:203], v[84:87]\n"
"ds_read_b64_tr_b16 a[252:253], v7 offset:256\n"
"v_cvt_pk_bf16_f32 v49, v50, v51\n"
"v_add_f32 v238, v238, v58\n"
"v_mfma_f32_16x16x32_bf16 v[92:95], v[240:243], v[180:183], v[220:223]\n"
"ds_read_b64_tr_b16 a[254:255], v7 offset:4352\n"
"v_add_f32 v238, v238, v59\n"
"v_mfma_f32_16x16x32_bf16 v[92:95], v[244:247], v[188:191], v[92:95]\n"
"v_cvt_pk_bf16_f32 v57, v58, v59\n"
"v_add_f32 v233, v233, v20\n"
"v_mfma_f32_16x16x32_bf16 v[92:95], v[248:251], v[196:199], v[92:95]\n"
"v_add_f32 v233, v233, v21\n"
"v_add_f32 v234, v234, v28\n"
"v_mfma_f32_16x16x32_bf16 v[92:95], v[252:255], v[204:207], v[92:95]\n"
"v_add_f32 v234, v234, v29\n"
"v_add_f32 v235, v235, v36\n"
"v_add_f32 v235, v235, v37\n"
"v_mfma_f32_16x16x32_bf16 v[100:103], v[240:243], v[144:147], v[224:227]\n"
"v_cvt_pk_bf16_f32 v34, v36, v37\n"
"v_add_f32 v236, v236, v44\n"
"v_mfma_f32_16x16x32_bf16 v[100:103], v[244:247], v[152:155], v[100:103]\n"
"v_add_f32 v236, v236, v45\n"
"v_cvt_pk_bf16_f32 v42, v44, v45\n"
"v_mfma_f32_16x16x32_bf16 v[100:103], v[248:251], v[160:163], v[100:103]\n"
"v_add_f32 v237, v237, v52\n"
"v_add_f32 v237, v237, v53\n"
"v_mfma_f32_16x16x32_bf16 v[100:103], v[252:255], v[168:171], v[100:103]\n"
"v_cvt_pk_bf16_f32 v50, v52, v53\n"
"v_add_f32 v238, v238, v60\n"
"v_add_f32 v238, v238, v61\n"
"s_add_u32 m0, s79, 106496\n"
"v_mfma_f32_16x16x32_bf16 v[108:111], v[240:243], v[148:151], v[228:231]\n"
"s_waitcnt vmcnt(4)\n"
"buffer_load_dwordx4 v3, s[20:23], s75 offen nt lds\n"
"v_mfma_f32_16x16x32_bf16 v[108:111], v[244:247], v[156:159], v[108:111]\n"
"v_cvt_pk_bf16_f32 v58, v60, v61\n"
"v_mfma_f32_16x16x32_bf16 v[108:111], v[248:251], v[164:167], v[108:111]\n"
"v_add_f32 v233, v233, v22\n"
"v_add_f32 v233, v233, v23\n"
"v_add_f32 v234, v234, v30\n"
"v_add_f32 v234, v234, v31\n"
"v_add_f32 v235, v235, v38\n"
"v_add_f32 v235, v235, v39\n"
"v_cvt_pk_bf16_f32 v35, v38, v39\n"
"v_add_f32 v236, v236, v46\n"
"v_add_f32 v236, v236, v47\n"
"v_cvt_pk_bf16_f32 v43, v46, v47\n"
"v_mfma_f32_16x16x32_bf16 v[108:111], v[252:255], v[172:175], v[108:111]\n"
"v_add_f32 v237, v237, v54\n"
"v_add_f32 v237, v237, v55\n"
"v_cvt_pk_bf16_f32 v51, v54, v55\n"
"v_add_f32 v238, v238, v62\n"
"v_add_f32 v238, v238, v63\n"
"v_cvt_pk_bf16_f32 v59, v62, v63\n"
"v_max3_f32 v118, v233, v234, v235\n"
"v_max3_f32 v118, v118, v236, v237\n"
"v_max_f32 v118, v118, v238\n"
"v_cmp_nge_f32 vcc, 0x47800000, v118\n"
"s_nop 0\n"
"s_cbranch_vccnz Ltr6_%=\n"
"Lrt6_%=:\n"
"s_waitcnt lgkmcnt(0)\n"
"v_mfma_f32_16x16x32_bf16 a[0:3], a[240:243], v[16:19], a[0:3]\n"
"v_exp_f32 v64, v64\n"
"v_mfma_f32_16x16x32_bf16 a[4:7], a[240:243], v[24:27], a[4:7]\n"
"v_exp_f32 v65, v65\n"
"v_mfma_f32_16x16x32_bf16 a[64:67], a[240:243], v[32:35], a[64:67]\n"
"v_exp_f32 v72, v72\n"
"v_mfma_f32_16x16x32_bf16 a[68:71], a[240:243], v[40:43], a[68:71]\n"
"v_exp_f32 v73, v73\n"
"v_add_f32 v112, v112, v233\n"
"v_mfma_f32_16x16x32_bf16 a[128:131], a[240:243], v[48:51], a[128:131]\n"
"v_exp_f32 v66, v66\n"
"v_mfma_f32_16x16x32_bf16 a[132:135], a[240:243], v[56:59], a[132:135]\n"
"ds_read_b64_tr_b16 a[240:241], v6 offset:512\n"
"ds_read_b64_tr_b16 a[242:243], v6 offset:4608\n"
"v_mfma_f32_16x16x32_bf16 a[136:139], a[244:247], v[48:51], a[136:139]\n"
"v_exp_f32 v67, v67\n"
"v_mfma_f32_16x16x32_bf16 a[140:143], a[244:247], v[56:59], a[140:143]\n"
"v_exp_f32 v74, v74\n"
"v_add_f32 v113, v113, v234\n"
"s_add_u32 m0, s79, 106496\n"
"v_mfma_f32_16x16x32_bf16 a[72:75], a[244:247], v[32:35], a[72:75]\n"
"s_waitcnt vmcnt(3)\n"
"buffer_load_dwordx4 v14, s[20:23], s75 offen offset:1024 nt lds\n"
"v_mfma_f32_16x16x32_bf16 a[76:79], a[244:247], v[40:43], a[76:79]\n"
"v_add_f32 v233, v64, v65\n"
"v_mfma_f32_16x16x32_bf16 a[8:11], a[244:247], v[16:19], a[8:11]\n"
"v_cvt_pk_bf16_f32 v64, v64, v65\n"
"v_mfma_f32_16x16x32_bf16 a[12:15], a[244:247], v[24:27], a[12:15]\n"
"ds_read_b64_tr_b16 a[244:245], v7 offset:512\n"
"ds_read_b64_tr_b16 a[246:247], v7 offset:4608\n"
"s_add_u32 s77, s77, 8192\n"
"s_and_b32 s77, s77, 0x1ffff\n"
"v_mfma_f32_16x16x32_bf16 a[16:19], a[248:251], v[16:19], a[16:19]\n"
"v_exp_f32 v75, v75\n"
"v_mfma_f32_16x16x32_bf16 a[20:23], a[248:251], v[24:27], a[20:23]\n"
"v_exp_f32 v68, v68\n"
"v_mfma_f32_16x16x32_bf16 a[80:83], a[248:251], v[32:35], a[80:83]\n"
"v_exp_f32 v69, v69\n"
"v_mfma_f32_16x16x32_bf16 a[84:87], a[248:251], v[40:43], a[84:87]\n"
"v_exp_f32 v76, v76\n"
"v_add_f32 v234, v72, v73\n"
"v_mfma_f32_16x16x32_bf16 a[144:147], a[248:251], v[48:51], a[144:147]\n"
"v_exp_f32 v77, v77\n"
"v_mfma_f32_16x16x32_bf16 a[148:151], a[248:251], v[56:59], a[148:151]\n"
"ds_read_b64_tr_b16 a[248:249], v6 offset:768\n"
"ds_read_b64_tr_b16 a[250:251], v6 offset:4864\n"
"v_mfma_f32_16x16x32_bf16 a[152:155], a[252:255], v[48:51], a[152:155]\n"
"v_exp_f32 v70, v70\n"
"v_mfma_f32_16x16x32_bf16 a[156:159], a[252:255], v[56:59], a[156:159]\n"
"v_exp_f32 v71, v71\n"
"v_cvt_pk_bf16_f32 v72, v72, v73\n"
"s_add_u32 m0, s79, 57344\n"
"v_mfma_f32_16x16x32_bf16 a[88:91], a[252:255], v[32:35], a[88:91]\n"
"buffer_load_dwordx4 v4, s[48:51], s77 offen nt lds\n"
"v_mfma_f32_16x16x32_bf16 a[92:95], a[252:255], v[40:43], a[92:95]\n"
"v_add_f32 v233, v233, v66\n"
"v_mfma_f32_16x16x32_bf16 a[24:27], a[252:255], v[16:19], a[24:27]\n"
"v_exp_f32 v78, v78\n"
"v_mfma_f32_16x16x32_bf16 a[28:31], a[252:255], v[24:27], a[28:31]\n"
"ds_read_b64_tr_b16 a[252:253], v7 offset:768\n"
"ds_read_b64_tr_b16 a[254:255], v7 offset:4864\n"
"s_waitcnt lgkmcnt(2)\n"
"v_mfma_f32_16x16x32_bf16 a[32:35], a[240:243], v[16:19], a[32:35]\n"
"v_exp_f32 v80, v80\n"
"v_mfma_f32_16x16x32_bf16 a[36:39], a[240:243], v[24:27], a[36:39]\n"
"v_exp_f32 v79, v79\n"
"v_mfma_f32_16x16x32_bf16 a[96:99], a[240:243], v[32:35], a[96:99]\n"
"v_exp_f32 v81, v81\n"
"v_mfma_f32_16x16x32_bf16 a[100:103], a[240:243], v[40:43], a[100:103]\n"
"v_exp_f32 v88, v88\n"
"v_add_f32 v233, v233, v67\n"
"v_mfma_f32_16x16x32_bf16 a[160:163], a[240:243], v[48:51], a[160:163]\n"
"s_barrier\n"
"v_mfma_f32_16x16x32_bf16 a[164:167], a[240:243], v[56:59], a[164:167]\n"
"ds_read_b128 a[224:227], v5 offset:16384\n"
"ds_read_b128 a[228:231], v5 offset:16640\n"
"v_mfma_f32_16x16x32_bf16 a[168:171], a[244:247], v[48:51], a[168:171]\n"
"ds_read_b128 a[232:235], v5 offset:16896\n"
"v_cvt_pk_bf16_f32 v65, v66, v67\n"
"v_mfma_f32_16x16x32_bf16 a[172:175], a[244:247], v[56:59], a[172:175]\n"
"ds_read_b128 a[236:239], v5 offset:17152\n"
"v_exp_f32 v89, v89\n"
"v_mfma_f32_16x16x32_bf16 a[104:107], a[244:247], v[32:35], a[104:107]\n"
"ds_read_b128 v[240:243], v5 offset:20480\n"
"v_mfma_f32_16x16x32_bf16 a[108:111], a[244:247], v[40:43], a[108:111]\n"
"ds_read_b128 v[244:247], v5 offset:20736\n"
"v_exp_f32 v96, v96\n"
"v_mfma_f32_16x16x32_bf16 a[40:43], a[244:247], v[16:19], a[40:43]\n"
"ds_read_b128 v[248:251], v5 offset:20992\n"
"v_add_f32 v234, v234, v74\n"
"s_add_u32 m0, s79, 57344\n"
"v_mfma_f32_16x16x32_bf16 a[44:47], a[244:247], v[24:27], a[44:47]\n"
"s_waitcnt vmcnt(4)\n"
"buffer_load_dwordx4 v232, s[48:51], s77 offen offset:1024 nt lds\n"
"v_mfma_f32_16x16x32_bf16 a[48:51], a[248:251], v[16:19], a[48:51]\n"
"ds_read_b128 v[252:255], v5 offset:21248\n"
"v_add_f32 v234, v234, v75\n"
"v_mfma_f32_16x16x32_bf16 a[52:55], a[248:251], v[24:27], a[52:55]\n"
"v_exp_f32 v97, v97\n"
"v_mfma_f32_16x16x32_bf16 a[112:115], a[248:251], v[32:35], a[112:115]\n"
"v_exp_f32 v104, v104\n"
"v_mfma_f32_16x16x32_bf16 a[116:119], a[248:251], v[40:43], a[116:119]\n"
"v_exp_f32 v105, v105\n"
"v_cvt_pk_bf16_f32 v73, v74, v75\n"
"v_mfma_f32_16x16x32_bf16 a[176:179], a[248:251], v[48:51], a[176:179]\n"
"v_exp_f32 v82, v82\n"
"v_mfma_f32_16x16x32_bf16 a[180:183], a[248:251], v[56:59], a[180:183]\n"
"v_cvt_pk_bf16_f32 v66, v68, v69\n"
"s_waitcnt lgkmcnt(1)\n"
"v_mfma_f32_16x16x32_bf16 a[184:187], a[252:255], v[48:51], a[184:187]\n"
"v_exp_f32 v83, v83\n"
"v_mfma_f32_16x16x32_bf16 a[188:191], a[252:255], v[56:59], a[188:191]\n"
"v_exp_f32 v90, v90\n"
"v_cvt_pk_bf16_f32 v74, v76, v77\n"
"v_mfma_f32_16x16x32_bf16 a[120:123], a[252:255], v[32:35], a[120:123]\n"
"v_exp_f32 v91, v91\n"
"v_mfma_f32_16x16x32_bf16 a[124:127], a[252:255], v[40:43], a[124:127]\n"
"v_exp_f32 v98, v98\n"
"s_add_u32 m0, s79, 139264\n"
"v_mfma_f32_16x16x32_bf16 a[56:59], a[252:255], v[16:19], a[56:59]\n"
"s_waitcnt vmcnt(3)\n"
"buffer_load_dwordx4 v4, s[24:27], s77 offen nt lds\n"
"v_mfma_f32_16x16x32_bf16 a[60:63], a[252:255], v[24:27], a[60:63]\n"
"v_exp_f32 v99, v99\n"
"v_mfma_f32_16x16x32_bf16 v[16:19], a[224:227], a[192:195], v[208:211]\n"
"v_exp_f32 v106, v106\n"
"v_mfma_f32_16x16x32_bf16 v[16:19], a[228:231], a[200:203], v[16:19]\n"
"v_exp_f32 v107, v107\n"
"v_mfma_f32_16x16x32_bf16 v[16:19], a[232:235], a[208:211], v[16:19]\n"
"v_exp_f32 v84, v84\n"
"v_mfma_f32_16x16x32_bf16 v[16:19], a[236:239], a[216:219], v[16:19]\n"
"v_exp_f32 v85, v85\n"
"v_cvt_pk_bf16_f32 v67, v70, v71\n"
"v_mfma_f32_16x16x32_bf16 v[24:27], a[224:227], a[196:199], v[212:215]\n"
"v_exp_f32 v92, v92\n"
"v_mfma_f32_16x16x32_bf16 v[24:27], a[228:231], a[204:207], v[24:27]\n"
"v_exp_f32 v93, v93\n"
"v_mfma_f32_16x16x32_bf16 v[24:27], a[232:235], a[212:215], v[24:27]\n"
"v_exp_f32 v100, v100\n"
"v_mfma_f32_16x16x32_bf16 v[24:27], a[236:239], a[220:223], v[24:27]\n"
"v_exp_f32 v101, v101\n"
"v_cvt_pk_bf16_f32 v75, v78, v79\n"
"v_mfma_f32_16x16x32_bf16 v[32:35], a[224:227], v[176:179], v[216:219]\n"
"v_exp_f32 v108, v108\n"
"v_mfma_f32_16x16x32_bf16 v[32:35], a[228:231], v[184:187], v[32:35]\n"
"v_exp_f32 v109, v109\n"
"s_add_u32 m0, s79, 139264\n"
"v_mfma_f32_16x16x32_bf16 v[32:35], a[232:235], v[192:195], v[32:35]\n"
"buffer_load_dwordx4 v232, s[24:27], s77 offen offset:1024 nt lds\n"
"v_mfma_f32_16x16x32_bf16 v[32:35], a[236:239], v[200:203], v[32:35]\n"
"s_add_u32 s75, s75, 8192\n"
"s_and_b32 s75, s75, 0x1ffff\n"
"v_mfma_f32_16x16x32_bf16 v[40:43], a[224:227], v[180:183], v[220:223]\n"
"v_exp_f32 v86, v86\n"
"v_mfma_f32_16x16x32_bf16 v[40:43], a[228:231], v[188:191], v[40:43]\n"
"v_exp_f32 v87, v87\n"
"v_mfma_f32_16x16x32_bf16 v[40:43], a[232:235], v[196:199], v[40:43]\n"
"v_exp_f32 v94, v94\n"
"v_mfma_f32_16x16x32_bf16 v[40:43], a[236:239], v[204:207], v[40:43]\n"
"v_exp_f32 v95, v95\n"
"v_add_f32 v114, v114, v235\n"
"v_mfma_f32_16x16x32_bf16 v[48:51], a[224:227], v[144:147], v[224:227]\n"
"v_exp_f32 v102, v102\n"
"v_mfma_f32_16x16x32_bf16 v[48:51], a[228:231], v[152:155], v[48:51]\n"
"v_exp_f32 v103, v103\n"
"v_mfma_f32_16x16x32_bf16 v[48:51], a[232:235], v[160:163], v[48:51]\n"
"v_exp_f32 v110, v110\n"
"v_mfma_f32_16x16x32_bf16 v[48:51], a[236:239], v[168:171], v[48:51]\n"
"v_exp_f32 v111, v111\n"
"v_add_f32 v115, v115, v236\n"
"v_mfma_f32_16x16x32_bf16 v[56:59], a[224:227], v[148:151], v[228:231]\n"
"v_add_f32 v116, v116, v237\n"
"v_add_f32 v117, v117, v238\n"
"v_mfma_f32_16x16x32_bf16 v[56:59], a[228:231], v[156:159], v[56:59]\n"
"v_add_f32 v235, v80, v81\n"
"v_cvt_pk_bf16_f32 v80, v80, v81\n"
"s_add_u32 m0, s79, 32768\n"
"v_mfma_f32_16x16x32_bf16 v[56:59], a[232:235], v[164:167], v[56:59]\n"
"s_waitcnt vmcnt(4)\n"
"buffer_load_dwordx4 v3, s[44:47], s75 offen nt lds\n"
"v_mfma_f32_16x16x32_bf16 v[56:59], a[236:239], v[172:175], v[56:59]\n"
"v_add_f32 v236, v88, v89\n"
"v_cvt_pk_bf16_f32 v88, v88, v89\n"
"v_mfma_f32_16x16x32_bf16 v[20:23], v[240:243], a[192:195], v[208:211]\n"
"v_add_f32 v237, v96, v97\n"
"v_cvt_pk_bf16_f32 v96, v96, v97\n"
"v_mfma_f32_16x16x32_bf16 v[20:23], v[244:247], a[200:203], v[20:23]\n"
"v_add_f32 v238, v104, v105\n"
"v_mfma_f32_16x16x32_bf16 v[20:23], v[248:251], a[208:211], v[20:23]\n"
"v_cvt_pk_bf16_f32 v104, v104, v105\n"
"v_add_f32 v235, v235, v82\n"
"s_waitcnt lgkmcnt(0)\n"
"v_mfma_f32_16x16x32_bf16 v[20:23], v[252:255], a[216:219], v[20:23]\n"
"v_add_f32 v235, v235, v83\n"
"v_cvt_pk_bf16_f32 v81, v82, v83\n"
"v_add_f32 v236, v236, v90\n"
"v_mfma_f32_16x16x32_bf16 v[28:31], v[240:243], a[196:199], v[212:215]\n"
"s_barrier\n"
"v_mfma_f32_16x16x32_bf16 v[28:31], v[244:247], a[204:207], v[28:31]\n"
"ds_read_b64_tr_b16 a[240:241], v6 offset:8192\n"
"ds_read_b64_tr_b16 a[242:243], v6 offset:12288\n"
"v_mfma_f32_16x16x32_bf16 v[28:31], v[248:251], a[212:215], v[28:31]\n"
"ds_read_b64_tr_b16 a[244:245], v7 offset:8192\n"
"v_add_f32 v236, v236, v91\n"
"v_mfma_f32_16x16x32_bf16 v[28:31], v[252:255], a[220:223], v[28:31]\n"
"ds_read_b64_tr_b16 a[246:247], v7 offset:12288\n"
"v_cvt_pk_bf16_f32 v89, v90, v91\n"
"v_add_f32 v237, v237, v98\n"
"v_mfma_f32_16x16x32_bf16 v[36:39], v[240:243], v[176:179], v[216:219]\n"
"ds_read_b64_tr_b16 a[248:249], v6 offset:8448\n"
"v_add_f32 v237, v237, v99\n"
"v_mfma_f32_16x16x32_bf16 v[36:39], v[244:247], v[184:187], v[36:39]\n"
"ds_read_b64_tr_b16 a[250:251], v6 offset:12544\n"
"v_cvt_pk_bf16_f32 v97, v98, v99\n"
"v_mfma_f32_16x16x32_bf16 v[36:39], v[248:251], v[192:195], v[36:39]\n"
"ds_read_b64_tr_b16 a[252:253], v7 offset:8448\n"
"v_add_f32 v238, v238, v106\n"
"v_mfma_f32_16x16x32_bf16 v[36:39], v[252:255], v[200:203], v[36:39]\n"
"ds_read_b64_tr_b16 a[254:255], v7 offset:12544\n"
"v_add_f32 v238, v238, v107\n"
"v_cvt_pk_bf16_f32 v105, v106, v107\n"
"s_add_u32 m0, s79, 32768\n"
"v_mfma_f32_16x16x32_bf16 v[44:47], v[240:243], v[180:183], v[220:223]\n"
"s_waitcnt vmcnt(3)\n"
"buffer_load_dwordx4 v14, s[44:47], s75 offen offset:1024 nt lds\n"
"v_mfma_f32_16x16x32_bf16 v[44:47], v[244:247], v[188:191], v[44:47]\n"
"v_add_f32 v233, v233, v68\n"
"v_mfma_f32_16x16x32_bf16 v[44:47], v[248:251], v[196:199], v[44:47]\n"
"v_add_f32 v233, v233, v69\n"
"v_add_f32 v234, v234, v76\n"
"v_mfma_f32_16x16x32_bf16 v[44:47], v[252:255], v[204:207], v[44:47]\n"
"v_add_f32 v234, v234, v77\n"
"v_add_f32 v235, v235, v84\n"
"v_add_f32 v235, v235, v85\n"
"v_mfma_f32_16x16x32_bf16 v[52:55], v[240:243], v[144:147], v[224:227]\n"
"v_cvt_pk_bf16_f32 v82, v84, v85\n"
"v_add_f32 v236, v236, v92\n"
"v_mfma_f32_16x16x32_bf16 v[52:55], v[244:247], v[152:155], v[52:55]\n"
"v_add_f32 v236, v236, v93\n"
"v_cvt_pk_bf16_f32 v90, v92, v93\n"
"v_mfma_f32_16x16x32_bf16 v[52:55], v[248:251], v[160:163], v[52:55]\n"
"v_add_f32 v237, v237, v100\n"
"v_add_f32 v237, v237, v101\n"
"v_mfma_f32_16x16x32_bf16 v[52:55], v[252:255], v[168:171], v[52:55]\n"
"v_cvt_pk_bf16_f32 v98, v100, v101\n"
"v_add_f32 v238, v238, v108\n"
"v_add_f32 v238, v238, v109\n"
"v_mfma_f32_16x16x32_bf16 v[60:63], v[240:243], v[148:151], v[228:231]\n"
"v_cvt_pk_bf16_f32 v106, v108, v109\n"
"v_add_f32 v233, v233, v70\n"
"v_mfma_f32_16x16x32_bf16 v[60:63], v[244:247], v[156:159], v[60:63]\n"
"v_add_f32 v233, v233, v71\n"
"v_add_f32 v234, v234, v78\n"
"v_mfma_f32_16x16x32_bf16 v[60:63], v[248:251], v[164:167], v[60:63]\n"
"v_add_f32 v234, v234, v79\n"
"v_add_f32 v235, v235, v86\n"
"v_add_f32 v235, v235, v87\n"
"v_cvt_pk_bf16_f32 v83, v86, v87\n"
"v_add_f32 v236, v236, v94\n"
"v_add_f32 v236, v236, v95\n"
"v_cvt_pk_bf16_f32 v91, v94, v95\n"
"v_add_f32 v237, v237, v102\n"
"v_add_f32 v237, v237, v103\n"
"v_mfma_f32_16x16x32_bf16 v[60:63], v[252:255], v[172:175], v[60:63]\n"
"v_cvt_pk_bf16_f32 v99, v102, v103\n"
"v_add_f32 v238, v238, v110\n"
"v_add_f32 v238, v238, v111\n"
"v_cvt_pk_bf16_f32 v107, v110, v111\n"
"v_max3_f32 v118, v233, v234, v235\n"
"v_max3_f32 v118, v118, v236, v237\n"
"v_max_f32 v118, v118, v238\n"
"v_cmp_nge_f32 vcc, 0x47800000, v118\n"
"s_nop 0\n"
"s_cbranch_vccnz Ltr7_%=\n"
"Lrt7_%=:\n"
"s_add_u32 m0, s79, 114688\n"
"s_waitcnt lgkmcnt(0)\n"
"v_mfma_f32_16x16x32_bf16 a[0:3], a[240:243], v[64:67], a[0:3]\n"
"buffer_load_dwordx4 v3, s[20:23], s75 offen nt lds\n"
"v_mfma_f32_16x16x32_bf16 a[4:7], a[240:243], v[72:75], a[4:7]\n"
"v_add_f32 v112, v112, v233\n"
"v_mfma_f32_16x16x32_bf16 a[64:67], a[240:243], v[80:83], a[64:67]\n"
"v_exp_f32 v16, v16\n"
"v_mfma_f32_16x16x32_bf16 a[68:71], a[240:243], v[88:91], a[68:71]\n"
"v_exp_f32 v17, v17\n"
"v_add_f32 v113, v113, v234\n"
"v_mfma_f32_16x16x32_bf16 a[128:131], a[240:243], v[96:99], a[128:131]\n"
"v_exp_f32 v24, v24\n"
"v_mfma_f32_16x16x32_bf16 a[132:135], a[240:243], v[104:107], a[132:135]\n"
"ds_read_b64_tr_b16 a[240:241], v6 offset:8704\n"
"ds_read_b64_tr_b16 a[242:243], v6 offset:12800\n"
"v_mfma_f32_16x16x32_bf16 a[136:139], a[244:247], v[96:99], a[136:139]\n"
"v_exp_f32 v25, v25\n"
"v_mfma_f32_16x16x32_bf16 a[140:143], a[244:247], v[104:107], a[140:143]\n"
"v_exp_f32 v18, v18\n"
"v_add_f32 v233, v16, v17\n"
"v_mfma_f32_16x16x32_bf16 a[72:75], a[244:247], v[80:83], a[72:75]\n"
"v_exp_f32 v19, v19\n"
"v_mfma_f32_16x16x32_bf16 a[76:79], a[244:247], v[88:91], a[76:79]\n"
"v_exp_f32 v26, v26\n"
"v_mfma_f32_16x16x32_bf16 a[8:11], a[244:247], v[64:67], a[8:11]\n"
"v_exp_f32 v27, v27\n"
"v_mfma_f32_16x16x32_bf16 a[12:15], a[244:247], v[72:75], a[12:15]\n"
"ds_read_b64_tr_b16 a[244:245], v7 offset:8704\n"
"ds_read_b64_tr_b16 a[246:247], v7 offset:12800\n"
"v_cvt_pk_bf16_f32 v16, v16, v17\n"
"s_add_u32 m0, s79, 114688\n"
"v_mfma_f32_16x16x32_bf16 a[16:19], a[248:251], v[64:67], a[16:19]\n"
"s_waitcnt vmcnt(4)\n"
"buffer_load_dwordx4 v14, s[20:23], s75 offen offset:1024 nt lds\n"
"v_mfma_f32_16x16x32_bf16 a[20:23], a[248:251], v[72:75], a[20:23]\n"
"v_add_f32 v234, v24, v25\n"
"v_mfma_f32_16x16x32_bf16 a[80:83], a[248:251], v[80:83], a[80:83]\n"
"v_exp_f32 v20, v20\n"
"v_mfma_f32_16x16x32_bf16 a[84:87], a[248:251], v[88:91], a[84:87]\n"
"v_cvt_pk_bf16_f32 v24, v24, v25\n"
"s_add_u32 s77, s77, 8192\n"
"s_and_b32 s77, s77, 0x1ffff\n"
"v_mfma_f32_16x16x32_bf16 a[144:147], a[248:251], v[96:99], a[144:147]\n"
"v_exp_f32 v21, v21\n"
"v_mfma_f32_16x16x32_bf16 a[148:151], a[248:251], v[104:107], a[148:151]\n"
"ds_read_b64_tr_b16 a[248:249], v6 offset:8960\n"
"ds_read_b64_tr_b16 a[250:251], v6 offset:13056\n"
"v_mfma_f32_16x16x32_bf16 a[152:155], a[252:255], v[96:99], a[152:155]\n"
"v_exp_f32 v28, v28\n"
"v_mfma_f32_16x16x32_bf16 a[156:159], a[252:255], v[104:107], a[156:159]\n"
"v_exp_f32 v29, v29\n"
"v_add_f32 v233, v233, v18\n"
"v_mfma_f32_16x16x32_bf16 a[88:91], a[252:255], v[80:83], a[88:91]\n"
"v_exp_f32 v22, v22\n"
"v_mfma_f32_16x16x32_bf16 a[92:95], a[252:255], v[88:91], a[92:95]\n"
"v_exp_f32 v23, v23\n"
"v_mfma_f32_16x16x32_bf16 a[24:27], a[252:255], v[64:67], a[24:27]\n"
"v_exp_f32 v30, v30\n"
"v_mfma_f32_16x16x32_bf16 a[28:31], a[252:255], v[72:75], a[28:31]\n"
"ds_read_b64_tr_b16 a[252:253], v7 offset:8960\n"
"ds_read_b64_tr_b16 a[254:255], v7 offset:13056\n"
"s_waitcnt lgkmcnt(2)\n"
"v_mfma_f32_16x16x32_bf16 a[32:35], a[240:243], v[64:67], a[32:35]\n"
"v_exp_f32 v32, v32\n"
"v_mfma_f32_16x16x32_bf16 a[36:39], a[240:243], v[72:75], a[36:39]\n"
"v_exp_f32 v31, v31\n"
"s_add_u32 m0, s79, 65536\n"
"v_mfma_f32_16x16x32_bf16 a[96:99], a[240:243], v[80:83], a[96:99]\n"
"s_waitcnt vmcnt(4)\n"
"buffer_load_dwordx4 v4, s[48:51], s77 offen nt lds\n"
"v_mfma_f32_16x16x32_bf16 a[100:103], a[240:243], v[88:91], a[100:103]\n"
"v_exp_f32 v33, v33\n"
"v_mfma_f32_16x16x32_bf16 a[160:163], a[240:243], v[96:99], a[160:163]\n"
"s_barrier\n"
"v_mfma_f32_16x16x32_bf16 a[164:167], a[240:243], v[104:107], a[164:167]\n"
"ds_read_b128 a[224:227], v5 offset:24576\n"
"ds_read_b128 a[228:231], v5 offset:24832\n"
"v_mfma_f32_16x16x32_bf16 a[168:171], a[244:247], v[96:99], a[168:171]\n"
"ds_read_b128 a[232:235], v5 offset:25088\n"
"v_add_f32 v233, v233, v19\n"
"v_mfma_f32_16x16x32_bf16 a[172:175], a[244:247], v[104:107], a[172:175]\n"
"ds_read_b128 a[236:239], v5 offset:25344\n"
"v_exp_f32 v40, v40\n"
"v_mfma_f32_16x16x32_bf16 a[104:107], a[244:247], v[80:83], a[104:107]\n"
"ds_read_b128 v[240:243], v5 offset:28672\n"
"v_mfma_f32_16x16x32_bf16 a[108:111], a[244:247], v[88:91], a[108:111]\n"
"ds_read_b128 v[244:247], v5 offset:28928\n"
"v_exp_f32 v41, v41\n"
"v_mfma_f32_16x16x32_bf16 a[40:43], a[244:247], v[64:67], a[40:43]\n"
"ds_read_b128 v[248:251], v5 offset:29184\n"
"v_cvt_pk_bf16_f32 v17, v18, v19\n"
"v_mfma_f32_16x16x32_bf16 a[44:47], a[244:247], v[72:75], a[44:47]\n"
"ds_read_b128 v[252:255], v5 offset:29440\n"
"v_exp_f32 v48, v48\n"
"v_mfma_f32_16x16x32_bf16 a[48:51], a[248:251], v[64:67], a[48:51]\n"
"v_exp_f32 v49, v49\n"
"v_mfma_f32_16x16x32_bf16 a[52:55], a[248:251], v[72:75], a[52:55]\n"
"v_exp_f32 v56, v56\n"
"s_add_u32 m0, s79, 65536\n"
"v_mfma_f32_16x16x32_bf16 a[112:115], a[248:251], v[80:83], a[112:115]\n"
"s_waitcnt vmcnt(3)\n"
"buffer_load_dwordx4 v232, s[48:51], s77 offen offset:1024 nt lds\n"
"v_mfma_f32_16x16x32_bf16 a[116:119], a[248:251], v[88:91], a[116:119]\n"
"v_exp_f32 v57, v57\n"
"v_mfma_f32_16x16x32_bf16 a[176:179], a[248:251], v[96:99], a[176:179]\n"
"v_add_f32 v234, v234, v26\n"
"v_add_f32 v234, v234, v27\n"
"v_mfma_f32_16x16x32_bf16 a[180:183], a[248:251], v[104:107], a[180:183]\n"
"v_cvt_pk_bf16_f32 v25, v26, v27\n"
"s_waitcnt lgkmcnt(0)\n"
"v_mfma_f32_16x16x32_bf16 a[184:187], a[252:255], v[96:99], a[184:187]\n"
"v_exp_f32 v34, v34\n"
"v_mfma_f32_16x16x32_bf16 a[188:191], a[252:255], v[104:107], a[188:191]\n"
"v_exp_f32 v35, v35\n"
"v_cvt_pk_bf16_f32 v18, v20, v21\n"
"v_mfma_f32_16x16x32_bf16 a[120:123], a[252:255], v[80:83], a[120:123]\n"
"v_exp_f32 v42, v42\n"
"v_mfma_f32_16x16x32_bf16 a[124:127], a[252:255], v[88:91], a[124:127]\n"
"v_exp_f32 v43, v43\n"
"v_mfma_f32_16x16x32_bf16 a[56:59], a[252:255], v[64:67], a[56:59]\n"
"v_exp_f32 v50, v50\n"
"v_mfma_f32_16x16x32_bf16 a[60:63], a[252:255], v[72:75], a[60:63]\n"
"v_exp_f32 v51, v51\n"
"v_cvt_pk_bf16_f32 v26, v28, v29\n"
"v_mfma_f32_16x16x32_bf16 v[64:67], a[224:227], a[192:195], v[208:211]\n"
"v_exp_f32 v58, v58\n"
"v_mfma_f32_16x16x32_bf16 v[64:67], a[228:231], a[200:203], v[64:67]\n"
"v_exp_f32 v59, v59\n"
"s_add_u32 m0, s79, 147456\n"
"v_mfma_f32_16x16x32_bf16 v[64:67], a[232:235], a[208:211], v[64:67]\n"
"buffer_load_dwordx4 v4, s[24:27], s77 offen nt lds\n"
"v_mfma_f32_16x16x32_bf16 v[64:67], a[236:239], a[216:219], v[64:67]\n"
"v_exp_f32 v36, v36\n"
"v_mfma_f32_16x16x32_bf16 v[72:75], a[224:227], a[196:199], v[212:215]\n"
"v_exp_f32 v37, v37\n"
"v_mfma_f32_16x16x32_bf16 v[72:75], a[228:231], a[204:207], v[72:75]\n"
"v_exp_f32 v44, v44\n"
"v_mfma_f32_16x16x32_bf16 v[72:75], a[232:235], a[212:215], v[72:75]\n"
"v_exp_f32 v45, v45\n"
"v_mfma_f32_16x16x32_bf16 v[72:75], a[236:239], a[220:223], v[72:75]\n"
"v_exp_f32 v52, v52\n"
"v_cvt_pk_bf16_f32 v19, v22, v23\n"
"v_mfma_f32_16x16x32_bf16 v[80:83], a[224:227], v[176:179], v[216:219]\n"
"v_exp_f32 v53, v53\n"
"v_mfma_f32_16x16x32_bf16 v[80:83], a[228:231], v[184:187], v[80:83]\n"
"v_exp_f32 v60, v60\n"
"v_mfma_f32_16x16x32_bf16 v[80:83], a[232:235], v[192:195], v[80:83]\n"
"v_exp_f32 v61, v61\n"
"v_mfma_f32_16x16x32_bf16 v[80:83], a[236:239], v[200:203], v[80:83]\n"
"v_exp_f32 v38, v38\n"
"v_cvt_pk_bf16_f32 v27, v30, v31\n"
"v_mfma_f32_16x16x32_bf16 v[88:91], a[224:227], v[180:183], v[220:223]\n"
"v_exp_f32 v39, v39\n"
"v_mfma_f32_16x16x32_bf16 v[88:91], a[228:231], v[188:191], v[88:91]\n"
"v_exp_f32 v46, v46\n"
"v_mfma_f32_16x16x32_bf16 v[88:91], a[232:235], v[196:199], v[88:91]\n"
"v_exp_f32 v47, v47\n"
"v_mfma_f32_16x16x32_bf16 v[88:91], a[236:239], v[204:207], v[88:91]\n"
"v_exp_f32 v54, v54\n"
"v_add_f32 v114, v114, v235\n"
"s_add_u32 m0, s79, 147456\n"
"v_mfma_f32_16x16x32_bf16 v[96:99], a[224:227], v[144:147], v[224:227]\n"
"s_waitcnt vmcnt(4)\n"
"buffer_load_dwordx4 v232, s[24:27], s77 offen offset:1024 nt lds\n"
"v_mfma_f32_16x16x32_bf16 v[96:99], a[228:231], v[152:155], v[96:99]\n"
"v_add_f32 v115, v115, v236\n"
"v_mfma_f32_16x16x32_bf16 v[96:99], a[232:235], v[160:163], v[96:99]\n"
"v_exp_f32 v55, v55\n"
"v_mfma_f32_16x16x32_bf16 v[96:99], a[236:239], v[168:171], v[96:99]\n"
"v_add_f32 v116, v116, v237\n"
"s_add_u32 s75, s75, 8192\n"
"s_and_b32 s75, s75, 0x1ffff\n"
"v_mfma_f32_16x16x32_bf16 v[104:107], a[224:227], v[148:151], v[228:231]\n"
"v_exp_f32 v62, v62\n"
"v_mfma_f32_16x16x32_bf16 v[104:107], a[228:231], v[156:159], v[104:107]\n"
"v_exp_f32 v63, v63\n"
"v_mfma_f32_16x16x32_bf16 v[104:107], a[232:235], v[164:167], v[104:107]\n"
"v_add_f32 v117, v117, v238\n"
"v_add_f32 v235, v32, v33\n"
"v_mfma_f32_16x16x32_bf16 v[104:107], a[236:239], v[172:175], v[104:107]\n"
"v_cvt_pk_bf16_f32 v32, v32, v33\n"
"v_add_f32 v236, v40, v41\n"
"v_cvt_pk_bf16_f32 v40, v40, v41\n"
"v_mfma_f32_16x16x32_bf16 v[68:71], v[240:243], a[192:195], v[208:211]\n"
"v_add_f32 v237, v48, v49\n"
"v_cvt_pk_bf16_f32 v48, v48, v49\n"
"v_mfma_f32_16x16x32_bf16 v[68:71], v[244:247], a[200:203], v[68:71]\n"
"v_add_f32 v238, v56, v57\n"
"v_cvt_pk_bf16_f32 v56, v56, v57\n"
"v_mfma_f32_16x16x32_bf16 v[68:71], v[248:251], a[208:211], v[68:71]\n"
"v_add_f32 v235, v235, v34\n"
"v_add_f32 v235, v235, v35\n"
"v_mfma_f32_16x16x32_bf16 v[68:71], v[252:255], a[216:219], v[68:71]\n"
"v_cvt_pk_bf16_f32 v33, v34, v35\n"
"v_add_f32 v236, v236, v42\n"
"v_add_f32 v236, v236, v43\n"
"v_mfma_f32_16x16x32_bf16 v[76:79], v[240:243], a[196:199], v[212:215]\n"
"s_barrier\n"
"v_mfma_f32_16x16x32_bf16 v[76:79], v[244:247], a[204:207], v[76:79]\n"
"ds_read_b64_tr_b16 a[240:241], v6 offset:16384\n"
"ds_read_b64_tr_b16 a[242:243], v6 offset:20480\n"
"v_mfma_f32_16x16x32_bf16 v[76:79], v[248:251], a[212:215], v[76:79]\n"
"ds_read_b64_tr_b16 a[244:245], v7 offset:16384\n"
"v_cvt_pk_bf16_f32 v41, v42, v43\n"
"s_add_u32 m0, s79, 0\n"
"v_mfma_f32_16x16x32_bf16 v[76:79], v[252:255], a[220:223], v[76:79]\n"
"s_waitcnt vmcnt(3)\n"
"buffer_load_dwordx4 v3, s[44:47], s75 offen nt lds\n"
"v_mfma_f32_16x16x32_bf16 v[84:87], v[240:243], v[176:179], v[216:219]\n"
"ds_read_b64_tr_b16 a[246:247], v7 offset:20480\n"
"v_add_f32 v237, v237, v50\n"
"v_mfma_f32_16x16x32_bf16 v[84:87], v[244:247], v[184:187], v[84:87]\n"
"ds_read_b64_tr_b16 a[248:249], v6 offset:16640\n"
"v_add_f32 v237, v237, v51\n"
"v_mfma_f32_16x16x32_bf16 v[84:87], v[248:251], v[192:195], v[84:87]\n"
"ds_read_b64_tr_b16 a[250:251], v6 offset:20736\n"
"v_cvt_pk_bf16_f32 v49, v50, v51\n"
"v_mfma_f32_16x16x32_bf16 v[84:87], v[252:255], v[200:203], v[84:87]\n"
"ds_read_b64_tr_b16 a[252:253], v7 offset:16640\n"
"v_add_f32 v238, v238, v58\n"
"v_add_f32 v238, v238, v59\n"
"v_mfma_f32_16x16x32_bf16 v[92:95], v[240:243], v[180:183], v[220:223]\n"
"ds_read_b64_tr_b16 a[254:255], v7 offset:20736\n"
"v_cvt_pk_bf16_f32 v57, v58, v59\n"
"v_mfma_f32_16x16x32_bf16 v[92:95], v[244:247], v[188:191], v[92:95]\n"
"v_add_f32 v233, v233, v20\n"
"v_add_f32 v233, v233, v21\n"
"v_mfma_f32_16x16x32_bf16 v[92:95], v[248:251], v[196:199], v[92:95]\n"
"v_add_f32 v234, v234, v28\n"
"v_add_f32 v234, v234, v29\n"
"v_mfma_f32_16x16x32_bf16 v[92:95], v[252:255], v[204:207], v[92:95]\n"
"v_add_f32 v235, v235, v36\n"
"v_add_f32 v235, v235, v37\n"
"v_cvt_pk_bf16_f32 v34, v36, v37\n"
"s_add_u32 m0, s79, 0\n"
"v_mfma_f32_16x16x32_bf16 v[100:103], v[240:243], v[144:147], v[224:227]\n"
"buffer_load_dwordx4 v14, s[44:47], s75 offen offset:1024 nt lds\n"
"v_mfma_f32_16x16x32_bf16 v[100:103], v[244:247], v[152:155], v[100:103]\n"
"v_add_f32 v236, v236, v44\n"
"v_mfma_f32_16x16x32_bf16 v[100:103], v[248:251], v[160:163], v[100:103]\n"
"v_add_f32 v236, v236, v45\n"
"v_cvt_pk_bf16_f32 v42, v44, v45\n"
"v_mfma_f32_16x16x32_bf16 v[100:103], v[252:255], v[168:171], v[100:103]\n"
"v_add_f32 v237, v237, v52\n"
"v_add_f32 v237, v237, v53\n"
"v_cvt_pk_bf16_f32 v50, v52, v53\n"
"v_mfma_f32_16x16x32_bf16 v[108:111], v[240:243], v[148:151], v[228:231]\n"
"v_add_f32 v238, v238, v60\n"
"v_add_f32 v238, v238, v61\n"
"v_mfma_f32_16x16x32_bf16 v[108:111], v[244:247], v[156:159], v[108:111]\n"
"v_cvt_pk_bf16_f32 v58, v60, v61\n"
"v_add_f32 v233, v233, v22\n"
"v_mfma_f32_16x16x32_bf16 v[108:111], v[248:251], v[164:167], v[108:111]\n"
"v_add_f32 v233, v233, v23\n"
"v_add_f32 v234, v234, v30\n"
"v_add_f32 v234, v234, v31\n"
"v_add_f32 v235, v235, v38\n"
"v_add_f32 v235, v235, v39\n"
"v_cvt_pk_bf16_f32 v35, v38, v39\n"
"v_add_f32 v236, v236, v46\n"
"v_add_f32 v236, v236, v47\n"
"v_cvt_pk_bf16_f32 v43, v46, v47\n"
"v_add_f32 v237, v237, v54\n"
"v_mfma_f32_16x16x32_bf16 v[108:111], v[252:255], v[172:175], v[108:111]\n"
"v_add_f32 v237, v237, v55\n"
"v_cvt_pk_bf16_f32 v51, v54, v55\n"
"v_add_f32 v238, v238, v62\n"
"v_add_f32 v238, v238, v63\n"
"v_cvt_pk_bf16_f32 v59, v62, v63\n"
"v_max3_f32 v118, v233, v234, v235\n"
"v_max3_f32 v118, v118, v236, v237\n"
"v_max_f32 v118, v118, v238\n"
"v_cmp_nge_f32 vcc, 0x47800000, v118\n"
"s_nop 0\n"
"s_cbranch_vccnz Ltr8_%=\n"
"Lrt8_%=:\n"
"s_waitcnt lgkmcnt(0)\n"
"v_mfma_f32_16x16x32_bf16 a[0:3], a[240:243], v[16:19], a[0:3]\n"
"v_exp_f32 v64, v64\n"
"v_mfma_f32_16x16x32_bf16 a[4:7], a[240:243], v[24:27], a[4:7]\n"
"v_exp_f32 v65, v65\n"
"v_mfma_f32_16x16x32_bf16 a[64:67], a[240:243], v[32:35], a[64:67]\n"
"v_exp_f32 v72, v72\n"
"v_mfma_f32_16x16x32_bf16 a[68:71], a[240:243], v[40:43], a[68:71]\n"
"v_exp_f32 v73, v73\n"
"v_add_f32 v112, v112, v233\n"
"v_mfma_f32_16x16x32_bf16 a[128:131], a[240:243], v[48:51], a[128:131]\n"
"v_mfma_f32_16x16x32_bf16 a[132:135], a[240:243], v[56:59], a[132:135]\n"
"ds_read_b64_tr_b16 a[240:241], v6 offset:16896\n"
"s_add_u32 m0, s79, 81920\n"
"s_waitcnt vmcnt(4)\n"
"buffer_load_dwordx4 v3, s[20:23], s75 offen nt lds\n"
"v_mfma_f32_16x16x32_bf16 a[136:139], a[244:247], v[48:51], a[136:139]\n"
"ds_read_b64_tr_b16 a[242:243], v6 offset:20992\n"
"v_mfma_f32_16x16x32_bf16 a[140:143], a[244:247], v[56:59], a[140:143]\n"
"v_exp_f32 v66, v66\n"
"v_exp_f32 v67, v67\n"
"v_mfma_f32_16x16x32_bf16 a[72:75], a[244:247], v[32:35], a[72:75]\n"
"v_exp_f32 v74, v74\n"
"v_mfma_f32_16x16x32_bf16 a[76:79], a[244:247], v[40:43], a[76:79]\n"
"v_exp_f32 v75, v75\n"
"v_mfma_f32_16x16x32_bf16 a[8:11], a[244:247], v[16:19], a[8:11]\n"
"v_exp_f32 v68, v68\n"
"v_mfma_f32_16x16x32_bf16 a[12:15], a[244:247], v[24:27], a[12:15]\n"
"ds_read_b64_tr_b16 a[244:245], v7 offset:16896\n"
"ds_read_b64_tr_b16 a[246:247], v7 offset:20992\n"
"v_add_f32 v113, v113, v234\n"
"v_mfma_f32_16x16x32_bf16 a[16:19], a[248:251], v[16:19], a[16:19]\n"
"v_exp_f32 v69, v69\n"
"v_mfma_f32_16x16x32_bf16 a[20:23], a[248:251], v[24:27], a[20:23]\n"
"v_exp_f32 v76, v76\n"
"v_mfma_f32_16x16x32_bf16 a[80:83], a[248:251], v[32:35], a[80:83]\n"
"v_exp_f32 v77, v77\n"
"v_mfma_f32_16x16x32_bf16 a[84:87], a[248:251], v[40:43], a[84:87]\n"
"v_exp_f32 v70, v70\n"
"v_add_f32 v233, v64, v65\n"
"v_mfma_f32_16x16x32_bf16 a[144:147], a[248:251], v[48:51], a[144:147]\n"
"v_exp_f32 v71, v71\n"
"v_mfma_f32_16x16x32_bf16 a[148:151], a[248:251], v[56:59], a[148:151]\n"
"ds_read_b64_tr_b16 a[248:249], v6 offset:17152\n"
"ds_read_b64_tr_b16 a[250:251], v6 offset:21248\n"
"s_add_u32 m0, s79, 81920\n"
"v_mfma_f32_16x16x32_bf16 a[152:155], a[252:255], v[48:51], a[152:155]\n"
"s_waitcnt vmcnt(3)\n"
"buffer_load_dwordx4 v14, s[20:23], s75 offen offset:1024 nt lds\n"
"v_mfma_f32_16x16x32_bf16 a[156:159], a[252:255], v[56:59], a[156:159]\n"
"s_add_u32 s77, s77, 8192\n"
"s_and_b32 s77, s77, 0x1ffff\n"
"v_mfma_f32_16x16x32_bf16 a[88:91], a[252:255], v[32:35], a[88:91]\n"
"v_exp_f32 v78, v78\n"
"v_mfma_f32_16x16x32_bf16 a[92:95], a[252:255], v[40:43], a[92:95]\n"
"v_exp_f32 v79, v79\n"
"v_mfma_f32_16x16x32_bf16 a[24:27], a[252:255], v[16:19], a[24:27]\n"
"v_cvt_pk_bf16_f32 v64, v64, v65\n"
"v_add_f32 v234, v72, v73\n"
"v_mfma_f32_16x16x32_bf16 a[28:31], a[252:255], v[24:27], a[28:31]\n"
"ds_read_b64_tr_b16 a[252:253], v7 offset:17152\n"
"ds_read_b64_tr_b16 a[254:255], v7 offset:21248\n"
"s_waitcnt lgkmcnt(2)\n"
"v_mfma_f32_16x16x32_bf16 a[32:35], a[240:243], v[16:19], a[32:35]\n"
"v_exp_f32 v80, v80\n"
"v_mfma_f32_16x16x32_bf16 a[36:39], a[240:243], v[24:27], a[36:39]\n"
"v_cvt_pk_bf16_f32 v72, v72, v73\n"
"v_add_f32 v233, v233, v66\n"
"v_mfma_f32_16x16x32_bf16 a[96:99], a[240:243], v[32:35], a[96:99]\n"
"v_exp_f32 v81, v81\n"
"v_mfma_f32_16x16x32_bf16 a[100:103], a[240:243], v[40:43], a[100:103]\n"
"v_exp_f32 v88, v88\n"
"v_add_f32 v233, v233, v67\n"
"v_mfma_f32_16x16x32_bf16 a[160:163], a[240:243], v[48:51], a[160:163]\n"
"s_barrier\n"
"v_mfma_f32_16x16x32_bf16 a[164:167], a[240:243], v[56:59], a[164:167]\n"
"ds_read_b128 a[224:227], v5 offset:32768\n"
"ds_read_b128 a[228:231], v5 offset:33024\n"
"v_mfma_f32_16x16x32_bf16 a[168:171], a[244:247], v[48:51], a[168:171]\n"
"ds_read_b128 a[232:235], v5 offset:33280\n"
"v_cvt_pk_bf16_f32 v65, v66, v67\n"
"s_add_u32 m0, s79, 73728\n"
"v_mfma_f32_16x16x32_bf16 a[172:175], a[244:247], v[56:59], a[172:175]\n"
"buffer_load_dwordx4 v4, s[48:51], s77 offen nt lds\n"
"v_mfma_f32_16x16x32_bf16 a[104:107], a[244:247], v[32:35], a[104:107]\n"
"ds_read_b128 a[236:239], v5 offset:33536\n"
"v_mfma_f32_16x16x32_bf16 a[108:111], a[244:247], v[40:43], a[108:111]\n"
"ds_read_b128 v[240:243], v5 offset:36864\n"
"v_exp_f32 v89, v89\n"
"v_mfma_f32_16x16x32_bf16 a[40:43], a[244:247], v[16:19], a[40:43]\n"
"ds_read_b128 v[244:247], v5 offset:37120\n"
"v_add_f32 v234, v234, v74\n"
"v_mfma_f32_16x16x32_bf16 a[44:47], a[244:247], v[24:27], a[44:47]\n"
"ds_read_b128 v[248:251], v5 offset:37376\n"
"v_exp_f32 v96, v96\n"
"v_mfma_f32_16x16x32_bf16 a[48:51], a[248:251], v[16:19], a[48:51]\n"
"ds_read_b128 v[252:255], v5 offset:37632\n"
"v_add_f32 v234, v234, v75\n"
"v_mfma_f32_16x16x32_bf16 a[52:55], a[248:251], v[24:27], a[52:55]\n"
"v_exp_f32 v97, v97\n"
"v_mfma_f32_16x16x32_bf16 a[112:115], a[248:251], v[32:35], a[112:115]\n"
"v_exp_f32 v104, v104\n"
"v_mfma_f32_16x16x32_bf16 a[116:119], a[248:251], v[40:43], a[116:119]\n"
"v_exp_f32 v105, v105\n"
"v_cvt_pk_bf16_f32 v73, v74, v75\n"
"v_mfma_f32_16x16x32_bf16 a[176:179], a[248:251], v[48:51], a[176:179]\n"
"v_exp_f32 v82, v82\n"
"v_mfma_f32_16x16x32_bf16 a[180:183], a[248:251], v[56:59], a[180:183]\n"
"v_cvt_pk_bf16_f32 v66, v68, v69\n"
"s_add_u32 m0, s79, 73728\n"
"s_waitcnt lgkmcnt(1)\n"
"v_mfma_f32_16x16x32_bf16 a[184:187], a[252:255], v[48:51], a[184:187]\n"
"s_waitcnt vmcnt(4)\n"
"buffer_load_dwordx4 v232, s[48:51], s77 offen offset:1024 nt lds\n"
"v_mfma_f32_16x16x32_bf16 a[188:191], a[252:255], v[56:59], a[188:191]\n"
"v_exp_f32 v83, v83\n"
"v_mfma_f32_16x16x32_bf16 a[120:123], a[252:255], v[32:35], a[120:123]\n"
"v_exp_f32 v90, v90\n"
"v_mfma_f32_16x16x32_bf16 a[124:127], a[252:255], v[40:43], a[124:127]\n"
"v_exp_f32 v91, v91\n"
"v_mfma_f32_16x16x32_bf16 a[56:59], a[252:255], v[16:19], a[56:59]\n"
"v_exp_f32 v98, v98\n"
"v_mfma_f32_16x16x32_bf16 a[60:63], a[252:255], v[24:27], a[60:63]\n"
"v_exp_f32 v99, v99\n"
"v_cvt_pk_bf16_f32 v74, v76, v77\n"
"v_mfma_f32_16x16x32_bf16 v[16:19], a[224:227], a[192:195], v[208:211]\n"
"v_exp_f32 v106, v106\n"
"v_mfma_f32_16x16x32_bf16 v[16:19], a[228:231], a[200:203], v[16:19]\n"
"v_exp_f32 v107, v107\n"
"v_mfma_f32_16x16x32_bf16 v[16:19], a[232:235], a[208:211], v[16:19]\n"
"v_exp_f32 v84, v84\n"
"v_mfma_f32_16x16x32_bf16 v[16:19], a[236:239], a[216:219], v[16:19]\n"
"v_exp_f32 v85, v85\n"
"v_cvt_pk_bf16_f32 v67, v70, v71\n"
"v_mfma_f32_16x16x32_bf16 v[24:27], a[224:227], a[196:199], v[212:215]\n"
"v_exp_f32 v92, v92\n"
"v_mfma_f32_16x16x32_bf16 v[24:27], a[228:231], a[204:207], v[24:27]\n"
"v_exp_f32 v93, v93\n"
"v_mfma_f32_16x16x32_bf16 v[24:27], a[232:235], a[212:215], v[24:27]\n"
"v_exp_f32 v100, v100\n"
"v_mfma_f32_16x16x32_bf16 v[24:27], a[236:239], a[220:223], v[24:27]\n"
"v_exp_f32 v101, v101\n"
"v_cvt_pk_bf16_f32 v75, v78, v79\n"
"s_add_u32 m0, s79, 155648\n"
"v_mfma_f32_16x16x32_bf16 v[32:35], a[224:227], v[176:179], v[216:219]\n"
"s_waitcnt vmcnt(4)\n"
"buffer_load_dwordx4 v4, s[24:27], s77 offen nt lds\n"
"v_mfma_f32_16x16x32_bf16 v[32:35], a[228:231], v[184:187], v[32:35]\n"
"v_add_f32 v114, v114, v235\n"
"v_mfma_f32_16x16x32_bf16 v[32:35], a[232:235], v[192:195], v[32:35]\n"
"v_exp_f32 v108, v108\n"
"v_mfma_f32_16x16x32_bf16 v[32:35], a[236:239], v[200:203], v[32:35]\n"
"v_exp_f32 v109, v109\n"
"v_add_f32 v115, v115, v236\n"
"v_mfma_f32_16x16x32_bf16 v[40:43], a[224:227], v[180:183], v[220:223]\n"
"v_exp_f32 v86, v86\n"
"v_mfma_f32_16x16x32_bf16 v[40:43], a[228:231], v[188:191], v[40:43]\n"
"v_exp_f32 v87, v87\n"
"v_mfma_f32_16x16x32_bf16 v[40:43], a[232:235], v[196:199], v[40:43]\n"
"v_exp_f32 v94, v94\n"
"v_mfma_f32_16x16x32_bf16 v[40:43], a[236:239], v[204:207], v[40:43]\n"
"v_exp_f32 v95, v95\n"
"v_add_f32 v116, v116, v237\n"
"v_mfma_f32_16x16x32_bf16 v[48:51], a[224:227], v[144:147], v[224:227]\n"
"v_exp_f32 v102, v102\n"
"v_mfma_f32_16x16x32_bf16 v[48:51], a[228:231], v[152:155], v[48:51]\n"
"v_exp_f32 v103, v103\n"
"v_mfma_f32_16x16x32_bf16 v[48:51], a[232:235], v[160:163], v[48:51]\n"
"v_exp_f32 v110, v110\n"
"v_mfma_f32_16x16x32_bf16 v[48:51], a[236:239], v[168:171], v[48:51]\n"
"v_exp_f32 v111, v111\n"
"v_add_f32 v117, v117, v238\n"
"s_add_u32 m0, s79, 155648\n"
"v_mfma_f32_16x16x32_bf16 v[56:59], a[224:227], v[148:151], v[228:231]\n"
"s_waitcnt vmcnt(3)\n"
"buffer_load_dwordx4 v232, s[24:27], s77 offen offset:1024 nt lds\n"
"v_mfma_f32_16x16x32_bf16 v[56:59], a[228:231], v[156:159], v[56:59]\n"
"v_add_f32 v235, v80, v81\n"
"v_mfma_f32_16x16x32_bf16 v[56:59], a[232:235], v[164:167], v[56:59]\n"
"v_cvt_pk_bf16_f32 v80, v80, v81\n"
"v_add_f32 v236, v88, v89\n"
"v_mfma_f32_16x16x32_bf16 v[56:59], a[236:239], v[172:175], v[56:59]\n"
"v_cvt_pk_bf16_f32 v88, v88, v89\n"
"s_add_u32 s75, s75, 8192\n"
"s_and_b32 s75, s75, 0x1ffff\n"
"v_mfma_f32_16x16x32_bf16 v[20:23], v[240:243], a[192:195], v[208:211]\n"
"v_add_f32 v237, v96, v97\n"
"v_cvt_pk_bf16_f32 v96, v96, v97\n"
"v_mfma_f32_16x16x32_bf16 v[20:23], v[244:247], a[200:203], v[20:23]\n"
"v_add_f32 v238, v104, v105\n"
"v_mfma_f32_16x16x32_bf16 v[20:23], v[248:251], a[208:211], v[20:23]\n"
"v_cvt_pk_bf16_f32 v104, v104, v105\n"
"v_add_f32 v235, v235, v82\n"
"s_waitcnt lgkmcnt(0)\n"
"v_mfma_f32_16x16x32_bf16 v[20:23], v[252:255], a[216:219], v[20:23]\n"
"v_add_f32 v235, v235, v83\n"
"v_cvt_pk_bf16_f32 v81, v82, v83\n"
"v_add_f32 v236, v236, v90\n"
"v_mfma_f32_16x16x32_bf16 v[28:31], v[240:243], a[196:199], v[212:215]\n"
"s_barrier\n"
"v_mfma_f32_16x16x32_bf16 v[28:31], v[244:247], a[204:207], v[28:31]\n"
"ds_read_b64_tr_b16 a[240:241], v6 offset:24576\n"
"ds_read_b64_tr_b16 a[242:243], v6 offset:28672\n"
"v_mfma_f32_16x16x32_bf16 v[28:31], v[248:251], a[212:215], v[28:31]\n"
"ds_read_b64_tr_b16 a[244:245], v7 offset:24576\n"
"v_add_f32 v236, v236, v91\n"
"v_mfma_f32_16x16x32_bf16 v[28:31], v[252:255], a[220:223], v[28:31]\n"
"ds_read_b64_tr_b16 a[246:247], v7 offset:28672\n"
"v_cvt_pk_bf16_f32 v89, v90, v91\n"
"v_add_f32 v237, v237, v98\n"
"v_mfma_f32_16x16x32_bf16 v[36:39], v[240:243], v[176:179], v[216:219]\n"
"ds_read_b64_tr_b16 a[248:249], v6 offset:24832\n"
"s_add_u32 m0, s79, 8192\n"
"v_mfma_f32_16x16x32_bf16 v[36:39], v[244:247], v[184:187], v[36:39]\n"
"buffer_load_dwordx4 v3, s[44:47], s75 offen nt lds\n"
"v_mfma_f32_16x16x32_bf16 v[36:39], v[248:251], v[192:195], v[36:39]\n"
"ds_read_b64_tr_b16 a[250:251], v6 offset:28928\n"
"v_add_f32 v237, v237, v99\n"
"v_mfma_f32_16x16x32_bf16 v[36:39], v[252:255], v[200:203], v[36:39]\n"
"ds_read_b64_tr_b16 a[252:253], v7 offset:24832\n"
"v_cvt_pk_bf16_f32 v97, v98, v99\n"
"v_add_f32 v238, v238, v106\n"
"v_mfma_f32_16x16x32_bf16 v[44:47], v[240:243], v[180:183], v[220:223]\n"
"ds_read_b64_tr_b16 a[254:255], v7 offset:28928\n"
"v_add_f32 v238, v238, v107\n"
"v_mfma_f32_16x16x32_bf16 v[44:47], v[244:247], v[188:191], v[44:47]\n"
"v_cvt_pk_bf16_f32 v105, v106, v107\n"
"v_add_f32 v233, v233, v68\n"
"v_mfma_f32_16x16x32_bf16 v[44:47], v[248:251], v[196:199], v[44:47]\n"
"v_add_f32 v233, v233, v69\n"
"v_add_f32 v234, v234, v76\n"
"v_mfma_f32_16x16x32_bf16 v[44:47], v[252:255], v[204:207], v[44:47]\n"
"v_add_f32 v234, v234, v77\n"
"v_add_f32 v235, v235, v84\n"
"v_add_f32 v235, v235, v85\n"
"v_mfma_f32_16x16x32_bf16 v[52:55], v[240:243], v[144:147], v[224:227]\n"
"v_cvt_pk_bf16_f32 v82, v84, v85\n"
"v_add_f32 v236, v236, v92\n"
"v_mfma_f32_16x16x32_bf16 v[52:55], v[244:247], v[152:155], v[52:55]\n"
"v_add_f32 v236, v236, v93\n"
"v_cvt_pk_bf16_f32 v90, v92, v93\n"
"v_mfma_f32_16x16x32_bf16 v[52:55], v[248:251], v[160:163], v[52:55]\n"
"v_add_f32 v237, v237, v100\n"
"v_add_f32 v237, v237, v101\n"
"v_mfma_f32_16x16x32_bf16 v[52:55], v[252:255], v[168:171], v[52:55]\n"
"v_cvt_pk_bf16_f32 v98, v100, v101\n"
"v_add_f32 v238, v238, v108\n"
"v_add_f32 v238, v238, v109\n"
"v_mfma_f32_16x16x32_bf16 v[60:63], v[240:243], v[148:151], v[228:231]\n"
"v_cvt_pk_bf16_f32 v106, v108, v109\n"
"v_add_f32 v233, v233, v70\n"
"v_mfma_f32_16x16x32_bf16 v[60:63], v[244:247], v[156:159], v[60:63]\n"
"v_add_f32 v233, v233, v71\n"
"v_add_f32 v234, v234, v78\n"
"v_mfma_f32_16x16x32_bf16 v[60:63], v[248:251], v[164:167], v[60:63]\n"
"v_add_f32 v234, v234, v79\n"
"v_add_f32 v235, v235, v86\n"
"v_add_f32 v235, v235, v87\n"
"v_cvt_pk_bf16_f32 v83, v86, v87\n"
"v_add_f32 v236, v236, v94\n"
"v_add_f32 v236, v236, v95\n"
"v_cvt_pk_bf16_f32 v91, v94, v95\n"
"v_add_f32 v237, v237, v102\n"
"v_add_f32 v237, v237, v103\n"
"v_mfma_f32_16x16x32_bf16 v[60:63], v[252:255], v[172:175], v[60:63]\n"
"v_cvt_pk_bf16_f32 v99, v102, v103\n"
"v_add_f32 v238, v238, v110\n"
"v_add_f32 v238, v238, v111\n"
"v_cvt_pk_bf16_f32 v107, v110, v111\n"
"v_max3_f32 v118, v233, v234, v235\n"
"v_max3_f32 v118, v118, v236, v237\n"
"v_max_f32 v118, v118, v238\n"
"v_cmp_nge_f32 vcc, 0x47800000, v118\n"
"s_nop 0\n"
"s_cbranch_vccnz Ltr9_%=\n"
"Lrt9_%=:\n"
"s_add_u32 m0, s79, 8192\n"
"s_waitcnt lgkmcnt(0)\n"
"v_mfma_f32_16x16x32_bf16 a[0:3], a[240:243], v[64:67], a[0:3]\n"
"s_waitcnt vmcnt(4)\n"
"buffer_load_dwordx4 v14, s[44:47], s75 offen offset:1024 nt lds\n"
"v_mfma_f32_16x16x32_bf16 a[4:7], a[240:243], v[72:75], a[4:7]\n"
"v_add_f32 v112, v112, v233\n"
"v_mfma_f32_16x16x32_bf16 a[64:67], a[240:243], v[80:83], a[64:67]\n"
"v_exp_f32 v16, v16\n"
"v_mfma_f32_16x16x32_bf16 a[68:71], a[240:243], v[88:91], a[68:71]\n"
"v_exp_f32 v17, v17\n"
"v_add_f32 v113, v113, v234\n"
"v_mfma_f32_16x16x32_bf16 a[128:131], a[240:243], v[96:99], a[128:131]\n"
"v_exp_f32 v24, v24\n"
"v_mfma_f32_16x16x32_bf16 a[132:135], a[240:243], v[104:107], a[132:135]\n"
"ds_read_b64_tr_b16 a[240:241], v6 offset:25088\n"
"ds_read_b64_tr_b16 a[242:243], v6 offset:29184\n"
"v_mfma_f32_16x16x32_bf16 a[136:139], a[244:247], v[96:99], a[136:139]\n"
"v_exp_f32 v25, v25\n"
"v_mfma_f32_16x16x32_bf16 a[140:143], a[244:247], v[104:107], a[140:143]\n"
"v_exp_f32 v18, v18\n"
"v_add_f32 v233, v16, v17\n"
"v_mfma_f32_16x16x32_bf16 a[72:75], a[244:247], v[80:83], a[72:75]\n"
"v_exp_f32 v19, v19\n"
"v_mfma_f32_16x16x32_bf16 a[76:79], a[244:247], v[88:91], a[76:79]\n"
"v_exp_f32 v26, v26\n"
"v_mfma_f32_16x16x32_bf16 a[8:11], a[244:247], v[64:67], a[8:11]\n"
"v_cvt_pk_bf16_f32 v16, v16, v17\n"
"v_mfma_f32_16x16x32_bf16 a[12:15], a[244:247], v[72:75], a[12:15]\n"
"ds_read_b64_tr_b16 a[244:245], v7 offset:25088\n"
"s_add_u32 m0, s79, 90112\n"
"s_waitcnt vmcnt(3)\n"
"buffer_load_dwordx4 v3, s[20:23], s75 offen nt lds\n"
"v_mfma_f32_16x16x32_bf16 a[16:19], a[248:251], v[64:67], a[16:19]\n"
"ds_read_b64_tr_b16 a[246:247], v7 offset:29184\n"
"v_add_f32 v234, v24, v25\n"
"v_mfma_f32_16x16x32_bf16 a[20:23], a[248:251], v[72:75], a[20:23]\n"
"v_exp_f32 v27, v27\n"
"v_mfma_f32_16x16x32_bf16 a[80:83], a[248:251], v[80:83], a[80:83]\n"
"v_exp_f32 v20, v20\n"
"v_mfma_f32_16x16x32_bf16 a[84:87], a[248:251], v[88:91], a[84:87]\n"
"v_exp_f32 v21, v21\n"
"v_cvt_pk_bf16_f32 v24, v24, v25\n"
"v_mfma_f32_16x16x32_bf16 a[144:147], a[248:251], v[96:99], a[144:147]\n"
"v_exp_f32 v28, v28\n"
"v_mfma_f32_16x16x32_bf16 a[148:151], a[248:251], v[104:107], a[148:151]\n"
"ds_read_b64_tr_b16 a[248:249], v6 offset:25344\n"
"ds_read_b64_tr_b16 a[250:251], v6 offset:29440\n"
"v_mfma_f32_16x16x32_bf16 a[152:155], a[252:255], v[96:99], a[152:155]\n"
"v_exp_f32 v29, v29\n"
"v_mfma_f32_16x16x32_bf16 a[156:159], a[252:255], v[104:107], a[156:159]\n"
"v_exp_f32 v22, v22\n"
"v_add_f32 v233, v233, v18\n"
"v_mfma_f32_16x16x32_bf16 a[88:91], a[252:255], v[80:83], a[88:91]\n"
"v_exp_f32 v23, v23\n"
"v_mfma_f32_16x16x32_bf16 a[92:95], a[252:255], v[88:91], a[92:95]\n"
"v_exp_f32 v30, v30\n"
"s_add_u32 m0, s79, 90112\n"
"v_mfma_f32_16x16x32_bf16 a[24:27], a[252:255], v[64:67], a[24:27]\n"
"buffer_load_dwordx4 v14, s[20:23], s75 offen offset:1024 nt lds\n"
"v_mfma_f32_16x16x32_bf16 a[28:31], a[252:255], v[72:75], a[28:31]\n"
"s_waitcnt lgkmcnt(0)\n"
"v_mfma_f32_16x16x32_bf16 a[32:35], a[240:243], v[64:67], a[32:35]\n"
"ds_read_b64_tr_b16 a[252:253], v7 offset:25344\n"
"v_mfma_f32_16x16x32_bf16 a[36:39], a[240:243], v[72:75], a[36:39]\n"
"ds_read_b64_tr_b16 a[254:255], v7 offset:29440\n"
"s_add_u32 s77, s77, 8192\n"
"s_and_b32 s77, s77, 0x1ffff\n"
"v_mfma_f32_16x16x32_bf16 a[96:99], a[240:243], v[80:83], a[96:99]\n"
"v_exp_f32 v32, v32\n"
"v_mfma_f32_16x16x32_bf16 a[100:103], a[240:243], v[88:91], a[100:103]\n"
"v_exp_f32 v31, v31\n"
"v_add_f32 v233, v233, v19\n"
"v_mfma_f32_16x16x32_bf16 a[160:163], a[240:243], v[96:99], a[160:163]\n"
"s_barrier\n"
"v_mfma_f32_16x16x32_bf16 a[164:167], a[240:243], v[104:107], a[164:167]\n"
"ds_read_b128 a[224:227], v5 offset:0\n"
"ds_read_b128 a[228:231], v5 offset:256\n"
"v_mfma_f32_16x16x32_bf16 a[168:171], a[244:247], v[96:99], a[168:171]\n"
"ds_read_b128 a[232:235], v5 offset:512\n"
"v_cvt_pk_bf16_f32 v17, v18, v19\n"
"v_mfma_f32_16x16x32_bf16 a[172:175], a[244:247], v[104:107], a[172:175]\n"
"ds_read_b128 a[236:239], v5 offset:768\n"
"v_exp_f32 v33, v33\n"
"v_mfma_f32_16x16x32_bf16 a[104:107], a[244:247], v[80:83], a[104:107]\n"
"ds_read_b128 v[240:243], v5 offset:4096\n"
"v_mfma_f32_16x16x32_bf16 a[108:111], a[244:247], v[88:91], a[108:111]\n"
"ds_read_b128 v[244:247], v5 offset:4352\n"
"v_exp_f32 v40, v40\n"
"v_mfma_f32_16x16x32_bf16 a[40:43], a[244:247], v[64:67], a[40:43]\n"
"ds_read_b128 v[248:251], v5 offset:4608\n"
"v_add_f32 v234, v234, v26\n"
"s_add_u32 m0, s79, 40960\n"
"v_mfma_f32_16x16x32_bf16 a[44:47], a[244:247], v[72:75], a[44:47]\n"
"s_waitcnt vmcnt(4)\n"
"buffer_load_dwordx4 v4, s[48:51], s77 offen nt lds\n"
"v_mfma_f32_16x16x32_bf16 a[48:51], a[248:251], v[64:67], a[48:51]\n"
"ds_read_b128 v[252:255], v5 offset:4864\n"
"v_add_f32 v234, v234, v27\n"
"v_mfma_f32_16x16x32_bf16 a[52:55], a[248:251], v[72:75], a[52:55]\n"
"v_exp_f32 v41, v41\n"
"v_mfma_f32_16x16x32_bf16 a[112:115], a[248:251], v[80:83], a[112:115]\n"
"v_exp_f32 v48, v48\n"
"v_mfma_f32_16x16x32_bf16 a[116:119], a[248:251], v[88:91], a[116:119]\n"
"v_exp_f32 v49, v49\n"
"v_cvt_pk_bf16_f32 v25, v26, v27\n"
"v_mfma_f32_16x16x32_bf16 a[176:179], a[248:251], v[96:99], a[176:179]\n"
"v_exp_f32 v56, v56\n"
"v_mfma_f32_16x16x32_bf16 a[180:183], a[248:251], v[104:107], a[180:183]\n"
"v_cvt_pk_bf16_f32 v18, v20, v21\n"
"s_waitcnt lgkmcnt(1)\n"
"v_mfma_f32_16x16x32_bf16 a[184:187], a[252:255], v[96:99], a[184:187]\n"
"v_exp_f32 v57, v57\n"
"v_mfma_f32_16x16x32_bf16 a[188:191], a[252:255], v[104:107], a[188:191]\n"
"v_exp_f32 v34, v34\n"
"v_cvt_pk_bf16_f32 v26, v28, v29\n"
"v_mfma_f32_16x16x32_bf16 a[120:123], a[252:255], v[80:83], a[120:123]\n"
"v_exp_f32 v35, v35\n"
"v_mfma_f32_16x16x32_bf16 a[124:127], a[252:255], v[88:91], a[124:127]\n"
"v_exp_f32 v42, v42\n"
"v_mfma_f32_16x16x32_bf16 a[56:59], a[252:255], v[64:67], a[56:59]\n"
"v_exp_f32 v43, v43\n"
"v_mfma_f32_16x16x32_bf16 a[60:63], a[252:255], v[72:75], a[60:63]\n"
"v_exp_f32 v50, v50\n"
"v_cvt_pk_bf16_f32 v19, v22, v23\n"
"s_add_u32 m0, s79, 40960\n"
"v_mfma_f32_16x16x32_bf16 v[64:67], a[224:227], a[192:195], v[208:211]\n"
"s_waitcnt vmcnt(3)\n"
"buffer_load_dwordx4 v232, s[48:51], s77 offen offset:1024 nt lds\n"
"v_mfma_f32_16x16x32_bf16 v[64:67], a[228:231], a[200:203], v[64:67]\n"
"v_cvt_pk_bf16_f32 v27, v30, v31\n"
"v_mfma_f32_16x16x32_bf16 v[64:67], a[232:235], a[208:211], v[64:67]\n"
"v_exp_f32 v51, v51\n"
"v_mfma_f32_16x16x32_bf16 v[64:67], a[236:239], a[216:219], v[64:67]\n"
"v_exp_f32 v58, v58\n"
"v_add_f32 v114, v114, v235\n"
"v_mfma_f32_16x16x32_bf16 v[72:75], a[224:227], a[196:199], v[212:215]\n"
"v_exp_f32 v59, v59\n"
"v_mfma_f32_16x16x32_bf16 v[72:75], a[228:231], a[204:207], v[72:75]\n"
"v_exp_f32 v36, v36\n"
"v_mfma_f32_16x16x32_bf16 v[72:75], a[232:235], a[212:215], v[72:75]\n"
"v_exp_f32 v37, v37\n"
"v_mfma_f32_16x16x32_bf16 v[72:75], a[236:239], a[220:223], v[72:75]\n"
"v_exp_f32 v44, v44\n"
"v_add_f32 v115, v115, v236\n"
"v_mfma_f32_16x16x32_bf16 v[80:83], a[224:227], v[176:179], v[216:219]\n"
"v_exp_f32 v45, v45\n"
"v_mfma_f32_16x16x32_bf16 v[80:83], a[228:231], v[184:187], v[80:83]\n"
"v_exp_f32 v52, v52\n"
"v_mfma_f32_16x16x32_bf16 v[80:83], a[232:235], v[192:195], v[80:83]\n"
"v_exp_f32 v53, v53\n"
"v_mfma_f32_16x16x32_bf16 v[80:83], a[236:239], v[200:203], v[80:83]\n"
"v_exp_f32 v60, v60\n"
"v_add_f32 v116, v116, v237\n"
"s_add_u32 m0, s79, 122880\n"
"v_mfma_f32_16x16x32_bf16 v[88:91], a[224:227], v[180:183], v[220:223]\n"
"buffer_load_dwordx4 v4, s[24:27], s77 offen nt lds\n"
"v_mfma_f32_16x16x32_bf16 v[88:91], a[228:231], v[188:191], v[88:91]\n"
"v_add_f32 v117, v117, v238\n"
"v_mfma_f32_16x16x32_bf16 v[88:91], a[232:235], v[196:199], v[88:91]\n"
"v_exp_f32 v61, v61\n"
"v_mfma_f32_16x16x32_bf16 v[88:91], a[236:239], v[204:207], v[88:91]\n"
"v_exp_f32 v38, v38\n"
"v_add_f32 v235, v32, v33\n"
"v_mfma_f32_16x16x32_bf16 v[96:99], a[224:227], v[144:147], v[224:227]\n"
"v_exp_f32 v39, v39\n"
"v_mfma_f32_16x16x32_bf16 v[96:99], a[228:231], v[152:155], v[96:99]\n"
"v_exp_f32 v46, v46\n"
"v_mfma_f32_16x16x32_bf16 v[96:99], a[232:235], v[160:163], v[96:99]\n"
"v_exp_f32 v47, v47\n"
"v_mfma_f32_16x16x32_bf16 v[96:99], a[236:239], v[168:171], v[96:99]\n"
"v_exp_f32 v54, v54\n"
"v_cvt_pk_bf16_f32 v32, v32, v33\n"
"v_mfma_f32_16x16x32_bf16 v[104:107], a[224:227], v[148:151], v[228:231]\n"
"v_exp_f32 v55, v55\n"
"v_mfma_f32_16x16x32_bf16 v[104:107], a[228:231], v[156:159], v[104:107]\n"
"v_exp_f32 v62, v62\n"
"v_mfma_f32_16x16x32_bf16 v[104:107], a[232:235], v[164:167], v[104:107]\n"
"v_exp_f32 v63, v63\n"
"v_mfma_f32_16x16x32_bf16 v[104:107], a[236:239], v[172:175], v[104:107]\n"
"v_add_f32 v236, v40, v41\n"
"v_cvt_pk_bf16_f32 v40, v40, v41\n"
"v_add_f32 v237, v48, v49\n"
"s_add_u32 m0, s79, 122880\n"
"v_mfma_f32_16x16x32_bf16 v[68:71], v[240:243], a[192:195], v[208:211]\n"
"s_waitcnt vmcnt(4)\n"
"buffer_load_dwordx4 v232, s[24:27], s77 offen offset:1024 nt lds\n"
"v_mfma_f32_16x16x32_bf16 v[68:71], v[244:247], a[200:203], v[68:71]\n"
"v_mfma_f32_16x16x32_bf16 v[68:71], v[248:251], a[208:211], v[68:71]\n"
"v_cvt_pk_bf16_f32 v48, v48, v49\n"
"v_add_f32 v238, v56, v57\n"
"s_waitcnt lgkmcnt(0)\n"
"v_mfma_f32_16x16x32_bf16 v[68:71], v[252:255], a[216:219], v[68:71]\n"
"v_cvt_pk_bf16_f32 v56, v56, v57\n"
"s_add_u32 s75, s75, 8192\n"
"s_and_b32 s75, s75, 0x1ffff\n"
"v_mfma_f32_16x16x32_bf16 v[76:79], v[240:243], a[196:199], v[212:215]\n"
"s_barrier\n"
"v_mfma_f32_16x16x32_bf16 v[76:79], v[244:247], a[204:207], v[76:79]\n"
"ds_read_b64_tr_b16 a[240:241], v6 offset:32768\n"
"ds_read_b64_tr_b16 a[242:243], v6 offset:36864\n"
"v_mfma_f32_16x16x32_bf16 v[76:79], v[248:251], a[212:215], v[76:79]\n"
"ds_read_b64_tr_b16 a[244:245], v7 offset:32768\n"
"v_add_f32 v235, v235, v34\n"
"v_mfma_f32_16x16x32_bf16 v[76:79], v[252:255], a[220:223], v[76:79]\n"
"ds_read_b64_tr_b16 a[246:247], v7 offset:36864\n"
"v_add_f32 v235, v235, v35\n"
"v_cvt_pk_bf16_f32 v33, v34, v35\n"
"v_mfma_f32_16x16x32_bf16 v[84:87], v[240:243], v[176:179], v[216:219]\n"
"ds_read_b64_tr_b16 a[248:249], v6 offset:33024\n"
"v_add_f32 v236, v236, v42\n"
"v_mfma_f32_16x16x32_bf16 v[84:87], v[244:247], v[184:187], v[84:87]\n"
"ds_read_b64_tr_b16 a[250:251], v6 offset:37120\n"
"v_add_f32 v236, v236, v43\n"
"v_mfma_f32_16x16x32_bf16 v[84:87], v[248:251], v[192:195], v[84:87]\n"
"ds_read_b64_tr_b16 a[252:253], v7 offset:33024\n"
"v_cvt_pk_bf16_f32 v41, v42, v43\n"
"v_mfma_f32_16x16x32_bf16 v[84:87], v[252:255], v[200:203], v[84:87]\n"
"ds_read_b64_tr_b16 a[254:255], v7 offset:37120\n"
"v_add_f32 v237, v237, v50\n"
"v_add_f32 v237, v237, v51\n"
"v_mfma_f32_16x16x32_bf16 v[92:95], v[240:243], v[180:183], v[220:223]\n"
"v_cvt_pk_bf16_f32 v49, v50, v51\n"
"v_add_f32 v238, v238, v58\n"
"v_mfma_f32_16x16x32_bf16 v[92:95], v[244:247], v[188:191], v[92:95]\n"
"v_add_f32 v238, v238, v59\n"
"v_cvt_pk_bf16_f32 v57, v58, v59\n"
"s_add_u32 m0, s79, 16384\n"
"v_mfma_f32_16x16x32_bf16 v[92:95], v[248:251], v[196:199], v[92:95]\n"
"s_waitcnt vmcnt(4)\n"
"buffer_load_dwordx4 v3, s[44:47], s75 offen nt lds\n"
"v_mfma_f32_16x16x32_bf16 v[92:95], v[252:255], v[204:207], v[92:95]\n"
"v_add_f32 v233, v233, v20\n"
"v_add_f32 v233, v233, v21\n"
"v_mfma_f32_16x16x32_bf16 v[100:103], v[240:243], v[144:147], v[224:227]\n"
"v_add_f32 v234, v234, v28\n"
"v_add_f32 v234, v234, v29\n"
"v_mfma_f32_16x16x32_bf16 v[100:103], v[244:247], v[152:155], v[100:103]\n"
"v_add_f32 v235, v235, v36\n"
"v_add_f32 v235, v235, v37\n"
"v_mfma_f32_16x16x32_bf16 v[100:103], v[248:251], v[160:163], v[100:103]\n"
"v_cvt_pk_bf16_f32 v34, v36, v37\n"
"v_add_f32 v236, v236, v44\n"
"v_mfma_f32_16x16x32_bf16 v[100:103], v[252:255], v[168:171], v[100:103]\n"
"v_add_f32 v236, v236, v45\n"
"v_cvt_pk_bf16_f32 v42, v44, v45\n"
"v_add_f32 v237, v237, v52\n"
"v_mfma_f32_16x16x32_bf16 v[108:111], v[240:243], v[148:151], v[228:231]\n"
"v_add_f32 v237, v237, v53\n"
"v_cvt_pk_bf16_f32 v50, v52, v53\n"
"v_mfma_f32_16x16x32_bf16 v[108:111], v[244:247], v[156:159], v[108:111]\n"
"v_add_f32 v238, v238, v60\n"
"v_add_f32 v238, v238, v61\n"
"v_mfma_f32_16x16x32_bf16 v[108:111], v[248:251], v[164:167], v[108:111]\n"
"v_cvt_pk_bf16_f32 v58, v60, v61\n"
"v_add_f32 v233, v233, v22\n"
"v_add_f32 v233, v233, v23\n"
"v_add_f32 v234, v234, v30\n"
"v_add_f32 v234, v234, v31\n"
"v_add_f32 v235, v235, v38\n"
"v_add_f32 v235, v235, v39\n"
"v_cvt_pk_bf16_f32 v35, v38, v39\n"
"v_add_f32 v236, v236, v46\n"
"v_add_f32 v236, v236, v47\n"
"v_cvt_pk_bf16_f32 v43, v46, v47\n"
"v_mfma_f32_16x16x32_bf16 v[108:111], v[252:255], v[172:175], v[108:111]\n"
"v_add_f32 v237, v237, v54\n"
"v_add_f32 v237, v237, v55\n"
"v_cvt_pk_bf16_f32 v51, v54, v55\n"
"v_add_f32 v238, v238, v62\n"
"v_add_f32 v238, v238, v63\n"
"v_cvt_pk_bf16_f32 v59, v62, v63\n"
"v_max3_f32 v118, v233, v234, v235\n"
"v_max3_f32 v118, v118, v236, v237\n"
"v_max_f32 v118, v118, v238\n"
"v_cmp_nge_f32 vcc, 0x47800000, v118\n"
"s_nop 0\n"
"s_cbranch_vccnz Ltr10_%=\n"
"Lrt10_%=:\n"
"s_waitcnt lgkmcnt(0)\n"
"v_mfma_f32_16x16x32_bf16 a[0:3], a[240:243], v[16:19], a[0:3]\n"
"v_exp_f32 v64, v64\n"
"v_mfma_f32_16x16x32_bf16 a[4:7], a[240:243], v[24:27], a[4:7]\n"
"v_exp_f32 v65, v65\n"
"s_add_u32 m0, s79, 16384\n"
"v_mfma_f32_16x16x32_bf16 a[64:67], a[240:243], v[32:35], a[64:67]\n"
"s_waitcnt vmcnt(3)\n"
"buffer_load_dwordx4 v14, s[44:47], s75 offen offset:1024 nt lds\n"
"v_mfma_f32_16x16x32_bf16 a[68:71], a[240:243], v[40:43], a[68:71]\n"
"v_exp_f32 v72, v72\n"
"v_mfma_f32_16x16x32_bf16 a[128:131], a[240:243], v[48:51], a[128:131]\n"
"v_exp_f32 v73, v73\n"
"v_mfma_f32_16x16x32_bf16 a[132:135], a[240:243], v[56:59], a[132:135]\n"
"ds_read_b64_tr_b16 a[240:241], v6 offset:33280\n"
"ds_read_b64_tr_b16 a[242:243], v6 offset:37376\n"
"v_mfma_f32_16x16x32_bf16 a[136:139], a[244:247], v[48:51], a[136:139]\n"
"v_exp_f32 v66, v66\n"
"v_mfma_f32_16x16x32_bf16 a[140:143], a[244:247], v[56:59], a[140:143]\n"
"v_exp_f32 v67, v67\n"
"v_add_f32 v112, v112, v233\n"
"v_mfma_f32_16x16x32_bf16 a[72:75], a[244:247], v[32:35], a[72:75]\n"
"v_exp_f32 v74, v74\n"
"v_mfma_f32_16x16x32_bf16 a[76:79], a[244:247], v[40:43], a[76:79]\n"
"v_exp_f32 v75, v75\n"
"v_mfma_f32_16x16x32_bf16 a[8:11], a[244:247], v[16:19], a[8:11]\n"
"v_exp_f32 v68, v68\n"
"v_mfma_f32_16x16x32_bf16 a[12:15], a[244:247], v[24:27], a[12:15]\n"
"ds_read_b64_tr_b16 a[244:245], v7 offset:33280\n"
"ds_read_b64_tr_b16 a[246:247], v7 offset:37376\n"
"v_add_f32 v113, v113, v234\n"
"v_mfma_f32_16x16x32_bf16 a[16:19], a[248:251], v[16:19], a[16:19]\n"
"v_exp_f32 v69, v69\n"
"v_mfma_f32_16x16x32_bf16 a[20:23], a[248:251], v[24:27], a[20:23]\n"
"v_exp_f32 v76, v76\n"
"s_add_u32 m0, s79, 98304\n"
"v_mfma_f32_16x16x32_bf16 a[80:83], a[248:251], v[32:35], a[80:83]\n"
"buffer_load_dwordx4 v3, s[20:23], s75 offen nt lds\n"
"v_mfma_f32_16x16x32_bf16 a[84:87], a[248:251], v[40:43], a[84:87]\n"
"v_exp_f32 v77, v77\n"
"v_mfma_f32_16x16x32_bf16 a[144:147], a[248:251], v[48:51], a[144:147]\n"
"v_exp_f32 v70, v70\n"
"v_mfma_f32_16x16x32_bf16 a[148:151], a[248:251], v[56:59], a[148:151]\n"
"ds_read_b64_tr_b16 a[248:249], v6 offset:33536\n"
"ds_read_b64_tr_b16 a[250:251], v6 offset:37632\n"
"v_mfma_f32_16x16x32_bf16 a[152:155], a[252:255], v[48:51], a[152:155]\n"
"v_exp_f32 v71, v71\n"
"v_mfma_f32_16x16x32_bf16 a[156:159], a[252:255], v[56:59], a[156:159]\n"
"v_exp_f32 v78, v78\n"
"v_add_f32 v233, v64, v65\n"
"v_mfma_f32_16x16x32_bf16 a[88:91], a[252:255], v[32:35], a[88:91]\n"
"v_exp_f32 v79, v79\n"
"v_mfma_f32_16x16x32_bf16 a[92:95], a[252:255], v[40:43], a[92:95]\n"
"v_cvt_pk_bf16_f32 v64, v64, v65\n"
"v_add_f32 v234, v72, v73\n"
"v_mfma_f32_16x16x32_bf16 a[24:27], a[252:255], v[16:19], a[24:27]\n"
"v_cvt_pk_bf16_f32 v72, v72, v73\n"
"v_add_f32 v233, v233, v66\n"
"v_mfma_f32_16x16x32_bf16 a[28:31], a[252:255], v[24:27], a[28:31]\n"
"ds_read_b64_tr_b16 a[252:253], v7 offset:33536\n"
"ds_read_b64_tr_b16 a[254:255], v7 offset:37632\n"
"s_waitcnt lgkmcnt(2)\n"
"v_mfma_f32_16x16x32_bf16 a[32:35], a[240:243], v[16:19], a[32:35]\n"
"v_exp_f32 v80, v80\n"
"v_mfma_f32_16x16x32_bf16 a[36:39], a[240:243], v[24:27], a[36:39]\n"
"v_exp_f32 v81, v81\n"
"v_mfma_f32_16x16x32_bf16 a[96:99], a[240:243], v[32:35], a[96:99]\n"
"v_exp_f32 v88, v88\n"
"v_mfma_f32_16x16x32_bf16 a[100:103], a[240:243], v[40:43], a[100:103]\n"
"v_exp_f32 v89, v89\n"
"v_add_f32 v233, v233, v67\n"
"v_mfma_f32_16x16x32_bf16 a[160:163], a[240:243], v[48:51], a[160:163]\n"
"s_barrier\n"
"v_mfma_f32_16x16x32_bf16 a[164:167], a[240:243], v[56:59], a[164:167]\n"
"ds_read_b128 a[224:227], v5 offset:8192\n"
"ds_read_b128 a[228:231], v5 offset:8448\n"
"s_add_u32 m0, s79, 98304\n"
"v_mfma_f32_16x16x32_bf16 a[168:171], a[244:247], v[48:51], a[168:171]\n"
"s_waitcnt vmcnt(4)\n"
"buffer_load_dwordx4 v14, s[20:23], s75 offen offset:1024 nt lds\n"
"v_mfma_f32_16x16x32_bf16 a[172:175], a[244:247], v[56:59], a[172:175]\n"
"s_add_u32 s77, s77, 8192\n"
"s_and_b32 s77, s77, 0x1ffff\n"
"v_mfma_f32_16x16x32_bf16 a[104:107], a[244:247], v[32:35], a[104:107]\n"
"ds_read_b128 a[232:235], v5 offset:8704\n"
"v_mfma_f32_16x16x32_bf16 a[108:111], a[244:247], v[40:43], a[108:111]\n"
"ds_read_b128 a[236:239], v5 offset:8960\n"
"v_exp_f32 v96, v96\n"
"v_mfma_f32_16x16x32_bf16 a[40:43], a[244:247], v[16:19], a[40:43]\n"
"ds_read_b128 v[240:243], v5 offset:12288\n"
"v_cvt_pk_bf16_f32 v65, v66, v67\n"
"v_mfma_f32_16x16x32_bf16 a[44:47], a[244:247], v[24:27], a[44:47]\n"
"ds_read_b128 v[244:247], v5 offset:12544\n"
"v_exp_f32 v97, v97\n"
"v_mfma_f32_16x16x32_bf16 a[48:51], a[248:251], v[16:19], a[48:51]\n"
"ds_read_b128 v[248:251], v5 offset:12800\n"
"v_mfma_f32_16x16x32_bf16 a[52:55], a[248:251], v[24:27], a[52:55]\n"
"ds_read_b128 v[252:255], v5 offset:13056\n"
"v_exp_f32 v104, v104\n"
"v_mfma_f32_16x16x32_bf16 a[112:115], a[248:251], v[32:35], a[112:115]\n"
"v_exp_f32 v105, v105\n"
"v_mfma_f32_16x16x32_bf16 a[116:119], a[248:251], v[40:43], a[116:119]\n"
"v_add_f32 v234, v234, v74\n"
"v_add_f32 v234, v234, v75\n"
"v_cvt_pk_bf16_f32 v73, v74, v75\n"
"s_add_u32 m0, s79, 49152\n"
"v_mfma_f32_16x16x32_bf16 a[176:179], a[248:251], v[48:51], a[176:179]\n"
"s_waitcnt vmcnt(3)\n"
"buffer_load_dwordx4 v4, s[48:51], s77 offen nt lds\n"
"v_mfma_f32_16x16x32_bf16 a[180:183], a[248:251], v[56:59], a[180:183]\n"
"s_waitcnt lgkmcnt(2)\n"
"v_mfma_f32_16x16x32_bf16 a[184:187], a[252:255], v[48:51], a[184:187]\n"
"v_exp_f32 v82, v82\n"
"v_mfma_f32_16x16x32_bf16 a[188:191], a[252:255], v[56:59], a[188:191]\n"
"v_exp_f32 v83, v83\n"
"v_cvt_pk_bf16_f32 v66, v68, v69\n"
"v_mfma_f32_16x16x32_bf16 a[120:123], a[252:255], v[32:35], a[120:123]\n"
"v_exp_f32 v90, v90\n"
"v_mfma_f32_16x16x32_bf16 a[124:127], a[252:255], v[40:43], a[124:127]\n"
"v_exp_f32 v91, v91\n"
"v_mfma_f32_16x16x32_bf16 a[56:59], a[252:255], v[16:19], a[56:59]\n"
"v_exp_f32 v98, v98\n"
"v_mfma_f32_16x16x32_bf16 a[60:63], a[252:255], v[24:27], a[60:63]\n"
"v_exp_f32 v99, v99\n"
"v_cvt_pk_bf16_f32 v74, v76, v77\n"
"v_mfma_f32_16x16x32_bf16 v[16:19], a[224:227], a[192:195], v[208:211]\n"
"v_exp_f32 v106, v106\n"
"v_mfma_f32_16x16x32_bf16 v[16:19], a[228:231], a[200:203], v[16:19]\n"
"v_exp_f32 v107, v107\n"
"v_mfma_f32_16x16x32_bf16 v[16:19], a[232:235], a[208:211], v[16:19]\n"
"v_exp_f32 v84, v84\n"
"v_mfma_f32_16x16x32_bf16 v[16:19], a[236:239], a[216:219], v[16:19]\n"
"v_exp_f32 v85, v85\n"
"v_cvt_pk_bf16_f32 v67, v70, v71\n"
"s_add_u32 m0, s79, 49152\n"
"v_mfma_f32_16x16x32_bf16 v[24:27], a[224:227], a[196:199], v[212:215]\n"
"buffer_load_dwordx4 v232, s[48:51], s77 offen offset:1024 nt lds\n"
"v_mfma_f32_16x16x32_bf16 v[24:27], a[228:231], a[204:207], v[24:27]\n"
"v_cvt_pk_bf16_f32 v75, v78, v79\n"
"v_mfma_f32_16x16x32_bf16 v[24:27], a[232:235], a[212:215], v[24:27]\n"
"v_exp_f32 v92, v92\n"
"v_mfma_f32_16x16x32_bf16 v[24:27], a[236:239], a[220:223], v[24:27]\n"
"v_exp_f32 v93, v93\n"
"v_add_f32 v114, v114, v235\n"
"v_mfma_f32_16x16x32_bf16 v[32:35], a[224:227], v[176:179], v[216:219]\n"
"v_exp_f32 v100, v100\n"
"v_mfma_f32_16x16x32_bf16 v[32:35], a[228:231], v[184:187], v[32:35]\n"
"v_exp_f32 v101, v101\n"
"v_mfma_f32_16x16x32_bf16 v[32:35], a[232:235], v[192:195], v[32:35]\n"
"v_exp_f32 v108, v108\n"
"v_mfma_f32_16x16x32_bf16 v[32:35], a[236:239], v[200:203], v[32:35]\n"
"v_exp_f32 v109, v109\n"
"v_add_f32 v115, v115, v236\n"
"v_mfma_f32_16x16x32_bf16 v[40:43], a[224:227], v[180:183], v[220:223]\n"
"v_exp_f32 v86, v86\n"
"v_mfma_f32_16x16x32_bf16 v[40:43], a[228:231], v[188:191], v[40:43]\n"
"v_exp_f32 v87, v87\n"
"v_mfma_f32_16x16x32_bf16 v[40:43], a[232:235], v[196:199], v[40:43]\n"
"v_exp_f32 v94, v94\n"
"v_mfma_f32_16x16x32_bf16 v[40:43], a[236:239], v[204:207], v[40:43]\n"
"v_exp_f32 v95, v95\n"
"v_add_f32 v116, v116, v237\n"
"s_add_u32 m0, s79, 131072\n"
"v_mfma_f32_16x16x32_bf16 v[48:51], a[224:227], v[144:147], v[224:227]\n"
"s_waitcnt vmcnt(4)\n"
"buffer_load_dwordx4 v4, s[24:27], s77 offen nt lds\n"
"v_mfma_f32_16x16x32_bf16 v[48:51], a[228:231], v[152:155], v[48:51]\n"
"v_add_f32 v117, v117, v238\n"
"v_mfma_f32_16x16x32_bf16 v[48:51], a[232:235], v[160:163], v[48:51]\n"
"v_exp_f32 v102, v102\n"
"v_mfma_f32_16x16x32_bf16 v[48:51], a[236:239], v[168:171], v[48:51]\n"
"v_exp_f32 v103, v103\n"
"v_add_f32 v235, v80, v81\n"
"v_mfma_f32_16x16x32_bf16 v[56:59], a[224:227], v[148:151], v[228:231]\n"
"v_exp_f32 v110, v110\n"
"v_mfma_f32_16x16x32_bf16 v[56:59], a[228:231], v[156:159], v[56:59]\n"
"v_exp_f32 v111, v111\n"
"v_mfma_f32_16x16x32_bf16 v[56:59], a[232:235], v[164:167], v[56:59]\n"
"v_cvt_pk_bf16_f32 v80, v80, v81\n"
"v_add_f32 v236, v88, v89\n"
"v_mfma_f32_16x16x32_bf16 v[56:59], a[236:239], v[172:175], v[56:59]\n"
"v_cvt_pk_bf16_f32 v88, v88, v89\n"
"v_add_f32 v237, v96, v97\n"
"v_cvt_pk_bf16_f32 v96, v96, v97\n"
"v_mfma_f32_16x16x32_bf16 v[20:23], v[240:243], a[192:195], v[208:211]\n"
"v_add_f32 v238, v104, v105\n"
"v_cvt_pk_bf16_f32 v104, v104, v105\n"
"v_mfma_f32_16x16x32_bf16 v[20:23], v[244:247], a[200:203], v[20:23]\n"
"v_add_f32 v235, v235, v82\n"
"s_waitcnt lgkmcnt(0)\n"
"v_mfma_f32_16x16x32_bf16 v[20:23], v[248:251], a[208:211], v[20:23]\n"
"v_add_f32 v235, v235, v83\n"
"v_cvt_pk_bf16_f32 v81, v82, v83\n"
"v_mfma_f32_16x16x32_bf16 v[20:23], v[252:255], a[216:219], v[20:23]\n"
"v_add_f32 v236, v236, v90\n"
"v_add_f32 v236, v236, v91\n"
"v_cvt_pk_bf16_f32 v89, v90, v91\n"
"v_mfma_f32_16x16x32_bf16 v[28:31], v[240:243], a[196:199], v[212:215]\n"
"s_barrier\n"
"v_mfma_f32_16x16x32_bf16 v[28:31], v[244:247], a[204:207], v[28:31]\n"
"ds_read_b64_tr_b16 a[240:241], v6 offset:0\n"
"ds_read_b64_tr_b16 a[242:243], v6 offset:4096\n"
"s_add_u32 m0, s79, 131072\n"
"v_mfma_f32_16x16x32_bf16 v[28:31], v[248:251], a[212:215], v[28:31]\n"
"s_waitcnt vmcnt(3)\n"
"buffer_load_dwordx4 v232, s[24:27], s77 offen offset:1024 nt lds\n"
"v_mfma_f32_16x16x32_bf16 v[28:31], v[252:255], a[220:223], v[28:31]\n"
"s_add_u32 s75, s75, 8192\n"
"s_and_b32 s75, s75, 0x1ffff\n"
"v_mfma_f32_16x16x32_bf16 v[36:39], v[240:243], v[176:179], v[216:219]\n"
"ds_read_b64_tr_b16 a[244:245], v7 offset:0\n"
"v_add_f32 v237, v237, v98\n"
"v_mfma_f32_16x16x32_bf16 v[36:39], v[244:247], v[184:187], v[36:39]\n"
"ds_read_b64_tr_b16 a[246:247], v7 offset:4096\n"
"v_add_f32 v237, v237, v99\n"
"v_mfma_f32_16x16x32_bf16 v[36:39], v[248:251], v[192:195], v[36:39]\n"
"ds_read_b64_tr_b16 a[248:249], v6 offset:256\n"
"v_cvt_pk_bf16_f32 v97, v98, v99\n"
"v_mfma_f32_16x16x32_bf16 v[36:39], v[252:255], v[200:203], v[36:39]\n"
"ds_read_b64_tr_b16 a[250:251], v6 offset:4352\n"
"v_add_f32 v238, v238, v106\n"
"v_add_f32 v238, v238, v107\n"
"v_mfma_f32_16x16x32_bf16 v[44:47], v[240:243], v[180:183], v[220:223]\n"
"ds_read_b64_tr_b16 a[252:253], v7 offset:256\n"
"v_cvt_pk_bf16_f32 v105, v106, v107\n"
"v_mfma_f32_16x16x32_bf16 v[44:47], v[244:247], v[188:191], v[44:47]\n"
"ds_read_b64_tr_b16 a[254:255], v7 offset:4352\n"
"v_add_f32 v233, v233, v68\n"
"v_mfma_f32_16x16x32_bf16 v[44:47], v[248:251], v[196:199], v[44:47]\n"
"v_add_f32 v233, v233, v69\n"
"v_add_f32 v234, v234, v76\n"
"v_mfma_f32_16x16x32_bf16 v[44:47], v[252:255], v[204:207], v[44:47]\n"
"v_add_f32 v234, v234, v77\n"
"v_add_f32 v235, v235, v84\n"
"v_add_f32 v235, v235, v85\n"
"v_mfma_f32_16x16x32_bf16 v[52:55], v[240:243], v[144:147], v[224:227]\n"
"v_cvt_pk_bf16_f32 v82, v84, v85\n"
"v_add_f32 v236, v236, v92\n"
"v_mfma_f32_16x16x32_bf16 v[52:55], v[244:247], v[152:155], v[52:55]\n"
"v_add_f32 v236, v236, v93\n"
"v_cvt_pk_bf16_f32 v90, v92, v93\n"
"s_add_u32 m0, s79, 24576\n"
"v_mfma_f32_16x16x32_bf16 v[52:55], v[248:251], v[160:163], v[52:55]\n"
"buffer_load_dwordx4 v3, s[44:47], s75 offen nt lds\n"
"v_mfma_f32_16x16x32_bf16 v[52:55], v[252:255], v[168:171], v[52:55]\n"
"v_add_f32 v237, v237, v100\n"
"v_add_f32 v237, v237, v101\n"
"v_mfma_f32_16x16x32_bf16 v[60:63], v[240:243], v[148:151], v[228:231]\n"
"v_cvt_pk_bf16_f32 v98, v100, v101\n"
"v_add_f32 v238, v238, v108\n"
"v_mfma_f32_16x16x32_bf16 v[60:63], v[244:247], v[156:159], v[60:63]\n"
"v_add_f32 v238, v238, v109\n"
"v_cvt_pk_bf16_f32 v106, v108, v109\n"
"v_mfma_f32_16x16x32_bf16 v[60:63], v[248:251], v[164:167], v[60:63]\n"
"v_add_f32 v233, v233, v70\n"
"v_add_f32 v233, v233, v71\n"
"v_add_f32 v234, v234, v78\n"
"v_add_f32 v234, v234, v79\n"
"v_add_f32 v235, v235, v86\n"
"v_add_f32 v235, v235, v87\n"
"v_cvt_pk_bf16_f32 v83, v86, v87\n"
"v_add_f32 v236, v236, v94\n"
"v_add_f32 v236, v236, v95\n"
"v_cvt_pk_bf16_f32 v91, v94, v95\n"
"v_mfma_f32_16x16x32_bf16 v[60:63], v[252:255], v[172:175], v[60:63]\n"
"v_add_f32 v237, v237, v102\n"
"v_add_f32 v237, v237, v103\n"
"v_cvt_pk_bf16_f32 v99, v102, v103\n"
"v_add_f32 v238, v238, v110\n"
"v_add_f32 v238, v238, v111\n"
"v_cvt_pk_bf16_f32 v107, v110, v111\n"
"v_max3_f32 v118, v233, v234, v235\n"
"v_max3_f32 v118, v118, v236, v237\n"
"v_max_f32 v118, v118, v238\n"
"v_cmp_nge_f32 vcc, 0x47800000, v118\n"
"s_nop 0\n"
"s_cbranch_vccnz Ltr11_%=\n"
"Lrt11_%=:\n"
"s_waitcnt lgkmcnt(0)\n"
"v_mfma_f32_16x16x32_bf16 a[0:3], a[240:243], v[64:67], a[0:3]\n"
"v_exp_f32 v16, v16\n"
"v_mfma_f32_16x16x32_bf16 a[4:7], a[240:243], v[72:75], a[4:7]\n"
"v_exp_f32 v17, v17\n"
"v_mfma_f32_16x16x32_bf16 a[64:67], a[240:243], v[80:83], a[64:67]\n"
"v_exp_f32 v24, v24\n"
"v_mfma_f32_16x16x32_bf16 a[68:71], a[240:243], v[88:91], a[68:71]\n"
"v_exp_f32 v25, v25\n"
"v_add_f32 v112, v112, v233\n"
"v_mfma_f32_16x16x32_bf16 a[128:131], a[240:243], v[96:99], a[128:131]\n"
"v_exp_f32 v18, v18\n"
"v_mfma_f32_16x16x32_bf16 a[132:135], a[240:243], v[104:107], a[132:135]\n"
"ds_read_b64_tr_b16 a[240:241], v6 offset:512\n"
"ds_read_b64_tr_b16 a[242:243], v6 offset:4608\n"
"s_add_u32 m0, s79, 24576\n"
"v_mfma_f32_16x16x32_bf16 a[136:139], a[244:247], v[96:99], a[136:139]\n"
"s_waitcnt vmcnt(4)\n"
"buffer_load_dwordx4 v14, s[44:47], s75 offen offset:1024 nt lds\n"
"v_mfma_f32_16x16x32_bf16 a[140:143], a[244:247], v[104:107], a[140:143]\n"
"v_exp_f32 v19, v19\n"
"v_mfma_f32_16x16x32_bf16 a[72:75], a[244:247], v[80:83], a[72:75]\n"
"v_exp_f32 v26, v26\n"
"v_mfma_f32_16x16x32_bf16 a[76:79], a[244:247], v[88:91], a[76:79]\n"
"v_exp_f32 v27, v27\n"
"v_mfma_f32_16x16x32_bf16 a[8:11], a[244:247], v[64:67], a[8:11]\n"
"v_exp_f32 v20, v20\n"
"v_mfma_f32_16x16x32_bf16 a[12:15], a[244:247], v[72:75], a[12:15]\n"
"ds_read_b64_tr_b16 a[244:245], v7 offset:512\n"
"ds_read_b64_tr_b16 a[246:247], v7 offset:4608\n"
"v_add_f32 v113, v113, v234\n"
"v_mfma_f32_16x16x32_bf16 a[16:19], a[248:251], v[64:67], a[16:19]\n"
"v_exp_f32 v21, v21\n"
"v_mfma_f32_16x16x32_bf16 a[20:23], a[248:251], v[72:75], a[20:23]\n"
"v_exp_f32 v28, v28\n"
"v_mfma_f32_16x16x32_bf16 a[80:83], a[248:251], v[80:83], a[80:83]\n"
"v_exp_f32 v29, v29\n"
"v_mfma_f32_16x16x32_bf16 a[84:87], a[248:251], v[88:91], a[84:87]\n"
"v_exp_f32 v22, v22\n"
"v_add_f32 v233, v16, v17\n"
"v_mfma_f32_16x16x32_bf16 a[144:147], a[248:251], v[96:99], a[144:147]\n"
"v_exp_f32 v23, v23\n"
"v_mfma_f32_16x16x32_bf16 a[148:151], a[248:251], v[104:107], a[148:151]\n"
"ds_read_b64_tr_b16 a[248:249], v6 offset:768\n"
"ds_read_b64_tr_b16 a[250:251], v6 offset:4864\n"
"v_mfma_f32_16x16x32_bf16 a[152:155], a[252:255], v[96:99], a[152:155]\n"
"v_exp_f32 v30, v30\n"
"v_mfma_f32_16x16x32_bf16 a[156:159], a[252:255], v[104:107], a[156:159]\n"
"v_exp_f32 v31, v31\n"
"v_cvt_pk_bf16_f32 v16, v16, v17\n"
"s_add_u32 m0, s79, 106496\n"
"v_mfma_f32_16x16x32_bf16 a[88:91], a[252:255], v[80:83], a[88:91]\n"
"s_waitcnt vmcnt(4)\n"
"buffer_load_dwordx4 v3, s[20:23], s75 offen nt lds\n"
"v_mfma_f32_16x16x32_bf16 a[92:95], a[252:255], v[88:91], a[92:95]\n"
"v_add_f32 v234, v24, v25\n"
"v_mfma_f32_16x16x32_bf16 a[24:27], a[252:255], v[64:67], a[24:27]\n"
"v_cvt_pk_bf16_f32 v24, v24, v25\n"
"s_add_u32 s86, s82, 0\n"
"v_mfma_f32_16x16x32_bf16 a[28:31], a[252:255], v[72:75], a[28:31]\n"
"ds_read_b64_tr_b16 a[252:253], v7 offset:768\n"
"ds_read_b64_tr_b16 a[254:255], v7 offset:4864\n"
"s_waitcnt lgkmcnt(2)\n"
"v_mfma_f32_16x16x32_bf16 a[32:35], a[240:243], v[64:67], a[32:35]\n"
"v_exp_f32 v32, v32\n"
"v_mfma_f32_16x16x32_bf16 a[36:39], a[240:243], v[72:75], a[36:39]\n"
"v_exp_f32 v33, v33\n"
"v_mfma_f32_16x16x32_bf16 a[96:99], a[240:243], v[80:83], a[96:99]\n"
"v_exp_f32 v40, v40\n"
"v_mfma_f32_16x16x32_bf16 a[100:103], a[240:243], v[88:91], a[100:103]\n"
"v_exp_f32 v41, v41\n"
"v_add_f32 v233, v233, v18\n"
"v_mfma_f32_16x16x32_bf16 a[160:163], a[240:243], v[96:99], a[160:163]\n"
"s_barrier\n"
"v_mfma_f32_16x16x32_bf16 a[164:167], a[240:243], v[104:107], a[164:167]\n"
"ds_read_b128 a[224:227], v5 offset:16384\n"
"ds_read_b128 a[228:231], v5 offset:16640\n"
"v_mfma_f32_16x16x32_bf16 a[168:171], a[244:247], v[96:99], a[168:171]\n"
"ds_read_b128 a[232:235], v5 offset:16896\n"
"v_add_f32 v233, v233, v19\n"
"v_mfma_f32_16x16x32_bf16 a[172:175], a[244:247], v[104:107], a[172:175]\n"
"ds_read_b128 a[236:239], v5 offset:17152\n"
"v_exp_f32 v48, v48\n"
"v_mfma_f32_16x16x32_bf16 a[104:107], a[244:247], v[80:83], a[104:107]\n"
"ds_read_b128 v[240:243], v5 offset:20480\n"
"s_add_u32 m0, s79, 106496\n"
"v_mfma_f32_16x16x32_bf16 a[108:111], a[244:247], v[88:91], a[108:111]\n"
"s_waitcnt vmcnt(3)\n"
"buffer_load_dwordx4 v14, s[20:23], s75 offen offset:1024 nt lds\n"
"v_mfma_f32_16x16x32_bf16 a[40:43], a[244:247], v[64:67], a[40:43]\n"
"ds_read_b128 v[244:247], v5 offset:20736\n"
"s_add_u32 m0, s81, 0\n"
"v_mfma_f32_16x16x32_bf16 a[44:47], a[244:247], v[72:75], a[44:47]\n"
"buffer_load_dwordx4 v120, s[52:55], s86 offen lds\n"
"s_add_u32 s77, s77, 8192\n"
"s_and_b32 s77, s77, 0x1ffff\n"
"v_mfma_f32_16x16x32_bf16 a[48:51], a[248:251], v[64:67], a[48:51]\n"
"ds_read_b128 v[248:251], v5 offset:20992\n"
"v_mfma_f32_16x16x32_bf16 a[52:55], a[248:251], v[72:75], a[52:55]\n"
"ds_read_b128 v[252:255], v5 offset:21248\n"
"v_exp_f32 v49, v49\n"
"v_mfma_f32_16x16x32_bf16 a[112:115], a[248:251], v[80:83], a[112:115]\n"
"v_exp_f32 v56, v56\n"
"v_mfma_f32_16x16x32_bf16 a[116:119], a[248:251], v[88:91], a[116:119]\n"
"v_exp_f32 v57, v57\n"
"v_cvt_pk_bf16_f32 v17, v18, v19\n"
"v_mfma_f32_16x16x32_bf16 a[176:179], a[248:251], v[96:99], a[176:179]\n"
"v_add_f32 v234, v234, v26\n"
"v_add_f32 v234, v234, v27\n"
"v_mfma_f32_16x16x32_bf16 a[180:183], a[248:251], v[104:107], a[180:183]\n"
"v_cvt_pk_bf16_f32 v25, v26, v27\n"
"s_waitcnt lgkmcnt(2)\n"
"v_mfma_f32_16x16x32_bf16 a[184:187], a[252:255], v[96:99], a[184:187]\n"
"v_exp_f32 v34, v34\n"
"v_mfma_f32_16x16x32_bf16 a[188:191], a[252:255], v[104:107], a[188:191]\n"
"v_exp_f32 v35, v35\n"
"v_cvt_pk_bf16_f32 v18, v20, v21\n"
"s_add_u32 m0, s79, 57344\n"
"v_mfma_f32_16x16x32_bf16 a[120:123], a[252:255], v[80:83], a[120:123]\n"
"buffer_load_dwordx4 v4, s[48:51], s77 offen nt lds\n"
"v_mfma_f32_16x16x32_bf16 a[124:127], a[252:255], v[88:91], a[124:127]\n"
"v_cvt_pk_bf16_f32 v26, v28, v29\n"
"s_add_u32 m0, s81, 896\n"
"v_mfma_f32_16x16x32_bf16 a[56:59], a[252:255], v[64:67], a[56:59]\n"
"buffer_load_dwordx4 v120, s[52:55], s86 offen offset:128 lds\n"
"v_cvt_pk_bf16_f32 v19, v22, v23\n"
"v_mfma_f32_16x16x32_bf16 a[60:63], a[252:255], v[72:75], a[60:63]\n"
"v_exp_f32 v42, v42\n"
"v_mfma_f32_16x16x32_bf16 v[64:67], a[224:227], a[192:195], v[208:211]\n"
"v_exp_f32 v43, v43\n"
"v_mfma_f32_16x16x32_bf16 v[64:67], a[228:231], a[200:203], v[64:67]\n"
"v_exp_f32 v50, v50\n"
"v_mfma_f32_16x16x32_bf16 v[64:67], a[232:235], a[208:211], v[64:67]\n"
"v_exp_f32 v51, v51\n"
"v_mfma_f32_16x16x32_bf16 v[64:67], a[236:239], a[216:219], v[64:67]\n"
"v_exp_f32 v58, v58\n"
"v_cvt_pk_bf16_f32 v27, v30, v31\n"
"v_mfma_f32_16x16x32_bf16 v[72:75], a[224:227], a[196:199], v[212:215]\n"
"v_exp_f32 v59, v59\n"
"v_mfma_f32_16x16x32_bf16 v[72:75], a[228:231], a[204:207], v[72:75]\n"
"v_exp_f32 v36, v36\n"
"v_mfma_f32_16x16x32_bf16 v[72:75], a[232:235], a[212:215], v[72:75]\n"
"v_exp_f32 v37, v37\n"
"v_mfma_f32_16x16x32_bf16 v[72:75], a[236:239], a[220:223], v[72:75]\n"
"v_exp_f32 v44, v44\n"
"v_add_f32 v114, v114, v235\n"
"v_mfma_f32_16x16x32_bf16 v[80:83], a[224:227], v[176:179], v[216:219]\n"
"v_exp_f32 v45, v45\n"
"v_mfma_f32_16x16x32_bf16 v[80:83], a[228:231], v[184:187], v[80:83]\n"
"v_exp_f32 v52, v52\n"
"s_add_u32 m0, s79, 57344\n"
"v_mfma_f32_16x16x32_bf16 v[80:83], a[232:235], v[192:195], v[80:83]\n"
"s_waitcnt vmcnt(6)\n"
"buffer_load_dwordx4 v232, s[48:51], s77 offen offset:1024 nt lds\n"
"v_mfma_f32_16x16x32_bf16 v[80:83], a[236:239], v[200:203], v[80:83]\n"
"v_exp_f32 v53, v53\n"
"s_add_u32 m0, s81, 2048\n"
"v_mfma_f32_16x16x32_bf16 v[88:91], a[224:227], v[180:183], v[220:223]\n"
"buffer_load_dwordx4 v121, s[52:55], s86 offen lds\n"
"v_mfma_f32_16x16x32_bf16 v[88:91], a[228:231], v[188:191], v[88:91]\n"
"v_exp_f32 v60, v60\n"
"v_mfma_f32_16x16x32_bf16 v[88:91], a[232:235], v[196:199], v[88:91]\n"
"v_exp_f32 v61, v61\n"
"v_mfma_f32_16x16x32_bf16 v[88:91], a[236:239], v[204:207], v[88:91]\n"
"v_exp_f32 v38, v38\n"
"v_add_f32 v115, v115, v236\n"
"v_mfma_f32_16x16x32_bf16 v[96:99], a[224:227], v[144:147], v[224:227]\n"
"v_exp_f32 v39, v39\n"
"v_mfma_f32_16x16x32_bf16 v[96:99], a[228:231], v[152:155], v[96:99]\n"
"v_exp_f32 v46, v46\n"
"v_mfma_f32_16x16x32_bf16 v[96:99], a[232:235], v[160:163], v[96:99]\n"
"v_exp_f32 v47, v47\n"
"v_mfma_f32_16x16x32_bf16 v[96:99], a[236:239], v[168:171], v[96:99]\n"
"v_exp_f32 v54, v54\n"
"v_add_f32 v116, v116, v237\n"
"v_mfma_f32_16x16x32_bf16 v[104:107], a[224:227], v[148:151], v[228:231]\n"
"v_exp_f32 v55, v55\n"
"v_mfma_f32_16x16x32_bf16 v[104:107], a[228:231], v[156:159], v[104:107]\n"
"v_exp_f32 v62, v62\n"
"s_add_u32 m0, s79, 139264\n"
"v_mfma_f32_16x16x32_bf16 v[104:107], a[232:235], v[164:167], v[104:107]\n"
"s_waitcnt vmcnt(6)\n"
"buffer_load_dwordx4 v4, s[24:27], s77 offen nt lds\n"
"v_mfma_f32_16x16x32_bf16 v[104:107], a[236:239], v[172:175], v[104:107]\n"
"v_exp_f32 v63, v63\n"
"s_add_u32 m0, s81, 2944\n"
"v_mfma_f32_16x16x32_bf16 v[68:71], v[240:243], a[192:195], v[208:211]\n"
"buffer_load_dwordx4 v121, s[52:55], s86 offen offset:128 lds\n"
"v_add_f32 v117, v117, v238\n"
"v_mfma_f32_16x16x32_bf16 v[68:71], v[244:247], a[200:203], v[68:71]\n"
"s_waitcnt lgkmcnt(0)\n"
"v_mfma_f32_16x16x32_bf16 v[68:71], v[248:251], a[208:211], v[68:71]\n"
"v_add_f32 v235, v32, v33\n"
"v_cvt_pk_bf16_f32 v32, v32, v33\n"
"v_mfma_f32_16x16x32_bf16 v[68:71], v[252:255], a[216:219], v[68:71]\n"
"v_add_f32 v236, v40, v41\n"
"v_cvt_pk_bf16_f32 v40, v40, v41\n"
"v_add_f32 v237, v48, v49\n"
"v_mfma_f32_16x16x32_bf16 v[76:79], v[240:243], a[196:199], v[212:215]\n"
"s_barrier\n"
"v_mfma_f32_16x16x32_bf16 v[76:79], v[244:247], a[204:207], v[76:79]\n"
"ds_read_b64_tr_b16 a[240:241], v6 offset:8192\n"
"ds_read_b64_tr_b16 a[242:243], v6 offset:12288\n"
"v_mfma_f32_16x16x32_bf16 v[76:79], v[248:251], a[212:215], v[76:79]\n"
"ds_read_b64_tr_b16 a[244:245], v7 offset:8192\n"
"v_cvt_pk_bf16_f32 v48, v48, v49\n"
"v_mfma_f32_16x16x32_bf16 v[76:79], v[252:255], a[220:223], v[76:79]\n"
"ds_read_b64_tr_b16 a[246:247], v7 offset:12288\n"
"v_add_f32 v238, v56, v57\n"
"v_cvt_pk_bf16_f32 v56, v56, v57\n"
"v_mfma_f32_16x16x32_bf16 v[84:87], v[240:243], v[176:179], v[216:219]\n"
"ds_read_b64_tr_b16 a[248:249], v6 offset:8448\n"
"v_add_f32 v235, v235, v34\n"
"v_mfma_f32_16x16x32_bf16 v[84:87], v[244:247], v[184:187], v[84:87]\n"
"ds_read_b64_tr_b16 a[250:251], v6 offset:12544\n"
"v_add_f32 v235, v235, v35\n"
"s_add_u32 m0, s79, 139264\n"
"v_mfma_f32_16x16x32_bf16 v[84:87], v[248:251], v[192:195], v[84:87]\n"
"buffer_load_dwordx4 v232, s[24:27], s77 offen offset:1024 nt lds\n"
"v_mfma_f32_16x16x32_bf16 v[84:87], v[252:255], v[200:203], v[84:87]\n"
"s_add_u32 s75, s75, 8192\n"
"s_and_b32 s75, s75, 0x1ffff\n"
"v_mfma_f32_16x16x32_bf16 v[92:95], v[240:243], v[180:183], v[220:223]\n"
"ds_read_b64_tr_b16 a[252:253], v7 offset:8448\n"
"v_cvt_pk_bf16_f32 v33, v34, v35\n"
"s_add_u32 m0, s81, 4096\n"
"v_mfma_f32_16x16x32_bf16 v[92:95], v[244:247], v[188:191], v[92:95]\n"
"buffer_load_dwordx4 v122, s[52:55], s86 offen lds\n"
"v_mfma_f32_16x16x32_bf16 v[92:95], v[248:251], v[196:199], v[92:95]\n"
"ds_read_b64_tr_b16 a[254:255], v7 offset:12544\n"
"v_add_f32 v236, v236, v42\n"
"v_mfma_f32_16x16x32_bf16 v[92:95], v[252:255], v[204:207], v[92:95]\n"
"v_add_f32 v236, v236, v43\n"
"v_cvt_pk_bf16_f32 v41, v42, v43\n"
"v_add_f32 v237, v237, v50\n"
"v_mfma_f32_16x16x32_bf16 v[100:103], v[240:243], v[144:147], v[224:227]\n"
"v_add_f32 v237, v237, v51\n"
"v_cvt_pk_bf16_f32 v49, v50, v51\n"
"v_mfma_f32_16x16x32_bf16 v[100:103], v[244:247], v[152:155], v[100:103]\n"
"v_add_f32 v238, v238, v58\n"
"v_add_f32 v238, v238, v59\n"
"v_mfma_f32_16x16x32_bf16 v[100:103], v[248:251], v[160:163], v[100:103]\n"
"v_cvt_pk_bf16_f32 v57, v58, v59\n"
"v_add_f32 v233, v233, v20\n"
"v_mfma_f32_16x16x32_bf16 v[100:103], v[252:255], v[168:171], v[100:103]\n"
"v_add_f32 v233, v233, v21\n"
"v_add_f32 v234, v234, v28\n"
"v_add_f32 v234, v234, v29\n"
"v_mfma_f32_16x16x32_bf16 v[108:111], v[240:243], v[148:151], v[228:231]\n"
"v_add_f32 v235, v235, v36\n"
"v_add_f32 v235, v235, v37\n"
"v_mfma_f32_16x16x32_bf16 v[108:111], v[244:247], v[156:159], v[108:111]\n"
"v_cvt_pk_bf16_f32 v34, v36, v37\n"
"v_add_f32 v236, v236, v44\n"
"v_mfma_f32_16x16x32_bf16 v[108:111], v[248:251], v[164:167], v[108:111]\n"
"v_add_f32 v236, v236, v45\n"
"v_cvt_pk_bf16_f32 v42, v44, v45\n"
"v_add_f32 v237, v237, v52\n"
"v_add_f32 v237, v237, v53\n"
"v_cvt_pk_bf16_f32 v50, v52, v53\n"
"v_add_f32 v238, v238, v60\n"
"v_add_f32 v238, v238, v61\n"
"v_cvt_pk_bf16_f32 v58, v60, v61\n"
"v_add_f32 v233, v233, v22\n"
"v_add_f32 v233, v233, v23\n"
"v_add_f32 v234, v234, v30\n"
"v_add_f32 v234, v234, v31\n"
"v_add_f32 v235, v235, v38\n"
"v_add_f32 v235, v235, v39\n"
"v_mfma_f32_16x16x32_bf16 v[108:111], v[252:255], v[172:175], v[108:111]\n"
"v_cvt_pk_bf16_f32 v35, v38, v39\n"
"v_add_f32 v236, v236, v46\n"
"v_add_f32 v236, v236, v47\n"
"v_cvt_pk_bf16_f32 v43, v46, v47\n"
"v_add_f32 v237, v237, v54\n"
"v_add_f32 v237, v237, v55\n"
"v_cvt_pk_bf16_f32 v51, v54, v55\n"
"v_add_f32 v238, v238, v62\n"
"v_add_f32 v238, v238, v63\n"
"v_cvt_pk_bf16_f32 v59, v62, v63\n"
"v_max3_f32 v118, v233, v234, v235\n"
"v_max3_f32 v118, v118, v236, v237\n"
"v_max_f32 v118, v118, v238\n"
"v_cmp_nge_f32 vcc, 0x47800000, v118\n"
"s_nop 0\n"
"s_cbranch_vccnz Ltr12_%=\n"
"Lrt12_%=:\n"
"s_add_u32 m0, s79, 32768\n"
"s_waitcnt lgkmcnt(0)\n"
"v_mfma_f32_16x16x32_bf16 a[0:3], a[240:243], v[16:19], a[0:3]\n"
"s_waitcnt vmcnt(9)\n"
"buffer_load_dwordx4 v3, s[44:47], s75 offen nt lds\n"
"v_mfma_f32_16x16x32_bf16 a[4:7], a[240:243], v[24:27], a[4:7]\n"
"v_add_f32 v112, v112, v233\n"
"v_mfma_f32_16x16x32_bf16 a[64:67], a[240:243], v[32:35], a[64:67]\n"
"v_exp_f32 v64, v64\n"
"v_mfma_f32_16x16x32_bf16 a[68:71], a[240:243], v[40:43], a[68:71]\n"
"v_exp_f32 v65, v65\n"
"v_add_f32 v114, v114, v235\n"
"v_mfma_f32_16x16x32_bf16 a[128:131], a[240:243], v[48:51], a[128:131]\n"
"v_exp_f32 v66, v66\n"
"v_mfma_f32_16x16x32_bf16 a[132:135], a[240:243], v[56:59], a[132:135]\n"
"ds_read_b64_tr_b16 a[240:241], v6 offset:8704\n"
"ds_read_b64_tr_b16 a[242:243], v6 offset:12800\n"
"v_mfma_f32_16x16x32_bf16 a[136:139], a[244:247], v[48:51], a[136:139]\n"
"v_exp_f32 v67, v67\n"
"v_mfma_f32_16x16x32_bf16 a[140:143], a[244:247], v[56:59], a[140:143]\n"
"v_exp_f32 v68, v68\n"
"v_add_f32 v233, v64, v65\n"
"v_mfma_f32_16x16x32_bf16 a[72:75], a[244:247], v[32:35], a[72:75]\n"
"v_exp_f32 v69, v69\n"
"v_mfma_f32_16x16x32_bf16 a[76:79], a[244:247], v[40:43], a[76:79]\n"
"v_exp_f32 v70, v70\n"
"v_mfma_f32_16x16x32_bf16 a[8:11], a[244:247], v[16:19], a[8:11]\n"
"v_exp_f32 v71, v71\n"
"v_mfma_f32_16x16x32_bf16 a[12:15], a[244:247], v[24:27], a[12:15]\n"
"ds_read_b64_tr_b16 a[244:245], v7 offset:8704\n"
"ds_read_b64_tr_b16 a[246:247], v7 offset:12800\n"
"v_add_f32 v233, v233, v66\n"
"s_add_u32 m0, s79, 32768\n"
"v_mfma_f32_16x16x32_bf16 a[16:19], a[248:251], v[16:19], a[16:19]\n"
"s_waitcnt vmcnt(6)\n"
"buffer_load_dwordx4 v14, s[44:47], s75 offen offset:1024 nt lds\n"
"v_mfma_f32_16x16x32_bf16 a[20:23], a[248:251], v[24:27], a[20:23]\n"
"v_add_f32 v233, v233, v67\n"
"v_mfma_f32_16x16x32_bf16 a[80:83], a[248:251], v[32:35], a[80:83]\n"
"v_exp_f32 v80, v80\n"
"v_mfma_f32_16x16x32_bf16 a[84:87], a[248:251], v[40:43], a[84:87]\n"
"v_exp_f32 v81, v81\n"
"v_add_f32 v233, v233, v68\n"
"v_mfma_f32_16x16x32_bf16 a[144:147], a[248:251], v[48:51], a[144:147]\n"
"v_exp_f32 v82, v82\n"
"v_mfma_f32_16x16x32_bf16 a[148:151], a[248:251], v[56:59], a[148:151]\n"
"ds_read_b64_tr_b16 a[248:249], v6 offset:8960\n"
"ds_read_b64_tr_b16 a[250:251], v6 offset:13056\n"
"v_mfma_f32_16x16x32_bf16 a[152:155], a[252:255], v[48:51], a[152:155]\n"
"v_exp_f32 v83, v83\n"
"v_mfma_f32_16x16x32_bf16 a[156:159], a[252:255], v[56:59], a[156:159]\n"
"v_exp_f32 v84, v84\n"
"v_add_f32 v233, v233, v69\n"
"v_mfma_f32_16x16x32_bf16 a[88:91], a[252:255], v[32:35], a[88:91]\n"
"v_exp_f32 v85, v85\n"
"v_mfma_f32_16x16x32_bf16 a[92:95], a[252:255], v[40:43], a[92:95]\n"
"v_add_f32 v233, v233, v70\n"
"v_add_f32 v233, v233, v71\n"
"v_mfma_f32_16x16x32_bf16 a[24:27], a[252:255], v[16:19], a[24:27]\n"
"v_add_f32 v235, v80, v81\n"
"s_add_u32 s86, s82, 40960\n"
"v_mfma_f32_16x16x32_bf16 a[28:31], a[252:255], v[24:27], a[28:31]\n"
"ds_read_b64_tr_b16 a[252:253], v7 offset:8960\n"
"ds_read_b64_tr_b16 a[254:255], v7 offset:13056\n"
"s_add_u32 m0, s79, 114688\n"
"s_waitcnt lgkmcnt(2)\n"
"v_mfma_f32_16x16x32_bf16 a[32:35], a[240:243], v[16:19], a[32:35]\n"
"buffer_load_dwordx4 v3, s[20:23], s75 offen nt lds\n"
"v_mfma_f32_16x16x32_bf16 a[36:39], a[240:243], v[24:27], a[36:39]\n"
"v_add_f32 v235, v235, v82\n"
"v_mfma_f32_16x16x32_bf16 a[96:99], a[240:243], v[32:35], a[96:99]\n"
"v_exp_f32 v86, v86\n"
"v_mfma_f32_16x16x32_bf16 a[100:103], a[240:243], v[40:43], a[100:103]\n"
"v_exp_f32 v87, v87\n"
"v_add_f32 v235, v235, v83\n"
"v_mfma_f32_16x16x32_bf16 a[160:163], a[240:243], v[48:51], a[160:163]\n"
"s_barrier\n"
"v_mfma_f32_16x16x32_bf16 a[164:167], a[240:243], v[56:59], a[164:167]\n"
"ds_read_b128 a[224:227], v5 offset:24576\n"
"ds_read_b128 a[228:231], v5 offset:24832\n"
"v_mfma_f32_16x16x32_bf16 a[168:171], a[244:247], v[48:51], a[168:171]\n"
"ds_read_b128 a[232:235], v5 offset:25088\n"
"v_add_f32 v235, v235, v84\n"
"v_mfma_f32_16x16x32_bf16 a[172:175], a[244:247], v[56:59], a[172:175]\n"
"ds_read_b128 a[236:239], v5 offset:25344\n"
"v_exp_f32 v96, v96\n"
"v_mfma_f32_16x16x32_bf16 a[104:107], a[244:247], v[32:35], a[104:107]\n"
"ds_read_b128 v[240:243], v5 offset:28672\n"
"v_mfma_f32_16x16x32_bf16 a[108:111], a[244:247], v[40:43], a[108:111]\n"
"ds_read_b128 v[244:247], v5 offset:28928\n"
"v_exp_f32 v97, v97\n"
"v_mfma_f32_16x16x32_bf16 a[40:43], a[244:247], v[16:19], a[40:43]\n"
"ds_read_b128 v[248:251], v5 offset:29184\n"
"s_add_u32 m0, s81, 10240\n"
"v_mfma_f32_16x16x32_bf16 a[44:47], a[244:247], v[24:27], a[44:47]\n"
"buffer_load_dwordx4 v120, s[52:55], s86 offen lds\n"
"v_exp_f32 v98, v98\n"
"v_mfma_f32_16x16x32_bf16 a[48:51], a[248:251], v[16:19], a[48:51]\n"
"ds_read_b128 v[252:255], v5 offset:29440\n"
"s_add_u32 m0, s79, 114688\n"
"v_mfma_f32_16x16x32_bf16 a[52:55], a[248:251], v[24:27], a[52:55]\n"
"s_waitcnt vmcnt(7)\n"
"buffer_load_dwordx4 v14, s[20:23], s75 offen offset:1024 nt lds\n"
"v_mfma_f32_16x16x32_bf16 a[112:115], a[248:251], v[32:35], a[112:115]\n"
"v_exp_f32 v99, v99\n"
"v_mfma_f32_16x16x32_bf16 a[116:119], a[248:251], v[40:43], a[116:119]\n"
"v_add_f32 v235, v235, v85\n"
"s_add_u32 s77, s77, 8192\n"
"s_and_b32 s77, s77, 0x1ffff\n"
"v_mfma_f32_16x16x32_bf16 a[176:179], a[248:251], v[48:51], a[176:179]\n"
"v_exp_f32 v100, v100\n"
"v_mfma_f32_16x16x32_bf16 a[180:183], a[248:251], v[56:59], a[180:183]\n"
"v_add_f32 v235, v235, v86\n"
"s_waitcnt lgkmcnt(1)\n"
"v_mfma_f32_16x16x32_bf16 a[184:187], a[252:255], v[48:51], a[184:187]\n"
"v_exp_f32 v101, v101\n"
"v_mfma_f32_16x16x32_bf16 a[188:191], a[252:255], v[56:59], a[188:191]\n"
"v_exp_f32 v102, v102\n"
"v_add_f32 v235, v235, v87\n"
"v_mfma_f32_16x16x32_bf16 a[120:123], a[252:255], v[32:35], a[120:123]\n"
"v_exp_f32 v103, v103\n"
"v_mfma_f32_16x16x32_bf16 a[124:127], a[252:255], v[40:43], a[124:127]\n"
"v_add_f32 v116, v116, v237\n"
"v_add_f32 v237, v96, v97\n"
"s_add_u32 m0, s81, 11136\n"
"v_mfma_f32_16x16x32_bf16 a[56:59], a[252:255], v[16:19], a[56:59]\n"
"buffer_load_dwordx4 v120, s[52:55], s86 offen offset:128 lds\n"
"v_add_f32 v237, v237, v98\n"
"v_mfma_f32_16x16x32_bf16 a[60:63], a[252:255], v[24:27], a[60:63]\n"
"v_exp_f32 v72, v72\n"
"v_mfma_f32_16x16x32_bf16 v[16:19], a[224:227], a[192:195], v[208:211]\n"
"v_exp_f32 v73, v73\n"
"v_mfma_f32_16x16x32_bf16 v[16:19], a[228:231], a[200:203], v[16:19]\n"
"v_exp_f32 v74, v74\n"
"s_add_u32 m0, s79, 65536\n"
"v_mfma_f32_16x16x32_bf16 v[16:19], a[232:235], a[208:211], v[16:19]\n"
"s_waitcnt vmcnt(7)\n"
"buffer_load_dwordx4 v4, s[48:51], s77 offen nt lds\n"
"v_mfma_f32_16x16x32_bf16 v[16:19], a[236:239], a[216:219], v[16:19]\n"
"v_exp_f32 v75, v75\n"
"v_mfma_f32_16x16x32_bf16 v[24:27], a[224:227], a[196:199], v[212:215]\n"
"v_exp_f32 v76, v76\n"
"v_mfma_f32_16x16x32_bf16 v[24:27], a[228:231], a[204:207], v[24:27]\n"
"v_add_f32 v237, v237, v99\n"
"v_add_f32 v237, v237, v100\n"
"v_mfma_f32_16x16x32_bf16 v[24:27], a[232:235], a[212:215], v[24:27]\n"
"v_exp_f32 v77, v77\n"
"v_mfma_f32_16x16x32_bf16 v[24:27], a[236:239], a[220:223], v[24:27]\n"
"v_exp_f32 v78, v78\n"
"v_add_f32 v237, v237, v101\n"
"v_mfma_f32_16x16x32_bf16 v[32:35], a[224:227], v[176:179], v[216:219]\n"
"v_exp_f32 v79, v79\n"
"v_mfma_f32_16x16x32_bf16 v[32:35], a[228:231], v[184:187], v[32:35]\n"
"v_add_f32 v237, v237, v102\n"
"v_add_f32 v237, v237, v103\n"
"s_add_u32 m0, s81, 12288\n"
"v_mfma_f32_16x16x32_bf16 v[32:35], a[232:235], v[192:195], v[32:35]\n"
"buffer_load_dwordx4 v121, s[52:55], s86 offen lds\n"
"v_add_f32 v113, v113, v234\n"
"v_mfma_f32_16x16x32_bf16 v[32:35], a[236:239], v[200:203], v[32:35]\n"
"v_add_f32 v234, v72, v73\n"
"v_add_f32 v234, v234, v74\n"
"v_mfma_f32_16x16x32_bf16 v[40:43], a[224:227], v[180:183], v[220:223]\n"
"v_exp_f32 v88, v88\n"
"v_mfma_f32_16x16x32_bf16 v[40:43], a[228:231], v[188:191], v[40:43]\n"
"v_exp_f32 v89, v89\n"
"s_add_u32 m0, s79, 65536\n"
"v_mfma_f32_16x16x32_bf16 v[40:43], a[232:235], v[196:199], v[40:43]\n"
"s_waitcnt vmcnt(6)\n"
"buffer_load_dwordx4 v232, s[48:51], s77 offen offset:1024 nt lds\n"
"v_mfma_f32_16x16x32_bf16 v[40:43], a[236:239], v[204:207], v[40:43]\n"
"v_add_f32 v234, v234, v75\n"
"v_add_f32 v234, v234, v76\n"
"v_mfma_f32_16x16x32_bf16 v[48:51], a[224:227], v[144:147], v[224:227]\n"
"v_exp_f32 v90, v90\n"
"v_mfma_f32_16x16x32_bf16 v[48:51], a[228:231], v[152:155], v[48:51]\n"
"v_exp_f32 v91, v91\n"
"v_mfma_f32_16x16x32_bf16 v[48:51], a[232:235], v[160:163], v[48:51]\n"
"v_exp_f32 v92, v92\n"
"v_mfma_f32_16x16x32_bf16 v[48:51], a[236:239], v[168:171], v[48:51]\n"
"v_exp_f32 v93, v93\n"
"v_add_f32 v234, v234, v77\n"
"v_mfma_f32_16x16x32_bf16 v[56:59], a[224:227], v[148:151], v[228:231]\n"
"v_exp_f32 v94, v94\n"
"v_mfma_f32_16x16x32_bf16 v[56:59], a[228:231], v[156:159], v[56:59]\n"
"v_add_f32 v234, v234, v78\n"
"v_add_f32 v234, v234, v79\n"
"s_add_u32 m0, s81, 13184\n"
"v_mfma_f32_16x16x32_bf16 v[56:59], a[232:235], v[164:167], v[56:59]\n"
"buffer_load_dwordx4 v121, s[52:55], s86 offen offset:128 lds\n"
"v_add_f32 v115, v115, v236\n"
"v_mfma_f32_16x16x32_bf16 v[56:59], a[236:239], v[172:175], v[56:59]\n"
"v_exp_f32 v95, v95\n"
"v_mfma_f32_16x16x32_bf16 v[20:23], v[240:243], a[192:195], v[208:211]\n"
"v_exp_f32 v104, v104\n"
"v_mfma_f32_16x16x32_bf16 v[20:23], v[244:247], a[200:203], v[20:23]\n"
"v_add_f32 v236, v88, v89\n"
"s_add_u32 m0, s79, 147456\n"
"v_mfma_f32_16x16x32_bf16 v[20:23], v[248:251], a[208:211], v[20:23]\n"
"buffer_load_dwordx4 v4, s[24:27], s77 offen nt lds\n"
"s_waitcnt lgkmcnt(0)\n"
"v_mfma_f32_16x16x32_bf16 v[20:23], v[252:255], a[216:219], v[20:23]\n"
"v_exp_f32 v105, v105\n"
"v_mfma_f32_16x16x32_bf16 v[28:31], v[240:243], a[196:199], v[212:215]\n"
"s_barrier\n"
"v_mfma_f32_16x16x32_bf16 v[28:31], v[244:247], a[204:207], v[28:31]\n"
"ds_read_b64_tr_b16 a[240:241], v6 offset:16384\n"
"ds_read_b64_tr_b16 a[242:243], v6 offset:20480\n"
"v_mfma_f32_16x16x32_bf16 v[28:31], v[248:251], a[212:215], v[28:31]\n"
"ds_read_b64_tr_b16 a[244:245], v7 offset:16384\n"
"v_add_f32 v236, v236, v90\n"
"v_mfma_f32_16x16x32_bf16 v[28:31], v[252:255], a[220:223], v[28:31]\n"
"ds_read_b64_tr_b16 a[246:247], v7 offset:20480\n"
"v_add_f32 v236, v236, v91\n"
"v_add_f32 v236, v236, v92\n"
"v_mfma_f32_16x16x32_bf16 v[36:39], v[240:243], v[176:179], v[216:219]\n"
"ds_read_b64_tr_b16 a[248:249], v6 offset:16640\n"
"v_mfma_f32_16x16x32_bf16 v[36:39], v[244:247], v[184:187], v[36:39]\n"
"ds_read_b64_tr_b16 a[250:251], v6 offset:20736\n"
"v_exp_f32 v106, v106\n"
"v_mfma_f32_16x16x32_bf16 v[36:39], v[248:251], v[192:195], v[36:39]\n"
"ds_read_b64_tr_b16 a[252:253], v7 offset:16640\n"
"v_add_f32 v236, v236, v93\n"
"s_add_u32 m0, s81, 14336\n"
"v_mfma_f32_16x16x32_bf16 v[36:39], v[252:255], v[200:203], v[36:39]\n"
"buffer_load_dwordx4 v122, s[52:55], s86 offen lds\n"
"s_add_u32 s86, s83, 0\n"
"v_mfma_f32_16x16x32_bf16 v[44:47], v[240:243], v[180:183], v[220:223]\n"
"ds_read_b64_tr_b16 a[254:255], v7 offset:20736\n"
"v_add_f32 v236, v236, v94\n"
"v_mfma_f32_16x16x32_bf16 v[44:47], v[244:247], v[188:191], v[44:47]\n"
"v_exp_f32 v107, v107\n"
"v_mfma_f32_16x16x32_bf16 v[44:47], v[248:251], v[196:199], v[44:47]\n"
"v_exp_f32 v108, v108\n"
"v_mfma_f32_16x16x32_bf16 v[44:47], v[252:255], v[204:207], v[44:47]\n"
"v_exp_f32 v109, v109\n"
"v_add_f32 v236, v236, v95\n"
"s_add_u32 m0, s79, 147456\n"
"v_mfma_f32_16x16x32_bf16 v[52:55], v[240:243], v[144:147], v[224:227]\n"
"s_waitcnt vmcnt(9)\n"
"buffer_load_dwordx4 v232, s[24:27], s77 offen offset:1024 nt lds\n"
"v_mfma_f32_16x16x32_bf16 v[52:55], v[244:247], v[152:155], v[52:55]\n"
"v_add_f32 v117, v117, v238\n"
"v_mfma_f32_16x16x32_bf16 v[52:55], v[248:251], v[160:163], v[52:55]\n"
"v_exp_f32 v110, v110\n"
"v_mfma_f32_16x16x32_bf16 v[52:55], v[252:255], v[168:171], v[52:55]\n"
"v_add_f32 v238, v104, v105\n"
"s_add_u32 s77, s77, 8192\n"
"s_and_b32 s77, s77, 0x1ffff\n"
"v_mfma_f32_16x16x32_bf16 v[60:63], v[240:243], v[148:151], v[228:231]\n"
"buffer_load_dword v11, v8, s[56:59], 0 offen offset:256 sc1\n"
"v_add_f32 v238, v238, v106\n"
"v_mfma_f32_16x16x32_bf16 v[60:63], v[244:247], v[156:159], v[60:63]\n"
"v_exp_f32 v111, v111\n"
"v_mfma_f32_16x16x32_bf16 v[60:63], v[248:251], v[164:167], v[60:63]\n"
"v_cvt_pk_bf16_f32 v64, v64, v65\n"
"v_cvt_pk_bf16_f32 v72, v72, v73\n"
"v_cvt_pk_bf16_f32 v80, v80, v81\n"
"v_cvt_pk_bf16_f32 v88, v88, v89\n"
"v_cvt_pk_bf16_f32 v96, v96, v97\n"
"v_cvt_pk_bf16_f32 v104, v104, v105\n"
"v_cvt_pk_bf16_f32 v65, v66, v67\n"
"v_cvt_pk_bf16_f32 v73, v74, v75\n"
"v_cvt_pk_bf16_f32 v81, v82, v83\n"
"v_cvt_pk_bf16_f32 v89, v90, v91\n"
"v_cvt_pk_bf16_f32 v97, v98, v99\n"
"v_add_f32 v238, v238, v107\n"
"v_cvt_pk_bf16_f32 v105, v106, v107\n"
"v_cvt_pk_bf16_f32 v66, v68, v69\n"
"v_cvt_pk_bf16_f32 v74, v76, v77\n"
"v_cvt_pk_bf16_f32 v82, v84, v85\n"
"v_cvt_pk_bf16_f32 v90, v92, v93\n"
"v_mfma_f32_16x16x32_bf16 v[60:63], v[252:255], v[172:175], v[60:63]\n"
"v_cvt_pk_bf16_f32 v98, v100, v101\n"
"v_add_f32 v238, v238, v108\n"
"v_add_f32 v238, v238, v109\n"
"v_cvt_pk_bf16_f32 v106, v108, v109\n"
"v_cvt_pk_bf16_f32 v67, v70, v71\n"
"v_cvt_pk_bf16_f32 v75, v78, v79\n"
"v_cvt_pk_bf16_f32 v83, v86, v87\n"
"v_cvt_pk_bf16_f32 v91, v94, v95\n"
"v_cvt_pk_bf16_f32 v99, v102, v103\n"
"v_add_f32 v238, v238, v110\n"
"v_add_f32 v238, v238, v111\n"
"v_cvt_pk_bf16_f32 v107, v110, v111\n"
"v_max3_f32 v118, v233, v234, v235\n"
"v_max3_f32 v118, v118, v236, v237\n"
"v_max_f32 v118, v118, v238\n"
"v_cmp_nge_f32 vcc, 0x47800000, v118\n"
"s_nop 0\n"
"s_cbranch_vccnz Ltr13_%=\n"
"Lrt13_%=:\n"
"s_add_u32 m0, s81, 40960\n"
"s_waitcnt lgkmcnt(0)\n"
"v_mfma_f32_16x16x32_bf16 a[0:3], a[240:243], v[64:67], a[0:3]\n"
"buffer_load_dwordx4 v120, s[52:55], s86 offen lds\n"
"v_mfma_f32_16x16x32_bf16 a[4:7], a[240:243], v[72:75], a[4:7]\n"
"v_exp_f32 v16, v16\n"
"v_mfma_f32_16x16x32_bf16 a[64:67], a[240:243], v[80:83], a[64:67]\n"
"v_exp_f32 v17, v17\n"
"v_mfma_f32_16x16x32_bf16 a[68:71], a[240:243], v[88:91], a[68:71]\n"
"v_exp_f32 v18, v18\n"
"v_add_f32 v112, v112, v233\n"
"v_mfma_f32_16x16x32_bf16 a[128:131], a[240:243], v[96:99], a[128:131]\n"
"v_mfma_f32_16x16x32_bf16 a[132:135], a[240:243], v[104:107], a[132:135]\n"
"ds_read_b64_tr_b16 a[240:241], v6 offset:16896\n"
"s_add_u32 m0, s79, 73728\n"
"s_waitcnt vmcnt(8)\n"
"buffer_load_dwordx4 v4, s[48:51], s77 offen nt lds\n"
"v_mfma_f32_16x16x32_bf16 a[136:139], a[244:247], v[96:99], a[136:139]\n"
"ds_read_b64_tr_b16 a[242:243], v6 offset:20992\n"
"s_add_u32 m0, s81, 41856\n"
"v_mfma_f32_16x16x32_bf16 a[140:143], a[244:247], v[104:107], a[140:143]\n"
"buffer_load_dwordx4 v120, s[52:55], s86 offen offset:128 lds\n"
"v_exp_f32 v19, v19\n"
"v_mfma_f32_16x16x32_bf16 a[72:75], a[244:247], v[80:83], a[72:75]\n"
"v_exp_f32 v20, v20\n"
"v_mfma_f32_16x16x32_bf16 a[76:79], a[244:247], v[88:91], a[76:79]\n"
"v_exp_f32 v21, v21\n"
"v_mfma_f32_16x16x32_bf16 a[8:11], a[244:247], v[64:67], a[8:11]\n"
"v_add_f32 v233, v16, v17\n"
"v_mfma_f32_16x16x32_bf16 a[12:15], a[244:247], v[72:75], a[12:15]\n"
"ds_read_b64_tr_b16 a[244:245], v7 offset:16896\n"
"s_add_u32 m0, s81, 43008\n"
"s_nop 0\n"
"buffer_load_dwordx4 v121, s[52:55], s86 offen lds\n"
"v_mfma_f32_16x16x32_bf16 a[16:19], a[248:251], v[64:67], a[16:19]\n"
"ds_read_b64_tr_b16 a[246:247], v7 offset:20992\n"
"v_add_f32 v233, v233, v18\n"
"v_mfma_f32_16x16x32_bf16 a[20:23], a[248:251], v[72:75], a[20:23]\n"
"v_exp_f32 v22, v22\n"
"v_mfma_f32_16x16x32_bf16 a[80:83], a[248:251], v[80:83], a[80:83]\n"
"v_exp_f32 v32, v32\n"
"v_mfma_f32_16x16x32_bf16 a[84:87], a[248:251], v[88:91], a[84:87]\n"
"v_exp_f32 v23, v23\n"
"v_add_f32 v233, v233, v19\n"
"s_add_u32 m0, s79, 73728\n"
"v_mfma_f32_16x16x32_bf16 a[144:147], a[248:251], v[96:99], a[144:147]\n"
"buffer_load_dwordx4 v232, s[48:51], s77 offen offset:1024 nt lds\n"
"v_mfma_f32_16x16x32_bf16 a[148:151], a[248:251], v[104:107], a[148:151]\n"
"v_mfma_f32_16x16x32_bf16 a[152:155], a[252:255], v[96:99], a[152:155]\n"
"ds_read_b64_tr_b16 a[248:249], v6 offset:17152\n"
"s_add_u32 m0, s81, 43904\n"
"v_mfma_f32_16x16x32_bf16 a[156:159], a[252:255], v[104:107], a[156:159]\n"
"buffer_load_dwordx4 v121, s[52:55], s86 offen offset:128 lds\n"
"v_exp_f32 v33, v33\n"
"v_mfma_f32_16x16x32_bf16 a[88:91], a[252:255], v[80:83], a[88:91]\n"
"ds_read_b64_tr_b16 a[250:251], v6 offset:21248\n"
"v_add_f32 v233, v233, v20\n"
"v_mfma_f32_16x16x32_bf16 a[92:95], a[252:255], v[88:91], a[92:95]\n"
"v_exp_f32 v34, v34\n"
"v_mfma_f32_16x16x32_bf16 a[24:27], a[252:255], v[64:67], a[24:27]\n"
"v_exp_f32 v35, v35\n"
"v_mfma_f32_16x16x32_bf16 a[28:31], a[252:255], v[72:75], a[28:31]\n"
"ds_read_b64_tr_b16 a[252:253], v7 offset:17152\n"
"ds_read_b64_tr_b16 a[254:255], v7 offset:21248\n"
"s_add_u32 m0, s81, 45056\n"
"s_waitcnt lgkmcnt(4)\n"
"v_mfma_f32_16x16x32_bf16 a[32:35], a[240:243], v[64:67], a[32:35]\n"
"buffer_load_dwordx4 v122, s[52:55], s86 offen lds\n"
"v_add_f32 v233, v233, v21\n"
"v_mfma_f32_16x16x32_bf16 a[36:39], a[240:243], v[72:75], a[36:39]\n"
"s_add_u32 s86, s82, 81920\n"
"v_mfma_f32_16x16x32_bf16 a[96:99], a[240:243], v[80:83], a[96:99]\n"
"v_exp_f32 v36, v36\n"
"v_mfma_f32_16x16x32_bf16 a[100:103], a[240:243], v[88:91], a[100:103]\n"
"v_exp_f32 v37, v37\n"
"v_add_f32 v233, v233, v22\n"
"v_mfma_f32_16x16x32_bf16 a[160:163], a[240:243], v[96:99], a[160:163]\n"
"s_barrier\n"
"v_mfma_f32_16x16x32_bf16 a[164:167], a[240:243], v[104:107], a[164:167]\n"
"ds_read_b128 a[224:227], v5 offset:32768\n"
"ds_read_b128 a[228:231], v5 offset:33024\n"
"v_mfma_f32_16x16x32_bf16 a[168:171], a[244:247], v[96:99], a[168:171]\n"
"ds_read_b128 a[232:235], v5 offset:33280\n"
"v_add_f32 v233, v233, v23\n"
"s_add_u32 m0, s79, 155648\n"
"v_mfma_f32_16x16x32_bf16 a[172:175], a[244:247], v[104:107], a[172:175]\n"
"s_waitcnt vmcnt(12)\n"
"buffer_load_dwordx4 v4, s[24:27], s77 offen nt lds\n"
"v_mfma_f32_16x16x32_bf16 a[104:107], a[244:247], v[80:83], a[104:107]\n"
"ds_read_b128 a[236:239], v5 offset:33536\n"
"v_add_f32 v114, v114, v235\n"
"s_add_u32 m0, s81, 20480\n"
"v_mfma_f32_16x16x32_bf16 a[108:111], a[244:247], v[88:91], a[108:111]\n"
"buffer_load_dwordx4 v120, s[52:55], s86 offen lds\n"
"v_mfma_f32_16x16x32_bf16 a[40:43], a[244:247], v[64:67], a[40:43]\n"
"ds_read_b128 v[240:243], v5 offset:36864\n"
"v_mfma_f32_16x16x32_bf16 a[44:47], a[244:247], v[72:75], a[44:47]\n"
"ds_read_b128 v[244:247], v5 offset:37120\n"
"v_exp_f32 v38, v38\n"
"s_waitcnt lgkmcnt(4)\n"
"v_mfma_f32_16x16x32_bf16 a[48:51], a[248:251], v[64:67], a[48:51]\n"
"ds_read_b128 v[248:251], v5 offset:37376\n"
"v_add_f32 v235, v32, v33\n"
"s_add_u32 m0, s81, 21376\n"
"v_mfma_f32_16x16x32_bf16 a[52:55], a[248:251], v[72:75], a[52:55]\n"
"buffer_load_dwordx4 v120, s[52:55], s86 offen offset:128 lds\n"
"v_mfma_f32_16x16x32_bf16 a[112:115], a[248:251], v[80:83], a[112:115]\n"
"ds_read_b128 v[252:255], v5 offset:37632\n"
"v_mfma_f32_16x16x32_bf16 a[116:119], a[248:251], v[88:91], a[116:119]\n"
"v_exp_f32 v48, v48\n"
"v_exp_f32 v39, v39\n"
"v_mfma_f32_16x16x32_bf16 a[176:179], a[248:251], v[96:99], a[176:179]\n"
"v_exp_f32 v49, v49\n"
"v_mfma_f32_16x16x32_bf16 a[180:183], a[248:251], v[104:107], a[180:183]\n"
"v_add_f32 v235, v235, v34\n"
"v_add_f32 v235, v235, v35\n"
"s_add_u32 m0, s79, 155648\n"
"v_mfma_f32_16x16x32_bf16 a[184:187], a[252:255], v[96:99], a[184:187]\n"
"s_waitcnt vmcnt(11)\n"
"buffer_load_dwordx4 v232, s[24:27], s77 offen offset:1024 nt lds\n"
"v_mfma_f32_16x16x32_bf16 a[188:191], a[252:255], v[104:107], a[188:191]\n"
"v_exp_f32 v50, v50\n"
"v_mfma_f32_16x16x32_bf16 a[120:123], a[252:255], v[80:83], a[120:123]\n"
"v_exp_f32 v51, v51\n"
"v_mfma_f32_16x16x32_bf16 a[124:127], a[252:255], v[88:91], a[124:127]\n"
"v_add_f32 v235, v235, v36\n"
"v_add_f32 v235, v235, v37\n"
"s_add_u32 m0, s81, 22528\n"
"v_mfma_f32_16x16x32_bf16 a[56:59], a[252:255], v[64:67], a[56:59]\n"
"buffer_load_dwordx4 v121, s[52:55], s86 offen lds\n"
"v_add_f32 v235, v235, v38\n"
"v_mfma_f32_16x16x32_bf16 a[60:63], a[252:255], v[72:75], a[60:63]\n"
"v_exp_f32 v52, v52\n"
"v_mfma_f32_16x16x32_bf16 v[64:67], a[224:227], a[192:195], v[208:211]\n"
"v_exp_f32 v53, v53\n"
"v_mfma_f32_16x16x32_bf16 v[64:67], a[228:231], a[200:203], v[64:67]\n"
"v_add_f32 v235, v235, v39\n"
"s_add_u32 m0, s81, 23424\n"
"s_waitcnt lgkmcnt(0)\n"
"v_mfma_f32_16x16x32_bf16 v[64:67], a[232:235], a[208:211], v[64:67]\n"
"buffer_load_dwordx4 v121, s[52:55], s86 offen offset:128 lds\n"
"v_add_f32 v116, v116, v237\n"
"v_mfma_f32_16x16x32_bf16 v[64:67], a[236:239], a[216:219], v[64:67]\n"
"v_exp_f32 v54, v54\n"
"v_mfma_f32_16x16x32_bf16 v[72:75], a[224:227], a[196:199], v[212:215]\n"
"v_exp_f32 v24, v24\n"
"v_mfma_f32_16x16x32_bf16 v[72:75], a[228:231], a[204:207], v[72:75]\n"
"v_exp_f32 v55, v55\n"
"v_mfma_f32_16x16x32_bf16 v[72:75], a[232:235], a[212:215], v[72:75]\n"
"v_exp_f32 v25, v25\n"
"v_mfma_f32_16x16x32_bf16 v[72:75], a[236:239], a[220:223], v[72:75]\n"
"v_exp_f32 v26, v26\n"
"v_add_f32 v237, v48, v49\n"
"v_mfma_f32_16x16x32_bf16 v[80:83], a[224:227], v[176:179], v[216:219]\n"
"v_exp_f32 v27, v27\n"
"v_mfma_f32_16x16x32_bf16 v[80:83], a[228:231], v[184:187], v[80:83]\n"
"v_add_f32 v237, v237, v50\n"
"v_add_f32 v237, v237, v51\n"
"s_add_u32 m0, s81, 24576\n"
"v_mfma_f32_16x16x32_bf16 v[80:83], a[232:235], v[192:195], v[80:83]\n"
"buffer_load_dwordx4 v122, s[52:55], s86 offen lds\n"
"v_add_f32 v237, v237, v52\n"
"v_mfma_f32_16x16x32_bf16 v[80:83], a[236:239], v[200:203], v[80:83]\n"
"v_exp_f32 v28, v28\n"
"v_mfma_f32_16x16x32_bf16 v[88:91], a[224:227], v[180:183], v[220:223]\n"
"v_exp_f32 v29, v29\n"
"v_mfma_f32_16x16x32_bf16 v[88:91], a[228:231], v[188:191], v[88:91]\n"
"v_add_f32 v237, v237, v53\n"
"v_add_f32 v237, v237, v54\n"
"v_mfma_f32_16x16x32_bf16 v[88:91], a[232:235], v[196:199], v[88:91]\n"
"v_exp_f32 v30, v30\n"
"v_mfma_f32_16x16x32_bf16 v[88:91], a[236:239], v[204:207], v[88:91]\n"
"v_exp_f32 v31, v31\n"
"v_add_f32 v237, v237, v55\n"
"v_mfma_f32_16x16x32_bf16 v[96:99], a[224:227], v[144:147], v[224:227]\n"
"v_add_f32 v113, v113, v234\n"
"v_cvt_pk_bf16_f32 v16, v16, v17\n"
"v_mfma_f32_16x16x32_bf16 v[96:99], a[228:231], v[152:155], v[96:99]\n"
"v_add_f32 v234, v24, v25\n"
"v_cvt_pk_bf16_f32 v17, v18, v19\n"
"v_mfma_f32_16x16x32_bf16 v[96:99], a[232:235], v[160:163], v[96:99]\n"
"v_exp_f32 v40, v40\n"
"v_mfma_f32_16x16x32_bf16 v[96:99], a[236:239], v[168:171], v[96:99]\n"
"v_exp_f32 v41, v41\n"
"v_add_f32 v234, v234, v26\n"
"v_mfma_f32_16x16x32_bf16 v[104:107], a[224:227], v[148:151], v[228:231]\n"
"v_exp_f32 v42, v42\n"
"v_mfma_f32_16x16x32_bf16 v[104:107], a[228:231], v[156:159], v[104:107]\n"
"v_add_f32 v234, v234, v27\n"
"v_cvt_pk_bf16_f32 v18, v20, v21\n"
"v_mfma_f32_16x16x32_bf16 v[104:107], a[232:235], v[164:167], v[104:107]\n"
"v_exp_f32 v43, v43\n"
"v_mfma_f32_16x16x32_bf16 v[104:107], a[236:239], v[172:175], v[104:107]\n"
"v_add_f32 v234, v234, v28\n"
"v_add_f32 v234, v234, v29\n"
"v_cvt_pk_bf16_f32 v19, v22, v23\n"
"v_mfma_f32_16x16x32_bf16 v[68:71], v[240:243], a[192:195], v[208:211]\n"
"v_exp_f32 v44, v44\n"
"v_mfma_f32_16x16x32_bf16 v[68:71], v[244:247], a[200:203], v[68:71]\n"
"v_exp_f32 v45, v45\n"
"v_mfma_f32_16x16x32_bf16 v[68:71], v[248:251], a[208:211], v[68:71]\n"
"v_exp_f32 v46, v46\n"
"v_mfma_f32_16x16x32_bf16 v[68:71], v[252:255], a[216:219], v[68:71]\n"
"v_add_f32 v115, v115, v236\n"
"v_add_f32 v234, v234, v30\n"
"v_add_f32 v234, v234, v31\n"
"v_mfma_f32_16x16x32_bf16 v[76:79], v[240:243], a[196:199], v[212:215]\n"
"s_barrier\n"
"v_mfma_f32_16x16x32_bf16 v[76:79], v[244:247], a[204:207], v[76:79]\n"
"ds_read_b64_tr_b16 a[240:241], v6 offset:24576\n"
"ds_read_b64_tr_b16 a[242:243], v6 offset:28672\n"
"v_mfma_f32_16x16x32_bf16 v[76:79], v[248:251], a[212:215], v[76:79]\n"
"ds_read_b64_tr_b16 a[244:245], v7 offset:24576\n"
"s_add_u32 s86, s84, 0\n"
"v_mfma_f32_16x16x32_bf16 v[76:79], v[252:255], a[220:223], v[76:79]\n"
"ds_read_b64_tr_b16 a[246:247], v7 offset:28672\n"
"v_exp_f32 v47, v47\n"
"v_mfma_f32_16x16x32_bf16 v[84:87], v[240:243], v[176:179], v[216:219]\n"
"ds_read_b64_tr_b16 a[248:249], v6 offset:24832\n"
"v_cvt_pk_bf16_f32 v24, v24, v25\n"
"v_mfma_f32_16x16x32_bf16 v[84:87], v[244:247], v[184:187], v[84:87]\n"
"ds_read_b64_tr_b16 a[250:251], v6 offset:28928\n"
"v_add_f32 v236, v40, v41\n"
"v_mfma_f32_16x16x32_bf16 v[84:87], v[248:251], v[192:195], v[84:87]\n"
"ds_read_b64_tr_b16 a[252:253], v7 offset:24832\n"
"v_cvt_pk_bf16_f32 v25, v26, v27\n"
"v_mfma_f32_16x16x32_bf16 v[84:87], v[252:255], v[200:203], v[84:87]\n"
"ds_read_b64_tr_b16 a[254:255], v7 offset:28928\n"
"v_exp_f32 v56, v56\n"
"v_mfma_f32_16x16x32_bf16 v[92:95], v[240:243], v[180:183], v[220:223]\n"
"v_exp_f32 v57, v57\n"
"v_mfma_f32_16x16x32_bf16 v[92:95], v[244:247], v[188:191], v[92:95]\n"
"v_add_f32 v236, v236, v42\n"
"v_add_f32 v236, v236, v43\n"
"v_mfma_f32_16x16x32_bf16 v[92:95], v[248:251], v[196:199], v[92:95]\n"
"v_exp_f32 v58, v58\n"
"v_mfma_f32_16x16x32_bf16 v[92:95], v[252:255], v[204:207], v[92:95]\n"
"v_exp_f32 v59, v59\n"
"v_cvt_pk_bf16_f32 v26, v28, v29\n"
"s_add_u32 m0, s81, 51200\n"
"v_mfma_f32_16x16x32_bf16 v[100:103], v[240:243], v[144:147], v[224:227]\n"
"buffer_load_dwordx4 v120, s[52:55], s86 offen lds\n"
"v_add_f32 v236, v236, v44\n"
"v_mfma_f32_16x16x32_bf16 v[100:103], v[244:247], v[152:155], v[100:103]\n"
"v_add_f32 v236, v236, v45\n"
"v_mfma_f32_16x16x32_bf16 v[100:103], v[248:251], v[160:163], v[100:103]\n"
"v_exp_f32 v60, v60\n"
"v_mfma_f32_16x16x32_bf16 v[100:103], v[252:255], v[168:171], v[100:103]\n"
"v_exp_f32 v61, v61\n"
"v_cvt_pk_bf16_f32 v27, v30, v31\n"
"v_mfma_f32_16x16x32_bf16 v[108:111], v[240:243], v[148:151], v[228:231]\n"
"v_exp_f32 v62, v62\n"
"v_add_f32 v236, v236, v46\n"
"v_mfma_f32_16x16x32_bf16 v[108:111], v[244:247], v[156:159], v[108:111]\n"
"v_exp_f32 v63, v63\n"
"v_add_f32 v236, v236, v47\n"
"v_mfma_f32_16x16x32_bf16 v[108:111], v[248:251], v[164:167], v[108:111]\n"
"v_add_f32 v117, v117, v238\n"
"v_cvt_pk_bf16_f32 v32, v32, v33\n"
"v_cvt_pk_bf16_f32 v40, v40, v41\n"
"v_cvt_pk_bf16_f32 v48, v48, v49\n"
"v_add_f32 v238, v56, v57\n"
"v_cvt_pk_bf16_f32 v56, v56, v57\n"
"v_cvt_pk_bf16_f32 v33, v34, v35\n"
"v_cvt_pk_bf16_f32 v41, v42, v43\n"
"v_cvt_pk_bf16_f32 v49, v50, v51\n"
"v_add_f32 v238, v238, v58\n"
"v_add_f32 v238, v238, v59\n"
"v_cvt_pk_bf16_f32 v57, v58, v59\n"
"v_cvt_pk_bf16_f32 v34, v36, v37\n"
"v_cvt_pk_bf16_f32 v42, v44, v45\n"
"v_mfma_f32_16x16x32_bf16 v[108:111], v[252:255], v[172:175], v[108:111]\n"
"v_cvt_pk_bf16_f32 v50, v52, v53\n"
"v_add_f32 v238, v238, v60\n"
"v_add_f32 v238, v238, v61\n"
"v_cvt_pk_bf16_f32 v58, v60, v61\n"
"v_cvt_pk_bf16_f32 v35, v38, v39\n"
"v_cvt_pk_bf16_f32 v43, v46, v47\n"
"v_cvt_pk_bf16_f32 v51, v54, v55\n"
"v_add_f32 v238, v238, v62\n"
"v_add_f32 v238, v238, v63\n"
"v_cvt_pk_bf16_f32 v59, v62, v63\n"
"v_max3_f32 v118, v233, v234, v235\n"
"v_max3_f32 v118, v118, v236, v237\n"
"v_max_f32 v118, v118, v238\n"
"v_cmp_nge_f32 vcc, 0x47800000, v118\n"
"s_nop 0\n"
"s_cbranch_vccnz Ltr14_%=\n"
"Lrt14_%=:\n"
"s_add_u32 m0, s81, 52096\n"
"s_waitcnt lgkmcnt(0)\n"
"v_mfma_f32_16x16x32_bf16 a[0:3], a[240:243], v[16:19], a[0:3]\n"
"buffer_load_dwordx4 v120, s[52:55], s86 offen offset:128 lds\n"
"v_mfma_f32_16x16x32_bf16 a[4:7], a[240:243], v[24:27], a[4:7]\n"
"v_exp_f32 v64, v64\n"
"v_mfma_f32_16x16x32_bf16 a[64:67], a[240:243], v[32:35], a[64:67]\n"
"v_exp_f32 v65, v65\n"
"v_mfma_f32_16x16x32_bf16 a[68:71], a[240:243], v[40:43], a[68:71]\n"
"v_exp_f32 v66, v66\n"
"v_add_f32 v112, v112, v233\n"
"v_mfma_f32_16x16x32_bf16 a[128:131], a[240:243], v[48:51], a[128:131]\n"
"v_mfma_f32_16x16x32_bf16 a[132:135], a[240:243], v[56:59], a[132:135]\n"
"ds_read_b64_tr_b16 a[240:241], v6 offset:25088\n"
"s_add_u32 m0, s81, 53248\n"
"s_nop 0\n"
"buffer_load_dwordx4 v121, s[52:55], s86 offen lds\n"
"v_mfma_f32_16x16x32_bf16 a[136:139], a[244:247], v[48:51], a[136:139]\n"
"ds_read_b64_tr_b16 a[242:243], v6 offset:29184\n"
"v_mfma_f32_16x16x32_bf16 a[140:143], a[244:247], v[56:59], a[140:143]\n"
"v_exp_f32 v67, v67\n"
"v_exp_f32 v68, v68\n"
"v_mfma_f32_16x16x32_bf16 a[72:75], a[244:247], v[32:35], a[72:75]\n"
"v_exp_f32 v69, v69\n"
"v_mfma_f32_16x16x32_bf16 a[76:79], a[244:247], v[40:43], a[76:79]\n"
"v_exp_f32 v70, v70\n"
"v_mfma_f32_16x16x32_bf16 a[8:11], a[244:247], v[16:19], a[8:11]\n"
"v_add_f32 v233, v64, v65\n"
"v_mfma_f32_16x16x32_bf16 a[12:15], a[244:247], v[24:27], a[12:15]\n"
"ds_read_b64_tr_b16 a[244:245], v7 offset:25088\n"
"s_add_u32 m0, s81, 54144\n"
"s_nop 0\n"
"buffer_load_dwordx4 v121, s[52:55], s86 offen offset:128 lds\n"
"v_mfma_f32_16x16x32_bf16 a[16:19], a[248:251], v[16:19], a[16:19]\n"
"ds_read_b64_tr_b16 a[246:247], v7 offset:29184\n"
"v_add_f32 v233, v233, v66\n"
"v_mfma_f32_16x16x32_bf16 a[20:23], a[248:251], v[24:27], a[20:23]\n"
"v_exp_f32 v71, v71\n"
"v_mfma_f32_16x16x32_bf16 a[80:83], a[248:251], v[32:35], a[80:83]\n"
"v_exp_f32 v80, v80\n"
"v_mfma_f32_16x16x32_bf16 a[84:87], a[248:251], v[40:43], a[84:87]\n"
"v_exp_f32 v81, v81\n"
"v_add_f32 v233, v233, v67\n"
"v_mfma_f32_16x16x32_bf16 a[144:147], a[248:251], v[48:51], a[144:147]\n"
"v_mfma_f32_16x16x32_bf16 a[148:151], a[248:251], v[56:59], a[148:151]\n"
"ds_read_b64_tr_b16 a[248:249], v6 offset:25344\n"
"s_add_u32 m0, s81, 55296\n"
"s_nop 0\n"
"buffer_load_dwordx4 v122, s[52:55], s86 offen lds\n"
"v_mfma_f32_16x16x32_bf16 a[152:155], a[252:255], v[48:51], a[152:155]\n"
"ds_read_b64_tr_b16 a[250:251], v6 offset:29440\n"
"v_add_f32 v233, v233, v68\n"
"v_mfma_f32_16x16x32_bf16 a[156:159], a[252:255], v[56:59], a[156:159]\n"
"v_exp_f32 v82, v82\n"
"s_add_u32 s86, s82, 122880\n"
"v_mfma_f32_16x16x32_bf16 a[88:91], a[252:255], v[32:35], a[88:91]\n"
"v_exp_f32 v83, v83\n"
"v_mfma_f32_16x16x32_bf16 a[92:95], a[252:255], v[40:43], a[92:95]\n"
"v_add_f32 v233, v233, v69\n"
"v_add_f32 v233, v233, v70\n"
"v_mfma_f32_16x16x32_bf16 a[24:27], a[252:255], v[16:19], a[24:27]\n"
"v_exp_f32 v84, v84\n"
"v_mfma_f32_16x16x32_bf16 a[28:31], a[252:255], v[24:27], a[28:31]\n"
"ds_read_b64_tr_b16 a[252:253], v7 offset:25344\n"
"ds_read_b64_tr_b16 a[254:255], v7 offset:29440\n"
"s_waitcnt lgkmcnt(3)\n"
"v_mfma_f32_16x16x32_bf16 a[32:35], a[240:243], v[16:19], a[32:35]\n"
"v_exp_f32 v85, v85\n"
"v_mfma_f32_16x16x32_bf16 a[36:39], a[240:243], v[24:27], a[36:39]\n"
"v_add_f32 v114, v114, v235\n"
"v_add_f32 v233, v233, v71\n"
"v_mfma_f32_16x16x32_bf16 a[96:99], a[240:243], v[32:35], a[96:99]\n"
"v_exp_f32 v86, v86\n"
"v_mfma_f32_16x16x32_bf16 a[100:103], a[240:243], v[40:43], a[100:103]\n"
"v_exp_f32 v87, v87\n"
"v_add_f32 v235, v80, v81\n"
"v_mfma_f32_16x16x32_bf16 a[160:163], a[240:243], v[48:51], a[160:163]\n"
"s_waitcnt vmcnt(8)\n"
"s_barrier\n"
"v_mfma_f32_16x16x32_bf16 a[164:167], a[240:243], v[56:59], a[164:167]\n"
"ds_read_b64_tr_b16 a[240:241], v6 offset:32768\n"
"ds_read_b64_tr_b16 a[242:243], v6 offset:36864\n"
"s_add_u32 m0, s81, 30720\n"
"v_mfma_f32_16x16x32_bf16 a[168:171], a[244:247], v[48:51], a[168:171]\n"
"buffer_load_dwordx4 v120, s[52:55], s86 offen lds\n"
"v_add_f32 v235, v235, v82\n"
"v_mfma_f32_16x16x32_bf16 a[172:175], a[244:247], v[56:59], a[172:175]\n"
"v_exp_f32 v96, v96\n"
"v_mfma_f32_16x16x32_bf16 a[104:107], a[244:247], v[32:35], a[104:107]\n"
"v_exp_f32 v97, v97\n"
"v_mfma_f32_16x16x32_bf16 a[108:111], a[244:247], v[40:43], a[108:111]\n"
"v_add_f32 v235, v235, v83\n"
"v_add_f32 v235, v235, v84\n"
"v_mfma_f32_16x16x32_bf16 a[40:43], a[244:247], v[16:19], a[40:43]\n"
"v_exp_f32 v98, v98\n"
"v_mfma_f32_16x16x32_bf16 a[44:47], a[244:247], v[24:27], a[44:47]\n"
"ds_read_b64_tr_b16 a[244:245], v7 offset:32768\n"
"ds_read_b64_tr_b16 a[246:247], v7 offset:36864\n"
"s_add_u32 m0, s81, 31616\n"
"s_waitcnt lgkmcnt(2)\n"
"v_mfma_f32_16x16x32_bf16 a[48:51], a[248:251], v[16:19], a[48:51]\n"
"buffer_load_dwordx4 v120, s[52:55], s86 offen offset:128 lds\n"
"v_mfma_f32_16x16x32_bf16 a[52:55], a[248:251], v[24:27], a[52:55]\n"
"v_exp_f32 v99, v99\n"
"v_mfma_f32_16x16x32_bf16 a[112:115], a[248:251], v[32:35], a[112:115]\n"
"v_exp_f32 v100, v100\n"
"v_mfma_f32_16x16x32_bf16 a[116:119], a[248:251], v[40:43], a[116:119]\n"
"v_exp_f32 v101, v101\n"
"v_add_f32 v235, v235, v85\n"
"v_mfma_f32_16x16x32_bf16 a[176:179], a[248:251], v[48:51], a[176:179]\n"
"v_add_f32 v235, v235, v86\n"
"v_add_f32 v235, v235, v87\n"
"v_mfma_f32_16x16x32_bf16 a[180:183], a[248:251], v[56:59], a[180:183]\n"
"ds_read_b64_tr_b16 a[248:249], v6 offset:33024\n"
"ds_read_b64_tr_b16 a[250:251], v6 offset:37120\n"
"s_add_u32 m0, s81, 32768\n"
"v_mfma_f32_16x16x32_bf16 a[184:187], a[252:255], v[48:51], a[184:187]\n"
"buffer_load_dwordx4 v121, s[52:55], s86 offen lds\n"
"v_add_f32 v116, v116, v237\n"
"v_mfma_f32_16x16x32_bf16 a[188:191], a[252:255], v[56:59], a[188:191]\n"
"v_exp_f32 v102, v102\n"
"v_mfma_f32_16x16x32_bf16 a[120:123], a[252:255], v[32:35], a[120:123]\n"
"v_exp_f32 v72, v72\n"
"v_exp_f32 v73, v73\n"
"v_exp_f32 v88, v88\n"
"v_exp_f32 v89, v89\n"
"v_exp_f32 v104, v104\n"
"v_exp_f32 v105, v105\n"
"v_exp_f32 v74, v74\n"
"v_exp_f32 v75, v75\n"
"v_exp_f32 v90, v90\n"
"v_exp_f32 v91, v91\n"
"v_exp_f32 v106, v106\n"
"v_exp_f32 v107, v107\n"
"v_mfma_f32_16x16x32_bf16 a[124:127], a[252:255], v[40:43], a[124:127]\n"
"v_exp_f32 v76, v76\n"
"v_exp_f32 v77, v77\n"
"v_exp_f32 v92, v92\n"
"v_exp_f32 v93, v93\n"
"v_exp_f32 v108, v108\n"
"v_exp_f32 v109, v109\n"
"v_exp_f32 v78, v78\n"
"v_exp_f32 v79, v79\n"
"v_exp_f32 v94, v94\n"
"v_exp_f32 v95, v95\n"
"v_exp_f32 v103, v103\n"
"v_exp_f32 v110, v110\n"
"v_exp_f32 v111, v111\n"
"v_mfma_f32_16x16x32_bf16 a[56:59], a[252:255], v[16:19], a[56:59]\n"
"v_add_f32 v113, v113, v234\n"
"v_add_f32 v115, v115, v236\n"
"v_add_f32 v117, v117, v238\n"
"v_cvt_pk_bf16_f32 v64, v64, v65\n"
"v_add_f32 v234, v72, v73\n"
"v_cvt_pk_bf16_f32 v72, v72, v73\n"
"v_cvt_pk_bf16_f32 v80, v80, v81\n"
"v_add_f32 v236, v88, v89\n"
"v_cvt_pk_bf16_f32 v88, v88, v89\n"
"v_add_f32 v237, v96, v97\n"
"v_cvt_pk_bf16_f32 v96, v96, v97\n"
"v_add_f32 v238, v104, v105\n"
"v_cvt_pk_bf16_f32 v104, v104, v105\n"
"v_cvt_pk_bf16_f32 v65, v66, v67\n"
"v_add_f32 v234, v234, v74\n"
"v_add_f32 v234, v234, v75\n"
"v_cvt_pk_bf16_f32 v73, v74, v75\n"
"v_cvt_pk_bf16_f32 v81, v82, v83\n"
"v_add_f32 v236, v236, v90\n"
"v_add_f32 v236, v236, v91\n"
"v_cvt_pk_bf16_f32 v89, v90, v91\n"
"v_add_f32 v237, v237, v98\n"
"v_add_f32 v237, v237, v99\n"
"v_cvt_pk_bf16_f32 v97, v98, v99\n"
"v_add_f32 v238, v238, v106\n"
"v_add_f32 v238, v238, v107\n"
"v_cvt_pk_bf16_f32 v105, v106, v107\n"
"v_cvt_pk_bf16_f32 v66, v68, v69\n"
"v_add_f32 v234, v234, v76\n"
"v_add_f32 v234, v234, v77\n"
"v_mfma_f32_16x16x32_bf16 a[60:63], a[252:255], v[24:27], a[60:63]\n"
"v_cvt_pk_bf16_f32 v74, v76, v77\n"
"v_cvt_pk_bf16_f32 v82, v84, v85\n"
"v_add_f32 v236, v236, v92\n"
"v_add_f32 v236, v236, v93\n"
"v_cvt_pk_bf16_f32 v90, v92, v93\n"
"v_add_f32 v237, v237, v100\n"
"v_add_f32 v237, v237, v101\n"
"v_cvt_pk_bf16_f32 v98, v100, v101\n"
"v_add_f32 v238, v238, v108\n"
"v_add_f32 v238, v238, v109\n"
"v_cvt_pk_bf16_f32 v106, v108, v109\n"
"v_cvt_pk_bf16_f32 v67, v70, v71\n"
"v_add_f32 v234, v234, v78\n"
"v_add_f32 v234, v234, v79\n"
"v_cvt_pk_bf16_f32 v75, v78, v79\n"
"v_cvt_pk_bf16_f32 v83, v86, v87\n"
"v_add_f32 v236, v236, v94\n"
"v_add_f32 v236, v236, v95\n"
"v_cvt_pk_bf16_f32 v91, v94, v95\n"
"v_add_f32 v237, v237, v102\n"
"v_add_f32 v237, v237, v103\n"
"v_cvt_pk_bf16_f32 v99, v102, v103\n"
"v_add_f32 v238, v238, v110\n"
"v_add_f32 v238, v238, v111\n"
"v_cvt_pk_bf16_f32 v107, v110, v111\n"
"v_max3_f32 v118, v233, v234, v235\n"
"v_max3_f32 v118, v118, v236, v237\n"
"v_max_f32 v118, v118, v238\n"
"v_cmp_nge_f32 vcc, 0x47800000, v118\n"
"s_nop 0\n"
"s_cbranch_vccnz Ltr15_%=\n"
"Lrt15_%=:\n"
"v_mfma_f32_16x16x32_bf16 a[0:3], a[240:243], v[64:67], a[0:3]\n"
"ds_read_b64_tr_b16 a[252:253], v7 offset:33024\n"
"v_add_f32 v112, v112, v233\n"
"s_add_u32 m0, s81, 33664\n"
"v_mfma_f32_16x16x32_bf16 a[4:7], a[240:243], v[72:75], a[4:7]\n"
"buffer_load_dwordx4 v121, s[52:55], s86 offen offset:128 lds\n"
"v_mfma_f32_16x16x32_bf16 a[64:67], a[240:243], v[80:83], a[64:67]\n"
"ds_read_b64_tr_b16 a[254:255], v7 offset:37120\n"
"v_add_f32 v114, v114, v235\n"
"v_mfma_f32_16x16x32_bf16 a[68:71], a[240:243], v[88:91], a[68:71]\n"
"v_add_f32 v116, v116, v237\n"
"v_mov_b32 v131, v112\n"
"v_mov_b32 v133, v114\n"
"v_mfma_f32_16x16x32_bf16 a[128:131], a[240:243], v[96:99], a[128:131]\n"
"v_permlane32_swap_b32 v131, v112\n"
"v_mfma_f32_16x16x32_bf16 a[132:135], a[240:243], v[104:107], a[132:135]\n"
"ds_read_b64_tr_b16 a[240:241], v6 offset:33280\n"
"ds_read_b64_tr_b16 a[242:243], v6 offset:37376\n"
"s_add_u32 m0, s81, 34816\n"
"s_waitcnt lgkmcnt(4)\n"
"v_mfma_f32_16x16x32_bf16 a[136:139], a[244:247], v[96:99], a[136:139]\n"
"buffer_load_dwordx4 v122, s[52:55], s86 offen lds\n"
"v_add_f32 v112, v112, v131\n"
"v_mfma_f32_16x16x32_bf16 a[140:143], a[244:247], v[104:107], a[140:143]\n"
"v_mov_b32 v131, v112\n"
"v_permlane32_swap_b32 v133, v114\n"
"v_mfma_f32_16x16x32_bf16 a[72:75], a[244:247], v[80:83], a[72:75]\n"
"v_permlane16_swap_b32 v131, v112\n"
"v_add_f32 v112, v112, v131\n"
"v_mfma_f32_16x16x32_bf16 a[76:79], a[244:247], v[88:91], a[76:79]\n"
"v_add_f32 v114, v114, v133\n"
"v_mov_b32 v133, v114\n"
"v_mfma_f32_16x16x32_bf16 a[8:11], a[244:247], v[64:67], a[8:11]\n"
"v_rcp_f32 v131, v112\n"
"v_mfma_f32_16x16x32_bf16 a[12:15], a[244:247], v[72:75], a[12:15]\n"
"ds_read_b64_tr_b16 a[244:245], v7 offset:33280\n"
"ds_read_b64_tr_b16 a[246:247], v7 offset:37376\n"
"v_mov_b32 v135, v116\n"
"v_mfma_f32_16x16x32_bf16 a[16:19], a[248:251], v[64:67], a[16:19]\n"
"v_add_f32 v113, v113, v234\n"
"v_mov_b32 v132, v113\n"
"v_mfma_f32_16x16x32_bf16 a[20:23], a[248:251], v[72:75], a[20:23]\n"
"v_permlane16_swap_b32 v133, v114\n"
"v_add_f32 v114, v114, v133\n"
"v_mfma_f32_16x16x32_bf16 a[80:83], a[248:251], v[80:83], a[80:83]\n"
"v_rcp_f32 v133, v114\n"
"v_mfma_f32_16x16x32_bf16 a[84:87], a[248:251], v[88:91], a[84:87]\n"
"v_permlane32_swap_b32 v135, v116\n"
"v_add_f32 v116, v116, v135\n"
"v_mov_b32 v135, v116\n"
"v_mfma_f32_16x16x32_bf16 a[144:147], a[248:251], v[96:99], a[144:147]\n"
"v_accvgpr_read_b32 v16, a0\n"
"v_mfma_f32_16x16x32_bf16 a[148:151], a[248:251], v[104:107], a[148:151]\n"
"ds_read_b64_tr_b16 a[248:249], v6 offset:33536\n"
"ds_read_b64_tr_b16 a[250:251], v6 offset:37632\n"
"s_waitcnt lgkmcnt(2)\n"
"v_mfma_f32_16x16x32_bf16 a[152:155], a[252:255], v[96:99], a[152:155]\n"
"v_mul_f32 v16, v16, v131\n"
"v_accvgpr_read_b32 v17, a1\n"
"v_mfma_f32_16x16x32_bf16 a[156:159], a[252:255], v[104:107], a[156:159]\n"
"v_mul_f32 v17, v17, v131\n"
"v_cvt_pk_bf16_f32 v144, v16, v17\n"
"v_accvgpr_read_b32 v18, a2\n"
"v_mfma_f32_16x16x32_bf16 a[88:91], a[252:255], v[80:83], a[88:91]\n"
"s_barrier\n"
"v_mfma_f32_16x16x32_bf16 a[92:95], a[252:255], v[88:91], a[92:95]\n"
"ds_read_b64_tr_b16 v[40:41], v127 offset:10240\n"
"ds_read_b64_tr_b16 v[42:43], v127 offset:15360\n"
"v_mfma_f32_16x16x32_bf16 a[24:27], a[252:255], v[64:67], a[24:27]\n"
"ds_read_b64_tr_b16 v[44:45], v127 offset:10248\n"
"v_mul_f32 v18, v18, v131\n"
"v_mfma_f32_16x16x32_bf16 a[28:31], a[252:255], v[72:75], a[28:31]\n"
"ds_read_b64_tr_b16 v[46:47], v127 offset:15368\n"
"v_accvgpr_read_b32 v19, a3\n"
"v_mul_f32 v19, v19, v131\n"
"v_mfma_f32_16x16x32_bf16 a[32:35], a[240:243], v[64:67], a[32:35]\n"
"ds_read_b64_tr_b16 v[32:33], v127 offset:0\n"
"v_cvt_pk_bf16_f32 v145, v18, v19\n"
"v_mfma_f32_16x16x32_bf16 a[36:39], a[240:243], v[72:75], a[36:39]\n"
"ds_read_b64_tr_b16 v[34:35], v127 offset:5120\n"
"v_accvgpr_read_b32 v20, a8\n"
"v_mfma_f32_16x16x32_bf16 a[96:99], a[240:243], v[80:83], a[96:99]\n"
"ds_read_b64_tr_b16 v[36:37], v127 offset:8\n"
"v_mul_f32 v20, v20, v131\n"
"v_mfma_f32_16x16x32_bf16 a[100:103], a[240:243], v[88:91], a[100:103]\n"
"ds_read_b64_tr_b16 v[38:39], v127 offset:5128\n"
"v_accvgpr_read_b32 v21, a9\n"
"v_mul_f32 v21, v21, v131\n"
"v_mfma_f32_16x16x32_bf16 a[160:163], a[240:243], v[96:99], a[160:163]\n"
"ds_read_b64_tr_b16 a[252:253], v7 offset:33536\n"
"v_cvt_pk_bf16_f32 v146, v20, v21\n"
"v_mfma_f32_16x16x32_bf16 a[164:167], a[240:243], v[104:107], a[164:167]\n"
"ds_read_b64_tr_b16 a[254:255], v7 offset:37632\n"
"v_accvgpr_read_b32 v22, a10\n"
"v_mfma_f32_16x16x32_bf16 a[168:171], a[244:247], v[96:99], a[168:171]\n"
"ds_read_b64_tr_b16 v[240:241], v123 offset:0\n"
"v_mul_f32 v22, v22, v131\n"
"v_mfma_f32_16x16x32_bf16 a[172:175], a[244:247], v[104:107], a[172:175]\n"
"ds_read_b64_tr_b16 v[242:243], v123 offset:5120\n"
"v_accvgpr_read_b32 v23, a11\n"
"v_mul_f32 v23, v23, v131\n"
"v_mfma_f32_16x16x32_bf16 a[104:107], a[244:247], v[80:83], a[104:107]\n"
"ds_read_b64_tr_b16 v[244:245], v123 offset:8\n"
"v_add_f32 v115, v115, v236\n"
"v_mfma_f32_16x16x32_bf16 a[108:111], a[244:247], v[88:91], a[108:111]\n"
"ds_read_b64_tr_b16 v[246:247], v123 offset:5128\n"
"v_cvt_pk_bf16_f32 v147, v22, v23\n"
"v_mfma_f32_16x16x32_bf16 a[40:43], a[244:247], v[64:67], a[40:43]\n"
"buffer_store_dwordx4 v[144:147], v13, s[56:59], s76 offen sc1\n"
"v_permlane32_swap_b32 v132, v113\n"
"v_mfma_f32_16x16x32_bf16 a[44:47], a[244:247], v[72:75], a[44:47]\n"
"ds_read_b64_tr_b16 v[248:249], v124 offset:0\n"
"v_add_f32 v113, v113, v132\n"
"s_waitcnt lgkmcnt(5)\n"
"v_mfma_f32_16x16x32_bf16 a[48:51], a[248:251], v[64:67], a[48:51]\n"
"ds_read_b64_tr_b16 v[250:251], v124 offset:5120\n"
"v_mov_b32 v132, v113\n"
"v_mfma_f32_16x16x32_bf16 a[52:55], a[248:251], v[72:75], a[52:55]\n"
"ds_read_b64_tr_b16 v[252:253], v124 offset:8\n"
"v_mov_b32 v134, v115\n"
"v_mfma_f32_16x16x32_bf16 a[112:115], a[248:251], v[80:83], a[112:115]\n"
"ds_read_b64_tr_b16 v[254:255], v124 offset:5128\n"
"v_permlane16_swap_b32 v135, v116\n"
"v_mfma_f32_16x16x32_bf16 a[116:119], a[248:251], v[88:91], a[116:119]\n"
"ds_read_b64_tr_b16 a[240:241], v123 offset:10240\n"
"v_add_f32 v116, v116, v135\n"
"v_accvgpr_read_b32 v24, a64\n"
"v_mfma_f32_16x16x32_bf16 a[176:179], a[248:251], v[96:99], a[176:179]\n"
"ds_read_b64_tr_b16 a[242:243], v123 offset:15360\n"
"v_mfma_f32_16x16x32_bf16 a[180:183], a[248:251], v[104:107], a[180:183]\n"
"ds_read_b64_tr_b16 a[244:245], v124 offset:10240\n"
"v_rcp_f32 v135, v116\n"
"v_mfma_f32_16x16x32_bf16 a[184:187], a[252:255], v[96:99], a[184:187]\n"
"ds_read_b64_tr_b16 a[246:247], v124 offset:15360\n"
"v_add_f32 v117, v117, v238\n"
"v_permlane16_swap_b32 v132, v113\n"
"v_add_f32 v113, v113, v132\n"
"v_permlane32_swap_b32 v134, v115\n"
"v_add_f32 v115, v115, v134\n"
"v_mfma_f32_16x16x32_bf16 a[188:191], a[252:255], v[104:107], a[188:191]\n"
"ds_read_b64_tr_b16 a[248:249], v123 offset:10248\n"
"v_mov_b32 v134, v115\n"
"v_mov_b32 v136, v117\n"
"v_mul_f32 v24, v24, v133\n"
"v_accvgpr_read_b32 v25, a65\n"
"v_mul_f32 v25, v25, v133\n"
"v_mfma_f32_16x16x32_bf16 a[120:123], a[252:255], v[80:83], a[120:123]\n"
"ds_read_b64_tr_b16 a[250:251], v123 offset:15368\n"
"v_rcp_f32 v132, v113\n"
"v_permlane16_swap_b32 v134, v115\n"
"v_add_f32 v115, v115, v134\n"
"v_mfma_f32_16x16x32_bf16 a[124:127], a[252:255], v[88:91], a[124:127]\n"
"v_permlane32_swap_b32 v136, v117\n"
"v_add_f32 v117, v117, v136\n"
"v_mov_b32 v136, v117\n"
"v_cvt_pk_bf16_f32 v148, v24, v25\n"
"v_accvgpr_read_b32 v26, a66\n"
"v_mul_f32 v26, v26, v133\n"
"v_mfma_f32_16x16x32_bf16 a[56:59], a[252:255], v[64:67], a[56:59]\n"
"v_rcp_f32 v134, v115\n"
"v_permlane16_swap_b32 v136, v117\n"
"v_add_f32 v117, v117, v136\n"
"v_accvgpr_read_b32 v27, a67\n"
"v_mul_f32 v27, v27, v133\n"
"v_cvt_pk_bf16_f32 v149, v26, v27\n"
"v_accvgpr_read_b32 v28, a72\n"
"v_mul_f32 v28, v28, v133\n"
"v_accvgpr_read_b32 v29, a73\n"
"v_mul_f32 v29, v29, v133\n"
"v_cvt_pk_bf16_f32 v150, v28, v29\n"
"v_accvgpr_read_b32 v30, a74\n"
"v_mul_f32 v30, v30, v133\n"
"v_accvgpr_read_b32 v31, a75\n"
"v_mul_f32 v31, v31, v133\n"
"v_cvt_pk_bf16_f32 v151, v30, v31\n"
"v_accvgpr_read_b32 v24, a4\n"
"v_mul_f32 v24, v24, v132\n"
"v_mfma_f32_16x16x32_bf16 a[60:63], a[252:255], v[72:75], a[60:63]\n"
"ds_read_b64_tr_b16 a[252:253], v124 offset:10248\n"
"ds_read_b64_tr_b16 a[254:255], v124 offset:15368\n"
"v_accvgpr_read_b32 v25, a5\n"
"v_mul_f32 v25, v25, v132\n"
"v_cvt_pk_bf16_f32 v156, v24, v25\n"
"v_accvgpr_read_b32 v26, a6\n"
"v_mul_f32 v26, v26, v132\n"
"v_accvgpr_read_b32 v27, a7\n"
"v_mul_f32 v27, v27, v132\n"
"v_cvt_pk_bf16_f32 v157, v26, v27\n"
"v_accvgpr_read_b32 v28, a12\n"
"v_mul_f32 v28, v28, v132\n"
"v_accvgpr_read_b32 v29, a13\n"
"v_mul_f32 v29, v29, v132\n"
"v_cvt_pk_bf16_f32 v158, v28, v29\n"
"v_accvgpr_read_b32 v30, a14\n"
"v_mul_f32 v30, v30, v132\n"
"v_accvgpr_read_b32 v31, a15\n"
"v_mul_f32 v31, v31, v132\n"
"v_cvt_pk_bf16_f32 v159, v30, v31\n"
"s_waitcnt lgkmcnt(5)\n"
"v_mfma_f32_16x16x32_bf16 a[192:195], v[240:243], v[144:147], 0\n"
"buffer_store_dwordx4 v[148:151], v13, s[56:59], s76 offen offset:1024 sc1\n"
"v_rcp_f32 v136, v117\n"
"v_accvgpr_read_b32 v16, a128\n"
"v_mul_f32 v16, v16, v135\n"
"v_accvgpr_read_b32 v17, a129\n"
"v_mul_f32 v17, v17, v135\n"
"v_cvt_pk_bf16_f32 v152, v16, v17\n"
"v_accvgpr_read_b32 v18, a130\n"
"v_mul_f32 v18, v18, v135\n"
"v_accvgpr_read_b32 v19, a131\n"
"v_mul_f32 v19, v19, v135\n"
"v_cvt_pk_bf16_f32 v153, v18, v19\n"
"v_accvgpr_read_b32 v20, a136\n"
"v_mul_f32 v20, v20, v135\n"
"v_accvgpr_read_b32 v21, a137\n"
"v_mul_f32 v21, v21, v135\n"
"v_cvt_pk_bf16_f32 v154, v20, v21\n"
"v_accvgpr_read_b32 v22, a138\n"
"v_mul_f32 v22, v22, v135\n"
"v_accvgpr_read_b32 v23, a139\n"
"v_mfma_f32_16x16x32_bf16 a[200:203], v[240:243], v[156:159], 0\n"
"v_mul_f32 v23, v23, v135\n"
"v_cvt_pk_bf16_f32 v155, v22, v23\n"
"v_accvgpr_read_b32 v16, a68\n"
"v_mul_f32 v16, v16, v134\n"
"v_accvgpr_read_b32 v17, a69\n"
"v_mul_f32 v17, v17, v134\n"
"v_cvt_pk_bf16_f32 v160, v16, v17\n"
"v_accvgpr_read_b32 v18, a70\n"
"v_mul_f32 v18, v18, v134\n"
"v_accvgpr_read_b32 v19, a71\n"
"v_mul_f32 v19, v19, v134\n"
"v_cvt_pk_bf16_f32 v161, v18, v19\n"
"v_accvgpr_read_b32 v20, a76\n"
"v_mul_f32 v20, v20, v134\n"
"v_accvgpr_read_b32 v21, a77\n"
"v_mul_f32 v21, v21, v134\n"
"v_cvt_pk_bf16_f32 v162, v20, v21\n"
"v_accvgpr_read_b32 v22, a78\n"
"v_mul_f32 v22, v22, v134\n"
"v_accvgpr_read_b32 v23, a79\n"
"v_mul_f32 v23, v23, v134\n"
"v_cvt_pk_bf16_f32 v163, v22, v23\n"
"v_mfma_f32_16x16x32_bf16 a[208:211], v[240:243], v[148:151], 0\n"
"buffer_store_dwordx4 v[152:155], v13, s[56:59], s76 offen offset:2048 sc1\n"
"v_accvgpr_read_b32 v24, a132\n"
"v_mul_f32 v24, v24, v136\n"
"v_accvgpr_read_b32 v25, a133\n"
"v_mul_f32 v25, v25, v136\n"
"v_cvt_pk_bf16_f32 v164, v24, v25\n"
"v_accvgpr_read_b32 v26, a134\n"
"v_mul_f32 v26, v26, v136\n"
"v_accvgpr_read_b32 v27, a135\n"
"v_mul_f32 v27, v27, v136\n"
"v_mfma_f32_16x16x32_bf16 a[216:219], v[240:243], v[160:163], 0\n"
"v_cvt_pk_bf16_f32 v165, v26, v27\n"
"v_accvgpr_read_b32 v28, a140\n"
"v_mul_f32 v28, v28, v136\n"
"v_accvgpr_read_b32 v29, a141\n"
"v_mul_f32 v29, v29, v136\n"
"v_cvt_pk_bf16_f32 v166, v28, v29\n"
"v_accvgpr_read_b32 v30, a142\n"
"v_mul_f32 v30, v30, v136\n"
"v_accvgpr_read_b32 v31, a143\n"
"v_mul_f32 v31, v31, v136\n"
"v_cvt_pk_bf16_f32 v167, v30, v31\n"
"v_mfma_f32_16x16x32_bf16 a[224:227], v[240:243], v[152:155], 0\n"
"buffer_store_dwordx4 v[156:159], v13, s[56:59], s76 offen offset:3072 sc1\n"
"s_add_u32 s86, s85, 0\n"
"v_mfma_f32_16x16x32_bf16 a[232:235], v[240:243], v[164:167], 0\n"
"ds_read_b64_tr_b16 v[240:241], v125 offset:0\n"
"s_add_u32 s76, s87, 4096\n"
"v_mfma_f32_16x16x32_bf16 a[236:239], v[244:247], v[164:167], 0\n"
"buffer_store_dwordx4 v[160:163], v13, s[56:59], s76 offen sc1\n"
"v_accvgpr_read_b32 v16, a16\n"
"v_mfma_f32_16x16x32_bf16 a[228:231], v[244:247], v[152:155], 0\n"
"ds_read_b64_tr_b16 v[242:243], v125 offset:5120\n"
"v_mul_f32 v16, v16, v131\n"
"v_mfma_f32_16x16x32_bf16 a[220:223], v[244:247], v[160:163], 0\n"
"v_accvgpr_read_b32 v17, a17\n"
"v_mul_f32 v17, v17, v131\n"
"v_mfma_f32_16x16x32_bf16 a[212:215], v[244:247], v[148:151], 0\n"
"v_cvt_pk_bf16_f32 v168, v16, v17\n"
"v_accvgpr_read_b32 v18, a18\n"
"v_mfma_f32_16x16x32_bf16 a[204:207], v[244:247], v[156:159], 0\n"
"buffer_store_dwordx4 v[164:167], v13, s[56:59], s76 offen offset:1024 sc1\n"
"v_mul_f32 v18, v18, v131\n"
"v_mfma_f32_16x16x32_bf16 a[196:199], v[244:247], v[144:147], 0\n"
"ds_read_b64_tr_b16 v[244:245], v125 offset:8\n"
"s_add_u32 s76, s87, 6144\n"
"v_mfma_f32_16x16x32_bf16 a[0:3], v[248:251], v[144:147], 0\n"
"ds_read_b64_tr_b16 v[246:247], v125 offset:5128\n"
"v_accvgpr_read_b32 v19, a19\n"
"v_mfma_f32_16x16x32_bf16 a[8:11], v[248:251], v[156:159], 0\n"
"v_mul_f32 v19, v19, v131\n"
"v_cvt_pk_bf16_f32 v169, v18, v19\n"
"v_mfma_f32_16x16x32_bf16 a[64:67], v[248:251], v[148:151], 0\n"
"v_accvgpr_read_b32 v16, a144\n"
"v_mul_f32 v16, v16, v135\n"
"v_mfma_f32_16x16x32_bf16 a[72:75], v[248:251], v[160:163], 0\n"
"v_accvgpr_read_b32 v17, a145\n"
"v_mul_f32 v17, v17, v135\n"
"v_mfma_f32_16x16x32_bf16 a[128:131], v[248:251], v[152:155], 0\n"
"s_waitcnt vmcnt(6)\n"
"s_barrier\n"
"v_mfma_f32_16x16x32_bf16 a[136:139], v[248:251], v[164:167], 0\n"
"ds_read_b64_tr_b16 v[48:49], v127 offset:20480\n"
"ds_read_b64_tr_b16 v[50:51], v127 offset:25600\n"
"v_mfma_f32_16x16x32_bf16 a[140:143], v[252:255], v[164:167], 0\n"
"ds_read_b64_tr_b16 v[52:53], v127 offset:20488\n"
"v_cvt_pk_bf16_f32 v176, v16, v17\n"
"v_mfma_f32_16x16x32_bf16 a[132:135], v[252:255], v[152:155], 0\n"
"ds_read_b64_tr_b16 v[54:55], v127 offset:25608\n"
"v_accvgpr_read_b32 v18, a146\n"
"s_add_u32 m0, s81, 61440\n"
"v_mfma_f32_16x16x32_bf16 a[76:79], v[252:255], v[160:163], 0\n"
"buffer_load_dwordx4 v120, s[52:55], s86 offen lds\n"
"s_lshl_b32 s96, s94, 2\n"
"v_mfma_f32_16x16x32_bf16 a[68:71], v[252:255], v[148:151], 0\n"
"ds_read_b64_tr_b16 v[248:249], v125 offset:10240\n"
"s_add_u32 m0, s81, 62336\n"
"v_mfma_f32_16x16x32_bf16 a[12:15], v[252:255], v[156:159], 0\n"
"buffer_load_dwordx4 v120, s[52:55], s86 offen offset:128 lds\n"
"v_accvgpr_read_b32 v20, a24\n"
"v_mul_f32 v20, v20, v131\n"
"v_accvgpr_read_b32 v21, a25\n"
"v_mul_f32 v21, v21, v131\n"
"v_cvt_pk_bf16_f32 v170, v20, v21\n"
"v_accvgpr_read_b32 v22, a26\n"
"v_mul_f32 v22, v22, v131\n"
"v_accvgpr_read_b32 v23, a27\n"
"v_mul_f32 v23, v23, v131\n"
"v_cvt_pk_bf16_f32 v171, v22, v23\n"
"v_accvgpr_read_b32 v24, a80\n"
"v_mul_f32 v24, v24, v133\n"
"v_accvgpr_read_b32 v25, a81\n"
"v_mul_f32 v25, v25, v133\n"
"v_cvt_pk_bf16_f32 v172, v24, v25\n"
"v_accvgpr_read_b32 v26, a82\n"
"v_mul_f32 v26, v26, v133\n"
"v_accvgpr_read_b32 v27, a83\n"
"v_mul_f32 v27, v27, v133\n"
"v_cvt_pk_bf16_f32 v173, v26, v27\n"
"v_accvgpr_read_b32 v28, a88\n"
"v_mul_f32 v28, v28, v133\n"
"v_accvgpr_read_b32 v29, a89\n"
"v_mul_f32 v29, v29, v133\n"
"v_cvt_pk_bf16_f32 v174, v28, v29\n"
"v_accvgpr_read_b32 v30, a90\n"
"v_mfma_f32_16x16x32_bf16 a[4:7], v[252:255], v[144:147], 0\n"
"ds_read_b64_tr_b16 v[250:251], v125 offset:15360\n"
"v_mul_f32 v30, v30, v133\n"
"v_accvgpr_read_b32 v31, a91\n"
"v_mul_f32 v31, v31, v133\n"
"v_cvt_pk_bf16_f32 v175, v30, v31\n"
"v_accvgpr_read_b32 v24, a20\n"
"v_mul_f32 v24, v24, v132\n"
"v_accvgpr_read_b32 v25, a21\n"
"v_mul_f32 v25, v25, v132\n"
"v_cvt_pk_bf16_f32 v180, v24, v25\n"
"v_accvgpr_read_b32 v26, a22\n"
"v_mul_f32 v26, v26, v132\n"
"v_accvgpr_read_b32 v27, a23\n"
"v_mul_f32 v27, v27, v132\n"
"v_cvt_pk_bf16_f32 v181, v26, v27\n"
"v_accvgpr_read_b32 v28, a28\n"
"v_mul_f32 v28, v28, v132\n"
"v_accvgpr_read_b32 v29, a29\n"
"v_mul_f32 v29, v29, v132\n"
"v_cvt_pk_bf16_f32 v182, v28, v29\n"
"v_accvgpr_read_b32 v30, a30\n"
"v_mul_f32 v30, v30, v132\n"
"v_accvgpr_read_b32 v31, a31\n"
"v_mul_f32 v31, v31, v132\n"
"v_cvt_pk_bf16_f32 v183, v30, v31\n"
"s_add_u32 s96, s96, s91\n"
"v_mfma_f32_16x16x32_bf16 a[192:195], a[240:243], v[168:171], a[192:195]\n"
"ds_read_b64_tr_b16 v[252:253], v125 offset:10248\n"
"v_mul_f32 v18, v18, v135\n"
"v_accvgpr_read_b32 v19, a147\n"
"v_mul_f32 v19, v19, v135\n"
"v_cvt_pk_bf16_f32 v177, v18, v19\n"
"v_accvgpr_read_b32 v20, a152\n"
"v_mul_f32 v20, v20, v135\n"
"v_accvgpr_read_b32 v21, a153\n"
"v_mul_f32 v21, v21, v135\n"
"v_cvt_pk_bf16_f32 v178, v20, v21\n"
"v_accvgpr_read_b32 v22, a154\n"
"v_mul_f32 v22, v22, v135\n"
"v_accvgpr_read_b32 v23, a155\n"
"v_mul_f32 v23, v23, v135\n"
"v_cvt_pk_bf16_f32 v179, v22, v23\n"
"v_accvgpr_read_b32 v16, a84\n"
"v_mul_f32 v16, v16, v134\n"
"v_accvgpr_read_b32 v17, a85\n"
"v_mul_f32 v17, v17, v134\n"
"v_mfma_f32_16x16x32_bf16 a[200:203], a[240:243], v[180:183], a[200:203]\n"
"ds_read_b64_tr_b16 v[254:255], v125 offset:15368\n"
"v_cvt_pk_bf16_f32 v184, v16, v17\n"
"v_accvgpr_read_b32 v18, a86\n"
"v_mul_f32 v18, v18, v134\n"
"v_accvgpr_read_b32 v19, a87\n"
"v_mul_f32 v19, v19, v134\n"
"v_cvt_pk_bf16_f32 v185, v18, v19\n"
"v_accvgpr_read_b32 v20, a92\n"
"v_mul_f32 v20, v20, v134\n"
"v_accvgpr_read_b32 v21, a93\n"
"v_mul_f32 v21, v21, v134\n"
"v_cvt_pk_bf16_f32 v186, v20, v21\n"
"v_accvgpr_read_b32 v22, a94\n"
"v_mul_f32 v22, v22, v134\n"
"v_accvgpr_read_b32 v23, a95\n"
"v_mul_f32 v23, v23, v134\n"
"v_cvt_pk_bf16_f32 v187, v22, v23\n"
"s_xor_b32 s97, s96, 4\n"
"s_mul_i32 s97, s97, 16384\n"
"v_mfma_f32_16x16x32_bf16 a[208:211], a[240:243], v[172:175], a[208:211]\n"
"v_accvgpr_read_b32 v24, a148\n"
"v_mul_f32 v24, v24, v136\n"
"v_accvgpr_read_b32 v25, a149\n"
"v_mul_f32 v25, v25, v136\n"
"v_cvt_pk_bf16_f32 v188, v24, v25\n"
"v_accvgpr_read_b32 v26, a150\n"
"v_mul_f32 v26, v26, v136\n"
"v_accvgpr_read_b32 v27, a151\n"
"v_mul_f32 v27, v27, v136\n"
"v_cvt_pk_bf16_f32 v189, v26, v27\n"
"v_accvgpr_read_b32 v28, a156\n"
"v_mul_f32 v28, v28, v136\n"
"v_mfma_f32_16x16x32_bf16 a[216:219], a[240:243], v[184:187], a[216:219]\n"
"v_accvgpr_read_b32 v29, a157\n"
"v_mul_f32 v29, v29, v136\n"
"v_cvt_pk_bf16_f32 v190, v28, v29\n"
"v_accvgpr_read_b32 v30, a158\n"
"v_mul_f32 v30, v30, v136\n"
"v_accvgpr_read_b32 v31, a159\n"
"v_mul_f32 v31, v31, v136\n"
"v_cvt_pk_bf16_f32 v191, v30, v31\n"
"s_bfe_u32 s29, s94, 0x10001\n"
"s_lshl_b32 s30, s29, 10\n"
"s_add_u32 s79, s81, s30\n"
"s_add_u32 s68, s97, 28311552\n"
"s_add_u32 m0, s81, 63488\n"
"v_mfma_f32_16x16x32_bf16 a[224:227], a[240:243], v[176:179], a[224:227]\n"
"buffer_load_dwordx4 v121, s[52:55], s86 offen lds\n"
"v_mfma_f32_16x16x32_bf16 a[232:235], a[240:243], v[188:191], a[232:235]\n"
"ds_read_b64_tr_b16 a[240:241], v126 offset:0\n"
"s_waitcnt lgkmcnt(3)\n"
"v_mfma_f32_16x16x32_bf16 a[136:139], a[244:247], v[188:191], a[136:139]\n"
"ds_read_b64_tr_b16 a[242:243], v126 offset:5120\n"
"s_xor_b32 s97, s96, 8\n"
"v_mfma_f32_16x16x32_bf16 a[128:131], a[244:247], v[176:179], a[128:131]\n"
"s_xor_b32 s30, s30, 1024\n"
"s_add_u32 s80, s81, s30\n"
"v_mfma_f32_16x16x32_bf16 a[72:75], a[244:247], v[184:187], a[72:75]\n"
"s_lshl_b32 s29, s29, 7\n"
"s_xor_b32 s30, s29, 128\n"
"v_mfma_f32_16x16x32_bf16 a[64:67], a[244:247], v[172:175], a[64:67]\n"
"s_mul_i32 s97, s97, 16384\n"
"s_add_u32 s69, s97, 28314624\n"
"s_add_u32 m0, s81, 64384\n"
"v_mfma_f32_16x16x32_bf16 a[8:11], a[244:247], v[180:183], a[8:11]\n"
"buffer_load_dwordx4 v121, s[52:55], s86 offen offset:128 lds\n"
"s_xor_b32 s97, s96, 12\n"
"v_mfma_f32_16x16x32_bf16 a[0:3], a[244:247], v[168:171], a[0:3]\n"
"ds_read_b64_tr_b16 a[244:245], v126 offset:8\n"
"v_mfma_f32_16x16x32_bf16 a[196:199], a[248:251], v[168:171], a[196:199]\n"
"ds_read_b64_tr_b16 a[246:247], v126 offset:5128\n"
"s_mul_i32 s97, s97, 16384\n"
"v_mfma_f32_16x16x32_bf16 a[204:207], a[248:251], v[180:183], a[204:207]\n"
"s_add_u32 s70, s97, 28317696\n"
"s_and_b32 s31, s94, 3\n"
"v_mfma_f32_16x16x32_bf16 a[212:215], a[248:251], v[172:175], a[212:215]\n"
"s_andn2_b32 s97, s96, 12\n"
"s_add_u32 s97, s97, 0\n"
"v_mfma_f32_16x16x32_bf16 a[220:223], a[248:251], v[184:187], a[220:223]\n"
"s_lshl_b32 s97, s97, 4\n"
"s_add_u32 s97, s97, s31\n"
"s_add_u32 m0, s81, 65536\n"
"v_mfma_f32_16x16x32_bf16 a[228:231], a[248:251], v[176:179], a[228:231]\n"
"buffer_load_dwordx4 v122, s[52:55], s86 offen lds\n"
"s_add_u32 s86, s83, 40960\n"
"v_mfma_f32_16x16x32_bf16 a[236:239], a[248:251], v[188:191], a[236:239]\n"
"ds_read_b64_tr_b16 a[248:249], v126 offset:10240\n"
"v_mfma_f32_16x16x32_bf16 a[140:143], a[252:255], v[188:191], a[140:143]\n"
"ds_read_b64_tr_b16 a[250:251], v126 offset:15360\n"
"s_mul_i32 s97, s97, 1024\n"
"s_add_u32 m0, s81, 71680\n"
"v_mfma_f32_16x16x32_bf16 a[132:135], a[252:255], v[176:179], a[132:135]\n"
"buffer_load_dwordx4 v120, s[52:55], s86 offen lds\n"
"v_mfma_f32_16x16x32_bf16 a[76:79], a[252:255], v[184:187], a[76:79]\n"
"s_add_u32 s71, s97, 28320768\n"
"s_andn2_b32 s97, s96, 12\n"
"v_mfma_f32_16x16x32_bf16 a[68:71], a[252:255], v[172:175], a[68:71]\n"
"s_add_u32 s97, s97, 4\n"
"s_lshl_b32 s97, s97, 4\n"
"v_mfma_f32_16x16x32_bf16 a[12:15], a[252:255], v[180:183], a[12:15]\n"
"v_mfma_f32_16x16x32_bf16 a[4:7], a[252:255], v[168:171], a[4:7]\n"
"ds_read_b64_tr_b16 a[252:253], v126 offset:10248\n"
"s_add_u32 m0, s81, 72576\n"
"s_nop 0\n"
"buffer_load_dwordx4 v120, s[52:55], s86 offen offset:128 lds\n"
"v_mfma_f32_16x16x32_bf16 a[16:19], v[240:243], v[144:147], 0\n"
"ds_read_b64_tr_b16 a[254:255], v126 offset:15368\n"
"s_add_u32 s97, s97, s31\n"
"v_mfma_f32_16x16x32_bf16 a[24:27], v[240:243], v[156:159], 0\n"
"s_mul_i32 s97, s97, 1024\n"
"s_add_u32 s72, s97, 28320768\n"
"s_add_u32 m0, s81, 73728\n"
"v_mfma_f32_16x16x32_bf16 a[80:83], v[240:243], v[148:151], 0\n"
"buffer_load_dwordx4 v121, s[52:55], s86 offen lds\n"
"s_andn2_b32 s97, s96, 12\n"
"v_mfma_f32_16x16x32_bf16 a[88:91], v[240:243], v[160:163], 0\n"
"s_add_u32 s97, s97, 8\n"
"v_mfma_f32_16x16x32_bf16 a[144:147], v[240:243], v[152:155], 0\n"
"s_lshl_b32 s97, s97, 4\n"
"s_add_u32 s97, s97, s31\n"
"v_mfma_f32_16x16x32_bf16 a[152:155], v[240:243], v[164:167], 0\n"
"ds_read_b64_tr_b16 v[240:241], v123 offset:20480\n"
"ds_read_b64_tr_b16 v[242:243], v123 offset:25600\n"
"s_add_u32 m0, s81, 74624\n"
"v_mfma_f32_16x16x32_bf16 a[156:159], v[244:247], v[164:167], 0\n"
"buffer_load_dwordx4 v121, s[52:55], s86 offen offset:128 lds\n"
"s_mul_i32 s97, s97, 1024\n"
"v_mfma_f32_16x16x32_bf16 a[148:151], v[244:247], v[152:155], 0\n"
"s_add_u32 s73, s97, 28320768\n"
"v_mfma_f32_16x16x32_bf16 a[92:95], v[244:247], v[160:163], 0\n"
"s_add_u32 s64, s14, 4096\n"
"s_add_u32 s65, s14, 9216\n"
"v_mfma_f32_16x16x32_bf16 a[84:87], v[244:247], v[148:151], 0\n"
"s_add_u32 s67, s14, 8192\n"
"s_bfe_u32 s96, s94, 0x10003\n"
"v_mfma_f32_16x16x32_bf16 a[28:31], v[244:247], v[156:159], 0\n"
"s_xor_b32 s97, s96, 1\n"
"s_lshl_b32 s97, s97, 8\n"
"v_mfma_f32_16x16x32_bf16 a[20:23], v[244:247], v[144:147], 0\n"
"ds_read_b64_tr_b16 v[244:245], v124 offset:20480\n"
"ds_read_b64_tr_b16 v[246:247], v124 offset:25600\n"
"v_mfma_f32_16x16x32_bf16 a[16:19], v[248:251], v[168:171], a[16:19]\n"
"s_mul_i32 s75, s31, 81920\n"
"s_add_u32 s75, s75, s97\n"
"v_mfma_f32_16x16x32_bf16 a[24:27], v[248:251], v[180:183], a[24:27]\n"
"s_add_u32 s96, s96, s31\n"
"s_lshl_b32 s66, s96, 6\n"
"s_add_u32 m0, s81, 75776\n"
"v_mfma_f32_16x16x32_bf16 a[80:83], v[248:251], v[172:175], a[80:83]\n"
"buffer_load_dwordx4 v122, s[52:55], s86 offen lds\n"
"v_accvgpr_read_b32 v16, a32\n"
"v_mfma_f32_16x16x32_bf16 a[88:91], v[248:251], v[184:187], a[88:91]\n"
"v_mul_f32 v16, v16, v131\n"
"v_mfma_f32_16x16x32_bf16 a[144:147], v[248:251], v[176:179], a[144:147]\n"
"v_accvgpr_read_b32 v17, a33\n"
"v_mfma_f32_16x16x32_bf16 a[152:155], v[248:251], v[188:191], a[152:155]\n"
"ds_read_b64_tr_b16 v[248:249], v125 offset:20480\n"
"ds_read_b64_tr_b16 v[250:251], v125 offset:25600\n"
"s_waitcnt lgkmcnt(2)\n"
"v_mfma_f32_16x16x32_bf16 a[156:159], v[252:255], v[188:191], a[156:159]\n"
"v_mul_f32 v17, v17, v131\n"
"v_mfma_f32_16x16x32_bf16 a[148:151], v[252:255], v[176:179], a[148:151]\n"
"s_cmp_eq_u32 s31, 3\n"
"s_cselect_b32 s75, 0x40000000, s75\n"
"s_cselect_b32 s65, 0x40000000, s65\n"
"v_mfma_f32_16x16x32_bf16 a[92:95], v[252:255], v[184:187], a[92:95]\n"
"v_cvt_pk_bf16_f32 v192, v16, v17\n"
"v_accvgpr_read_b32 v18, a34\n"
"v_mfma_f32_16x16x32_bf16 a[84:87], v[252:255], v[172:175], a[84:87]\n"
"v_mul_f32 v18, v18, v131\n"
"v_accvgpr_read_b32 v19, a35\n"
"v_mfma_f32_16x16x32_bf16 a[28:31], v[252:255], v[180:183], a[28:31]\n"
"v_mul_f32 v19, v19, v131\n"
"v_cvt_pk_bf16_f32 v193, v18, v19\n"
"v_accvgpr_read_b32 v20, a40\n"
"v_accvgpr_read_b32 v21, a41\n"
"v_mfma_f32_16x16x32_bf16 a[20:23], v[252:255], v[168:171], a[20:23]\n"
"ds_read_b64_tr_b16 v[252:253], v123 offset:20488\n"
"ds_read_b64_tr_b16 v[254:255], v123 offset:25608\n"
"v_accvgpr_read_b32 v22, a42\n"
"v_accvgpr_read_b32 v23, a43\n"
"v_mfma_f32_16x16x32_bf16 a[32:35], a[240:243], v[144:147], 0\n"
"v_accvgpr_read_b32 v24, a96\n"
"v_accvgpr_read_b32 v25, a97\n"
"v_accvgpr_read_b32 v26, a98\n"
"v_accvgpr_read_b32 v27, a99\n"
"v_accvgpr_read_b32 v28, a104\n"
"v_accvgpr_read_b32 v29, a105\n"
"v_mfma_f32_16x16x32_bf16 a[40:43], a[240:243], v[156:159], 0\n"
"v_accvgpr_read_b32 v30, a106\n"
"v_accvgpr_read_b32 v31, a107\n"
"v_accvgpr_read_b32 v16, a160\n"
"v_mul_f32 v16, v16, v135\n"
"v_accvgpr_read_b32 v17, a161\n"
"v_mul_f32 v17, v17, v135\n"
"v_mfma_f32_16x16x32_bf16 a[96:99], a[240:243], v[148:151], 0\n"
"v_mul_f32 v20, v20, v131\n"
"v_mul_f32 v21, v21, v131\n"
"v_cvt_pk_bf16_f32 v194, v20, v21\n"
"v_mul_f32 v22, v22, v131\n"
"v_mul_f32 v23, v23, v131\n"
"v_cvt_pk_bf16_f32 v195, v22, v23\n"
"v_cvt_pk_bf16_f32 v200, v16, v17\n"
"v_mfma_f32_16x16x32_bf16 a[104:107], a[240:243], v[160:163], 0\n"
"v_accvgpr_read_b32 v18, a162\n"
"v_mul_f32 v18, v18, v135\n"
"v_accvgpr_read_b32 v19, a163\n"
"v_accvgpr_read_b32 v20, a168\n"
"v_accvgpr_read_b32 v21, a169\n"
"v_accvgpr_read_b32 v22, a170\n"
"v_accvgpr_read_b32 v23, a171\n"
"v_mfma_f32_16x16x32_bf16 a[160:163], a[240:243], v[152:155], 0\n"
"v_mul_f32 v24, v24, v133\n"
"v_mul_f32 v25, v25, v133\n"
"v_cvt_pk_bf16_f32 v196, v24, v25\n"
"v_mul_f32 v26, v26, v133\n"
"v_mul_f32 v27, v27, v133\n"
"v_cvt_pk_bf16_f32 v197, v26, v27\n"
"v_mul_f32 v28, v28, v133\n"
"v_mul_f32 v29, v29, v133\n"
"v_cvt_pk_bf16_f32 v198, v28, v29\n"
"v_mul_f32 v30, v30, v133\n"
"v_mul_f32 v31, v31, v133\n"
"v_cvt_pk_bf16_f32 v199, v30, v31\n"
"v_mul_f32 v19, v19, v135\n"
"v_cvt_pk_bf16_f32 v201, v18, v19\n"
"v_accvgpr_read_b32 v24, a36\n"
"v_mul_f32 v24, v24, v132\n"
"v_accvgpr_read_b32 v25, a37\n"
"v_mul_f32 v25, v25, v132\n"
"v_cvt_pk_bf16_f32 v204, v24, v25\n"
"v_accvgpr_read_b32 v26, a38\n"
"v_mul_f32 v26, v26, v132\n"
"v_accvgpr_read_b32 v27, a39\n"
"v_mfma_f32_16x16x32_bf16 a[168:171], a[240:243], v[164:167], 0\n"
"ds_read_b64_tr_b16 a[240:241], v124 offset:20488\n"
"ds_read_b64_tr_b16 a[242:243], v124 offset:25608\n"
"v_mul_f32 v27, v27, v132\n"
"v_cvt_pk_bf16_f32 v205, v26, v27\n"
"v_accvgpr_read_b32 v28, a44\n"
"v_mul_f32 v28, v28, v132\n"
"v_accvgpr_read_b32 v29, a45\n"
"v_mul_f32 v29, v29, v132\n"
"v_cvt_pk_bf16_f32 v206, v28, v29\n"
"v_accvgpr_read_b32 v30, a46\n"
"v_mul_f32 v30, v30, v132\n"
"v_accvgpr_read_b32 v31, a47\n"
"v_mul_f32 v31, v31, v132\n"
"v_cvt_pk_bf16_f32 v207, v30, v31\n"
"v_accvgpr_read_b32 v24, a164\n"
"v_accvgpr_read_b32 v25, a165\n"
"v_accvgpr_read_b32 v26, a166\n"
"v_accvgpr_read_b32 v27, a167\n"
"v_accvgpr_read_b32 v28, a172\n"
"v_accvgpr_read_b32 v29, a173\n"
"v_accvgpr_read_b32 v30, a174\n"
"v_accvgpr_read_b32 v31, a175\n"
"v_mfma_f32_16x16x32_bf16 a[172:175], a[244:247], v[164:167], 0\n"
"v_mul_f32 v20, v20, v135\n"
"v_mul_f32 v21, v21, v135\n"
"v_cvt_pk_bf16_f32 v202, v20, v21\n"
"v_mul_f32 v22, v22, v135\n"
"v_mul_f32 v23, v23, v135\n"
"v_cvt_pk_bf16_f32 v203, v22, v23\n"
"v_accvgpr_read_b32 v16, a100\n"
"v_mul_f32 v16, v16, v134\n"
"v_mfma_f32_16x16x32_bf16 a[164:167], a[244:247], v[152:155], 0\n"
"v_accvgpr_read_b32 v17, a101\n"
"v_mul_f32 v17, v17, v134\n"
"v_accvgpr_read_b32 v18, a102\n"
"v_accvgpr_read_b32 v19, a103\n"
"v_accvgpr_read_b32 v20, a108\n"
"v_accvgpr_read_b32 v21, a109\n"
"v_accvgpr_read_b32 v22, a110\n"
"v_accvgpr_read_b32 v23, a111\n"
"v_mfma_f32_16x16x32_bf16 a[108:111], a[244:247], v[160:163], 0\n"
"v_cvt_pk_bf16_f32 v208, v16, v17\n"
"v_mul_f32 v18, v18, v134\n"
"v_mfma_f32_16x16x32_bf16 a[100:103], a[244:247], v[148:151], 0\n"
"v_mul_f32 v19, v19, v134\n"
"v_cvt_pk_bf16_f32 v209, v18, v19\n"
"v_mfma_f32_16x16x32_bf16 a[44:47], a[244:247], v[156:159], 0\n"
"v_accvgpr_read_b32 v16, a48\n"
"v_mul_f32 v16, v16, v131\n"
"v_mfma_f32_16x16x32_bf16 a[36:39], a[244:247], v[144:147], 0\n"
"ds_read_b64_tr_b16 a[244:245], v125 offset:20488\n"
"ds_read_b64_tr_b16 a[246:247], v125 offset:25608\n"
"v_mfma_f32_16x16x32_bf16 a[32:35], a[248:251], v[168:171], a[32:35]\n"
"v_accvgpr_read_b32 v17, a49\n"
"v_mul_f32 v17, v17, v131\n"
"v_mfma_f32_16x16x32_bf16 a[40:43], a[248:251], v[180:183], a[40:43]\n"
"v_cvt_pk_bf16_f32 v216, v16, v17\n"
"v_accvgpr_read_b32 v18, a50\n"
"v_mfma_f32_16x16x32_bf16 a[96:99], a[248:251], v[172:175], a[96:99]\n"
"v_mul_f32 v18, v18, v131\n"
"v_accvgpr_read_b32 v19, a51\n"
"v_mfma_f32_16x16x32_bf16 a[104:107], a[248:251], v[184:187], a[104:107]\n"
"v_mul_f32 v19, v19, v131\n"
"v_cvt_pk_bf16_f32 v217, v18, v19\n"
"v_mfma_f32_16x16x32_bf16 a[160:163], a[248:251], v[176:179], a[160:163]\n"
"v_accvgpr_read_b32 v16, a176\n"
"v_mul_f32 v16, v16, v135\n"
"v_mfma_f32_16x16x32_bf16 a[168:171], a[248:251], v[188:191], a[168:171]\n"
"ds_read_b64_tr_b16 a[248:249], v126 offset:20480\n"
"ds_read_b64_tr_b16 a[250:251], v126 offset:25600\n"
"v_mfma_f32_16x16x32_bf16 a[172:175], a[252:255], v[188:191], a[172:175]\n"
"v_accvgpr_read_b32 v17, a177\n"
"v_mul_f32 v17, v17, v135\n"
"v_mfma_f32_16x16x32_bf16 a[164:167], a[252:255], v[176:179], a[164:167]\n"
"v_cvt_pk_bf16_f32 v224, v16, v17\n"
"v_accvgpr_read_b32 v18, a178\n"
"v_mfma_f32_16x16x32_bf16 a[108:111], a[252:255], v[184:187], a[108:111]\n"
"v_mul_f32 v18, v18, v135\n"
"v_accvgpr_read_b32 v19, a179\n"
"v_mfma_f32_16x16x32_bf16 a[100:103], a[252:255], v[172:175], a[100:103]\n"
"v_mul_f32 v19, v19, v135\n"
"s_add_u32 s86, s84, 40960\n"
"v_mfma_f32_16x16x32_bf16 a[44:47], a[252:255], v[180:183], a[44:47]\n"
"v_cvt_pk_bf16_f32 v225, v18, v19\n"
"v_accvgpr_read_b32 v16, a116\n"
"v_mfma_f32_16x16x32_bf16 a[36:39], a[252:255], v[168:171], a[36:39]\n"
"ds_read_b64_tr_b16 a[252:253], v126 offset:20488\n"
"ds_read_b64_tr_b16 a[254:255], v126 offset:25608\n"
"v_mfma_f32_16x16x32_bf16 a[192:195], v[240:243], v[192:195], a[192:195]\n"
"v_mul_f32 v20, v20, v134\n"
"v_mul_f32 v21, v21, v134\n"
"v_cvt_pk_bf16_f32 v210, v20, v21\n"
"v_mul_f32 v22, v22, v134\n"
"v_mul_f32 v23, v23, v134\n"
"v_mfma_f32_16x16x32_bf16 a[200:203], v[240:243], v[204:207], a[200:203]\n"
"v_cvt_pk_bf16_f32 v211, v22, v23\n"
"v_mul_f32 v16, v16, v134\n"
"v_accvgpr_read_b32 v17, a117\n"
"v_mul_f32 v17, v17, v134\n"
"v_cvt_pk_bf16_f32 v232, v16, v17\n"
"v_mfma_f32_16x16x32_bf16 a[208:211], v[240:243], v[196:199], a[208:211]\n"
"v_mul_f32 v24, v24, v136\n"
"v_mul_f32 v25, v25, v136\n"
"v_cvt_pk_bf16_f32 v212, v24, v25\n"
"v_mul_f32 v26, v26, v136\n"
"v_mul_f32 v27, v27, v136\n"
"v_cvt_pk_bf16_f32 v213, v26, v27\n"
"v_mul_f32 v28, v28, v136\n"
"v_mul_f32 v29, v29, v136\n"
"v_mfma_f32_16x16x32_bf16 a[216:219], v[240:243], v[208:211], a[216:219]\n"
"v_cvt_pk_bf16_f32 v214, v28, v29\n"
"v_mul_f32 v30, v30, v136\n"
"v_mul_f32 v31, v31, v136\n"
"v_cvt_pk_bf16_f32 v215, v30, v31\n"
"v_accvgpr_read_b32 v18, a118\n"
"v_mul_f32 v18, v18, v134\n"
"v_accvgpr_read_b32 v19, a119\n"
"v_mul_f32 v19, v19, v134\n"
"v_mfma_f32_16x16x32_bf16 a[224:227], v[240:243], v[200:203], a[224:227]\n"
"v_mfma_f32_16x16x32_bf16 a[232:235], v[240:243], v[212:215], a[232:235]\n"
"s_waitcnt vmcnt(10)\n"
"s_mov_b32 exec_lo, 0xe0\n"
"s_mov_b32 exec_hi, 0x0\n"
"buffer_store_dword v11, v9, s[56:59], 0 offen sc1\n"
"s_mov_b64 exec, -1\n"
"v_mfma_f32_16x16x32_bf16 a[136:139], v[244:247], v[212:215], a[136:139]\n"
"ds_read_b64_tr_b16 v[240:241], v123 offset:30720\n"
"v_accvgpr_read_b32 v20, a56\n"
"v_mfma_f32_16x16x32_bf16 a[128:131], v[244:247], v[200:203], a[128:131]\n"
"ds_read_b64_tr_b16 v[242:243], v123 offset:35840\n"
"v_cvt_pk_bf16_f32 v233, v18, v19\n"
"v_mfma_f32_16x16x32_bf16 a[72:75], v[244:247], v[208:211], a[72:75]\n"
"s_barrier\n"
"s_add_u32 m0, s81, 0\n"
"v_mfma_f32_16x16x32_bf16 a[64:67], v[244:247], v[196:199], a[64:67]\n"
"buffer_load_dwordx4 v120, s[52:55], s86 offen lds\n"
"s_add_u32 m0, s81, 896\n"
"v_mfma_f32_16x16x32_bf16 a[8:11], v[244:247], v[204:207], a[8:11]\n"
"buffer_load_dwordx4 v120, s[52:55], s86 offen offset:128 lds\n"
"v_mfma_f32_16x16x32_bf16 a[0:3], v[244:247], v[192:195], a[0:3]\n"
"s_waitcnt lgkmcnt(2)\n"
"v_mfma_f32_16x16x32_bf16 a[16:19], v[248:251], v[192:195], a[16:19]\n"
"ds_read_b64_tr_b16 v[244:245], v124 offset:30720\n"
"v_mul_f32 v20, v20, v131\n"
"s_add_u32 m0, s81, 2048\n"
"v_mfma_f32_16x16x32_bf16 a[24:27], v[248:251], v[204:207], a[24:27]\n"
"buffer_load_dwordx4 v121, s[52:55], s86 offen lds\n"
"v_mfma_f32_16x16x32_bf16 a[80:83], v[248:251], v[196:199], a[80:83]\n"
"ds_read_b64_tr_b16 v[246:247], v124 offset:35840\n"
"v_accvgpr_read_b32 v21, a57\n"
"s_add_u32 m0, s81, 2944\n"
"v_mfma_f32_16x16x32_bf16 a[88:91], v[248:251], v[208:211], a[88:91]\n"
"buffer_load_dwordx4 v121, s[52:55], s86 offen offset:128 lds\n"
"v_mfma_f32_16x16x32_bf16 a[144:147], v[248:251], v[200:203], a[144:147]\n"
"v_mul_f32 v21, v21, v131\n"
"v_cvt_pk_bf16_f32 v218, v20, v21\n"
"v_mfma_f32_16x16x32_bf16 a[152:155], v[248:251], v[212:215], a[152:155]\n"
"ds_read_b64_tr_b16 v[248:249], v125 offset:30720\n"
"ds_read_b64_tr_b16 v[250:251], v125 offset:35840\n"
"v_mfma_f32_16x16x32_bf16 a[236:239], v[252:255], v[212:215], a[236:239]\n"
"v_accvgpr_read_b32 v22, a58\n"
"v_mul_f32 v22, v22, v131\n"
"v_mfma_f32_16x16x32_bf16 a[228:231], v[252:255], v[200:203], a[228:231]\n"
"v_accvgpr_read_b32 v23, a59\n"
"v_mul_f32 v23, v23, v131\n"
"v_mfma_f32_16x16x32_bf16 a[220:223], v[252:255], v[208:211], a[220:223]\n"
"v_cvt_pk_bf16_f32 v219, v22, v23\n"
"v_accvgpr_read_b32 v20, a184\n"
"v_mfma_f32_16x16x32_bf16 a[212:215], v[252:255], v[196:199], a[212:215]\n"
"v_mul_f32 v20, v20, v135\n"
"v_accvgpr_read_b32 v21, a185\n"
"s_add_u32 m0, s81, 4096\n"
"v_mfma_f32_16x16x32_bf16 a[204:207], v[252:255], v[204:207], a[204:207]\n"
"buffer_load_dwordx4 v122, s[52:55], s86 offen lds\n"
"s_add_u32 s86, s85, 40960\n"
"v_mfma_f32_16x16x32_bf16 a[196:199], v[252:255], v[192:195], a[196:199]\n"
"v_mfma_f32_16x16x32_bf16 a[4:7], a[240:243], v[192:195], a[4:7]\n"
"ds_read_b64_tr_b16 v[252:253], v126 offset:30720\n"
"v_mul_f32 v21, v21, v135\n"
"v_mfma_f32_16x16x32_bf16 a[12:15], a[240:243], v[204:207], a[12:15]\n"
"ds_read_b64_tr_b16 v[254:255], v126 offset:35840\n"
"v_cvt_pk_bf16_f32 v226, v20, v21\n"
"s_add_u32 m0, s81, 10240\n"
"v_mfma_f32_16x16x32_bf16 a[68:71], a[240:243], v[196:199], a[68:71]\n"
"buffer_load_dwordx4 v120, s[52:55], s86 offen lds\n"
"v_accvgpr_read_b32 v22, a186\n"
"v_mfma_f32_16x16x32_bf16 a[76:79], a[240:243], v[208:211], a[76:79]\n"
"v_mul_f32 v22, v22, v135\n"
"v_mfma_f32_16x16x32_bf16 a[132:135], a[240:243], v[200:203], a[132:135]\n"
"v_accvgpr_read_b32 v23, a187\n"
"v_mul_f32 v23, v23, v135\n"
"v_mfma_f32_16x16x32_bf16 a[140:143], a[240:243], v[212:215], a[140:143]\n"
"ds_read_b64_tr_b16 a[240:241], v123 offset:30728\n"
"ds_read_b64_tr_b16 a[242:243], v123 offset:35848\n"
"s_add_u32 m0, s81, 11136\n"
"v_mfma_f32_16x16x32_bf16 a[156:159], a[244:247], v[212:215], a[156:159]\n"
"buffer_load_dwordx4 v120, s[52:55], s86 offen offset:128 lds\n"
"v_cvt_pk_bf16_f32 v227, v22, v23\n"
"v_mfma_f32_16x16x32_bf16 a[148:151], a[244:247], v[200:203], a[148:151]\n"
"v_accvgpr_read_b32 v20, a124\n"
"v_mfma_f32_16x16x32_bf16 a[92:95], a[244:247], v[208:211], a[92:95]\n"
"v_mul_f32 v20, v20, v134\n"
"v_accvgpr_read_b32 v21, a125\n"
"v_mfma_f32_16x16x32_bf16 a[84:87], a[244:247], v[196:199], a[84:87]\n"
"v_mul_f32 v21, v21, v134\n"
"v_cvt_pk_bf16_f32 v234, v20, v21\n"
"v_mfma_f32_16x16x32_bf16 a[28:31], a[244:247], v[204:207], a[28:31]\n"
"v_accvgpr_read_b32 v22, a126\n"
"v_mul_f32 v22, v22, v134\n"
"v_mfma_f32_16x16x32_bf16 a[20:23], a[244:247], v[192:195], a[20:23]\n"
"ds_read_b64_tr_b16 a[244:245], v124 offset:30728\n"
"ds_read_b64_tr_b16 a[246:247], v124 offset:35848\n"
"s_add_u32 m0, s81, 12288\n"
"v_mfma_f32_16x16x32_bf16 a[32:35], a[248:251], v[192:195], a[32:35]\n"
"buffer_load_dwordx4 v121, s[52:55], s86 offen lds\n"
"v_accvgpr_read_b32 v23, a127\n"
"v_mfma_f32_16x16x32_bf16 a[40:43], a[248:251], v[204:207], a[40:43]\n"
"v_mul_f32 v23, v23, v134\n"
"v_mfma_f32_16x16x32_bf16 a[96:99], a[248:251], v[196:199], a[96:99]\n"
"v_accvgpr_read_b32 v24, a112\n"
"v_mul_f32 v24, v24, v133\n"
"v_mfma_f32_16x16x32_bf16 a[104:107], a[248:251], v[208:211], a[104:107]\n"
"v_accvgpr_read_b32 v25, a113\n"
"v_cvt_pk_bf16_f32 v235, v22, v23\n"
"v_mfma_f32_16x16x32_bf16 a[160:163], a[248:251], v[200:203], a[160:163]\n"
"v_mfma_f32_16x16x32_bf16 a[168:171], a[248:251], v[212:215], a[168:171]\n"
"ds_read_b64_tr_b16 a[248:249], v125 offset:30728\n"
"s_add_u32 m0, s81, 13184\n"
"s_nop 0\n"
"buffer_load_dwordx4 v121, s[52:55], s86 offen offset:128 lds\n"
"v_mfma_f32_16x16x32_bf16 a[172:175], a[252:255], v[212:215], a[172:175]\n"
"ds_read_b64_tr_b16 a[250:251], v125 offset:35848\n"
"v_mul_f32 v25, v25, v133\n"
"v_mfma_f32_16x16x32_bf16 a[164:167], a[252:255], v[200:203], a[164:167]\n"
"v_cvt_pk_bf16_f32 v220, v24, v25\n"
"v_accvgpr_read_b32 v26, a114\n"
"v_mfma_f32_16x16x32_bf16 a[108:111], a[252:255], v[208:211], a[108:111]\n"
"v_mul_f32 v26, v26, v133\n"
"v_accvgpr_read_b32 v27, a115\n"
"v_mfma_f32_16x16x32_bf16 a[100:103], a[252:255], v[196:199], a[100:103]\n"
"v_mul_f32 v27, v27, v133\n"
"v_cvt_pk_bf16_f32 v221, v26, v27\n"
"v_mfma_f32_16x16x32_bf16 a[44:47], a[252:255], v[204:207], a[44:47]\n"
"v_accvgpr_read_b32 v24, a52\n"
"v_mul_f32 v24, v24, v132\n"
"v_mfma_f32_16x16x32_bf16 a[36:39], a[252:255], v[192:195], a[36:39]\n"
"ds_read_b64_tr_b16 a[252:253], v126 offset:30728\n"
"ds_read_b64_tr_b16 a[254:255], v126 offset:35848\n"
"s_add_u32 m0, s81, 14336\n"
"v_mfma_f32_16x16x32_bf16 a[48:51], v[32:35], v[144:147], 0\n"
"buffer_load_dwordx4 v122, s[52:55], s86 offen lds\n"
"v_accvgpr_read_b32 v28, a120\n"
"v_accvgpr_read_b32 v29, a121\n"
"v_accvgpr_read_b32 v30, a122\n"
"v_mfma_f32_16x16x32_bf16 a[56:59], v[32:35], v[156:159], 0\n"
"v_accvgpr_read_b32 v31, a123\n"
"v_accvgpr_read_b32 v25, a53\n"
"v_mul_f32 v25, v25, v132\n"
"v_mfma_f32_16x16x32_bf16 a[112:115], v[32:35], v[148:151], 0\n"
"v_cvt_pk_bf16_f32 v228, v24, v25\n"
"v_accvgpr_read_b32 v26, a54\n"
"v_mfma_f32_16x16x32_bf16 a[120:123], v[32:35], v[160:163], 0\n"
"v_mul_f32 v26, v26, v132\n"
"v_accvgpr_read_b32 v27, a55\n"
"v_mfma_f32_16x16x32_bf16 a[176:179], v[32:35], v[152:155], 0\n"
"v_mul_f32 v28, v28, v133\n"
"v_mul_f32 v29, v29, v133\n"
"v_cvt_pk_bf16_f32 v222, v28, v29\n"
"v_mul_f32 v30, v30, v133\n"
"v_mul_f32 v31, v31, v133\n"
"v_cvt_pk_bf16_f32 v223, v30, v31\n"
"v_mul_f32 v27, v27, v132\n"
"v_cvt_pk_bf16_f32 v229, v26, v27\n"
"v_accvgpr_read_b32 v28, a60\n"
"v_mul_f32 v28, v28, v132\n"
"v_accvgpr_read_b32 v29, a61\n"
"v_mul_f32 v29, v29, v132\n"
"v_cvt_pk_bf16_f32 v230, v28, v29\n"
"v_mfma_f32_16x16x32_bf16 a[184:187], v[32:35], v[164:167], 0\n"
"v_accvgpr_read_b32 v30, a62\n"
"v_mul_f32 v30, v30, v132\n"
"v_accvgpr_read_b32 v31, a63\n"
"v_mul_f32 v31, v31, v132\n"
"v_cvt_pk_bf16_f32 v231, v30, v31\n"
"v_accvgpr_read_b32 v24, a180\n"
"v_mul_f32 v24, v24, v136\n"
"v_accvgpr_read_b32 v25, a181\n"
"v_accvgpr_read_b32 v26, a182\n"
"v_accvgpr_read_b32 v27, a183\n"
"v_accvgpr_read_b32 v28, a188\n"
"v_accvgpr_read_b32 v29, a189\n"
"v_accvgpr_read_b32 v30, a190\n"
"v_accvgpr_read_b32 v31, a191\n"
"v_mfma_f32_16x16x32_bf16 a[188:191], v[36:39], v[164:167], 0\n"
"buffer_load_dword v12, v8, s[56:59], 0 offen sc1\n"
"v_mul_f32 v25, v25, v136\n"
"v_mfma_f32_16x16x32_bf16 a[180:183], v[36:39], v[152:155], 0\n"
"v_cvt_pk_bf16_f32 v236, v24, v25\n"
"v_mul_f32 v26, v26, v136\n"
"v_mfma_f32_16x16x32_bf16 a[124:127], v[36:39], v[160:163], 0\n"
"buffer_store_dwordx4 v[168:171], v13, s[56:59], s76 offen sc1\n"
"v_mul_f32 v27, v27, v136\n"
"v_mfma_f32_16x16x32_bf16 a[116:119], v[36:39], v[148:151], 0\n"
"v_cvt_pk_bf16_f32 v237, v26, v27\n"
"v_mul_f32 v28, v28, v136\n"
"v_mfma_f32_16x16x32_bf16 a[60:63], v[36:39], v[156:159], 0\n"
"buffer_store_dwordx4 v[172:175], v13, s[56:59], s76 offen offset:1024 sc1\n"
"v_mul_f32 v29, v29, v136\n"
"v_mfma_f32_16x16x32_bf16 a[52:55], v[36:39], v[144:147], 0\n"
"v_cvt_pk_bf16_f32 v238, v28, v29\n"
"v_mul_f32 v30, v30, v136\n"
"v_mfma_f32_16x16x32_bf16 a[48:51], v[40:43], v[168:171], a[48:51]\n"
"v_mul_f32 v31, v31, v136\n"
"v_cvt_pk_bf16_f32 v239, v30, v31\n"
"v_mfma_f32_16x16x32_bf16 a[56:59], v[40:43], v[180:183], a[56:59]\n"
"v_add_u32 v123, 0xa000, v123\n"
"v_add_u32 v124, 0xa000, v124\n"
"v_mfma_f32_16x16x32_bf16 a[112:115], v[40:43], v[172:175], a[112:115]\n"
"buffer_store_dwordx4 v[176:179], v13, s[56:59], s76 offen offset:2048 sc1\n"
"v_add_u32 v125, 0xa000, v125\n"
"v_mfma_f32_16x16x32_bf16 a[120:123], v[40:43], v[184:187], a[120:123]\n"
"v_add_u32 v126, 0xa000, v126\n"
"v_mfma_f32_16x16x32_bf16 a[176:179], v[40:43], v[176:179], a[176:179]\n"
"v_mfma_f32_16x16x32_bf16 a[184:187], v[40:43], v[188:191], a[184:187]\n"
"v_mfma_f32_16x16x32_bf16 a[188:191], v[44:47], v[188:191], a[188:191]\n"
"v_mfma_f32_16x16x32_bf16 a[180:183], v[44:47], v[176:179], a[180:183]\n"
"v_mfma_f32_16x16x32_bf16 a[124:127], v[44:47], v[184:187], a[124:127]\n"
"buffer_store_dwordx4 v[180:183], v13, s[56:59], s76 offen offset:3072 sc1\n"
"s_add_u32 s76, s87, 10240\n"
"v_mfma_f32_16x16x32_bf16 a[116:119], v[44:47], v[172:175], a[116:119]\n"
"v_mfma_f32_16x16x32_bf16 a[60:63], v[44:47], v[180:183], a[60:63]\n"
"v_mfma_f32_16x16x32_bf16 a[52:55], v[44:47], v[168:171], a[52:55]\n"
"s_waitcnt lgkmcnt(0)\n"
"v_mfma_f32_16x16x32_bf16 a[192:195], v[240:243], v[216:219], a[192:195]\n"
"buffer_store_dwordx4 v[184:187], v13, s[56:59], s76 offen sc1\n"
"s_add_u32 s86, s83, 81920\n"
"v_mfma_f32_16x16x32_bf16 a[200:203], v[240:243], v[228:231], a[200:203]\n"
"v_mfma_f32_16x16x32_bf16 a[208:211], v[240:243], v[220:223], a[208:211]\n"
"v_mfma_f32_16x16x32_bf16 a[216:219], v[240:243], v[232:235], a[216:219]\n"
"v_mfma_f32_16x16x32_bf16 a[224:227], v[240:243], v[224:227], a[224:227]\n"
"buffer_store_dwordx4 v[188:191], v13, s[56:59], s76 offen offset:1024 sc1\n"
"s_add_u32 s76, s87, 12288\n"
"v_mfma_f32_16x16x32_bf16 a[232:235], v[240:243], v[236:239], a[232:235]\n"
"ds_read_b64_tr_b16 v[240:241], v127 offset:30720\n"
"v_mfma_f32_16x16x32_bf16 a[136:139], v[244:247], v[236:239], a[136:139]\n"
"v_mfma_f32_16x16x32_bf16 a[128:131], v[244:247], v[224:227], a[128:131]\n"
"s_waitcnt vmcnt(6)\n"
"v_cmp_eq_u32 vcc, v12, v10\n"
"s_nop 0\n"
"s_and_b32 s92, vcc_lo, 0xe0\n"
"s_cmp_eq_u32 s92, 0xe0\n"
"s_cbranch_scc0 Lpolla_%=\n"
"Lpra_%=:\n"
"buffer_load_dword v11, v8, s[56:59], 0 offen offset:256 sc1\n"
"v_mfma_f32_16x16x32_bf16 a[72:75], v[244:247], v[232:235], a[72:75]\n"
"ds_read_b64_tr_b16 v[242:243], v127 offset:35840\n"
"s_add_u32 s78, s88, 0\n"
"v_mfma_f32_16x16x32_bf16 a[64:67], v[244:247], v[220:223], a[64:67]\n"
"v_mfma_f32_16x16x32_bf16 a[8:11], v[244:247], v[228:231], a[8:11]\n"
"v_mfma_f32_16x16x32_bf16 a[0:3], v[244:247], v[216:219], a[0:3]\n"
"ds_read_b64_tr_b16 v[244:245], v127 offset:30728\n"
"buffer_load_dwordx4 v[64:67], v13, s[56:59], s78 offen sc1\n"
"v_mfma_f32_16x16x32_bf16 a[16:19], v[248:251], v[216:219], a[16:19]\n"
"s_barrier\n"
"v_mfma_f32_16x16x32_bf16 a[24:27], v[248:251], v[228:231], a[24:27]\n"
"ds_read_b64_tr_b16 v[246:247], v127 offset:35848\n"
"buffer_load_dwordx4 v[68:71], v13, s[56:59], s78 offen offset:1024 sc1\n"
"v_mfma_f32_16x16x32_bf16 a[80:83], v[248:251], v[220:223], a[80:83]\n"
"v_add_u32 v127, 0xa000, v127\n"
"v_mfma_f32_16x16x32_bf16 a[88:91], v[248:251], v[232:235], a[88:91]\n"
"v_mfma_f32_16x16x32_bf16 a[144:147], v[248:251], v[224:227], a[144:147]\n"
"v_mfma_f32_16x16x32_bf16 a[152:155], v[248:251], v[236:239], a[152:155]\n"
"v_mfma_f32_16x16x32_bf16 a[168:171], v[252:255], v[236:239], a[168:171]\n"
"buffer_load_dwordx4 v[72:75], v13, s[56:59], s78 offen offset:2048 sc1\n"
"v_mfma_f32_16x16x32_bf16 a[160:163], v[252:255], v[224:227], a[160:163]\n"
"v_mfma_f32_16x16x32_bf16 a[104:107], v[252:255], v[232:235], a[104:107]\n"
"v_mfma_f32_16x16x32_bf16 a[96:99], v[252:255], v[220:223], a[96:99]\n"
"v_mfma_f32_16x16x32_bf16 a[40:43], v[252:255], v[228:231], a[40:43]\n"
"buffer_load_dwordx4 v[76:79], v13, s[56:59], s78 offen offset:3072 sc1\n"
"s_add_u32 s78, s88, 4096\n"
"v_mfma_f32_16x16x32_bf16 a[32:35], v[252:255], v[216:219], a[32:35]\n"
"v_mfma_f32_16x16x32_bf16 a[196:199], a[240:243], v[216:219], a[196:199]\n"
"v_mfma_f32_16x16x32_bf16 a[204:207], a[240:243], v[228:231], a[204:207]\n"
"v_mfma_f32_16x16x32_bf16 a[212:215], a[240:243], v[220:223], a[212:215]\n"
"buffer_load_dwordx4 v[80:83], v13, s[56:59], s78 offen sc1\n"
"v_mfma_f32_16x16x32_bf16 a[220:223], a[240:243], v[232:235], a[220:223]\n"
"v_mfma_f32_16x16x32_bf16 a[228:231], a[240:243], v[224:227], a[228:231]\n"
"v_mfma_f32_16x16x32_bf16 a[236:239], a[240:243], v[236:239], a[236:239]\n"
"v_mfma_f32_16x16x32_bf16 a[140:143], a[244:247], v[236:239], a[140:143]\n"
"buffer_load_dwordx4 v[84:87], v13, s[56:59], s78 offen offset:1024 sc1\n"
"v_mfma_f32_16x16x32_bf16 a[132:135], a[244:247], v[224:227], a[132:135]\n"
"v_mfma_f32_16x16x32_bf16 a[76:79], a[244:247], v[232:235], a[76:79]\n"
"v_mfma_f32_16x16x32_bf16 a[68:71], a[244:247], v[220:223], a[68:71]\n"
"s_add_u32 m0, s81, 20480\n"
"v_mfma_f32_16x16x32_bf16 a[12:15], a[244:247], v[228:231], a[12:15]\n"
"buffer_load_dwordx4 v120, s[52:55], s86 offen lds\n"
"s_add_u32 s78, s89, 0\n"
"v_mfma_f32_16x16x32_bf16 a[4:7], a[244:247], v[216:219], a[4:7]\n"
"v_mfma_f32_16x16x32_bf16 a[20:23], a[248:251], v[216:219], a[20:23]\n"
"v_mfma_f32_16x16x32_bf16 a[28:31], a[248:251], v[228:231], a[28:31]\n"
"v_mfma_f32_16x16x32_bf16 a[84:87], a[248:251], v[220:223], a[84:87]\n"
"v_mfma_f32_16x16x32_bf16 a[92:95], a[248:251], v[232:235], a[92:95]\n"
"v_mfma_f32_16x16x32_bf16 a[148:151], a[248:251], v[224:227], a[148:151]\n"
"buffer_load_dwordx4 v[88:91], v13, s[56:59], s78 offen sc1\n"
"v_mfma_f32_16x16x32_bf16 a[156:159], a[248:251], v[236:239], a[156:159]\n"
"v_mfma_f32_16x16x32_bf16 a[172:175], a[252:255], v[236:239], a[172:175]\n"
"v_mfma_f32_16x16x32_bf16 a[164:167], a[252:255], v[224:227], a[164:167]\n"
"v_mfma_f32_16x16x32_bf16 a[108:111], a[252:255], v[232:235], a[108:111]\n"
"buffer_load_dwordx4 v[92:95], v13, s[56:59], s78 offen offset:1024 sc1\n"
"v_mfma_f32_16x16x32_bf16 a[100:103], a[252:255], v[220:223], a[100:103]\n"
"v_mfma_f32_16x16x32_bf16 a[44:47], a[252:255], v[228:231], a[44:47]\n"
"v_mfma_f32_16x16x32_bf16 a[36:39], a[252:255], v[216:219], a[36:39]\n"
"v_mfma_f32_16x16x32_bf16 a[48:51], v[48:51], v[192:195], a[48:51]\n"
"buffer_load_dwordx4 v[96:99], v13, s[56:59], s78 offen offset:2048 sc1\n"
"v_mfma_f32_16x16x32_bf16 a[56:59], v[48:51], v[204:207], a[56:59]\n"
"v_mfma_f32_16x16x32_bf16 a[112:115], v[48:51], v[196:199], a[112:115]\n"
"v_mfma_f32_16x16x32_bf16 a[120:123], v[48:51], v[208:211], a[120:123]\n"
"v_mfma_f32_16x16x32_bf16 a[176:179], v[48:51], v[200:203], a[176:179]\n"
"buffer_load_dwordx4 v[100:103], v13, s[56:59], s78 offen offset:3072 sc1\n"
"s_add_u32 s78, s89, 4096\n"
"v_mfma_f32_16x16x32_bf16 a[184:187], v[48:51], v[212:215], a[184:187]\n"
"v_mfma_f32_16x16x32_bf16 a[188:191], v[52:55], v[212:215], a[188:191]\n"
"v_mfma_f32_16x16x32_bf16 a[180:183], v[52:55], v[200:203], a[180:183]\n"
"v_mfma_f32_16x16x32_bf16 a[124:127], v[52:55], v[208:211], a[124:127]\n"
"s_barrier\n"
"v_mfma_f32_16x16x32_bf16 a[116:119], v[52:55], v[196:199], a[116:119]\n"
"buffer_load_dwordx4 v[104:107], v13, s[56:59], s78 offen sc1\n"
"ds_read_b64_tr_b16 v[248:249], v123 offset:0\n"
"v_mfma_f32_16x16x32_bf16 a[60:63], v[52:55], v[204:207], a[60:63]\n"
"ds_read_b64_tr_b16 v[250:251], v123 offset:5120\n"
"v_mfma_f32_16x16x32_bf16 a[52:55], v[52:55], v[192:195], a[52:55]\n"
"ds_read_b64_tr_b16 v[252:253], v124 offset:0\n"
"s_waitcnt lgkmcnt(3)\n"
"v_mfma_f32_16x16x32_bf16 a[48:51], v[240:243], v[216:219], a[48:51]\n"
"ds_read_b64_tr_b16 v[254:255], v124 offset:5120\n"
"v_mfma_f32_16x16x32_bf16 a[56:59], v[240:243], v[228:231], a[56:59]\n"
"ds_read_b64_tr_b16 a[240:241], v125 offset:0\n"
"v_mfma_f32_16x16x32_bf16 a[112:115], v[240:243], v[220:223], a[112:115]\n"
"buffer_load_dwordx4 v[108:111], v13, s[56:59], s78 offen offset:1024 sc1\n"
"s_add_u32 s78, s90, 0\n"
"v_mfma_f32_16x16x32_bf16 a[120:123], v[240:243], v[232:235], a[120:123]\n"
"ds_read_b64_tr_b16 a[242:243], v125 offset:5120\n"
"v_mfma_f32_16x16x32_bf16 a[176:179], v[240:243], v[224:227], a[176:179]\n"
"ds_read_b64_tr_b16 a[244:245], v126 offset:0\n"
"v_mfma_f32_16x16x32_bf16 a[184:187], v[240:243], v[236:239], a[184:187]\n"
"ds_read_b64_tr_b16 a[246:247], v126 offset:5120\n"
"v_mfma_f32_16x16x32_bf16 a[188:191], v[244:247], v[236:239], a[188:191]\n"
"buffer_load_dwordx4 v[40:43], v13, s[56:59], s78 offen sc1\n"
"v_mfma_f32_16x16x32_bf16 a[180:183], v[244:247], v[224:227], a[180:183]\n"
"ds_read_b64_tr_b16 a[248:249], v127 offset:0\n"
"v_mfma_f32_16x16x32_bf16 a[124:127], v[244:247], v[232:235], a[124:127]\n"
"ds_read_b64_tr_b16 a[250:251], v127 offset:5120\n"
"v_mfma_f32_16x16x32_bf16 a[116:119], v[244:247], v[220:223], a[116:119]\n"
"ds_read_b64_tr_b16 a[252:253], v123 offset:8\n"
"v_mfma_f32_16x16x32_bf16 a[60:63], v[244:247], v[228:231], a[60:63]\n"
"buffer_load_dwordx4 v[44:47], v13, s[56:59], s78 offen offset:1024 sc1\n"
"v_mfma_f32_16x16x32_bf16 a[52:55], v[244:247], v[216:219], a[52:55]\n"
"ds_read_b64_tr_b16 a[254:255], v123 offset:5128\n"
"s_waitcnt vmcnt(14) lgkmcnt(4)\n"
"v_mfma_f32_16x16x32_bf16 a[192:195], v[248:251], v[64:67], a[192:195]\n"
"ds_read_b64_tr_b16 v[240:241], v124 offset:8\n"
"s_waitcnt vmcnt(11)\n"
"v_mfma_f32_16x16x32_bf16 a[200:203], v[248:251], v[76:79], a[200:203]\n"
"ds_read_b64_tr_b16 v[242:243], v124 offset:5128\n"
"v_mfma_f32_16x16x32_bf16 a[208:211], v[248:251], v[68:71], a[208:211]\n"
"buffer_load_dwordx4 v[48:51], v13, s[56:59], s78 offen offset:2048 sc1\n"
"s_waitcnt vmcnt(11)\n"
"v_mfma_f32_16x16x32_bf16 a[216:219], v[248:251], v[80:83], a[216:219]\n"
"ds_read_b64_tr_b16 v[244:245], v125 offset:8\n"
"v_mfma_f32_16x16x32_bf16 a[224:227], v[248:251], v[72:75], a[224:227]\n"
"ds_read_b64_tr_b16 v[246:247], v125 offset:5128\n"
"s_waitcnt vmcnt(10)\n"
"v_mfma_f32_16x16x32_bf16 a[232:235], v[248:251], v[84:87], a[232:235]\n"
"ds_read_b64_tr_b16 v[248:249], v126 offset:8\n"
"v_mfma_f32_16x16x32_bf16 a[136:139], v[252:255], v[84:87], a[136:139]\n"
"buffer_load_dwordx4 v[52:55], v13, s[56:59], s78 offen offset:3072 sc1\n"
"s_add_u32 s78, s90, 4096\n"
"v_mfma_f32_16x16x32_bf16 a[128:131], v[252:255], v[72:75], a[128:131]\n"
"ds_read_b64_tr_b16 v[250:251], v126 offset:5128\n"
"v_mfma_f32_16x16x32_bf16 a[72:75], v[252:255], v[80:83], a[72:75]\n"
"v_mfma_f32_16x16x32_bf16 a[64:67], v[252:255], v[68:71], a[64:67]\n"
"v_mfma_f32_16x16x32_bf16 a[8:11], v[252:255], v[76:79], a[8:11]\n"
"v_mfma_f32_16x16x32_bf16 a[0:3], v[252:255], v[64:67], a[0:3]\n"
"ds_read_b64_tr_b16 v[252:253], v127 offset:8\n"
"ds_read_b64_tr_b16 v[254:255], v127 offset:5128\n"
"v_mfma_f32_16x16x32_bf16 a[16:19], a[240:243], v[64:67], a[16:19]\n"
"buffer_load_dwordx4 v[56:59], v13, s[56:59], s78 offen sc1\n"
"v_mfma_f32_16x16x32_bf16 a[24:27], a[240:243], v[76:79], a[24:27]\n"
"v_mfma_f32_16x16x32_bf16 a[80:83], a[240:243], v[68:71], a[80:83]\n"
"v_mfma_f32_16x16x32_bf16 a[88:91], a[240:243], v[80:83], a[88:91]\n"
"v_mfma_f32_16x16x32_bf16 a[144:147], a[240:243], v[72:75], a[144:147]\n"
"buffer_load_dwordx4 v[60:63], v13, s[56:59], s78 offen offset:1024 sc1\n"
"v_mfma_f32_16x16x32_bf16 a[152:155], a[240:243], v[84:87], a[152:155]\n"
"v_mfma_f32_16x16x32_bf16 a[168:171], a[244:247], v[84:87], a[168:171]\n"
"v_mfma_f32_16x16x32_bf16 a[160:163], a[244:247], v[72:75], a[160:163]\n"
"s_add_u32 m0, s81, 21376\n"
"v_mfma_f32_16x16x32_bf16 a[104:107], a[244:247], v[80:83], a[104:107]\n"
"buffer_load_dwordx4 v120, s[52:55], s86 offen offset:128 lds\n"
"v_mfma_f32_16x16x32_bf16 a[96:99], a[244:247], v[68:71], a[96:99]\n"
"v_mfma_f32_16x16x32_bf16 a[40:43], a[244:247], v[76:79], a[40:43]\n"
"v_mfma_f32_16x16x32_bf16 a[32:35], a[244:247], v[64:67], a[32:35]\n"
"s_mov_b32 exec_lo, 0xee0\n"
"s_mov_b32 exec_hi, 0x0\n"
"buffer_store_dword v11, v9, s[56:59], 0 offen sc1\n"
"s_mov_b64 exec, -1\n"
"s_add_u32 m0, s81, 22528\n"
"s_waitcnt lgkmcnt(0)\n"
"v_mfma_f32_16x16x32_bf16 a[48:51], a[248:251], v[64:67], a[48:51]\n"
"buffer_load_dwordx4 v121, s[52:55], s86 offen lds\n"
"v_mfma_f32_16x16x32_bf16 a[56:59], a[248:251], v[76:79], a[56:59]\n"
"v_mfma_f32_16x16x32_bf16 a[112:115], a[248:251], v[68:71], a[112:115]\n"
"v_mfma_f32_16x16x32_bf16 a[120:123], a[248:251], v[80:83], a[120:123]\n"
"s_add_u32 m0, s81, 23424\n"
"v_mfma_f32_16x16x32_bf16 a[176:179], a[248:251], v[72:75], a[176:179]\n"
"buffer_load_dwordx4 v121, s[52:55], s86 offen offset:128 lds\n"
"v_mfma_f32_16x16x32_bf16 a[184:187], a[248:251], v[84:87], a[184:187]\n"
"v_mfma_f32_16x16x32_bf16 a[236:239], a[252:255], v[84:87], a[236:239]\n"
"v_mfma_f32_16x16x32_bf16 a[228:231], a[252:255], v[72:75], a[228:231]\n"
"v_mfma_f32_16x16x32_bf16 a[220:223], a[252:255], v[80:83], a[220:223]\n"
"v_mfma_f32_16x16x32_bf16 a[212:215], a[252:255], v[68:71], a[212:215]\n"
"v_mfma_f32_16x16x32_bf16 a[204:207], a[252:255], v[76:79], a[204:207]\n"
"v_mfma_f32_16x16x32_bf16 a[196:199], a[252:255], v[64:67], a[196:199]\n"
"v_mfma_f32_16x16x32_bf16 a[4:7], v[240:243], v[64:67], a[4:7]\n"
"v_mfma_f32_16x16x32_bf16 a[12:15], v[240:243], v[76:79], a[12:15]\n"
"s_add_u32 m0, s81, 24576\n"
"v_mfma_f32_16x16x32_bf16 a[68:71], v[240:243], v[68:71], a[68:71]\n"
"buffer_load_dwordx4 v122, s[52:55], s86 offen lds\n"
"s_add_u32 s86, s84, 81920\n"
"v_mfma_f32_16x16x32_bf16 a[76:79], v[240:243], v[80:83], a[76:79]\n"
"v_mfma_f32_16x16x32_bf16 a[132:135], v[240:243], v[72:75], a[132:135]\n"
"v_mfma_f32_16x16x32_bf16 a[140:143], v[240:243], v[84:87], a[140:143]\n"
"v_mfma_f32_16x16x32_bf16 a[156:159], v[244:247], v[84:87], a[156:159]\n"
"v_mfma_f32_16x16x32_bf16 a[148:151], v[244:247], v[72:75], a[148:151]\n"
"v_mfma_f32_16x16x32_bf16 a[92:95], v[244:247], v[80:83], a[92:95]\n"
"s_barrier\n"
"v_mfma_f32_16x16x32_bf16 a[84:87], v[244:247], v[68:71], a[84:87]\n"
"ds_read_b64_tr_b16 a[240:241], v123 offset:10240\n"
"ds_read_b64_tr_b16 a[242:243], v123 offset:15360\n"
"v_mfma_f32_16x16x32_bf16 a[28:31], v[244:247], v[76:79], a[28:31]\n"
"ds_read_b64_tr_b16 a[244:245], v124 offset:10240\n"
"v_mfma_f32_16x16x32_bf16 a[20:23], v[244:247], v[64:67], a[20:23]\n"
"ds_read_b64_tr_b16 a[246:247], v124 offset:15360\n"
"v_mfma_f32_16x16x32_bf16 a[36:39], v[248:251], v[64:67], a[36:39]\n"
"ds_read_b64_tr_b16 a[248:249], v125 offset:10240\n"
"v_mfma_f32_16x16x32_bf16 a[44:47], v[248:251], v[76:79], a[44:47]\n"
"ds_read_b64_tr_b16 a[250:251], v125 offset:15360\n"
"v_mfma_f32_16x16x32_bf16 a[100:103], v[248:251], v[68:71], a[100:103]\n"
"ds_read_b64_tr_b16 a[252:253], v126 offset:10240\n"
"v_mfma_f32_16x16x32_bf16 a[108:111], v[248:251], v[80:83], a[108:111]\n"
"ds_read_b64_tr_b16 a[254:255], v126 offset:15360\n"
"v_mfma_f32_16x16x32_bf16 a[164:167], v[248:251], v[72:75], a[164:167]\n"
"ds_read_b64_tr_b16 v[240:241], v127 offset:10240\n"
"v_mfma_f32_16x16x32_bf16 a[172:175], v[248:251], v[84:87], a[172:175]\n"
"ds_read_b64_tr_b16 v[242:243], v127 offset:15360\n"
"v_mfma_f32_16x16x32_bf16 a[188:191], v[252:255], v[84:87], a[188:191]\n"
"ds_read_b64_tr_b16 v[244:245], v123 offset:10248\n"
"v_mfma_f32_16x16x32_bf16 a[180:183], v[252:255], v[72:75], a[180:183]\n"
"ds_read_b64_tr_b16 v[246:247], v123 offset:15368\n"
"v_mfma_f32_16x16x32_bf16 a[124:127], v[252:255], v[80:83], a[124:127]\n"
"ds_read_b64_tr_b16 v[248:249], v124 offset:10248\n"
"v_mfma_f32_16x16x32_bf16 a[116:119], v[252:255], v[68:71], a[116:119]\n"
"ds_read_b64_tr_b16 v[250:251], v124 offset:15368\n"
"v_mfma_f32_16x16x32_bf16 a[60:63], v[252:255], v[76:79], a[60:63]\n"
"v_mfma_f32_16x16x32_bf16 a[52:55], v[252:255], v[64:67], a[52:55]\n"
"ds_read_b64_tr_b16 v[252:253], v125 offset:10248\n"
"ds_read_b64_tr_b16 v[254:255], v125 offset:15368\n"
"s_add_u32 m0, s81, 30720\n"
"s_waitcnt vmcnt(16) lgkmcnt(6)\n"
"v_mfma_f32_16x16x32_bf16 a[192:195], a[240:243], v[88:91], a[192:195]\n"
"buffer_load_dwordx4 v120, s[52:55], s86 offen lds\n"
"s_waitcnt vmcnt(14)\n"
"v_mfma_f32_16x16x32_bf16 a[200:203], a[240:243], v[100:103], a[200:203]\n"
"s_add_u32 m0, s81, 31616\n"
"v_mfma_f32_16x16x32_bf16 a[208:211], a[240:243], v[92:95], a[208:211]\n"
"buffer_load_dwordx4 v120, s[52:55], s86 offen offset:128 lds\n"
"s_waitcnt vmcnt(14)\n"
"v_mfma_f32_16x16x32_bf16 a[216:219], a[240:243], v[104:107], a[216:219]\n"
"v_mfma_f32_16x16x32_bf16 a[224:227], a[240:243], v[96:99], a[224:227]\n"
"s_waitcnt vmcnt(13)\n"
"v_mfma_f32_16x16x32_bf16 a[232:235], a[240:243], v[108:111], a[232:235]\n"
"ds_read_b64_tr_b16 a[240:241], v126 offset:10248\n"
"ds_read_b64_tr_b16 a[242:243], v126 offset:15368\n"
"s_add_u32 m0, s81, 32768\n"
"v_mfma_f32_16x16x32_bf16 a[136:139], a[244:247], v[108:111], a[136:139]\n"
"buffer_load_dwordx4 v121, s[52:55], s86 offen lds\n"
"v_mfma_f32_16x16x32_bf16 a[128:131], a[244:247], v[96:99], a[128:131]\n"
"s_add_u32 m0, s81, 33664\n"
"v_mfma_f32_16x16x32_bf16 a[72:75], a[244:247], v[104:107], a[72:75]\n"
"buffer_load_dwordx4 v121, s[52:55], s86 offen offset:128 lds\n"
"v_mfma_f32_16x16x32_bf16 a[64:67], a[244:247], v[92:95], a[64:67]\n"
"v_mfma_f32_16x16x32_bf16 a[8:11], a[244:247], v[100:103], a[8:11]\n"
"v_mfma_f32_16x16x32_bf16 a[0:3], a[244:247], v[88:91], a[0:3]\n"
"ds_read_b64_tr_b16 a[244:245], v127 offset:10248\n"
"ds_read_b64_tr_b16 a[246:247], v127 offset:15368\n"
"v_mfma_f32_16x16x32_bf16 a[16:19], a[248:251], v[88:91], a[16:19]\n"
"buffer_load_dword v12, v8, s[56:59], 0 offen sc1\n"
"v_mfma_f32_16x16x32_bf16 a[24:27], a[248:251], v[100:103], a[24:27]\n"
"v_mfma_f32_16x16x32_bf16 a[80:83], a[248:251], v[92:95], a[80:83]\n"
"buffer_store_dwordx4 v[192:195], v13, s[56:59], s76 offen sc1\n"
"v_mfma_f32_16x16x32_bf16 a[88:91], a[248:251], v[104:107], a[88:91]\n"
"v_mfma_f32_16x16x32_bf16 a[144:147], a[248:251], v[96:99], a[144:147]\n"
"buffer_store_dwordx4 v[196:199], v13, s[56:59], s76 offen offset:1024 sc1\n"
"v_mfma_f32_16x16x32_bf16 a[152:155], a[248:251], v[108:111], a[152:155]\n"
"v_mfma_f32_16x16x32_bf16 a[168:171], a[252:255], v[108:111], a[168:171]\n"
"v_mfma_f32_16x16x32_bf16 a[160:163], a[252:255], v[96:99], a[160:163]\n"
"v_mfma_f32_16x16x32_bf16 a[104:107], a[252:255], v[104:107], a[104:107]\n"
"buffer_store_dwordx4 v[200:203], v13, s[56:59], s76 offen offset:2048 sc1\n"
"v_mfma_f32_16x16x32_bf16 a[96:99], a[252:255], v[92:95], a[96:99]\n"
"v_mfma_f32_16x16x32_bf16 a[40:43], a[252:255], v[100:103], a[40:43]\n"
"v_mfma_f32_16x16x32_bf16 a[32:35], a[252:255], v[88:91], a[32:35]\n"
"v_mfma_f32_16x16x32_bf16 a[48:51], v[240:243], v[88:91], a[48:51]\n"
"buffer_store_dwordx4 v[204:207], v13, s[56:59], s76 offen offset:3072 sc1\n"
"s_add_u32 s76, s87, 16384\n"
"v_mfma_f32_16x16x32_bf16 a[56:59], v[240:243], v[100:103], a[56:59]\n"
"v_mfma_f32_16x16x32_bf16 a[112:115], v[240:243], v[92:95], a[112:115]\n"
"v_mfma_f32_16x16x32_bf16 a[120:123], v[240:243], v[104:107], a[120:123]\n"
"v_mfma_f32_16x16x32_bf16 a[176:179], v[240:243], v[96:99], a[176:179]\n"
"buffer_store_dwordx4 v[208:211], v13, s[56:59], s76 offen sc1\n"
"v_mfma_f32_16x16x32_bf16 a[184:187], v[240:243], v[108:111], a[184:187]\n"
"s_waitcnt lgkmcnt(0)\n"
"v_mfma_f32_16x16x32_bf16 a[236:239], v[244:247], v[108:111], a[236:239]\n"
"v_mfma_f32_16x16x32_bf16 a[228:231], v[244:247], v[96:99], a[228:231]\n"
"v_mfma_f32_16x16x32_bf16 a[220:223], v[244:247], v[104:107], a[220:223]\n"
"buffer_store_dwordx4 v[212:215], v13, s[56:59], s76 offen offset:1024 sc1\n"
"s_add_u32 s76, s87, 18432\n"
"v_mfma_f32_16x16x32_bf16 a[212:215], v[244:247], v[92:95], a[212:215]\n"
"v_mfma_f32_16x16x32_bf16 a[204:207], v[244:247], v[100:103], a[204:207]\n"
"v_mfma_f32_16x16x32_bf16 a[196:199], v[244:247], v[88:91], a[196:199]\n"
"v_mfma_f32_16x16x32_bf16 a[4:7], v[248:251], v[88:91], a[4:7]\n"
"v_mfma_f32_16x16x32_bf16 a[12:15], v[248:251], v[100:103], a[12:15]\n"
"s_waitcnt vmcnt(6)\n"
"v_cmp_eq_u32 vcc, v12, v10\n"
"s_nop 0\n"
"s_and_b32 s92, vcc_lo, 0xe00\n"
"s_cmp_eq_u32 s92, 0xe00\n"
"s_cbranch_scc0 Lpollb_%=\n"
"Lprb_%=:\n"
"v_mfma_f32_16x16x32_bf16 a[68:71], v[248:251], v[92:95], a[68:71]\n"
"s_add_u32 s78, s88, 6144\n"
"v_mfma_f32_16x16x32_bf16 a[76:79], v[248:251], v[104:107], a[76:79]\n"
"v_mfma_f32_16x16x32_bf16 a[132:135], v[248:251], v[96:99], a[132:135]\n"
"buffer_load_dwordx4 v[16:19], v13, s[56:59], s78 offen sc1\n"
"v_mfma_f32_16x16x32_bf16 a[140:143], v[248:251], v[108:111], a[140:143]\n"
"v_mfma_f32_16x16x32_bf16 a[156:159], v[252:255], v[108:111], a[156:159]\n"
"buffer_load_dwordx4 v[20:23], v13, s[56:59], s78 offen offset:1024 sc1\n"
"v_mfma_f32_16x16x32_bf16 a[148:151], v[252:255], v[96:99], a[148:151]\n"
"v_mfma_f32_16x16x32_bf16 a[92:95], v[252:255], v[104:107], a[92:95]\n"
"s_barrier\n"
"v_mfma_f32_16x16x32_bf16 a[84:87], v[252:255], v[92:95], a[84:87]\n"
"ds_read_b64_tr_b16 a[248:249], v123 offset:20480\n"
"ds_read_b64_tr_b16 a[250:251], v123 offset:25600\n"
"v_mfma_f32_16x16x32_bf16 a[28:31], v[252:255], v[100:103], a[28:31]\n"
"buffer_load_dwordx4 v[24:27], v13, s[56:59], s78 offen offset:2048 sc1\n"
"v_mfma_f32_16x16x32_bf16 a[20:23], v[252:255], v[88:91], a[20:23]\n"
"ds_read_b64_tr_b16 a[252:253], v124 offset:20480\n"
"v_mfma_f32_16x16x32_bf16 a[36:39], a[240:243], v[88:91], a[36:39]\n"
"ds_read_b64_tr_b16 a[254:255], v124 offset:25600\n"
"v_mfma_f32_16x16x32_bf16 a[44:47], a[240:243], v[100:103], a[44:47]\n"
"ds_read_b64_tr_b16 v[240:241], v125 offset:20480\n"
"v_mfma_f32_16x16x32_bf16 a[100:103], a[240:243], v[92:95], a[100:103]\n"
"buffer_load_dwordx4 v[28:31], v13, s[56:59], s78 offen offset:3072 sc1\n"
"s_add_u32 s78, s88, 10240\n"
"v_mfma_f32_16x16x32_bf16 a[108:111], a[240:243], v[104:107], a[108:111]\n"
"ds_read_b64_tr_b16 v[242:243], v125 offset:25600\n"
"v_mfma_f32_16x16x32_bf16 a[164:167], a[240:243], v[96:99], a[164:167]\n"
"ds_read_b64_tr_b16 v[244:245], v126 offset:20480\n"
"v_mfma_f32_16x16x32_bf16 a[172:175], a[240:243], v[108:111], a[172:175]\n"
"ds_read_b64_tr_b16 v[246:247], v126 offset:25600\n"
"v_mfma_f32_16x16x32_bf16 a[188:191], a[244:247], v[108:111], a[188:191]\n"
"buffer_load_dwordx4 v[32:35], v13, s[56:59], s78 offen sc1\n"
"v_mfma_f32_16x16x32_bf16 a[180:183], a[244:247], v[96:99], a[180:183]\n"
"ds_read_b64_tr_b16 v[248:249], v127 offset:20480\n"
"v_mfma_f32_16x16x32_bf16 a[124:127], a[244:247], v[104:107], a[124:127]\n"
"ds_read_b64_tr_b16 v[250:251], v127 offset:25600\n"
"v_mfma_f32_16x16x32_bf16 a[116:119], a[244:247], v[92:95], a[116:119]\n"
"ds_read_b64_tr_b16 v[252:253], v123 offset:20488\n"
"v_mfma_f32_16x16x32_bf16 a[60:63], a[244:247], v[100:103], a[60:63]\n"
"ds_read_b64_tr_b16 v[254:255], v123 offset:25608\n"
"v_mfma_f32_16x16x32_bf16 a[52:55], a[244:247], v[88:91], a[52:55]\n"
"ds_read_b64_tr_b16 a[240:241], v124 offset:20488\n"
"s_waitcnt lgkmcnt(5)\n"
"v_mfma_f32_16x16x32_bf16 a[192:195], a[248:251], v[40:43], a[192:195]\n"
"buffer_load_dwordx4 v[36:39], v13, s[56:59], s78 offen offset:1024 sc1\n"
"v_mfma_f32_16x16x32_bf16 a[200:203], a[248:251], v[52:55], a[200:203]\n"
"ds_read_b64_tr_b16 a[242:243], v124 offset:25608\n"
"v_mfma_f32_16x16x32_bf16 a[208:211], a[248:251], v[44:47], a[208:211]\n"
"ds_read_b64_tr_b16 a[244:245], v125 offset:20488\n"
"v_mfma_f32_16x16x32_bf16 a[216:219], a[248:251], v[56:59], a[216:219]\n"
"ds_read_b64_tr_b16 a[246:247], v125 offset:25608\n"
"v_mfma_f32_16x16x32_bf16 a[224:227], a[248:251], v[48:51], a[224:227]\n"
"s_add_u32 s78, s89, 6144\n"
"v_mfma_f32_16x16x32_bf16 a[232:235], a[248:251], v[60:63], a[232:235]\n"
"ds_read_b64_tr_b16 a[248:249], v126 offset:20488\n"
"ds_read_b64_tr_b16 a[250:251], v126 offset:25608\n"
"v_mfma_f32_16x16x32_bf16 a[136:139], a[252:255], v[60:63], a[136:139]\n"
"buffer_load_dwordx4 v[144:147], v13, s[56:59], s78 offen sc1\n"
"v_mfma_f32_16x16x32_bf16 a[128:131], a[252:255], v[48:51], a[128:131]\n"
"v_mfma_f32_16x16x32_bf16 a[72:75], a[252:255], v[56:59], a[72:75]\n"
"buffer_load_dwordx4 v[148:151], v13, s[56:59], s78 offen offset:1024 sc1\n"
"v_mfma_f32_16x16x32_bf16 a[64:67], a[252:255], v[44:47], a[64:67]\n"
"v_mfma_f32_16x16x32_bf16 a[8:11], a[252:255], v[52:55], a[8:11]\n"
"v_mfma_f32_16x16x32_bf16 a[0:3], a[252:255], v[40:43], a[0:3]\n"
"ds_read_b64_tr_b16 a[252:253], v127 offset:20488\n"
"ds_read_b64_tr_b16 a[254:255], v127 offset:25608\n"
"v_mfma_f32_16x16x32_bf16 a[16:19], v[240:243], v[40:43], a[16:19]\n"
"buffer_load_dwordx4 v[152:155], v13, s[56:59], s78 offen offset:2048 sc1\n"
"v_mfma_f32_16x16x32_bf16 a[24:27], v[240:243], v[52:55], a[24:27]\n"
"v_mfma_f32_16x16x32_bf16 a[80:83], v[240:243], v[44:47], a[80:83]\n"
"v_mfma_f32_16x16x32_bf16 a[88:91], v[240:243], v[56:59], a[88:91]\n"
"v_mfma_f32_16x16x32_bf16 a[144:147], v[240:243], v[48:51], a[144:147]\n"
"buffer_load_dwordx4 v[156:159], v13, s[56:59], s78 offen offset:3072 sc1\n"
"s_add_u32 s78, s89, 10240\n"
"v_mfma_f32_16x16x32_bf16 a[152:155], v[240:243], v[60:63], a[152:155]\n"
"v_mfma_f32_16x16x32_bf16 a[168:171], v[244:247], v[60:63], a[168:171]\n"
"v_mfma_f32_16x16x32_bf16 a[160:163], v[244:247], v[48:51], a[160:163]\n"
"v_mfma_f32_16x16x32_bf16 a[104:107], v[244:247], v[56:59], a[104:107]\n"
"v_mfma_f32_16x16x32_bf16 a[96:99], v[244:247], v[44:47], a[96:99]\n"
"v_mfma_f32_16x16x32_bf16 a[40:43], v[244:247], v[52:55], a[40:43]\n"
"buffer_load_dwordx4 v[160:163], v13, s[56:59], s78 offen sc1\n"
"v_mfma_f32_16x16x32_bf16 a[32:35], v[244:247], v[40:43], a[32:35]\n"
"s_waitcnt lgkmcnt(0)\n"
"v_mfma_f32_16x16x32_bf16 a[48:51], v[248:251], v[40:43], a[48:51]\n"
"v_mfma_f32_16x16x32_bf16 a[56:59], v[248:251], v[52:55], a[56:59]\n"
"v_mfma_f32_16x16x32_bf16 a[112:115], v[248:251], v[44:47], a[112:115]\n"
"buffer_load_dwordx4 v[164:167], v13, s[56:59], s78 offen offset:1024 sc1\n"
"v_mfma_f32_16x16x32_bf16 a[120:123], v[248:251], v[56:59], a[120:123]\n"
"v_mfma_f32_16x16x32_bf16 a[176:179], v[248:251], v[48:51], a[176:179]\n"
"v_mfma_f32_16x16x32_bf16 a[184:187], v[248:251], v[60:63], a[184:187]\n"
"v_mfma_f32_16x16x32_bf16 a[236:239], v[252:255], v[60:63], a[236:239]\n"
"s_add_u32 s78, s90, 6144\n"
"v_mfma_f32_16x16x32_bf16 a[228:231], v[252:255], v[48:51], a[228:231]\n"
"v_mfma_f32_16x16x32_bf16 a[220:223], v[252:255], v[56:59], a[220:223]\n"
"buffer_load_dwordx4 v[168:171], v13, s[56:59], s78 offen sc1\n"
"v_mfma_f32_16x16x32_bf16 a[212:215], v[252:255], v[44:47], a[212:215]\n"
"v_mfma_f32_16x16x32_bf16 a[204:207], v[252:255], v[52:55], a[204:207]\n"
"buffer_load_dwordx4 v[172:175], v13, s[56:59], s78 offen offset:1024 sc1\n"
"v_mfma_f32_16x16x32_bf16 a[196:199], v[252:255], v[40:43], a[196:199]\n"
"v_mfma_f32_16x16x32_bf16 a[4:7], a[240:243], v[40:43], a[4:7]\n"
"v_mfma_f32_16x16x32_bf16 a[12:15], a[240:243], v[52:55], a[12:15]\n"
"v_mfma_f32_16x16x32_bf16 a[68:71], a[240:243], v[44:47], a[68:71]\n"
"buffer_load_dwordx4 v[176:179], v13, s[56:59], s78 offen offset:2048 sc1\n"
"v_mfma_f32_16x16x32_bf16 a[76:79], a[240:243], v[56:59], a[76:79]\n"
"v_mfma_f32_16x16x32_bf16 a[132:135], a[240:243], v[48:51], a[132:135]\n"
"v_mfma_f32_16x16x32_bf16 a[140:143], a[240:243], v[60:63], a[140:143]\n"
"v_mfma_f32_16x16x32_bf16 a[156:159], a[244:247], v[60:63], a[156:159]\n"
"v_mfma_f32_16x16x32_bf16 a[148:151], a[244:247], v[48:51], a[148:151]\n"
"v_mfma_f32_16x16x32_bf16 a[92:95], a[244:247], v[56:59], a[92:95]\n"
"s_barrier\n"
"v_mfma_f32_16x16x32_bf16 a[84:87], a[244:247], v[44:47], a[84:87]\n"
"buffer_load_dwordx4 v[180:183], v13, s[56:59], s78 offen offset:3072 sc1\n"
"s_add_u32 s78, s90, 10240\n"
"v_mfma_f32_16x16x32_bf16 a[28:31], a[244:247], v[52:55], a[28:31]\n"
"ds_read_b64_tr_b16 v[240:241], v123 offset:30720\n"
"v_mfma_f32_16x16x32_bf16 a[20:23], a[244:247], v[40:43], a[20:23]\n"
"ds_read_b64_tr_b16 v[242:243], v123 offset:35840\n"
"v_mfma_f32_16x16x32_bf16 a[36:39], a[248:251], v[40:43], a[36:39]\n"
"buffer_load_dwordx4 v[184:187], v13, s[56:59], s78 offen sc1\n"
"v_mfma_f32_16x16x32_bf16 a[44:47], a[248:251], v[52:55], a[44:47]\n"
"ds_read_b64_tr_b16 v[244:245], v124 offset:30720\n"
"v_mfma_f32_16x16x32_bf16 a[100:103], a[248:251], v[44:47], a[100:103]\n"
"ds_read_b64_tr_b16 v[246:247], v124 offset:35840\n"
"v_mfma_f32_16x16x32_bf16 a[108:111], a[248:251], v[56:59], a[108:111]\n"
"ds_read_b64_tr_b16 v[248:249], v125 offset:30720\n"
"v_mfma_f32_16x16x32_bf16 a[164:167], a[248:251], v[48:51], a[164:167]\n"
"buffer_load_dwordx4 v[188:191], v13, s[56:59], s78 offen offset:1024 sc1\n"
"v_mfma_f32_16x16x32_bf16 a[172:175], a[248:251], v[60:63], a[172:175]\n"
"ds_read_b64_tr_b16 v[250:251], v125 offset:35840\n"
"v_mfma_f32_16x16x32_bf16 a[188:191], a[252:255], v[60:63], a[188:191]\n"
"ds_read_b64_tr_b16 v[252:253], v126 offset:30720\n"
"v_mfma_f32_16x16x32_bf16 a[180:183], a[252:255], v[48:51], a[180:183]\n"
"ds_read_b64_tr_b16 v[254:255], v126 offset:35840\n"
"v_mfma_f32_16x16x32_bf16 a[124:127], a[252:255], v[56:59], a[124:127]\n"
"ds_read_b64_tr_b16 a[240:241], v127 offset:30720\n"
"v_mfma_f32_16x16x32_bf16 a[116:119], a[252:255], v[44:47], a[116:119]\n"
"ds_read_b64_tr_b16 a[242:243], v127 offset:35840\n"
"v_mfma_f32_16x16x32_bf16 a[60:63], a[252:255], v[52:55], a[60:63]\n"
"ds_read_b64_tr_b16 a[244:245], v123 offset:30728\n"
"v_mfma_f32_16x16x32_bf16 a[52:55], a[252:255], v[40:43], a[52:55]\n"
"ds_read_b64_tr_b16 a[246:247], v123 offset:35848\n"
"s_waitcnt vmcnt(17) lgkmcnt(6)\n"
"v_mfma_f32_16x16x32_bf16 a[192:195], v[240:243], v[16:19], a[192:195]\n"
"ds_read_b64_tr_b16 a[248:249], v124 offset:30728\n"
"v_add_u32 v123, 0xffff6000, v123\n"
"s_waitcnt vmcnt(14)\n"
"v_mfma_f32_16x16x32_bf16 a[200:203], v[240:243], v[28:31], a[200:203]\n"
"ds_read_b64_tr_b16 a[250:251], v124 offset:35848\n"
"v_mfma_f32_16x16x32_bf16 a[208:211], v[240:243], v[20:23], a[208:211]\n"
"ds_read_b64_tr_b16 a[252:253], v125 offset:30728\n"
"v_add_u32 v124, 0xffff6000, v124\n"
"s_waitcnt vmcnt(13)\n"
"v_mfma_f32_16x16x32_bf16 a[216:219], v[240:243], v[32:35], a[216:219]\n"
"ds_read_b64_tr_b16 a[254:255], v125 offset:35848\n"
"v_mfma_f32_16x16x32_bf16 a[224:227], v[240:243], v[24:27], a[224:227]\n"
"s_waitcnt vmcnt(12)\n"
"v_mfma_f32_16x16x32_bf16 a[232:235], v[240:243], v[36:39], a[232:235]\n"
"s_mov_b32 exec_lo, 0xe000\n"
"s_mov_b32 exec_hi, 0x0\n"
"buffer_store_dword v11, v9, s[56:59], 0 offen sc1\n"
"s_mov_b64 exec, -1\n"
"v_mfma_f32_16x16x32_bf16 a[136:139], v[244:247], v[36:39], a[136:139]\n"
"buffer_store_dwordx4 v[216:219], v13, s[56:59], s76 offen sc1\n"
"v_add_u32 v125, 0xffff6000, v125\n"
"v_mfma_f32_16x16x32_bf16 a[128:131], v[244:247], v[24:27], a[128:131]\n"
"ds_read_b64_tr_b16 v[240:241], v126 offset:30728\n"
"v_mfma_f32_16x16x32_bf16 a[72:75], v[244:247], v[32:35], a[72:75]\n"
"buffer_store_dwordx4 v[220:223], v13, s[56:59], s76 offen offset:1024 sc1\n"
"v_mfma_f32_16x16x32_bf16 a[64:67], v[244:247], v[20:23], a[64:67]\n"
"ds_read_b64_tr_b16 v[242:243], v126 offset:35848\n"
"v_add_u32 v126, 0xffff6000, v126\n"
"v_mfma_f32_16x16x32_bf16 a[8:11], v[244:247], v[28:31], a[8:11]\n"
"buffer_store_dwordx4 v[224:227], v13, s[56:59], s76 offen offset:2048 sc1\n"
"v_mfma_f32_16x16x32_bf16 a[0:3], v[244:247], v[16:19], a[0:3]\n"
"ds_read_b64_tr_b16 v[244:245], v127 offset:30728\n"
"v_mfma_f32_16x16x32_bf16 a[16:19], v[248:251], v[16:19], a[16:19]\n"
"buffer_store_dwordx4 v[228:231], v13, s[56:59], s76 offen offset:3072 sc1\n"
"s_add_u32 s76, s87, 22528\n"
"v_mfma_f32_16x16x32_bf16 a[24:27], v[248:251], v[28:31], a[24:27]\n"
"ds_read_b64_tr_b16 v[246:247], v127 offset:35848\n"
"v_add_u32 v127, 0xffff6000, v127\n"
"v_mfma_f32_16x16x32_bf16 a[80:83], v[248:251], v[20:23], a[80:83]\n"
"v_mfma_f32_16x16x32_bf16 a[88:91], v[248:251], v[32:35], a[88:91]\n"
"v_mfma_f32_16x16x32_bf16 a[144:147], v[248:251], v[24:27], a[144:147]\n"
"buffer_store_dwordx4 v[232:235], v13, s[56:59], s76 offen sc1\n"
"v_mfma_f32_16x16x32_bf16 a[152:155], v[248:251], v[36:39], a[152:155]\n"
"s_waitcnt lgkmcnt(1)\n"
"v_mfma_f32_16x16x32_bf16 a[168:171], v[252:255], v[36:39], a[168:171]\n"
"v_mfma_f32_16x16x32_bf16 a[160:163], v[252:255], v[24:27], a[160:163]\n"
"v_mfma_f32_16x16x32_bf16 a[104:107], v[252:255], v[32:35], a[104:107]\n"
"buffer_store_dwordx4 v[236:239], v13, s[56:59], s76 offen offset:1024 sc1\n"
"v_mfma_f32_16x16x32_bf16 a[96:99], v[252:255], v[20:23], a[96:99]\n"
"v_mfma_f32_16x16x32_bf16 a[40:43], v[252:255], v[28:31], a[40:43]\n"
"v_mfma_f32_16x16x32_bf16 a[32:35], v[252:255], v[16:19], a[32:35]\n"
"v_mfma_f32_16x16x32_bf16 a[48:51], a[240:243], v[16:19], a[48:51]\n"
"v_mfma_f32_16x16x32_bf16 a[56:59], a[240:243], v[28:31], a[56:59]\n"
"v_mfma_f32_16x16x32_bf16 a[112:115], a[240:243], v[20:23], a[112:115]\n"
"v_mfma_f32_16x16x32_bf16 a[120:123], a[240:243], v[32:35], a[120:123]\n"
"s_add_u32 m0, s81, 34816\n"
"v_mfma_f32_16x16x32_bf16 a[176:179], a[240:243], v[24:27], a[176:179]\n"
"buffer_load_dwordx4 v122, s[52:55], s86 offen lds\n"
"s_add_u32 s86, s85, 81920\n"
"v_mfma_f32_16x16x32_bf16 a[184:187], a[240:243], v[36:39], a[184:187]\n"
"v_mfma_f32_16x16x32_bf16 a[236:239], a[244:247], v[36:39], a[236:239]\n"
"v_mfma_f32_16x16x32_bf16 a[228:231], a[244:247], v[24:27], a[228:231]\n"
"v_mfma_f32_16x16x32_bf16 a[220:223], a[244:247], v[32:35], a[220:223]\n"
"v_mfma_f32_16x16x32_bf16 a[212:215], a[244:247], v[20:23], a[212:215]\n"
"v_mfma_f32_16x16x32_bf16 a[204:207], a[244:247], v[28:31], a[204:207]\n"
"v_mfma_f32_16x16x32_bf16 a[196:199], a[244:247], v[16:19], a[196:199]\n"
"v_mfma_f32_16x16x32_bf16 a[4:7], a[248:251], v[16:19], a[4:7]\n"
"v_mfma_f32_16x16x32_bf16 a[12:15], a[248:251], v[28:31], a[12:15]\n"
"s_add_u32 m0, s81, 45056\n"
"v_mfma_f32_16x16x32_bf16 a[68:71], a[248:251], v[20:23], a[68:71]\n"
"buffer_load_dwordx4 v122, s[52:55], s86 offen lds\n"
"v_mfma_f32_16x16x32_bf16 a[76:79], a[248:251], v[32:35], a[76:79]\n"
"s_add_u32 s86, s85, 81920\n"
"s_add_u32 s86, s86, s29\n"
"v_mfma_f32_16x16x32_bf16 a[132:135], a[248:251], v[24:27], a[132:135]\n"
"v_mfma_f32_16x16x32_bf16 a[140:143], a[248:251], v[36:39], a[140:143]\n"
"s_add_u32 m0, s79, 40960\n"
"v_mfma_f32_16x16x32_bf16 a[156:159], a[252:255], v[36:39], a[156:159]\n"
"buffer_load_dwordx4 v120, s[52:55], s86 offen lds\n"
"v_mfma_f32_16x16x32_bf16 a[148:151], a[252:255], v[24:27], a[148:151]\n"
"v_mfma_f32_16x16x32_bf16 a[92:95], a[252:255], v[32:35], a[92:95]\n"
"s_barrier\n"
"v_mfma_f32_16x16x32_bf16 a[84:87], a[252:255], v[20:23], a[84:87]\n"
"ds_read_b64_tr_b16 v[248:249], v123 offset:0\n"
"ds_read_b64_tr_b16 v[250:251], v123 offset:5120\n"
"v_mfma_f32_16x16x32_bf16 a[28:31], a[252:255], v[28:31], a[28:31]\n"
"ds_read_b64_tr_b16 v[252:253], v124 offset:0\n"
"v_mfma_f32_16x16x32_bf16 a[20:23], a[252:255], v[16:19], a[20:23]\n"
"ds_read_b64_tr_b16 v[254:255], v124 offset:5120\n"
"v_mfma_f32_16x16x32_bf16 a[36:39], v[240:243], v[16:19], a[36:39]\n"
"ds_read_b64_tr_b16 a[240:241], v125 offset:0\n"
"v_mfma_f32_16x16x32_bf16 a[44:47], v[240:243], v[28:31], a[44:47]\n"
"ds_read_b64_tr_b16 a[242:243], v125 offset:5120\n"
"v_mfma_f32_16x16x32_bf16 a[100:103], v[240:243], v[20:23], a[100:103]\n"
"ds_read_b64_tr_b16 a[244:245], v126 offset:0\n"
"v_mfma_f32_16x16x32_bf16 a[108:111], v[240:243], v[32:35], a[108:111]\n"
"ds_read_b64_tr_b16 a[246:247], v126 offset:5120\n"
"v_mfma_f32_16x16x32_bf16 a[164:167], v[240:243], v[24:27], a[164:167]\n"
"ds_read_b64_tr_b16 a[248:249], v127 offset:0\n"
"v_mfma_f32_16x16x32_bf16 a[172:175], v[240:243], v[36:39], a[172:175]\n"
"ds_read_b64_tr_b16 a[250:251], v127 offset:5120\n"
"s_waitcnt lgkmcnt(6)\n"
"v_mfma_f32_16x16x32_bf16 a[188:191], v[244:247], v[36:39], a[188:191]\n"
"buffer_load_dword v12, v8, s[56:59], 0 offen sc1\n"
"v_mfma_f32_16x16x32_bf16 a[180:183], v[244:247], v[24:27], a[180:183]\n"
"ds_read_b64_tr_b16 a[252:253], v123 offset:8\n"
"v_mfma_f32_16x16x32_bf16 a[124:127], v[244:247], v[32:35], a[124:127]\n"
"ds_read_b64_tr_b16 a[254:255], v123 offset:5128\n"
"v_mfma_f32_16x16x32_bf16 a[116:119], v[244:247], v[20:23], a[116:119]\n"
"ds_read_b64_tr_b16 v[240:241], v124 offset:8\n"
"v_mfma_f32_16x16x32_bf16 a[60:63], v[244:247], v[28:31], a[60:63]\n"
"ds_read_b64_tr_b16 v[242:243], v124 offset:5128\n"
"v_mfma_f32_16x16x32_bf16 a[52:55], v[244:247], v[16:19], a[52:55]\n"
"ds_read_b64_tr_b16 v[244:245], v125 offset:8\n"
"s_add_u32 m0, s79, 43008\n"
"s_waitcnt vmcnt(22)\n"
"v_mfma_f32_16x16x32_bf16 a[192:195], v[248:251], v[144:147], a[192:195]\n"
"buffer_load_dwordx4 v121, s[52:55], s86 offen lds\n"
"s_waitcnt vmcnt(20)\n"
"v_mfma_f32_16x16x32_bf16 a[200:203], v[248:251], v[156:159], a[200:203]\n"
"ds_read_b64_tr_b16 v[246:247], v125 offset:5128\n"
"v_mfma_f32_16x16x32_bf16 a[208:211], v[248:251], v[148:151], a[208:211]\n"
"s_add_u32 s86, s83, 122880\n"
"s_waitcnt vmcnt(19)\n"
"v_mfma_f32_16x16x32_bf16 a[216:219], v[248:251], v[160:163], a[216:219]\n"
"v_mfma_f32_16x16x32_bf16 a[224:227], v[248:251], v[152:155], a[224:227]\n"
"s_waitcnt vmcnt(18)\n"
"v_mfma_f32_16x16x32_bf16 a[232:235], v[248:251], v[164:167], a[232:235]\n"
"ds_read_b64_tr_b16 v[248:249], v126 offset:8\n"
"ds_read_b64_tr_b16 v[250:251], v126 offset:5128\n"
"s_add_u32 m0, s81, 55296\n"
"v_mfma_f32_16x16x32_bf16 a[136:139], v[252:255], v[164:167], a[136:139]\n"
"buffer_load_dwordx4 v122, s[52:55], s86 offen lds\n"
"v_mfma_f32_16x16x32_bf16 a[128:131], v[252:255], v[152:155], a[128:131]\n"
"s_add_u32 s86, s83, 122880\n"
"s_add_u32 s86, s86, s29\n"
"s_add_u32 m0, s79, 51200\n"
"v_mfma_f32_16x16x32_bf16 a[72:75], v[252:255], v[160:163], a[72:75]\n"
"buffer_load_dwordx4 v120, s[52:55], s86 offen lds\n"
"v_mfma_f32_16x16x32_bf16 a[64:67], v[252:255], v[148:151], a[64:67]\n"
"v_mfma_f32_16x16x32_bf16 a[8:11], v[252:255], v[156:159], a[8:11]\n"
"v_mfma_f32_16x16x32_bf16 a[0:3], v[252:255], v[144:147], a[0:3]\n"
"ds_read_b64_tr_b16 v[252:253], v127 offset:8\n"
"ds_read_b64_tr_b16 v[254:255], v127 offset:5128\n"
"s_add_u32 m0, s79, 53248\n"
"s_waitcnt lgkmcnt(2)\n"
"v_mfma_f32_16x16x32_bf16 a[16:19], a[240:243], v[144:147], a[16:19]\n"
"buffer_load_dwordx4 v121, s[52:55], s86 offen lds\n"
"s_add_u32 s86, s84, 122880\n"
"v_mfma_f32_16x16x32_bf16 a[24:27], a[240:243], v[156:159], a[24:27]\n"
"v_mfma_f32_16x16x32_bf16 a[80:83], a[240:243], v[148:151], a[80:83]\n"
"v_mfma_f32_16x16x32_bf16 a[88:91], a[240:243], v[160:163], a[88:91]\n"
"v_mfma_f32_16x16x32_bf16 a[144:147], a[240:243], v[152:155], a[144:147]\n"
"v_mfma_f32_16x16x32_bf16 a[152:155], a[240:243], v[164:167], a[152:155]\n"
"v_mfma_f32_16x16x32_bf16 a[168:171], a[244:247], v[164:167], a[168:171]\n"
"v_mfma_f32_16x16x32_bf16 a[160:163], a[244:247], v[152:155], a[160:163]\n"
"s_add_u32 m0, s81, 65536\n"
"v_mfma_f32_16x16x32_bf16 a[104:107], a[244:247], v[160:163], a[104:107]\n"
"buffer_load_dwordx4 v122, s[52:55], s86 offen lds\n"
"v_mfma_f32_16x16x32_bf16 a[96:99], a[244:247], v[148:151], a[96:99]\n"
"s_add_u32 s86, s84, 122880\n"
"s_add_u32 s86, s86, s29\n"
"v_mfma_f32_16x16x32_bf16 a[40:43], a[244:247], v[156:159], a[40:43]\n"
"v_mfma_f32_16x16x32_bf16 a[32:35], a[244:247], v[144:147], a[32:35]\n"
"s_add_u32 m0, s79, 61440\n"
"v_mfma_f32_16x16x32_bf16 a[48:51], a[248:251], v[144:147], a[48:51]\n"
"buffer_load_dwordx4 v120, s[52:55], s86 offen lds\n"
"v_mfma_f32_16x16x32_bf16 a[56:59], a[248:251], v[156:159], a[56:59]\n"
"v_mfma_f32_16x16x32_bf16 a[112:115], a[248:251], v[148:151], a[112:115]\n"
"v_mfma_f32_16x16x32_bf16 a[120:123], a[248:251], v[160:163], a[120:123]\n"
"s_add_u32 m0, s79, 63488\n"
"v_mfma_f32_16x16x32_bf16 a[176:179], a[248:251], v[152:155], a[176:179]\n"
"buffer_load_dwordx4 v121, s[52:55], s86 offen lds\n"
"v_mfma_f32_16x16x32_bf16 a[184:187], a[248:251], v[164:167], a[184:187]\n"
"v_mfma_f32_16x16x32_bf16 a[236:239], a[252:255], v[164:167], a[236:239]\n"
"v_mfma_f32_16x16x32_bf16 a[228:231], a[252:255], v[152:155], a[228:231]\n"
"v_mfma_f32_16x16x32_bf16 a[220:223], a[252:255], v[160:163], a[220:223]\n"
"s_add_u32 s86, s85, 122880\n"
"v_mfma_f32_16x16x32_bf16 a[212:215], a[252:255], v[148:151], a[212:215]\n"
"v_mfma_f32_16x16x32_bf16 a[204:207], a[252:255], v[156:159], a[204:207]\n"
"v_mfma_f32_16x16x32_bf16 a[196:199], a[252:255], v[144:147], a[196:199]\n"
"v_mfma_f32_16x16x32_bf16 a[4:7], v[240:243], v[144:147], a[4:7]\n"
"v_mfma_f32_16x16x32_bf16 a[12:15], v[240:243], v[156:159], a[12:15]\n"
"v_mfma_f32_16x16x32_bf16 a[68:71], v[240:243], v[148:151], a[68:71]\n"
"v_mfma_f32_16x16x32_bf16 a[76:79], v[240:243], v[160:163], a[76:79]\n"
"v_mfma_f32_16x16x32_bf16 a[132:135], v[240:243], v[152:155], a[132:135]\n"
"v_mfma_f32_16x16x32_bf16 a[140:143], v[240:243], v[164:167], a[140:143]\n"
"v_mfma_f32_16x16x32_bf16 a[156:159], v[244:247], v[164:167], a[156:159]\n"
"v_mfma_f32_16x16x32_bf16 a[148:151], v[244:247], v[152:155], a[148:151]\n"
"v_mfma_f32_16x16x32_bf16 a[92:95], v[244:247], v[160:163], a[92:95]\n"
"s_barrier\n"
"v_mfma_f32_16x16x32_bf16 a[84:87], v[244:247], v[148:151], a[84:87]\n"
"ds_read_b64_tr_b16 a[240:241], v123 offset:10240\n"
"ds_read_b64_tr_b16 a[242:243], v123 offset:15360\n"
"v_mfma_f32_16x16x32_bf16 a[28:31], v[244:247], v[156:159], a[28:31]\n"
"ds_read_b64_tr_b16 a[244:245], v124 offset:10240\n"
"v_mfma_f32_16x16x32_bf16 a[20:23], v[244:247], v[144:147], a[20:23]\n"
"ds_read_b64_tr_b16 a[246:247], v124 offset:15360\n"
"v_mfma_f32_16x16x32_bf16 a[36:39], v[248:251], v[144:147], a[36:39]\n"
"ds_read_b64_tr_b16 a[248:249], v125 offset:10240\n"
"v_mfma_f32_16x16x32_bf16 a[44:47], v[248:251], v[156:159], a[44:47]\n"
"ds_read_b64_tr_b16 a[250:251], v125 offset:15360\n"
"v_mfma_f32_16x16x32_bf16 a[100:103], v[248:251], v[148:151], a[100:103]\n"
"ds_read_b64_tr_b16 a[252:253], v126 offset:10240\n"
"v_mfma_f32_16x16x32_bf16 a[108:111], v[248:251], v[160:163], a[108:111]\n"
"ds_read_b64_tr_b16 a[254:255], v126 offset:15360\n"
"v_mfma_f32_16x16x32_bf16 a[164:167], v[248:251], v[152:155], a[164:167]\n"
"ds_read_b64_tr_b16 v[240:241], v127 offset:10240\n"
"v_mfma_f32_16x16x32_bf16 a[172:175], v[248:251], v[164:167], a[172:175]\n"
"ds_read_b64_tr_b16 v[242:243], v127 offset:15360\n"
"s_waitcnt lgkmcnt(6)\n"
"v_mfma_f32_16x16x32_bf16 a[188:191], v[252:255], v[164:167], a[188:191]\n"
"ds_read_b64_tr_b16 v[244:245], v123 offset:10248\n"
"v_mfma_f32_16x16x32_bf16 a[180:183], v[252:255], v[152:155], a[180:183]\n"
"ds_read_b64_tr_b16 v[246:247], v123 offset:15368\n"
"v_mfma_f32_16x16x32_bf16 a[124:127], v[252:255], v[160:163], a[124:127]\n"
"v_mfma_f32_16x16x32_bf16 a[116:119], v[252:255], v[148:151], a[116:119]\n"
"s_waitcnt vmcnt(11)\n"
"s_mov_b32 exec_lo, 0xe0000\n"
"s_mov_b32 exec_hi, 0x0\n"
"buffer_store_dword v11, v9, s[56:59], 0 offen sc1\n"
"s_mov_b64 exec, -1\n"
"s_waitcnt vmcnt(8)\n"
"v_cmp_eq_u32 vcc, v12, v10\n"
"s_nop 0\n"
"s_and_b32 s92, vcc_lo, 0xe000\n"
"s_cmp_eq_u32 s92, 0xe000\n"
"s_cbranch_scc0 Lpollc_%=\n"
"Lprc_%=:\n"
"v_mfma_f32_16x16x32_bf16 a[60:63], v[252:255], v[156:159], a[60:63]\n"
"ds_read_b64_tr_b16 v[248:249], v124 offset:10248\n"
"s_add_u32 s78, s88, 12288\n"
"v_mfma_f32_16x16x32_bf16 a[52:55], v[252:255], v[144:147], a[52:55]\n"
"ds_read_b64_tr_b16 v[250:251], v124 offset:15368\n"
"v_mfma_f32_16x16x32_bf16 a[192:195], a[240:243], v[168:171], a[192:195]\n"
"buffer_load_dwordx4 v[192:195], v13, s[56:59], s78 offen sc1\n"
"v_mfma_f32_16x16x32_bf16 a[200:203], a[240:243], v[180:183], a[200:203]\n"
"ds_read_b64_tr_b16 v[252:253], v125 offset:10248\n"
"v_mfma_f32_16x16x32_bf16 a[208:211], a[240:243], v[172:175], a[208:211]\n"
"buffer_load_dwordx4 v[196:199], v13, s[56:59], s78 offen offset:1024 sc1\n"
"v_mfma_f32_16x16x32_bf16 a[216:219], a[240:243], v[184:187], a[216:219]\n"
"ds_read_b64_tr_b16 v[254:255], v125 offset:15368\n"
"v_mfma_f32_16x16x32_bf16 a[224:227], a[240:243], v[176:179], a[224:227]\n"
"buffer_load_dwordx4 v[200:203], v13, s[56:59], s78 offen offset:2048 sc1\n"
"v_mfma_f32_16x16x32_bf16 a[232:235], a[240:243], v[188:191], a[232:235]\n"
"ds_read_b64_tr_b16 a[240:241], v126 offset:10248\n"
"v_mfma_f32_16x16x32_bf16 a[136:139], a[244:247], v[188:191], a[136:139]\n"
"buffer_load_dwordx4 v[204:207], v13, s[56:59], s78 offen offset:3072 sc1\n"
"s_add_u32 s78, s88, 16384\n"
"v_mfma_f32_16x16x32_bf16 a[128:131], a[244:247], v[176:179], a[128:131]\n"
"ds_read_b64_tr_b16 a[242:243], v126 offset:15368\n"
"v_mfma_f32_16x16x32_bf16 a[72:75], a[244:247], v[184:187], a[72:75]\n"
"v_mfma_f32_16x16x32_bf16 a[64:67], a[244:247], v[172:175], a[64:67]\n"
"v_mfma_f32_16x16x32_bf16 a[8:11], a[244:247], v[180:183], a[8:11]\n"
"buffer_load_dwordx4 v[208:211], v13, s[56:59], s78 offen sc1\n"
"v_mfma_f32_16x16x32_bf16 a[0:3], a[244:247], v[168:171], a[0:3]\n"
"ds_read_b64_tr_b16 a[244:245], v127 offset:10248\n"
"s_waitcnt lgkmcnt(2)\n"
"v_mfma_f32_16x16x32_bf16 a[16:19], a[248:251], v[168:171], a[16:19]\n"
"ds_read_b64_tr_b16 a[246:247], v127 offset:15368\n"
"v_mfma_f32_16x16x32_bf16 a[24:27], a[248:251], v[180:183], a[24:27]\n"
"v_mfma_f32_16x16x32_bf16 a[80:83], a[248:251], v[172:175], a[80:83]\n"
"buffer_load_dwordx4 v[212:215], v13, s[56:59], s78 offen offset:1024 sc1\n"
"v_mfma_f32_16x16x32_bf16 a[88:91], a[248:251], v[184:187], a[88:91]\n"
"v_mfma_f32_16x16x32_bf16 a[144:147], a[248:251], v[176:179], a[144:147]\n"
"v_mfma_f32_16x16x32_bf16 a[152:155], a[248:251], v[188:191], a[152:155]\n"
"v_mfma_f32_16x16x32_bf16 a[168:171], a[252:255], v[188:191], a[168:171]\n"
"v_mfma_f32_16x16x32_bf16 a[160:163], a[252:255], v[176:179], a[160:163]\n"
"v_mfma_f32_16x16x32_bf16 a[104:107], a[252:255], v[184:187], a[104:107]\n"
"v_mfma_f32_16x16x32_bf16 a[96:99], a[252:255], v[172:175], a[96:99]\n"
"s_add_u32 m0, s81, 75776\n"
"v_mfma_f32_16x16x32_bf16 a[40:43], a[252:255], v[180:183], a[40:43]\n"
"buffer_load_dwordx4 v122, s[52:55], s86 offen lds\n"
"v_mfma_f32_16x16x32_bf16 a[32:35], a[252:255], v[168:171], a[32:35]\n"
"s_add_u32 s86, s85, 122880\n"
"s_add_u32 s86, s86, s29\n"
"v_mfma_f32_16x16x32_bf16 a[48:51], v[240:243], v[168:171], a[48:51]\n"
"s_add_u32 s78, s89, 12288\n"
"v_mfma_f32_16x16x32_bf16 a[56:59], v[240:243], v[180:183], a[56:59]\n"
"v_mfma_f32_16x16x32_bf16 a[112:115], v[240:243], v[172:175], a[112:115]\n"
"v_mfma_f32_16x16x32_bf16 a[120:123], v[240:243], v[184:187], a[120:123]\n"
"v_mfma_f32_16x16x32_bf16 a[176:179], v[240:243], v[176:179], a[176:179]\n"
"buffer_load_dwordx4 v[216:219], v13, s[56:59], s78 offen sc1\n"
"v_mfma_f32_16x16x32_bf16 a[184:187], v[240:243], v[188:191], a[184:187]\n"
"v_mfma_f32_16x16x32_bf16 a[236:239], v[244:247], v[188:191], a[236:239]\n"
"v_mfma_f32_16x16x32_bf16 a[228:231], v[244:247], v[176:179], a[228:231]\n"
"v_mfma_f32_16x16x32_bf16 a[220:223], v[244:247], v[184:187], a[220:223]\n"
"buffer_load_dwordx4 v[220:223], v13, s[56:59], s78 offen offset:1024 sc1\n"
"v_mfma_f32_16x16x32_bf16 a[212:215], v[244:247], v[172:175], a[212:215]\n"
"v_mfma_f32_16x16x32_bf16 a[204:207], v[244:247], v[180:183], a[204:207]\n"
"v_mfma_f32_16x16x32_bf16 a[196:199], v[244:247], v[168:171], a[196:199]\n"
"v_mfma_f32_16x16x32_bf16 a[4:7], v[248:251], v[168:171], a[4:7]\n"
"buffer_load_dwordx4 v[224:227], v13, s[56:59], s78 offen offset:2048 sc1\n"
"v_mfma_f32_16x16x32_bf16 a[12:15], v[248:251], v[180:183], a[12:15]\n"
"v_mfma_f32_16x16x32_bf16 a[68:71], v[248:251], v[172:175], a[68:71]\n"
"v_mfma_f32_16x16x32_bf16 a[76:79], v[248:251], v[184:187], a[76:79]\n"
"v_mfma_f32_16x16x32_bf16 a[132:135], v[248:251], v[176:179], a[132:135]\n"
"buffer_load_dwordx4 v[228:231], v13, s[56:59], s78 offen offset:3072 sc1\n"
"s_add_u32 s78, s89, 16384\n"
"v_mfma_f32_16x16x32_bf16 a[140:143], v[248:251], v[188:191], a[140:143]\n"
"v_mfma_f32_16x16x32_bf16 a[156:159], v[252:255], v[188:191], a[156:159]\n"
"v_mfma_f32_16x16x32_bf16 a[148:151], v[252:255], v[176:179], a[148:151]\n"
"v_mfma_f32_16x16x32_bf16 a[92:95], v[252:255], v[184:187], a[92:95]\n"
"s_barrier\n"
"v_mfma_f32_16x16x32_bf16 a[84:87], v[252:255], v[172:175], a[84:87]\n"
"buffer_load_dwordx4 v[232:235], v13, s[56:59], s78 offen sc1\n"
"ds_read_b64_tr_b16 a[248:249], v123 offset:20480\n"
"v_mfma_f32_16x16x32_bf16 a[28:31], v[252:255], v[180:183], a[28:31]\n"
"buffer_load_dword v12, v8, s[56:59], 0 offen sc1\n"
"v_mfma_f32_16x16x32_bf16 a[20:23], v[252:255], v[168:171], a[20:23]\n"
"ds_read_b64_tr_b16 a[250:251], v123 offset:25600\n"
"s_waitcnt lgkmcnt(2)\n"
"v_mfma_f32_16x16x32_bf16 a[36:39], a[240:243], v[168:171], a[36:39]\n"
"ds_read_b64_tr_b16 a[252:253], v124 offset:20480\n"
"v_mfma_f32_16x16x32_bf16 a[44:47], a[240:243], v[180:183], a[44:47]\n"
"ds_read_b64_tr_b16 a[254:255], v124 offset:25600\n"
"v_mfma_f32_16x16x32_bf16 a[100:103], a[240:243], v[172:175], a[100:103]\n"
"buffer_load_dwordx4 v[236:239], v13, s[56:59], s78 offen offset:1024 sc1\n"
"s_add_u32 s78, s90, 12288\n"
"v_mfma_f32_16x16x32_bf16 a[108:111], a[240:243], v[184:187], a[108:111]\n"
"ds_read_b64_tr_b16 v[240:241], v125 offset:20480\n"
"v_mfma_f32_16x16x32_bf16 a[164:167], a[240:243], v[176:179], a[164:167]\n"
"ds_read_b64_tr_b16 v[242:243], v125 offset:25600\n"
"v_mfma_f32_16x16x32_bf16 a[172:175], a[240:243], v[188:191], a[172:175]\n"
"ds_read_b64_tr_b16 v[244:245], v126 offset:20480\n"
"v_mfma_f32_16x16x32_bf16 a[188:191], a[244:247], v[188:191], a[188:191]\n"
"buffer_load_dwordx4 v[64:67], v13, s[56:59], s78 offen sc1\n"
"v_mfma_f32_16x16x32_bf16 a[180:183], a[244:247], v[176:179], a[180:183]\n"
"ds_read_b64_tr_b16 v[246:247], v126 offset:25600\n"
"v_mfma_f32_16x16x32_bf16 a[124:127], a[244:247], v[184:187], a[124:127]\n"
"ds_read_b64_tr_b16 v[248:249], v127 offset:20480\n"
"v_mfma_f32_16x16x32_bf16 a[116:119], a[244:247], v[172:175], a[116:119]\n"
"ds_read_b64_tr_b16 v[250:251], v127 offset:25600\n"
"v_mfma_f32_16x16x32_bf16 a[60:63], a[244:247], v[180:183], a[60:63]\n"
"ds_read_b64_tr_b16 v[252:253], v123 offset:20488\n"
"v_mfma_f32_16x16x32_bf16 a[52:55], a[244:247], v[168:171], a[52:55]\n"
"ds_read_b64_tr_b16 v[254:255], v123 offset:25608\n"
"s_waitcnt vmcnt(14) lgkmcnt(5)\n"
"v_mfma_f32_16x16x32_bf16 a[192:195], a[248:251], v[192:195], a[192:195]\n"
"buffer_load_dwordx4 v[68:71], v13, s[56:59], s78 offen offset:1024 sc1\n"
"s_waitcnt vmcnt(12)\n"
"v_mfma_f32_16x16x32_bf16 a[200:203], a[248:251], v[204:207], a[200:203]\n"
"ds_read_b64_tr_b16 a[240:241], v124 offset:20488\n"
"v_mfma_f32_16x16x32_bf16 a[208:211], a[248:251], v[196:199], a[208:211]\n"
"ds_read_b64_tr_b16 a[242:243], v124 offset:25608\n"
"s_waitcnt vmcnt(11)\n"
"v_mfma_f32_16x16x32_bf16 a[216:219], a[248:251], v[208:211], a[216:219]\n"
"ds_read_b64_tr_b16 a[244:245], v125 offset:20488\n"
"v_mfma_f32_16x16x32_bf16 a[224:227], a[248:251], v[200:203], a[224:227]\n"
"buffer_load_dwordx4 v[72:75], v13, s[56:59], s78 offen offset:2048 sc1\n"
"s_waitcnt vmcnt(11)\n"
"v_mfma_f32_16x16x32_bf16 a[232:235], a[248:251], v[212:215], a[232:235]\n"
"ds_read_b64_tr_b16 a[246:247], v125 offset:25608\n"
"v_mfma_f32_16x16x32_bf16 a[136:139], a[252:255], v[212:215], a[136:139]\n"
"ds_read_b64_tr_b16 a[248:249], v126 offset:20488\n"
"v_mfma_f32_16x16x32_bf16 a[128:131], a[252:255], v[200:203], a[128:131]\n"
"ds_read_b64_tr_b16 a[250:251], v126 offset:25608\n"
"v_mfma_f32_16x16x32_bf16 a[72:75], a[252:255], v[208:211], a[72:75]\n"
"buffer_load_dwordx4 v[76:79], v13, s[56:59], s78 offen offset:3072 sc1\n"
"s_add_u32 s78, s90, 16384\n"
"v_mfma_f32_16x16x32_bf16 a[64:67], a[252:255], v[196:199], a[64:67]\n"
"v_mfma_f32_16x16x32_bf16 a[8:11], a[252:255], v[204:207], a[8:11]\n"
"v_mfma_f32_16x16x32_bf16 a[0:3], a[252:255], v[192:195], a[0:3]\n"
"s_waitcnt vmcnt(5)\n"
"v_cmp_eq_u32 vcc, v12, v10\n"
"s_nop 0\n"
"s_and_b32 s92, vcc_lo, 0xe0000\n"
"s_cmp_eq_u32 s92, 0xe0000\n"
"s_cbranch_scc0 Lpolld_%=\n"
"Lprd_%=:\n"
"v_mfma_f32_16x16x32_bf16 a[16:19], v[240:243], v[192:195], a[16:19]\n"
"buffer_load_dwordx4 v[80:83], v13, s[56:59], s78 offen sc1\n"
"v_mfma_f32_16x16x32_bf16 a[24:27], v[240:243], v[204:207], a[24:27]\n"
"ds_read_b64_tr_b16 a[252:253], v127 offset:20488\n"
"v_mfma_f32_16x16x32_bf16 a[80:83], v[240:243], v[196:199], a[80:83]\n"
"ds_read_b64_tr_b16 a[254:255], v127 offset:25608\n"
"v_mfma_f32_16x16x32_bf16 a[88:91], v[240:243], v[208:211], a[88:91]\n"
"v_mfma_f32_16x16x32_bf16 a[144:147], v[240:243], v[200:203], a[144:147]\n"
"buffer_load_dwordx4 v[84:87], v13, s[56:59], s78 offen offset:1024 sc1\n"
"s_add_u32 s78, s88, 18432\n"
"v_mfma_f32_16x16x32_bf16 a[152:155], v[240:243], v[212:215], a[152:155]\n"
"s_waitcnt lgkmcnt(2)\n"
"v_mfma_f32_16x16x32_bf16 a[168:171], v[244:247], v[212:215], a[168:171]\n"
"v_mfma_f32_16x16x32_bf16 a[160:163], v[244:247], v[200:203], a[160:163]\n"
"v_mfma_f32_16x16x32_bf16 a[104:107], v[244:247], v[208:211], a[104:107]\n"
"v_mfma_f32_16x16x32_bf16 a[96:99], v[244:247], v[196:199], a[96:99]\n"
"v_mfma_f32_16x16x32_bf16 a[40:43], v[244:247], v[204:207], a[40:43]\n"
"buffer_load_dwordx4 v[88:91], v13, s[56:59], s78 offen sc1\n"
"v_mfma_f32_16x16x32_bf16 a[32:35], v[244:247], v[192:195], a[32:35]\n"
"v_mfma_f32_16x16x32_bf16 a[48:51], v[248:251], v[192:195], a[48:51]\n"
"v_mfma_f32_16x16x32_bf16 a[56:59], v[248:251], v[204:207], a[56:59]\n"
"v_mfma_f32_16x16x32_bf16 a[112:115], v[248:251], v[196:199], a[112:115]\n"
"buffer_load_dwordx4 v[92:95], v13, s[56:59], s78 offen offset:1024 sc1\n"
"v_mfma_f32_16x16x32_bf16 a[120:123], v[248:251], v[208:211], a[120:123]\n"
"v_mfma_f32_16x16x32_bf16 a[176:179], v[248:251], v[200:203], a[176:179]\n"
"v_mfma_f32_16x16x32_bf16 a[184:187], v[248:251], v[212:215], a[184:187]\n"
"v_mfma_f32_16x16x32_bf16 a[236:239], v[252:255], v[212:215], a[236:239]\n"
"buffer_load_dwordx4 v[96:99], v13, s[56:59], s78 offen offset:2048 sc1\n"
"v_mfma_f32_16x16x32_bf16 a[228:231], v[252:255], v[200:203], a[228:231]\n"
"v_mfma_f32_16x16x32_bf16 a[220:223], v[252:255], v[208:211], a[220:223]\n"
"v_mfma_f32_16x16x32_bf16 a[212:215], v[252:255], v[196:199], a[212:215]\n"
"v_mfma_f32_16x16x32_bf16 a[204:207], v[252:255], v[204:207], a[204:207]\n"
"buffer_load_dwordx4 v[100:103], v13, s[56:59], s78 offen offset:3072 sc1\n"
"s_add_u32 s78, s88, 22528\n"
"v_mfma_f32_16x16x32_bf16 a[196:199], v[252:255], v[192:195], a[196:199]\n"
"v_mfma_f32_16x16x32_bf16 a[4:7], a[240:243], v[192:195], a[4:7]\n"
"v_mfma_f32_16x16x32_bf16 a[12:15], a[240:243], v[204:207], a[12:15]\n"
"v_mfma_f32_16x16x32_bf16 a[68:71], a[240:243], v[196:199], a[68:71]\n"
"buffer_load_dwordx4 v[104:107], v13, s[56:59], s78 offen sc1\n"
"v_mfma_f32_16x16x32_bf16 a[76:79], a[240:243], v[208:211], a[76:79]\n"
"v_mfma_f32_16x16x32_bf16 a[132:135], a[240:243], v[200:203], a[132:135]\n"
"v_mfma_f32_16x16x32_bf16 a[140:143], a[240:243], v[212:215], a[140:143]\n"
"v_mfma_f32_16x16x32_bf16 a[156:159], a[244:247], v[212:215], a[156:159]\n"
"v_mfma_f32_16x16x32_bf16 a[148:151], a[244:247], v[200:203], a[148:151]\n"
"v_mfma_f32_16x16x32_bf16 a[92:95], a[244:247], v[208:211], a[92:95]\n"
"s_barrier\n"
"v_mfma_f32_16x16x32_bf16 a[84:87], a[244:247], v[196:199], a[84:87]\n"
"buffer_load_dwordx4 v[108:111], v13, s[56:59], s78 offen offset:1024 sc1\n"
"s_add_u32 s78, s89, 18432\n"
"v_mfma_f32_16x16x32_bf16 a[28:31], a[244:247], v[204:207], a[28:31]\n"
"ds_read_b64_tr_b16 v[240:241], v123 offset:30720\n"
"v_mfma_f32_16x16x32_bf16 a[20:23], a[244:247], v[192:195], a[20:23]\n"
"ds_read_b64_tr_b16 v[242:243], v123 offset:35840\n"
"v_mfma_f32_16x16x32_bf16 a[36:39], a[248:251], v[192:195], a[36:39]\n"
"buffer_load_dwordx4 v[40:43], v13, s[56:59], s78 offen sc1\n"
"v_mfma_f32_16x16x32_bf16 a[44:47], a[248:251], v[204:207], a[44:47]\n"
"ds_read_b64_tr_b16 v[244:245], v124 offset:30720\n"
"v_mfma_f32_16x16x32_bf16 a[100:103], a[248:251], v[196:199], a[100:103]\n"
"ds_read_b64_tr_b16 v[246:247], v124 offset:35840\n"
"v_mfma_f32_16x16x32_bf16 a[108:111], a[248:251], v[208:211], a[108:111]\n"
"ds_read_b64_tr_b16 v[248:249], v125 offset:30720\n"
"v_mfma_f32_16x16x32_bf16 a[164:167], a[248:251], v[200:203], a[164:167]\n"
"buffer_load_dwordx4 v[44:47], v13, s[56:59], s78 offen offset:1024 sc1\n"
"v_mfma_f32_16x16x32_bf16 a[172:175], a[248:251], v[212:215], a[172:175]\n"
"ds_read_b64_tr_b16 v[250:251], v125 offset:35840\n"
"s_waitcnt lgkmcnt(4)\n"
"v_mfma_f32_16x16x32_bf16 a[188:191], a[252:255], v[212:215], a[188:191]\n"
"ds_read_b64_tr_b16 v[252:253], v126 offset:30720\n"
"v_mfma_f32_16x16x32_bf16 a[180:183], a[252:255], v[200:203], a[180:183]\n"
"ds_read_b64_tr_b16 v[254:255], v126 offset:35840\n"
"v_mfma_f32_16x16x32_bf16 a[124:127], a[252:255], v[208:211], a[124:127]\n"
"buffer_load_dwordx4 v[48:51], v13, s[56:59], s78 offen offset:2048 sc1\n"
"v_mfma_f32_16x16x32_bf16 a[116:119], a[252:255], v[196:199], a[116:119]\n"
"ds_read_b64_tr_b16 a[240:241], v127 offset:30720\n"
"v_mfma_f32_16x16x32_bf16 a[60:63], a[252:255], v[204:207], a[60:63]\n"
"ds_read_b64_tr_b16 a[242:243], v127 offset:35840\n"
"v_mfma_f32_16x16x32_bf16 a[52:55], a[252:255], v[192:195], a[52:55]\n"
"ds_read_b64_tr_b16 a[244:245], v123 offset:30728\n"
"v_mfma_f32_16x16x32_bf16 a[192:195], v[240:243], v[216:219], a[192:195]\n"
"buffer_load_dwordx4 v[52:55], v13, s[56:59], s78 offen offset:3072 sc1\n"
"s_add_u32 s78, s89, 22528\n"
"v_mfma_f32_16x16x32_bf16 a[200:203], v[240:243], v[228:231], a[200:203]\n"
"ds_read_b64_tr_b16 a[246:247], v123 offset:35848\n"
"v_add_u32 v123, 0xa000, v123\n"
"v_mfma_f32_16x16x32_bf16 a[208:211], v[240:243], v[220:223], a[208:211]\n"
"ds_read_b64_tr_b16 a[248:249], v124 offset:30728\n"
"v_mfma_f32_16x16x32_bf16 a[216:219], v[240:243], v[232:235], a[216:219]\n"
"ds_read_b64_tr_b16 a[250:251], v124 offset:35848\n"
"v_add_u32 v124, 0xa000, v124\n"
"v_mfma_f32_16x16x32_bf16 a[224:227], v[240:243], v[224:227], a[224:227]\n"
"ds_read_b64_tr_b16 a[252:253], v125 offset:30728\n"
"s_waitcnt vmcnt(16)\n"
"v_mfma_f32_16x16x32_bf16 a[232:235], v[240:243], v[236:239], a[232:235]\n"
"ds_read_b64_tr_b16 a[254:255], v125 offset:35848\n"
"v_add_u32 v125, 0xa000, v125\n"
"s_waitcnt lgkmcnt(5)\n"
"v_mfma_f32_16x16x32_bf16 a[136:139], v[244:247], v[236:239], a[136:139]\n"
"buffer_load_dwordx4 v[56:59], v13, s[56:59], s78 offen sc1\n"
"v_mfma_f32_16x16x32_bf16 a[128:131], v[244:247], v[224:227], a[128:131]\n"
"ds_read_b64_tr_b16 v[240:241], v126 offset:30728\n"
"v_mfma_f32_16x16x32_bf16 a[72:75], v[244:247], v[232:235], a[72:75]\n"
"ds_read_b64_tr_b16 v[242:243], v126 offset:35848\n"
"v_add_u32 v126, 0xa000, v126\n"
"v_mfma_f32_16x16x32_bf16 a[64:67], v[244:247], v[220:223], a[64:67]\n"
"v_mfma_f32_16x16x32_bf16 a[8:11], v[244:247], v[228:231], a[8:11]\n"
"buffer_load_dwordx4 v[60:63], v13, s[56:59], s78 offen offset:1024 sc1\n"
"s_add_u32 s78, s90, 18432\n"
"v_mfma_f32_16x16x32_bf16 a[0:3], v[244:247], v[216:219], a[0:3]\n"
"ds_read_b64_tr_b16 v[244:245], v127 offset:30728\n"
"v_mfma_f32_16x16x32_bf16 a[16:19], v[248:251], v[216:219], a[16:19]\n"
"ds_read_b64_tr_b16 v[246:247], v127 offset:35848\n"
"v_add_u32 v127, 0xa000, v127\n"
"v_mfma_f32_16x16x32_bf16 a[24:27], v[248:251], v[228:231], a[24:27]\n"
"v_mfma_f32_16x16x32_bf16 a[80:83], v[248:251], v[220:223], a[80:83]\n"
"buffer_load_dwordx4 v[16:19], v13, s[56:59], s78 offen sc1\n"
"v_mfma_f32_16x16x32_bf16 a[88:91], v[248:251], v[232:235], a[88:91]\n"
"v_mfma_f32_16x16x32_bf16 a[144:147], v[248:251], v[224:227], a[144:147]\n"
"v_mfma_f32_16x16x32_bf16 a[152:155], v[248:251], v[236:239], a[152:155]\n"
"v_mfma_f32_16x16x32_bf16 a[168:171], v[252:255], v[236:239], a[168:171]\n"
"buffer_load_dwordx4 v[20:23], v13, s[56:59], s78 offen offset:1024 sc1\n"
"v_mfma_f32_16x16x32_bf16 a[160:163], v[252:255], v[224:227], a[160:163]\n"
"v_mfma_f32_16x16x32_bf16 a[104:107], v[252:255], v[232:235], a[104:107]\n"
"v_mfma_f32_16x16x32_bf16 a[96:99], v[252:255], v[220:223], a[96:99]\n"
"v_mfma_f32_16x16x32_bf16 a[40:43], v[252:255], v[228:231], a[40:43]\n"
"buffer_load_dwordx4 v[24:27], v13, s[56:59], s78 offen offset:2048 sc1\n"
"v_mfma_f32_16x16x32_bf16 a[32:35], v[252:255], v[216:219], a[32:35]\n"
"v_mfma_f32_16x16x32_bf16 a[48:51], a[240:243], v[216:219], a[48:51]\n"
"v_mfma_f32_16x16x32_bf16 a[56:59], a[240:243], v[228:231], a[56:59]\n"
"v_mfma_f32_16x16x32_bf16 a[112:115], a[240:243], v[220:223], a[112:115]\n"
"v_mfma_f32_16x16x32_bf16 a[120:123], a[240:243], v[232:235], a[120:123]\n"
"s_add_u32 m0, s79, 71680\n"
"v_mfma_f32_16x16x32_bf16 a[176:179], a[240:243], v[224:227], a[176:179]\n"
"buffer_load_dwordx4 v120, s[52:55], s86 offen lds\n"
"v_mfma_f32_16x16x32_bf16 a[184:187], a[240:243], v[236:239], a[184:187]\n"
"s_waitcnt lgkmcnt(0)\n"
"v_mfma_f32_16x16x32_bf16 a[236:239], a[244:247], v[236:239], a[236:239]\n"
"v_mfma_f32_16x16x32_bf16 a[228:231], a[244:247], v[224:227], a[228:231]\n"
"s_add_u32 m0, s79, 73728\n"
"v_mfma_f32_16x16x32_bf16 a[220:223], a[244:247], v[232:235], a[220:223]\n"
"buffer_load_dwordx4 v121, s[52:55], s86 offen lds\n"
"v_mfma_f32_16x16x32_bf16 a[212:215], a[244:247], v[220:223], a[212:215]\n"
"s_add_u32 s86, s85, 81920\n"
"s_add_u32 s86, s86, s30\n"
"v_mfma_f32_16x16x32_bf16 a[204:207], a[244:247], v[228:231], a[204:207]\n"
"v_mfma_f32_16x16x32_bf16 a[196:199], a[244:247], v[216:219], a[196:199]\n"
"v_mfma_f32_16x16x32_bf16 a[4:7], a[248:251], v[216:219], a[4:7]\n"
"buffer_load_dwordx4 v[28:31], v13, s[56:59], s78 offen offset:3072 sc1\n"
"s_add_u32 s78, s90, 22528\n"
"v_mfma_f32_16x16x32_bf16 a[12:15], a[248:251], v[228:231], a[12:15]\n"
"v_mfma_f32_16x16x32_bf16 a[68:71], a[248:251], v[220:223], a[68:71]\n"
"v_mfma_f32_16x16x32_bf16 a[76:79], a[248:251], v[232:235], a[76:79]\n"
"v_mfma_f32_16x16x32_bf16 a[132:135], a[248:251], v[224:227], a[132:135]\n"
"buffer_load_dwordx4 v[32:35], v13, s[56:59], s78 offen sc1\n"
"v_mfma_f32_16x16x32_bf16 a[140:143], a[248:251], v[236:239], a[140:143]\n"
"v_mfma_f32_16x16x32_bf16 a[156:159], a[252:255], v[236:239], a[156:159]\n"
"v_mfma_f32_16x16x32_bf16 a[148:151], a[252:255], v[224:227], a[148:151]\n"
"v_mfma_f32_16x16x32_bf16 a[92:95], a[252:255], v[232:235], a[92:95]\n"
"s_barrier\n"
"s_add_u32 m0, s80, 40960\n"
"v_mfma_f32_16x16x32_bf16 a[84:87], a[252:255], v[220:223], a[84:87]\n"
"buffer_load_dwordx4 v120, s[52:55], s86 offen lds\n"
"v_mfma_f32_16x16x32_bf16 a[28:31], a[252:255], v[228:231], a[28:31]\n"
"ds_read_b64_tr_b16 v[248:249], v123 offset:0\n"
"v_mfma_f32_16x16x32_bf16 a[20:23], a[252:255], v[216:219], a[20:23]\n"
"ds_read_b64_tr_b16 v[250:251], v123 offset:5120\n"
"v_mfma_f32_16x16x32_bf16 a[36:39], v[240:243], v[216:219], a[36:39]\n"
"ds_read_b64_tr_b16 v[252:253], v123 offset:10240\n"
"v_mfma_f32_16x16x32_bf16 a[44:47], v[240:243], v[228:231], a[44:47]\n"
"ds_read_b64_tr_b16 v[254:255], v123 offset:15360\n"
"v_mfma_f32_16x16x32_bf16 a[100:103], v[240:243], v[220:223], a[100:103]\n"
"buffer_load_dwordx4 v[36:39], v13, s[56:59], s78 offen offset:1024 sc1\n"
"v_mfma_f32_16x16x32_bf16 a[108:111], v[240:243], v[232:235], a[108:111]\n"
"ds_read_b64_tr_b16 a[240:241], v123 offset:20480\n"
"v_mfma_f32_16x16x32_bf16 a[164:167], v[240:243], v[224:227], a[164:167]\n"
"ds_read_b64_tr_b16 a[242:243], v123 offset:25600\n"
"v_mfma_f32_16x16x32_bf16 a[172:175], v[240:243], v[236:239], a[172:175]\n"
"ds_read_b64_tr_b16 a[244:245], v123 offset:30720\n"
"s_add_u32 m0, s80, 43008\n"
"v_mfma_f32_16x16x32_bf16 a[188:191], v[244:247], v[236:239], a[188:191]\n"
"buffer_load_dwordx4 v121, s[52:55], s86 offen lds\n"
"v_mfma_f32_16x16x32_bf16 a[180:183], v[244:247], v[224:227], a[180:183]\n"
"s_add_u32 s86, s83, 122880\n"
"s_add_u32 s86, s86, s30\n"
"v_mfma_f32_16x16x32_bf16 a[124:127], v[244:247], v[232:235], a[124:127]\n"
"ds_read_b64_tr_b16 a[246:247], v123 offset:35840\n"
"v_mfma_f32_16x16x32_bf16 a[116:119], v[244:247], v[220:223], a[116:119]\n"
"ds_read_b64_tr_b16 a[248:249], v123 offset:8\n"
"s_add_u32 m0, s80, 51200\n"
"v_mfma_f32_16x16x32_bf16 a[60:63], v[244:247], v[228:231], a[60:63]\n"
"buffer_load_dwordx4 v120, s[52:55], s86 offen lds\n"
"v_mfma_f32_16x16x32_bf16 a[52:55], v[244:247], v[216:219], a[52:55]\n"
"s_waitcnt vmcnt(28) lgkmcnt(2)\n"
"v_mfma_f32_16x16x32_bf16 a[192:195], v[248:251], v[64:67], a[192:195]\n"
"ds_read_b64_tr_b16 a[250:251], v123 offset:5128\n"
"s_waitcnt vmcnt(25)\n"
"v_mfma_f32_16x16x32_bf16 a[200:203], v[248:251], v[76:79], a[200:203]\n"
"ds_read_b64_tr_b16 a[252:253], v123 offset:10248\n"
"s_add_u32 m0, s80, 53248\n"
"v_mfma_f32_16x16x32_bf16 a[208:211], v[248:251], v[68:71], a[208:211]\n"
"buffer_load_dwordx4 v121, s[52:55], s86 offen lds\n"
"s_waitcnt vmcnt(25)\n"
"v_mfma_f32_16x16x32_bf16 a[216:219], v[248:251], v[80:83], a[216:219]\n"
"ds_read_b64_tr_b16 a[254:255], v123 offset:15368\n"
"v_mfma_f32_16x16x32_bf16 a[224:227], v[248:251], v[72:75], a[224:227]\n"
"ds_read_b64_tr_b16 v[240:241], v123 offset:20488\n"
"s_waitcnt vmcnt(24)\n"
"v_mfma_f32_16x16x32_bf16 a[232:235], v[248:251], v[84:87], a[232:235]\n"
"s_add_u32 s86, s84, 122880\n"
"s_add_u32 s86, s86, s30\n"
"s_add_u32 m0, s80, 61440\n"
"s_waitcnt vmcnt(18)\n"
"v_mfma_f32_16x16x32_bf16 a[232:235], v[252:255], v[108:111], a[232:235]\n"
"buffer_load_dwordx4 v120, s[52:55], s86 offen lds\n"
"v_mfma_f32_16x16x32_bf16 a[224:227], v[252:255], v[96:99], a[224:227]\n"
"ds_read_b64_tr_b16 v[242:243], v123 offset:25608\n"
"v_mfma_f32_16x16x32_bf16 a[216:219], v[252:255], v[104:107], a[216:219]\n"
"ds_read_b64_tr_b16 v[244:245], v123 offset:30728\n"
"v_mfma_f32_16x16x32_bf16 a[208:211], v[252:255], v[92:95], a[208:211]\n"
"ds_read_b64_tr_b16 v[246:247], v123 offset:35848\n"
"v_mfma_f32_16x16x32_bf16 a[200:203], v[252:255], v[100:103], a[200:203]\n"
"v_mfma_f32_16x16x32_bf16 a[192:195], v[252:255], v[88:91], a[192:195]\n"
"s_add_u32 m0, s80, 63488\n"
"s_waitcnt vmcnt(18)\n"
"v_mfma_f32_16x16x32_bf16 a[192:195], a[240:243], v[40:43], a[192:195]\n"
"buffer_load_dwordx4 v121, s[52:55], s86 offen lds\n"
"s_waitcnt vmcnt(16)\n"
"v_mfma_f32_16x16x32_bf16 a[200:203], a[240:243], v[52:55], a[200:203]\n"
"v_mfma_f32_16x16x32_bf16 a[208:211], a[240:243], v[44:47], a[208:211]\n"
"s_add_u32 s86, s85, 122880\n"
"s_add_u32 s86, s86, s30\n"
"s_waitcnt vmcnt(15)\n"
"v_mfma_f32_16x16x32_bf16 a[216:219], a[240:243], v[56:59], a[216:219]\n"
"s_add_u32 m0, s80, 71680\n"
"v_mfma_f32_16x16x32_bf16 a[224:227], a[240:243], v[48:51], a[224:227]\n"
"buffer_load_dwordx4 v120, s[52:55], s86 offen lds\n"
"s_waitcnt vmcnt(15)\n"
"v_mfma_f32_16x16x32_bf16 a[232:235], a[240:243], v[60:63], a[232:235]\n"
"s_waitcnt vmcnt(6) lgkmcnt(0)\n"
"v_mfma_f32_16x16x32_bf16 a[232:235], a[244:247], v[36:39], a[232:235]\n"
"v_mfma_f32_16x16x32_bf16 a[224:227], a[244:247], v[24:27], a[224:227]\n"
"s_add_u32 m0, s80, 73728\n"
"v_mfma_f32_16x16x32_bf16 a[216:219], a[244:247], v[32:35], a[216:219]\n"
"buffer_load_dwordx4 v121, s[52:55], s86 offen lds\n"
"v_mfma_f32_16x16x32_bf16 a[208:211], a[244:247], v[20:23], a[208:211]\n"
"v_mfma_f32_16x16x32_bf16 a[200:203], a[244:247], v[28:31], a[200:203]\n"
"s_nop 1\n"
"ds_write_b128 v129, a[232:235] offset:4096\n"
"v_mfma_f32_16x16x32_bf16 a[192:195], a[244:247], v[16:19], a[192:195]\n"
"v_mfma_f32_16x16x32_bf16 a[196:199], a[248:251], v[64:67], a[196:199]\n"
"v_mfma_f32_16x16x32_bf16 a[204:207], a[248:251], v[76:79], a[204:207]\n"
"v_mfma_f32_16x16x32_bf16 a[212:215], a[248:251], v[68:71], a[212:215]\n"
"s_nop 0\n"
"ds_write_b128 v129, a[200:203] offset:0\n"
"v_mfma_f32_16x16x32_bf16 a[220:223], a[248:251], v[80:83], a[220:223]\n"
"v_mfma_f32_16x16x32_bf16 a[228:231], a[248:251], v[72:75], a[228:231]\n"
"v_mfma_f32_16x16x32_bf16 a[236:239], a[248:251], v[84:87], a[236:239]\n"
"v_mfma_f32_16x16x32_bf16 a[236:239], a[252:255], v[108:111], a[236:239]\n"
"ds_write_b128 v129, a[216:219] offset:2048\n"
"v_mfma_f32_16x16x32_bf16 a[228:231], a[252:255], v[96:99], a[228:231]\n"
"v_mfma_f32_16x16x32_bf16 a[220:223], a[252:255], v[104:107], a[220:223]\n"
"s_barrier\n"
"v_mfma_f32_16x16x32_bf16 a[212:215], a[252:255], v[92:95], a[212:215]\n"
"ds_read_b64_tr_b16 v[248:249], v124 offset:0\n"
"ds_read_b64_tr_b16 v[250:251], v124 offset:5120\n"
"v_mfma_f32_16x16x32_bf16 a[204:207], a[252:255], v[100:103], a[204:207]\n"
"ds_read_b64_tr_b16 v[252:253], v124 offset:10240\n"
"v_mfma_f32_16x16x32_bf16 a[196:199], a[252:255], v[88:91], a[196:199]\n"
"ds_read_b64_tr_b16 v[254:255], v124 offset:15360\n"
"v_mfma_f32_16x16x32_bf16 a[196:199], v[240:243], v[40:43], a[196:199]\n"
"ds_read_b64_tr_b16 a[240:241], v124 offset:20480\n"
"v_mfma_f32_16x16x32_bf16 a[204:207], v[240:243], v[52:55], a[204:207]\n"
"ds_read_b64_tr_b16 a[242:243], v124 offset:25600\n"
"v_mfma_f32_16x16x32_bf16 a[212:215], v[240:243], v[44:47], a[212:215]\n"
"ds_read_b64_tr_b16 a[244:245], v124 offset:30720\n"
"v_mfma_f32_16x16x32_bf16 a[220:223], v[240:243], v[56:59], a[220:223]\n"
"ds_read_b64_tr_b16 a[246:247], v124 offset:35840\n"
"v_mfma_f32_16x16x32_bf16 a[228:231], v[240:243], v[48:51], a[228:231]\n"
"ds_read_b64_tr_b16 a[248:249], v124 offset:8\n"
"v_mfma_f32_16x16x32_bf16 a[236:239], v[240:243], v[60:63], a[236:239]\n"
"ds_read_b64_tr_b16 a[250:251], v124 offset:5128\n"
"v_mfma_f32_16x16x32_bf16 a[236:239], v[244:247], v[36:39], a[236:239]\n"
"ds_read_b64_tr_b16 a[252:253], v124 offset:10248\n"
"v_mfma_f32_16x16x32_bf16 a[228:231], v[244:247], v[24:27], a[228:231]\n"
"ds_read_b64_tr_b16 a[254:255], v124 offset:15368\n"
"v_mfma_f32_16x16x32_bf16 a[220:223], v[244:247], v[32:35], a[220:223]\n"
"ds_read_b64_tr_b16 v[240:241], v124 offset:20488\n"
"v_mfma_f32_16x16x32_bf16 a[212:215], v[244:247], v[20:23], a[212:215]\n"
"ds_read_b64_tr_b16 v[242:243], v124 offset:25608\n"
"v_mfma_f32_16x16x32_bf16 a[204:207], v[244:247], v[28:31], a[204:207]\n"
"ds_write_b128 v129, a[236:239] offset:5120\n"
"v_mfma_f32_16x16x32_bf16 a[196:199], v[244:247], v[16:19], a[196:199]\n"
"ds_read_b64_tr_b16 v[244:245], v124 offset:30728\n"
"s_waitcnt lgkmcnt(6)\n"
"v_mfma_f32_16x16x32_bf16 a[0:3], v[248:251], v[64:67], a[0:3]\n"
"ds_read_b64_tr_b16 v[246:247], v124 offset:35848\n"
"v_mfma_f32_16x16x32_bf16 a[8:11], v[248:251], v[76:79], a[8:11]\n"
"v_mfma_f32_16x16x32_bf16 a[64:67], v[248:251], v[68:71], a[64:67]\n"
"ds_write_b128 v129, a[204:207] offset:1024\n"
"v_mfma_f32_16x16x32_bf16 a[72:75], v[248:251], v[80:83], a[72:75]\n"
"v_mfma_f32_16x16x32_bf16 a[128:131], v[248:251], v[72:75], a[128:131]\n"
"v_mfma_f32_16x16x32_bf16 a[136:139], v[248:251], v[84:87], a[136:139]\n"
"v_mfma_f32_16x16x32_bf16 a[136:139], v[252:255], v[108:111], a[136:139]\n"
"ds_write_b128 v129, a[220:223] offset:3072\n"
"v_mfma_f32_16x16x32_bf16 a[128:131], v[252:255], v[96:99], a[128:131]\n"
"v_mfma_f32_16x16x32_bf16 a[72:75], v[252:255], v[104:107], a[72:75]\n"
"v_mfma_f32_16x16x32_bf16 a[64:67], v[252:255], v[92:95], a[64:67]\n"
"v_mfma_f32_16x16x32_bf16 a[8:11], v[252:255], v[100:103], a[8:11]\n"
"v_mfma_f32_16x16x32_bf16 a[0:3], v[252:255], v[88:91], a[0:3]\n"
"v_mfma_f32_16x16x32_bf16 a[0:3], a[240:243], v[40:43], a[0:3]\n"
"v_mfma_f32_16x16x32_bf16 a[8:11], a[240:243], v[52:55], a[8:11]\n"
"v_mfma_f32_16x16x32_bf16 a[64:67], a[240:243], v[44:47], a[64:67]\n"
"v_mfma_f32_16x16x32_bf16 a[72:75], a[240:243], v[56:59], a[72:75]\n"
"v_mfma_f32_16x16x32_bf16 a[128:131], a[240:243], v[48:51], a[128:131]\n"
"v_mfma_f32_16x16x32_bf16 a[136:139], a[240:243], v[60:63], a[136:139]\n"
"v_mfma_f32_16x16x32_bf16 a[136:139], a[244:247], v[36:39], a[136:139]\n"
"v_mfma_f32_16x16x32_bf16 a[128:131], a[244:247], v[24:27], a[128:131]\n"
"v_mfma_f32_16x16x32_bf16 a[72:75], a[244:247], v[32:35], a[72:75]\n"
"v_mfma_f32_16x16x32_bf16 a[64:67], a[244:247], v[20:23], a[64:67]\n"
"v_mfma_f32_16x16x32_bf16 a[8:11], a[244:247], v[28:31], a[8:11]\n"
"s_nop 3\n"
"ds_write_b128 v129, a[136:139] offset:16384\n"
"v_mfma_f32_16x16x32_bf16 a[0:3], a[244:247], v[16:19], a[0:3]\n"
"v_mfma_f32_16x16x32_bf16 a[4:7], a[248:251], v[64:67], a[4:7]\n"
"v_mfma_f32_16x16x32_bf16 a[12:15], a[248:251], v[76:79], a[12:15]\n"
"v_mfma_f32_16x16x32_bf16 a[68:71], a[248:251], v[68:71], a[68:71]\n"
"ds_write_b128 v129, a[8:11] offset:12288\n"
"v_mfma_f32_16x16x32_bf16 a[76:79], a[248:251], v[80:83], a[76:79]\n"
"v_mfma_f32_16x16x32_bf16 a[132:135], a[248:251], v[72:75], a[132:135]\n"
"v_mfma_f32_16x16x32_bf16 a[140:143], a[248:251], v[84:87], a[140:143]\n"
"s_waitcnt lgkmcnt(1)\n"
"v_mfma_f32_16x16x32_bf16 a[140:143], a[252:255], v[108:111], a[140:143]\n"
"s_waitcnt vmcnt(0)\n"
"s_barrier\n"
"v_mfma_f32_16x16x32_bf16 a[132:135], a[252:255], v[96:99], a[132:135]\n"
"ds_read_b128 v[144:147], v130 offset:0\n"
"ds_read_b128 v[148:151], v130 offset:1024\n"
"v_accvgpr_read_b32 v168, a192\n"
"v_mfma_f32_16x16x32_bf16 a[76:79], a[252:255], v[104:107], a[76:79]\n"
"ds_read_b128 v[152:155], v130 offset:2048\n"
"v_accvgpr_read_b32 v169, a193\n"
"v_mfma_f32_16x16x32_bf16 a[68:71], a[252:255], v[92:95], a[68:71]\n"
"ds_read_b128 v[156:159], v130 offset:3072\n"
"v_accvgpr_read_b32 v170, a194\n"
"v_accvgpr_read_b32 v171, a195\n"
"v_mfma_f32_16x16x32_bf16 a[12:15], a[252:255], v[100:103], a[12:15]\n"
"ds_read_b128 v[160:163], v130 offset:4096\n"
"v_accvgpr_read_b32 v172, a196\n"
"v_mfma_f32_16x16x32_bf16 a[4:7], a[252:255], v[88:91], a[4:7]\n"
"ds_read_b128 v[164:167], v130 offset:5120\n"
"v_accvgpr_read_b32 v173, a197\n"
"v_accvgpr_read_b32 v174, a198\n"
"v_mfma_f32_16x16x32_bf16 a[4:7], v[240:243], v[40:43], a[4:7]\n"
"ds_read_b64_tr_b16 v[248:249], v125 offset:0\n"
"v_accvgpr_read_b32 v175, a199\n"
"v_mfma_f32_16x16x32_bf16 a[12:15], v[240:243], v[52:55], a[12:15]\n"
"ds_read_b64_tr_b16 v[250:251], v125 offset:5120\n"
"v_mfma_f32_16x16x32_bf16 a[68:71], v[240:243], v[44:47], a[68:71]\n"
"ds_read_b64_tr_b16 v[252:253], v125 offset:10240\n"
"s_waitcnt lgkmcnt(5)\n"
"v_add_f32 v144, v144, v168\n"
"v_mfma_f32_16x16x32_bf16 a[76:79], v[240:243], v[56:59], a[76:79]\n"
"ds_read_b64_tr_b16 v[254:255], v125 offset:15360\n"
"v_add_f32 v145, v145, v169\n"
"v_mfma_f32_16x16x32_bf16 a[132:135], v[240:243], v[48:51], a[132:135]\n"
"ds_read_b64_tr_b16 a[240:241], v125 offset:20480\n"
"v_add_f32 v146, v146, v170\n"
"v_mfma_f32_16x16x32_bf16 a[140:143], v[240:243], v[60:63], a[140:143]\n"
"ds_read_b64_tr_b16 a[242:243], v125 offset:25600\n"
"v_add_f32 v147, v147, v171\n"
"v_add_f32 v148, v148, v172\n"
"v_mfma_f32_16x16x32_bf16 a[140:143], v[244:247], v[36:39], a[140:143]\n"
"ds_write_b128 v129, a[72:75] offset:14336\n"
"v_add_f32 v149, v149, v173\n"
"v_mfma_f32_16x16x32_bf16 a[132:135], v[244:247], v[24:27], a[132:135]\n"
"ds_read_b64_tr_b16 a[244:245], v125 offset:30720\n"
"v_add_f32 v150, v150, v174\n"
"v_add_f32 v151, v151, v175\n"
"v_mfma_f32_16x16x32_bf16 a[76:79], v[244:247], v[32:35], a[76:79]\n"
"ds_read_b64_tr_b16 a[246:247], v125 offset:35840\n"
"v_cvt_pk_bf16_f32 v176, v144, v145\n"
"v_mfma_f32_16x16x32_bf16 a[68:71], v[244:247], v[20:23], a[68:71]\n"
"ds_read_b64_tr_b16 a[248:249], v125 offset:8\n"
"v_cvt_pk_bf16_f32 v177, v146, v147\n"
"v_cvt_pk_bf16_f32 v178, v148, v149\n"
"v_mfma_f32_16x16x32_bf16 a[12:15], v[244:247], v[28:31], a[12:15]\n"
"ds_write_b128 v129, a[140:143] offset:17408\n"
"v_cvt_pk_bf16_f32 v179, v150, v151\n"
"v_mfma_f32_16x16x32_bf16 a[4:7], v[244:247], v[16:19], a[4:7]\n"
"ds_read_b64_tr_b16 a[250:251], v125 offset:5128\n"
"v_accvgpr_read_b32 v168, a208\n"
"s_waitcnt lgkmcnt(6)\n"
"v_mfma_f32_16x16x32_bf16 a[16:19], v[248:251], v[64:67], a[16:19]\n"
"buffer_store_dwordx4 v[176:179], v13, s[56:59], s71 offen sc1\n"
"v_add_f32 v152, v152, v168\n"
"v_mfma_f32_16x16x32_bf16 a[24:27], v[248:251], v[76:79], a[24:27]\n"
"ds_read_b64_tr_b16 a[252:253], v125 offset:10248\n"
"v_accvgpr_read_b32 v169, a209\n"
"v_add_f32 v153, v153, v169\n"
"v_mfma_f32_16x16x32_bf16 a[80:83], v[248:251], v[68:71], a[80:83]\n"
"ds_write_b128 v129, a[12:15] offset:13312\n"
"v_accvgpr_read_b32 v170, a210\n"
"v_mfma_f32_16x16x32_bf16 a[88:91], v[248:251], v[80:83], a[88:91]\n"
"ds_read_b64_tr_b16 a[254:255], v125 offset:15368\n"
"v_add_f32 v154, v154, v170\n"
"v_accvgpr_read_b32 v171, a211\n"
"v_mfma_f32_16x16x32_bf16 a[144:147], v[248:251], v[72:75], a[144:147]\n"
"ds_read_b64_tr_b16 v[240:241], v125 offset:20488\n"
"v_add_f32 v155, v155, v171\n"
"v_mfma_f32_16x16x32_bf16 a[152:155], v[248:251], v[84:87], a[152:155]\n"
"ds_read_b64_tr_b16 v[242:243], v125 offset:25608\n"
"v_accvgpr_read_b32 v172, a212\n"
"v_add_f32 v156, v156, v172\n"
"v_mfma_f32_16x16x32_bf16 a[152:155], v[252:255], v[108:111], a[152:155]\n"
"ds_write_b128 v129, a[76:79] offset:15360\n"
"v_accvgpr_read_b32 v173, a213\n"
"v_mfma_f32_16x16x32_bf16 a[144:147], v[252:255], v[96:99], a[144:147]\n"
"ds_read_b64_tr_b16 v[244:245], v125 offset:30728\n"
"v_add_f32 v157, v157, v173\n"
"v_accvgpr_read_b32 v174, a214\n"
"v_mfma_f32_16x16x32_bf16 a[88:91], v[252:255], v[104:107], a[88:91]\n"
"ds_read_b64_tr_b16 v[246:247], v125 offset:35848\n"
"v_add_f32 v158, v158, v174\n"
"v_mfma_f32_16x16x32_bf16 a[80:83], v[252:255], v[92:95], a[80:83]\n"
"ds_read_b64_tr_b16 v[248:249], v126 offset:0\n"
"v_accvgpr_read_b32 v175, a215\n"
"v_add_f32 v159, v159, v175\n"
"v_mfma_f32_16x16x32_bf16 a[24:27], v[252:255], v[100:103], a[24:27]\n"
"ds_read_b64_tr_b16 v[250:251], v126 offset:5120\n"
"v_cvt_pk_bf16_f32 v180, v152, v153\n"
"v_mfma_f32_16x16x32_bf16 a[16:19], v[252:255], v[88:91], a[16:19]\n"
"ds_read_b64_tr_b16 v[252:253], v126 offset:10240\n"
"v_cvt_pk_bf16_f32 v181, v154, v155\n"
"v_cvt_pk_bf16_f32 v182, v156, v157\n"
"v_mfma_f32_16x16x32_bf16 a[16:19], a[240:243], v[40:43], a[16:19]\n"
"ds_read_b64_tr_b16 v[254:255], v126 offset:15360\n"
"v_cvt_pk_bf16_f32 v183, v158, v159\n"
"v_mfma_f32_16x16x32_bf16 a[24:27], a[240:243], v[52:55], a[24:27]\n"
"v_accvgpr_read_b32 v168, a224\n"
"v_add_f32 v160, v160, v168\n"
"v_accvgpr_read_b32 v169, a225\n"
"v_mfma_f32_16x16x32_bf16 a[80:83], a[240:243], v[44:47], a[80:83]\n"
"buffer_store_dwordx4 v[180:183], v13, s[56:59], s72 offen sc1\n"
"v_add_f32 v161, v161, v169\n"
"v_mfma_f32_16x16x32_bf16 a[88:91], a[240:243], v[56:59], a[88:91]\n"
"v_accvgpr_read_b32 v170, a226\n"
"v_add_f32 v162, v162, v170\n"
"v_accvgpr_read_b32 v171, a227\n"
"v_mfma_f32_16x16x32_bf16 a[144:147], a[240:243], v[48:51], a[144:147]\n"
"v_add_f32 v163, v163, v171\n"
"v_accvgpr_read_b32 v172, a228\n"
"v_mfma_f32_16x16x32_bf16 a[152:155], a[240:243], v[60:63], a[152:155]\n"
"ds_read_b64_tr_b16 a[240:241], v126 offset:20480\n"
"ds_read_b64_tr_b16 a[242:243], v126 offset:25600\n"
"s_waitcnt lgkmcnt(3)\n"
"v_mfma_f32_16x16x32_bf16 a[152:155], a[244:247], v[36:39], a[152:155]\n"
"s_barrier\n"
"v_mfma_f32_16x16x32_bf16 a[144:147], a[244:247], v[24:27], a[144:147]\n"
"ds_read_b128 v[144:147], v130 offset:12288\n"
"ds_read_b128 v[148:151], v130 offset:13312\n"
"v_add_f32 v164, v164, v172\n"
"v_mfma_f32_16x16x32_bf16 a[88:91], a[244:247], v[32:35], a[88:91]\n"
"ds_read_b128 v[152:155], v130 offset:14336\n"
"v_accvgpr_read_b32 v173, a229\n"
"v_mfma_f32_16x16x32_bf16 a[80:83], a[244:247], v[20:23], a[80:83]\n"
"ds_read_b128 v[156:159], v130 offset:15360\n"
"v_add_f32 v165, v165, v173\n"
"v_accvgpr_read_b32 v174, a230\n"
"v_mfma_f32_16x16x32_bf16 a[24:27], a[244:247], v[28:31], a[24:27]\n"
"ds_write_b128 v129, a[152:155] offset:4096\n"
"v_add_f32 v166, v166, v174\n"
"v_mfma_f32_16x16x32_bf16 a[16:19], a[244:247], v[16:19], a[16:19]\n"
"ds_read_b64_tr_b16 a[244:245], v126 offset:30720\n"
"v_accvgpr_read_b32 v175, a231\n"
"v_add_f32 v167, v167, v175\n"
"v_mfma_f32_16x16x32_bf16 a[20:23], a[248:251], v[64:67], a[20:23]\n"
"ds_read_b64_tr_b16 a[246:247], v126 offset:35840\n"
"v_cvt_pk_bf16_f32 v184, v160, v161\n"
"v_mfma_f32_16x16x32_bf16 a[28:31], a[248:251], v[76:79], a[28:31]\n"
"v_cvt_pk_bf16_f32 v185, v162, v163\n"
"v_cvt_pk_bf16_f32 v186, v164, v165\n"
"v_cvt_pk_bf16_f32 v187, v166, v167\n"
"v_mfma_f32_16x16x32_bf16 a[84:87], a[248:251], v[68:71], a[84:87]\n"
"buffer_store_dwordx4 v[184:187], v13, s[56:59], s73 offen sc1\n"
"v_accvgpr_read_b32 v168, a0\n"
"v_mfma_f32_16x16x32_bf16 a[92:95], a[248:251], v[80:83], a[92:95]\n"
"ds_read_b128 v[160:163], v130 offset:16384\n"
"s_waitcnt lgkmcnt(4)\n"
"v_add_f32 v144, v144, v168\n"
"v_mfma_f32_16x16x32_bf16 a[148:151], a[248:251], v[72:75], a[148:151]\n"
"ds_read_b128 v[164:167], v130 offset:17408\n"
"v_accvgpr_read_b32 v169, a1\n"
"v_mfma_f32_16x16x32_bf16 a[156:159], a[248:251], v[84:87], a[156:159]\n"
"ds_write_b128 v129, a[24:27] offset:0\n"
"v_add_f32 v145, v145, v169\n"
"v_accvgpr_read_b32 v170, a2\n"
"v_mfma_f32_16x16x32_bf16 a[156:159], a[252:255], v[108:111], a[156:159]\n"
"ds_read_b64_tr_b16 a[248:249], v126 offset:8\n"
"v_add_f32 v146, v146, v170\n"
"v_mfma_f32_16x16x32_bf16 a[148:151], a[252:255], v[96:99], a[148:151]\n"
"ds_read_b64_tr_b16 a[250:251], v126 offset:5128\n"
"v_accvgpr_read_b32 v171, a3\n"
"v_add_f32 v147, v147, v171\n"
"v_mfma_f32_16x16x32_bf16 a[92:95], a[252:255], v[104:107], a[92:95]\n"
"ds_write_b128 v129, a[88:91] offset:2048\n"
"v_accvgpr_read_b32 v172, a4\n"
"v_mfma_f32_16x16x32_bf16 a[84:87], a[252:255], v[92:95], a[84:87]\n"
"v_add_f32 v148, v148, v172\n"
"v_accvgpr_read_b32 v173, a5\n"
"v_add_f32 v149, v149, v173\n"
"v_mfma_f32_16x16x32_bf16 a[28:31], a[252:255], v[100:103], a[28:31]\n"
"v_accvgpr_read_b32 v174, a6\n"
"v_add_f32 v150, v150, v174\n"
"v_mfma_f32_16x16x32_bf16 a[20:23], a[252:255], v[88:91], a[20:23]\n"
"ds_read_b64_tr_b16 a[252:253], v126 offset:10248\n"
"ds_read_b64_tr_b16 a[254:255], v126 offset:15368\n"
"v_accvgpr_read_b32 v175, a7\n"
"v_mfma_f32_16x16x32_bf16 a[20:23], v[240:243], v[40:43], a[20:23]\n"
"v_add_f32 v151, v151, v175\n"
"v_cvt_pk_bf16_f32 v176, v144, v145\n"
"v_mfma_f32_16x16x32_bf16 a[28:31], v[240:243], v[52:55], a[28:31]\n"
"v_cvt_pk_bf16_f32 v177, v146, v147\n"
"v_cvt_pk_bf16_f32 v178, v148, v149\n"
"v_cvt_pk_bf16_f32 v179, v150, v151\n"
"v_mfma_f32_16x16x32_bf16 a[84:87], v[240:243], v[44:47], a[84:87]\n"
"buffer_store_dwordx4 v[176:179], v13, s[56:59], s68 offen sc1\n"
"v_accvgpr_read_b32 v168, a64\n"
"v_mfma_f32_16x16x32_bf16 a[92:95], v[240:243], v[56:59], a[92:95]\n"
"v_add_f32 v152, v152, v168\n"
"v_accvgpr_read_b32 v169, a65\n"
"v_add_f32 v153, v153, v169\n"
"v_mfma_f32_16x16x32_bf16 a[148:151], v[240:243], v[48:51], a[148:151]\n"
"v_accvgpr_read_b32 v170, a66\n"
"v_add_f32 v154, v154, v170\n"
"v_mfma_f32_16x16x32_bf16 a[156:159], v[240:243], v[60:63], a[156:159]\n"
"ds_read_b64_tr_b16 v[240:241], v126 offset:20488\n"
"ds_read_b64_tr_b16 v[242:243], v126 offset:25608\n"
"v_accvgpr_read_b32 v171, a67\n"
"v_mfma_f32_16x16x32_bf16 a[156:159], v[244:247], v[36:39], a[156:159]\n"
"v_add_f32 v155, v155, v171\n"
"v_accvgpr_read_b32 v172, a68\n"
"v_mfma_f32_16x16x32_bf16 a[148:151], v[244:247], v[24:27], a[148:151]\n"
"v_add_f32 v156, v156, v172\n"
"v_accvgpr_read_b32 v173, a69\n"
"v_add_f32 v157, v157, v173\n"
"v_mfma_f32_16x16x32_bf16 a[92:95], v[244:247], v[32:35], a[92:95]\n"
"v_accvgpr_read_b32 v174, a70\n"
"v_add_f32 v158, v158, v174\n"
"v_mfma_f32_16x16x32_bf16 a[84:87], v[244:247], v[20:23], a[84:87]\n"
"v_accvgpr_read_b32 v175, a71\n"
"v_add_f32 v159, v159, v175\n"
"v_cvt_pk_bf16_f32 v180, v152, v153\n"
"v_mfma_f32_16x16x32_bf16 a[28:31], v[244:247], v[28:31], a[28:31]\n"
"ds_write_b128 v129, a[156:159] offset:5120\n"
"v_cvt_pk_bf16_f32 v181, v154, v155\n"
"v_mfma_f32_16x16x32_bf16 a[20:23], v[244:247], v[16:19], a[20:23]\n"
"ds_read_b64_tr_b16 v[244:245], v126 offset:30728\n"
"v_cvt_pk_bf16_f32 v182, v156, v157\n"
"v_mfma_f32_16x16x32_bf16 a[32:35], v[248:251], v[64:67], a[32:35]\n"
"ds_read_b64_tr_b16 v[246:247], v126 offset:35848\n"
"v_cvt_pk_bf16_f32 v183, v158, v159\n"
"v_mfma_f32_16x16x32_bf16 a[40:43], v[248:251], v[76:79], a[40:43]\n"
"v_accvgpr_read_b32 v168, a128\n"
"s_waitcnt lgkmcnt(3)\n"
"v_add_f32 v160, v160, v168\n"
"v_mfma_f32_16x16x32_bf16 a[96:99], v[248:251], v[68:71], a[96:99]\n"
"buffer_store_dwordx4 v[180:183], v13, s[56:59], s68 offen offset:1024 sc1\n"
"v_accvgpr_read_b32 v169, a129\n"
"v_mfma_f32_16x16x32_bf16 a[104:107], v[248:251], v[80:83], a[104:107]\n"
"ds_write_b128 v129, a[28:31] offset:1024\n"
"v_add_f32 v161, v161, v169\n"
"v_accvgpr_read_b32 v170, a130\n"
"v_mfma_f32_16x16x32_bf16 a[160:163], v[248:251], v[72:75], a[160:163]\n"
"v_add_f32 v162, v162, v170\n"
"v_accvgpr_read_b32 v171, a131\n"
"v_mfma_f32_16x16x32_bf16 a[168:171], v[248:251], v[84:87], a[168:171]\n"
"ds_read_b64_tr_b16 v[248:249], v127 offset:0\n"
"ds_read_b64_tr_b16 v[250:251], v127 offset:5120\n"
"v_add_f32 v163, v163, v171\n"
"v_mfma_f32_16x16x32_bf16 a[168:171], v[252:255], v[108:111], a[168:171]\n"
"ds_write_b128 v129, a[92:95] offset:3072\n"
"v_accvgpr_read_b32 v172, a132\n"
"v_mfma_f32_16x16x32_bf16 a[160:163], v[252:255], v[96:99], a[160:163]\n"
"v_add_f32 v164, v164, v172\n"
"v_accvgpr_read_b32 v173, a133\n"
"v_add_f32 v165, v165, v173\n"
"v_mfma_f32_16x16x32_bf16 a[104:107], v[252:255], v[104:107], a[104:107]\n"
"v_accvgpr_read_b32 v174, a134\n"
"v_add_f32 v166, v166, v174\n"
"v_mfma_f32_16x16x32_bf16 a[96:99], v[252:255], v[92:95], a[96:99]\n"
"v_accvgpr_read_b32 v175, a135\n"
"v_add_f32 v167, v167, v175\n"
"v_cvt_pk_bf16_f32 v184, v160, v161\n"
"v_mfma_f32_16x16x32_bf16 a[40:43], v[252:255], v[100:103], a[40:43]\n"
"v_cvt_pk_bf16_f32 v185, v162, v163\n"
"v_cvt_pk_bf16_f32 v186, v164, v165\n"
"v_mfma_f32_16x16x32_bf16 a[32:35], v[252:255], v[88:91], a[32:35]\n"
"ds_read_b64_tr_b16 v[252:253], v127 offset:10240\n"
"ds_read_b64_tr_b16 v[254:255], v127 offset:15360\n"
"v_cvt_pk_bf16_f32 v187, v166, v167\n"
"v_mfma_f32_16x16x32_bf16 a[32:35], a[240:243], v[40:43], a[32:35]\n"
"buffer_store_dwordx4 v[184:187], v13, s[56:59], s68 offen offset:2048 sc1\n"
"v_mfma_f32_16x16x32_bf16 a[40:43], a[240:243], v[52:55], a[40:43]\n"
"v_mfma_f32_16x16x32_bf16 a[96:99], a[240:243], v[44:47], a[96:99]\n"
"v_mfma_f32_16x16x32_bf16 a[104:107], a[240:243], v[56:59], a[104:107]\n"
"v_mfma_f32_16x16x32_bf16 a[160:163], a[240:243], v[48:51], a[160:163]\n"
"v_mfma_f32_16x16x32_bf16 a[168:171], a[240:243], v[60:63], a[168:171]\n"
"ds_read_b64_tr_b16 a[240:241], v127 offset:20480\n"
"ds_read_b64_tr_b16 a[242:243], v127 offset:25600\n"
"v_mfma_f32_16x16x32_bf16 a[168:171], a[244:247], v[36:39], a[168:171]\n"
"s_waitcnt lgkmcnt(4)\n"
"s_barrier\n"
"v_mfma_f32_16x16x32_bf16 a[160:163], a[244:247], v[24:27], a[160:163]\n"
"ds_read_b128 v[144:147], v130 offset:0\n"
"ds_read_b128 v[148:151], v130 offset:1024\n"
"v_accvgpr_read_b32 v168, a16\n"
"v_mfma_f32_16x16x32_bf16 a[104:107], a[244:247], v[32:35], a[104:107]\n"
"ds_read_b128 v[152:155], v130 offset:2048\n"
"v_accvgpr_read_b32 v169, a17\n"
"v_mfma_f32_16x16x32_bf16 a[96:99], a[244:247], v[20:23], a[96:99]\n"
"ds_read_b128 v[156:159], v130 offset:3072\n"
"v_accvgpr_read_b32 v170, a18\n"
"v_accvgpr_read_b32 v171, a19\n"
"v_mfma_f32_16x16x32_bf16 a[40:43], a[244:247], v[28:31], a[40:43]\n"
"ds_read_b128 v[160:163], v130 offset:4096\n"
"v_accvgpr_read_b32 v172, a20\n"
"v_mfma_f32_16x16x32_bf16 a[32:35], a[244:247], v[16:19], a[32:35]\n"
"ds_read_b128 v[164:167], v130 offset:5120\n"
"v_accvgpr_read_b32 v173, a21\n"
"v_accvgpr_read_b32 v174, a22\n"
"v_mfma_f32_16x16x32_bf16 a[36:39], a[248:251], v[64:67], a[36:39]\n"
"ds_write_b128 v129, a[104:107] offset:14336\n"
"v_accvgpr_read_b32 v175, a23\n"
"v_mfma_f32_16x16x32_bf16 a[44:47], a[248:251], v[76:79], a[44:47]\n"
"ds_read_b64_tr_b16 a[244:245], v127 offset:30720\n"
"v_mfma_f32_16x16x32_bf16 a[100:103], a[248:251], v[68:71], a[100:103]\n"
"ds_read_b64_tr_b16 a[246:247], v127 offset:35840\n"
"s_waitcnt lgkmcnt(5)\n"
"v_add_f32 v144, v144, v168\n"
"v_mfma_f32_16x16x32_bf16 a[108:111], a[248:251], v[80:83], a[108:111]\n"
"v_add_f32 v145, v145, v169\n"
"v_add_f32 v146, v146, v170\n"
"v_mfma_f32_16x16x32_bf16 a[164:167], a[248:251], v[72:75], a[164:167]\n"
"ds_write_b128 v129, a[40:43] offset:12288\n"
"v_add_f32 v147, v147, v171\n"
"v_mfma_f32_16x16x32_bf16 a[172:175], a[248:251], v[84:87], a[172:175]\n"
"ds_read_b64_tr_b16 a[248:249], v127 offset:8\n"
"v_add_f32 v148, v148, v172\n"
"v_add_f32 v149, v149, v173\n"
"v_mfma_f32_16x16x32_bf16 a[172:175], a[252:255], v[108:111], a[172:175]\n"
"ds_read_b64_tr_b16 a[250:251], v127 offset:5128\n"
"v_add_f32 v150, v150, v174\n"
"v_mfma_f32_16x16x32_bf16 a[164:167], a[252:255], v[96:99], a[164:167]\n"
"v_add_f32 v151, v151, v175\n"
"v_cvt_pk_bf16_f32 v176, v144, v145\n"
"v_cvt_pk_bf16_f32 v177, v146, v147\n"
"v_mfma_f32_16x16x32_bf16 a[108:111], a[252:255], v[104:107], a[108:111]\n"
"ds_write_b128 v129, a[168:171] offset:16384\n"
"v_cvt_pk_bf16_f32 v178, v148, v149\n"
"v_mfma_f32_16x16x32_bf16 a[100:103], a[252:255], v[92:95], a[100:103]\n"
"v_cvt_pk_bf16_f32 v179, v150, v151\n"
"v_accvgpr_read_b32 v168, a80\n"
"v_add_f32 v152, v152, v168\n"
"v_mfma_f32_16x16x32_bf16 a[44:47], a[252:255], v[100:103], a[44:47]\n"
"buffer_store_dwordx4 v[176:179], v13, s[56:59], s69 offen sc1\n"
"v_accvgpr_read_b32 v169, a81\n"
"v_mfma_f32_16x16x32_bf16 a[36:39], a[252:255], v[88:91], a[36:39]\n"
"ds_read_b64_tr_b16 a[252:253], v127 offset:10248\n"
"v_add_f32 v153, v153, v169\n"
"v_accvgpr_read_b32 v170, a82\n"
"v_mfma_f32_16x16x32_bf16 a[36:39], v[240:243], v[40:43], a[36:39]\n"
"ds_read_b64_tr_b16 a[254:255], v127 offset:15368\n"
"v_add_f32 v154, v154, v170\n"
"v_mfma_f32_16x16x32_bf16 a[44:47], v[240:243], v[52:55], a[44:47]\n"
"v_accvgpr_read_b32 v171, a83\n"
"v_add_f32 v155, v155, v171\n"
"v_accvgpr_read_b32 v172, a84\n"
"v_mfma_f32_16x16x32_bf16 a[100:103], v[240:243], v[44:47], a[100:103]\n"
"v_add_f32 v156, v156, v172\n"
"v_accvgpr_read_b32 v173, a85\n"
"v_mfma_f32_16x16x32_bf16 a[108:111], v[240:243], v[56:59], a[108:111]\n"
"v_add_f32 v157, v157, v173\n"
"v_accvgpr_read_b32 v174, a86\n"
"v_add_f32 v158, v158, v174\n"
"v_mfma_f32_16x16x32_bf16 a[164:167], v[240:243], v[48:51], a[164:167]\n"
"v_accvgpr_read_b32 v175, a87\n"
"v_add_f32 v159, v159, v175\n"
"v_mfma_f32_16x16x32_bf16 a[172:175], v[240:243], v[60:63], a[172:175]\n"
"ds_read_b64_tr_b16 v[240:241], v127 offset:20488\n"
"ds_read_b64_tr_b16 v[242:243], v127 offset:25608\n"
"v_cvt_pk_bf16_f32 v180, v152, v153\n"
"v_mfma_f32_16x16x32_bf16 a[172:175], v[244:247], v[36:39], a[172:175]\n"
"v_cvt_pk_bf16_f32 v181, v154, v155\n"
"v_cvt_pk_bf16_f32 v182, v156, v157\n"
"v_mfma_f32_16x16x32_bf16 a[164:167], v[244:247], v[24:27], a[164:167]\n"
"v_cvt_pk_bf16_f32 v183, v158, v159\n"
"v_accvgpr_read_b32 v168, a144\n"
"v_mfma_f32_16x16x32_bf16 a[108:111], v[244:247], v[32:35], a[108:111]\n"
"buffer_store_dwordx4 v[180:183], v13, s[56:59], s69 offen offset:1024 sc1\n"
"s_waitcnt lgkmcnt(2)\n"
"v_add_f32 v160, v160, v168\n"
"v_mfma_f32_16x16x32_bf16 a[100:103], v[244:247], v[20:23], a[100:103]\n"
"v_accvgpr_read_b32 v169, a145\n"
"v_add_f32 v161, v161, v169\n"
"v_mfma_f32_16x16x32_bf16 a[44:47], v[244:247], v[28:31], a[44:47]\n"
"ds_write_b128 v129, a[172:175] offset:17408\n"
"v_accvgpr_read_b32 v170, a146\n"
"v_mfma_f32_16x16x32_bf16 a[36:39], v[244:247], v[16:19], a[36:39]\n"
"ds_read_b64_tr_b16 v[244:245], v127 offset:30728\n"
"v_add_f32 v162, v162, v170\n"
"v_accvgpr_read_b32 v171, a147\n"
"v_mfma_f32_16x16x32_bf16 a[48:51], v[248:251], v[64:67], a[48:51]\n"
"ds_read_b64_tr_b16 v[246:247], v127 offset:35848\n"
"v_add_f32 v163, v163, v171\n"
"v_mfma_f32_16x16x32_bf16 a[56:59], v[248:251], v[76:79], a[56:59]\n"
"v_accvgpr_read_b32 v172, a148\n"
"v_add_f32 v164, v164, v172\n"
"v_accvgpr_read_b32 v173, a149\n"
"v_mfma_f32_16x16x32_bf16 a[112:115], v[248:251], v[68:71], a[112:115]\n"
"ds_write_b128 v129, a[44:47] offset:13312\n"
"v_add_f32 v165, v165, v173\n"
"v_mfma_f32_16x16x32_bf16 a[120:123], v[248:251], v[80:83], a[120:123]\n"
"v_accvgpr_read_b32 v174, a150\n"
"v_add_f32 v166, v166, v174\n"
"v_accvgpr_read_b32 v175, a151\n"
"v_mfma_f32_16x16x32_bf16 a[176:179], v[248:251], v[72:75], a[176:179]\n"
"v_add_f32 v167, v167, v175\n"
"v_cvt_pk_bf16_f32 v184, v160, v161\n"
"v_mfma_f32_16x16x32_bf16 a[184:187], v[248:251], v[84:87], a[184:187]\n"
"v_cvt_pk_bf16_f32 v185, v162, v163\n"
"v_cvt_pk_bf16_f32 v186, v164, v165\n"
"v_cvt_pk_bf16_f32 v187, v166, v167\n"
"v_mfma_f32_16x16x32_bf16 a[184:187], v[252:255], v[108:111], a[184:187]\n"
"buffer_store_dwordx4 v[184:187], v13, s[56:59], s69 offen offset:2048 sc1\n"
"v_mfma_f32_16x16x32_bf16 a[176:179], v[252:255], v[96:99], a[176:179]\n"
"ds_write_b128 v129, a[108:111] offset:15360\n"
"v_mfma_f32_16x16x32_bf16 a[120:123], v[252:255], v[104:107], a[120:123]\n"
"v_mfma_f32_16x16x32_bf16 a[112:115], v[252:255], v[92:95], a[112:115]\n"
"v_mfma_f32_16x16x32_bf16 a[56:59], v[252:255], v[100:103], a[56:59]\n"
"v_mfma_f32_16x16x32_bf16 a[48:51], v[252:255], v[88:91], a[48:51]\n"
"v_mfma_f32_16x16x32_bf16 a[48:51], a[240:243], v[40:43], a[48:51]\n"
"v_mfma_f32_16x16x32_bf16 a[56:59], a[240:243], v[52:55], a[56:59]\n"
"v_mfma_f32_16x16x32_bf16 a[112:115], a[240:243], v[44:47], a[112:115]\n"
"v_mfma_f32_16x16x32_bf16 a[120:123], a[240:243], v[56:59], a[120:123]\n"
"v_mfma_f32_16x16x32_bf16 a[176:179], a[240:243], v[48:51], a[176:179]\n"
"v_mfma_f32_16x16x32_bf16 a[184:187], a[240:243], v[60:63], a[184:187]\n"
"v_mfma_f32_16x16x32_bf16 a[184:187], a[244:247], v[36:39], a[184:187]\n"
"s_waitcnt lgkmcnt(0)\n"
"s_barrier\n"
"v_accvgpr_read_b32 v168, a32\n"
"v_accvgpr_read_b32 v169, a33\n"
"v_accvgpr_read_b32 v170, a34\n"
"v_accvgpr_read_b32 v171, a35\n"
"v_mfma_f32_16x16x32_bf16 a[176:179], a[244:247], v[24:27], a[176:179]\n"
"ds_read_b128 v[144:147], v130 offset:12288\n"
"ds_read_b128 v[148:151], v130 offset:13312\n"
"v_accvgpr_read_b32 v172, a36\n"
"v_accvgpr_read_b32 v173, a37\n"
"v_accvgpr_read_b32 v174, a38\n"
"v_accvgpr_read_b32 v175, a39\n"
"v_mfma_f32_16x16x32_bf16 a[120:123], a[244:247], v[32:35], a[120:123]\n"
"ds_read_b128 v[152:155], v130 offset:14336\n"
"v_mfma_f32_16x16x32_bf16 a[112:115], a[244:247], v[20:23], a[112:115]\n"
"ds_read_b128 v[156:159], v130 offset:15360\n"
"v_mfma_f32_16x16x32_bf16 a[56:59], a[244:247], v[28:31], a[56:59]\n"
"ds_read_b128 v[160:163], v130 offset:16384\n"
"v_mfma_f32_16x16x32_bf16 a[48:51], a[244:247], v[16:19], a[48:51]\n"
"ds_read_b128 v[164:167], v130 offset:17408\n"
"s_waitcnt vmcnt(3)\n"
"s_mov_b32 exec_lo, 0x0\n"
"s_mov_b32 exec_hi, 0xe0\n"
"buffer_store_dword v11, v9, s[56:59], 0 offen sc1\n"
"s_mov_b64 exec, -1\n"
"v_mfma_f32_16x16x32_bf16 a[52:55], a[248:251], v[64:67], a[52:55]\n"
"v_mfma_f32_16x16x32_bf16 a[60:63], a[248:251], v[76:79], a[60:63]\n"
"v_mfma_f32_16x16x32_bf16 a[116:119], a[248:251], v[68:71], a[116:119]\n"
"s_waitcnt lgkmcnt(2)\n"
"v_add_f32 v144, v144, v168\n"
"v_add_f32 v145, v145, v169\n"
"v_add_f32 v146, v146, v170\n"
"v_add_f32 v147, v147, v171\n"
"v_add_f32 v148, v148, v172\n"
"v_mfma_f32_16x16x32_bf16 a[124:127], a[248:251], v[80:83], a[124:127]\n"
"v_add_f32 v149, v149, v173\n"
"v_add_f32 v150, v150, v174\n"
"v_add_f32 v151, v151, v175\n"
"v_cvt_pk_bf16_f32 v176, v144, v145\n"
"v_cvt_pk_bf16_f32 v177, v146, v147\n"
"v_cvt_pk_bf16_f32 v178, v148, v149\n"
"v_mfma_f32_16x16x32_bf16 a[180:183], a[248:251], v[72:75], a[180:183]\n"
"v_cvt_pk_bf16_f32 v179, v150, v151\n"
"v_accvgpr_read_b32 v168, a96\n"
"v_add_f32 v152, v152, v168\n"
"v_accvgpr_read_b32 v169, a97\n"
"v_add_f32 v153, v153, v169\n"
"v_accvgpr_read_b32 v170, a98\n"
"v_mfma_f32_16x16x32_bf16 a[188:191], a[248:251], v[84:87], a[188:191]\n"
"v_add_f32 v154, v154, v170\n"
"v_accvgpr_read_b32 v171, a99\n"
"v_add_f32 v155, v155, v171\n"
"v_accvgpr_read_b32 v172, a100\n"
"v_add_f32 v156, v156, v172\n"
"v_accvgpr_read_b32 v173, a101\n"
"v_mfma_f32_16x16x32_bf16 a[188:191], a[252:255], v[108:111], a[188:191]\n"
"buffer_store_dwordx4 v[176:179], v13, s[56:59], s70 offen sc1\n"
"v_add_f32 v157, v157, v173\n"
"v_mfma_f32_16x16x32_bf16 a[180:183], a[252:255], v[96:99], a[180:183]\n"
"v_accvgpr_read_b32 v174, a102\n"
"v_add_f32 v158, v158, v174\n"
"v_accvgpr_read_b32 v175, a103\n"
"v_mfma_f32_16x16x32_bf16 a[124:127], a[252:255], v[104:107], a[124:127]\n"
"v_add_f32 v159, v159, v175\n"
"v_cvt_pk_bf16_f32 v180, v152, v153\n"
"v_mfma_f32_16x16x32_bf16 a[116:119], a[252:255], v[92:95], a[116:119]\n"
"v_cvt_pk_bf16_f32 v181, v154, v155\n"
"v_cvt_pk_bf16_f32 v182, v156, v157\n"
"v_cvt_pk_bf16_f32 v183, v158, v159\n"
"v_mfma_f32_16x16x32_bf16 a[60:63], a[252:255], v[100:103], a[60:63]\n"
"buffer_store_dwordx4 v[180:183], v13, s[56:59], s70 offen offset:1024 sc1\n"
"v_accvgpr_read_b32 v168, a160\n"
"v_mfma_f32_16x16x32_bf16 a[52:55], a[252:255], v[88:91], a[52:55]\n"
"s_waitcnt lgkmcnt(0)\n"
"v_add_f32 v160, v160, v168\n"
"v_accvgpr_read_b32 v169, a161\n"
"v_mfma_f32_16x16x32_bf16 a[52:55], v[240:243], v[40:43], a[52:55]\n"
"v_add_f32 v161, v161, v169\n"
"v_accvgpr_read_b32 v170, a162\n"
"v_mfma_f32_16x16x32_bf16 a[60:63], v[240:243], v[52:55], a[60:63]\n"
"v_add_f32 v162, v162, v170\n"
"v_accvgpr_read_b32 v171, a163\n"
"v_add_f32 v163, v163, v171\n"
"v_mfma_f32_16x16x32_bf16 a[116:119], v[240:243], v[44:47], a[116:119]\n"
"v_accvgpr_read_b32 v172, a164\n"
"v_add_f32 v164, v164, v172\n"
"v_mfma_f32_16x16x32_bf16 a[124:127], v[240:243], v[56:59], a[124:127]\n"
"v_accvgpr_read_b32 v173, a165\n"
"v_add_f32 v165, v165, v173\n"
"buffer_load_dword v12, v8, s[56:59], 0 offen sc1\n"
"v_mfma_f32_16x16x32_bf16 a[180:183], v[240:243], v[48:51], a[180:183]\n"
"v_accvgpr_read_b32 v174, a166\n"
"v_add_f32 v166, v166, v174\n"
"v_mfma_f32_16x16x32_bf16 a[188:191], v[240:243], v[60:63], a[188:191]\n"
"v_accvgpr_read_b32 v175, a167\n"
"v_add_f32 v167, v167, v175\n"
"v_cvt_pk_bf16_f32 v184, v160, v161\n"
"v_mfma_f32_16x16x32_bf16 a[188:191], v[244:247], v[36:39], a[188:191]\n"
"v_cvt_pk_bf16_f32 v185, v162, v163\n"
"v_cvt_pk_bf16_f32 v186, v164, v165\n"
"v_mfma_f32_16x16x32_bf16 a[180:183], v[244:247], v[24:27], a[180:183]\n"
"v_cvt_pk_bf16_f32 v187, v166, v167\n"
"v_mfma_f32_16x16x32_bf16 a[124:127], v[244:247], v[32:35], a[124:127]\n"
"buffer_store_dwordx4 v[184:187], v13, s[56:59], s70 offen offset:2048 sc1\n"
"v_mfma_f32_16x16x32_bf16 a[116:119], v[244:247], v[20:23], a[116:119]\n"
"v_mfma_f32_16x16x32_bf16 a[60:63], v[244:247], v[28:31], a[60:63]\n"
"v_mfma_f32_16x16x32_bf16 a[52:55], v[244:247], v[16:19], a[52:55]\n"
"ds_write_b128 v129, a[56:59] offset:0\n"
"ds_write_b128 v129, a[120:123] offset:2048\n"
"ds_write_b128 v129, a[184:187] offset:4096\n"
"s_nop 3\n"
"ds_write_b128 v129, a[60:63] offset:1024\n"
"ds_write_b128 v129, a[124:127] offset:3072\n"
"ds_write_b128 v129, a[188:191] offset:5120\n"
"s_waitcnt lgkmcnt(0)\n"
"s_barrier\n"
"ds_read_b128 v[144:147], v130 offset:0\n"
"ds_read_b128 v[148:151], v130 offset:1024\n"
"ds_read_b128 v[152:155], v130 offset:2048\n"
"ds_read_b128 v[156:159], v130 offset:3072\n"
"ds_read_b128 v[160:163], v130 offset:4096\n"
"ds_read_b128 v[164:167], v130 offset:5120\n"
"v_accvgpr_read_b32 v168, a48\n"
"s_waitcnt lgkmcnt(5)\n"
"v_add_f32 v144, v144, v168\n"
"v_accvgpr_read_b32 v169, a49\n"
"v_add_f32 v145, v145, v169\n"
"v_accvgpr_read_b32 v170, a50\n"
"v_add_f32 v146, v146, v170\n"
"v_accvgpr_read_b32 v171, a51\n"
"v_add_f32 v147, v147, v171\n"
"v_accvgpr_read_b32 v172, a52\n"
"s_waitcnt lgkmcnt(4)\n"
"v_add_f32 v148, v148, v172\n"
"v_accvgpr_read_b32 v173, a53\n"
"v_add_f32 v149, v149, v173\n"
"v_accvgpr_read_b32 v174, a54\n"
"v_add_f32 v150, v150, v174\n"
"v_accvgpr_read_b32 v175, a55\n"
"v_add_f32 v151, v151, v175\n"
"v_accvgpr_read_b32 v168, a112\n"
"s_waitcnt lgkmcnt(3)\n"
"v_add_f32 v152, v152, v168\n"
"v_accvgpr_read_b32 v169, a113\n"
"v_add_f32 v153, v153, v169\n"
"v_accvgpr_read_b32 v170, a114\n"
"v_add_f32 v154, v154, v170\n"
"v_accvgpr_read_b32 v171, a115\n"
"v_add_f32 v155, v155, v171\n"
"v_accvgpr_read_b32 v172, a116\n"
"s_waitcnt lgkmcnt(2)\n"
"v_add_f32 v156, v156, v172\n"
"v_accvgpr_read_b32 v173, a117\n"
"v_add_f32 v157, v157, v173\n"
"v_accvgpr_read_b32 v174, a118\n"
"v_add_f32 v158, v158, v174\n"
"v_accvgpr_read_b32 v175, a119\n"
"v_add_f32 v159, v159, v175\n"
"v_accvgpr_read_b32 v168, a176\n"
"s_waitcnt lgkmcnt(1)\n"
"v_add_f32 v160, v160, v168\n"
"v_accvgpr_read_b32 v169, a177\n"
"v_add_f32 v161, v161, v169\n"
"v_accvgpr_read_b32 v170, a178\n"
"v_add_f32 v162, v162, v170\n"
"v_accvgpr_read_b32 v171, a179\n"
"v_add_f32 v163, v163, v171\n"
"v_accvgpr_read_b32 v172, a180\n"
"s_waitcnt lgkmcnt(0)\n"
"v_add_f32 v164, v164, v172\n"
"v_accvgpr_read_b32 v173, a181\n"
"v_add_f32 v165, v165, v173\n"
"v_accvgpr_read_b32 v174, a182\n"
"v_add_f32 v166, v166, v174\n"
"v_accvgpr_read_b32 v175, a183\n"
"v_add_f32 v167, v167, v175\n"
"s_waitcnt vmcnt(0)\n"
"s_mov_b32 exec_lo, 0x0\n"
"s_mov_b32 exec_hi, 0xc00\n"
"buffer_store_dword v11, v9, s[56:59], 0 offen sc1\n"
"s_mov_b64 exec, -1\n"
"v_cmp_eq_u32 vcc, v12, v10\n"
"s_nop 0\n"
"s_and_b32 s92, vcc_hi, 0xe0\n"
"s_cmp_eq_u32 s92, 0xe0\n"
"s_cbranch_scc0 LpollA_%=\n"
"LprA_%=:\n"
"buffer_load_dword v12, v8, s[56:59], 0 offen sc1\n"
"buffer_load_dword v134, v8, s[56:59], 0 offen offset:256 sc1\n"
"buffer_load_dwordx4 v[188:191], v13, s[56:59], s14 offen sc1\n"
"buffer_load_dwordx4 v[192:195], v13, s[56:59], s14 offen offset:1024 sc1\n"
"buffer_load_dwordx4 v[196:199], v13, s[56:59], s14 offen offset:2048 sc1\n"
"buffer_load_dwordx4 v[224:227], v13, s[56:59], s65 offen sc1\n"
"buffer_load_dwordx4 v[228:231], v13, s[56:59], s65 offen offset:1024 sc1\n"
"buffer_load_dwordx4 v[232:235], v13, s[56:59], s65 offen offset:2048 sc1\n"
"buffer_load_dwordx4 v[236:239], v13, s[56:59], s65 offen offset:3072 sc1\n"
"s_waitcnt vmcnt(7)\n"
"v_cmp_ne_u32 vcc, v134, v11\n"
"s_nop 0\n"
"s_cmp_lg_u32 vcc_hi, 0\n"
"s_cbranch_scc1 Lre_%=\n"
"Lrr_%=:\n"
"v_cmp_eq_u32 vcc, v12, v10\n"
"s_nop 0\n"
"s_and_b32 s92, vcc_hi, 0xc00\n"
"s_cmp_eq_u32 s92, 0xc00\n"
"s_cbranch_scc0 LpollC_%=\n"
"LprC_%=:\n"
"buffer_load_dwordx4 v[200:203], v13, s[56:59], s14 offen offset:3072 sc1\n"
"buffer_load_dwordx4 v[204:207], v13, s[56:59], s64 offen sc1\n"
"buffer_load_dwordx4 v[208:211], v13, s[56:59], s64 offen offset:1024 sc1\n"
"buffer_load_dwordx4 v[212:215], v13, s[56:59], s64 offen offset:2048 sc1\n"
"buffer_load_dwordx4 v[216:219], v13, s[56:59], s64 offen offset:3072 sc1\n"
"buffer_load_dwordx4 v[220:223], v13, s[56:59], s67 offen sc1\n"
"s_waitcnt vmcnt(9)\n"
"v_lshlrev_b32 v16, 16, v224\n"
"v_and_b32 v17, 0xffff0000, v224\n"
"v_lshlrev_b32 v18, 16, v225\n"
"v_and_b32 v19, 0xffff0000, v225\n"
"v_lshlrev_b32 v20, 16, v226\n"
"v_and_b32 v21, 0xffff0000, v226\n"
"v_lshlrev_b32 v22, 16, v227\n"
"v_and_b32 v23, 0xffff0000, v227\n"
"s_waitcnt vmcnt(8)\n"
"v_lshlrev_b32 v168, 16, v228\n"
"v_and_b32 v169, 0xffff0000, v228\n"
"v_add_f32 v16, v16, v168\n"
"v_add_f32 v17, v17, v169\n"
"v_lshlrev_b32 v168, 16, v229\n"
"v_and_b32 v169, 0xffff0000, v229\n"
"v_add_f32 v18, v18, v168\n"
"v_add_f32 v19, v19, v169\n"
"v_lshlrev_b32 v168, 16, v230\n"
"v_and_b32 v169, 0xffff0000, v230\n"
"v_add_f32 v20, v20, v168\n"
"v_add_f32 v21, v21, v169\n"
"v_lshlrev_b32 v168, 16, v231\n"
"v_and_b32 v169, 0xffff0000, v231\n"
"v_add_f32 v22, v22, v168\n"
"v_add_f32 v23, v23, v169\n"
"s_waitcnt vmcnt(7)\n"
"v_lshlrev_b32 v168, 16, v232\n"
"v_and_b32 v169, 0xffff0000, v232\n"
"v_add_f32 v16, v16, v168\n"
"v_add_f32 v17, v17, v169\n"
"v_lshlrev_b32 v168, 16, v233\n"
"v_and_b32 v169, 0xffff0000, v233\n"
"v_add_f32 v18, v18, v168\n"
"v_add_f32 v19, v19, v169\n"
"v_lshlrev_b32 v168, 16, v234\n"
"v_and_b32 v169, 0xffff0000, v234\n"
"v_add_f32 v20, v20, v168\n"
"v_add_f32 v21, v21, v169\n"
"v_lshlrev_b32 v168, 16, v235\n"
"v_and_b32 v169, 0xffff0000, v235\n"
"v_add_f32 v22, v22, v168\n"
"v_add_f32 v23, v23, v169\n"
"s_waitcnt vmcnt(6)\n"
"v_lshlrev_b32 v168, 16, v236\n"
"v_and_b32 v169, 0xffff0000, v236\n"
"v_add_f32 v16, v16, v168\n"
"v_add_f32 v17, v17, v169\n"
"v_lshlrev_b32 v168, 16, v237\n"
"v_and_b32 v169, 0xffff0000, v237\n"
"v_add_f32 v18, v18, v168\n"
"v_add_f32 v19, v19, v169\n"
"v_lshlrev_b32 v168, 16, v238\n"
"v_and_b32 v169, 0xffff0000, v238\n"
"v_add_f32 v20, v20, v168\n"
"v_add_f32 v21, v21, v169\n"
"v_lshlrev_b32 v168, 16, v239\n"
"v_and_b32 v169, 0xffff0000, v239\n"
"v_add_f32 v22, v22, v168\n"
"v_add_f32 v23, v23, v169\n"
"v_cvt_pk_bf16_f32 v24, v16, v17\n"
"v_cvt_pk_bf16_f32 v25, v18, v19\n"
"v_cvt_pk_bf16_f32 v26, v20, v21\n"
"v_cvt_pk_bf16_f32 v27, v22, v23\n"
"buffer_store_dwordx4 v[24:27], v128, s[60:63], s75 offen sc1\n"
"v_lshlrev_b32 v168, 16, v188\n"
"v_and_b32 v169, 0xffff0000, v188\n"
"v_add_f32 v144, v144, v168\n"
"v_add_f32 v145, v145, v169\n"
"v_lshlrev_b32 v168, 16, v189\n"
"v_and_b32 v169, 0xffff0000, v189\n"
"v_add_f32 v146, v146, v168\n"
"v_add_f32 v147, v147, v169\n"
"v_lshlrev_b32 v168, 16, v190\n"
"v_and_b32 v169, 0xffff0000, v190\n"
"v_add_f32 v148, v148, v168\n"
"v_add_f32 v149, v149, v169\n"
"v_lshlrev_b32 v168, 16, v191\n"
"v_and_b32 v169, 0xffff0000, v191\n"
"v_add_f32 v150, v150, v168\n"
"v_add_f32 v151, v151, v169\n"
"v_lshlrev_b32 v168, 16, v192\n"
"v_and_b32 v169, 0xffff0000, v192\n"
"v_add_f32 v152, v152, v168\n"
"v_add_f32 v153, v153, v169\n"
"v_lshlrev_b32 v168, 16, v193\n"
"v_and_b32 v169, 0xffff0000, v193\n"
"v_add_f32 v154, v154, v168\n"
"v_add_f32 v155, v155, v169\n"
"v_lshlrev_b32 v168, 16, v194\n"
"v_and_b32 v169, 0xffff0000, v194\n"
"v_add_f32 v156, v156, v168\n"
"v_add_f32 v157, v157, v169\n"
"v_lshlrev_b32 v168, 16, v195\n"
"v_and_b32 v169, 0xffff0000, v195\n"
"v_add_f32 v158, v158, v168\n"
"v_add_f32 v159, v159, v169\n"
"v_lshlrev_b32 v168, 16, v196\n"
"v_and_b32 v169, 0xffff0000, v196\n"
"v_add_f32 v160, v160, v168\n"
"v_add_f32 v161, v161, v169\n"
"v_lshlrev_b32 v168, 16, v197\n"
"v_and_b32 v169, 0xffff0000, v197\n"
"v_add_f32 v162, v162, v168\n"
"v_add_f32 v163, v163, v169\n"
"v_lshlrev_b32 v168, 16, v198\n"
"v_and_b32 v169, 0xffff0000, v198\n"
"v_add_f32 v164, v164, v168\n"
"v_add_f32 v165, v165, v169\n"
"v_lshlrev_b32 v168, 16, v199\n"
"v_and_b32 v169, 0xffff0000, v199\n"
"v_add_f32 v166, v166, v168\n"
"v_add_f32 v167, v167, v169\n"
"s_waitcnt vmcnt(6)\n"
"v_lshlrev_b32 v168, 16, v200\n"
"v_and_b32 v169, 0xffff0000, v200\n"
"v_add_f32 v144, v144, v168\n"
"v_add_f32 v145, v145, v169\n"
"v_lshlrev_b32 v168, 16, v201\n"
"v_and_b32 v169, 0xffff0000, v201\n"
"v_add_f32 v146, v146, v168\n"
"v_add_f32 v147, v147, v169\n"
"v_lshlrev_b32 v168, 16, v202\n"
"v_and_b32 v169, 0xffff0000, v202\n"
"v_add_f32 v148, v148, v168\n"
"v_add_f32 v149, v149, v169\n"
"v_lshlrev_b32 v168, 16, v203\n"
"v_and_b32 v169, 0xffff0000, v203\n"
"v_add_f32 v150, v150, v168\n"
"v_add_f32 v151, v151, v169\n"
"s_waitcnt vmcnt(5)\n"
"v_lshlrev_b32 v168, 16, v204\n"
"v_and_b32 v169, 0xffff0000, v204\n"
"v_add_f32 v152, v152, v168\n"
"v_add_f32 v153, v153, v169\n"
"v_lshlrev_b32 v168, 16, v205\n"
"v_and_b32 v169, 0xffff0000, v205\n"
"v_add_f32 v154, v154, v168\n"
"v_add_f32 v155, v155, v169\n"
"v_lshlrev_b32 v168, 16, v206\n"
"v_and_b32 v169, 0xffff0000, v206\n"
"v_add_f32 v156, v156, v168\n"
"v_add_f32 v157, v157, v169\n"
"v_lshlrev_b32 v168, 16, v207\n"
"v_and_b32 v169, 0xffff0000, v207\n"
"v_add_f32 v158, v158, v168\n"
"v_add_f32 v159, v159, v169\n"
"s_waitcnt vmcnt(4)\n"
"v_lshlrev_b32 v168, 16, v208\n"
"v_and_b32 v169, 0xffff0000, v208\n"
"v_add_f32 v160, v160, v168\n"
"v_add_f32 v161, v161, v169\n"
"v_lshlrev_b32 v168, 16, v209\n"
"v_and_b32 v169, 0xffff0000, v209\n"
"v_add_f32 v162, v162, v168\n"
"v_add_f32 v163, v163, v169\n"
"v_lshlrev_b32 v168, 16, v210\n"
"v_and_b32 v169, 0xffff0000, v210\n"
"v_add_f32 v164, v164, v168\n"
"v_add_f32 v165, v165, v169\n"
"v_lshlrev_b32 v168, 16, v211\n"
"v_and_b32 v169, 0xffff0000, v211\n"
"v_add_f32 v166, v166, v168\n"
"v_add_f32 v167, v167, v169\n"
"s_waitcnt vmcnt(3)\n"
"v_lshlrev_b32 v168, 16, v212\n"
"v_and_b32 v169, 0xffff0000, v212\n"
"v_add_f32 v144, v144, v168\n"
"v_add_f32 v145, v145, v169\n"
"v_lshlrev_b32 v168, 16, v213\n"
"v_and_b32 v169, 0xffff0000, v213\n"
"v_add_f32 v146, v146, v168\n"
"v_add_f32 v147, v147, v169\n"
"v_lshlrev_b32 v168, 16, v214\n"
"v_and_b32 v169, 0xffff0000, v214\n"
"v_add_f32 v148, v148, v168\n"
"v_add_f32 v149, v149, v169\n"
"v_lshlrev_b32 v168, 16, v215\n"
"v_and_b32 v169, 0xffff0000, v215\n"
"v_add_f32 v150, v150, v168\n"
"v_add_f32 v151, v151, v169\n"
"v_cvt_pk_bf16_f32 v176, v144, v145\n"
"v_cvt_pk_bf16_f32 v177, v146, v147\n"
"v_cvt_pk_bf16_f32 v178, v148, v149\n"
"v_cvt_pk_bf16_f32 v179, v150, v151\n"
"buffer_store_dwordx4 v[176:179], v128, s[60:63], s66 offen sc1\n"
"s_add_u32 s66, s66, 81920\n"
"s_waitcnt vmcnt(3)\n"
"v_lshlrev_b32 v168, 16, v216\n"
"v_and_b32 v169, 0xffff0000, v216\n"
"v_add_f32 v152, v152, v168\n"
"v_add_f32 v153, v153, v169\n"
"v_lshlrev_b32 v168, 16, v217\n"
"v_and_b32 v169, 0xffff0000, v217\n"
"v_add_f32 v154, v154, v168\n"
"v_add_f32 v155, v155, v169\n"
"v_lshlrev_b32 v168, 16, v218\n"
"v_and_b32 v169, 0xffff0000, v218\n"
"v_add_f32 v156, v156, v168\n"
"v_add_f32 v157, v157, v169\n"
"v_lshlrev_b32 v168, 16, v219\n"
"v_and_b32 v169, 0xffff0000, v219\n"
"v_add_f32 v158, v158, v168\n"
"v_add_f32 v159, v159, v169\n"
"v_cvt_pk_bf16_f32 v180, v152, v153\n"
"v_cvt_pk_bf16_f32 v181, v154, v155\n"
"v_cvt_pk_bf16_f32 v182, v156, v157\n"
"v_cvt_pk_bf16_f32 v183, v158, v159\n"
"buffer_store_dwordx4 v[180:183], v128, s[60:63], s66 offen sc1\n"
"s_add_u32 s66, s66, 81920\n"
"s_waitcnt vmcnt(3)\n"
"v_lshlrev_b32 v168, 16, v220\n"
"v_and_b32 v169, 0xffff0000, v220\n"
"v_add_f32 v160, v160, v168\n"
"v_add_f32 v161, v161, v169\n"
"v_lshlrev_b32 v168, 16, v221\n"
"v_and_b32 v169, 0xffff0000, v221\n"
"v_add_f32 v162, v162, v168\n"
"v_add_f32 v163, v163, v169\n"
"v_lshlrev_b32 v168, 16, v222\n"
"v_and_b32 v169, 0xffff0000, v222\n"
"v_add_f32 v164, v164, v168\n"
"v_add_f32 v165, v165, v169\n"
"v_lshlrev_b32 v168, 16, v223\n"
"v_and_b32 v169, 0xffff0000, v223\n"
"v_add_f32 v166, v166, v168\n"
"v_add_f32 v167, v167, v169\n"
"v_cvt_pk_bf16_f32 v184, v160, v161\n"
"v_cvt_pk_bf16_f32 v185, v162, v163\n"
"v_cvt_pk_bf16_f32 v186, v164, v165\n"
"v_cvt_pk_bf16_f32 v187, v166, v167\n"
"buffer_store_dwordx4 v[184:187], v128, s[60:63], s66 offen sc1\n"
"s_branch Lend_%=\n"
"Ltr0_%=:\n"
"s_add_u32 s97, s28, 0\n"
"s_and_b32 s97, s97, 0x1ffff\n"
"s_mov_b32 s96, 0xff7fffff\n"
"s_getpc_b64 s[98:99]\n"
"s_branch Lslow0_%=\n"
"s_branch Lrt0_%=\n"
"Ltr1_%=:\n"
"s_add_u32 s97, s28, 8192\n"
"s_and_b32 s97, s97, 0x1ffff\n"
"s_mov_b32 s96, 0\n"
"s_getpc_b64 s[98:99]\n"
"s_branch Lslow1_%=\n"
"s_branch Lrt1_%=\n"
"Ltr2_%=:\n"
"s_add_u32 s97, s28, 16384\n"
"s_and_b32 s97, s97, 0x1ffff\n"
"s_mov_b32 s96, 0\n"
"s_getpc_b64 s[98:99]\n"
"s_branch Lslow0_%=\n"
"s_branch Lrt2_%=\n"
"Ltr3_%=:\n"
"s_add_u32 s97, s28, 24576\n"
"s_and_b32 s97, s97, 0x1ffff\n"
"s_mov_b32 s96, 0\n"
"s_getpc_b64 s[98:99]\n"
"s_branch Lslow1_%=\n"
"s_branch Lrt3_%=\n"
"Ltr4_%=:\n"
"s_add_u32 s97, s28, 32768\n"
"s_and_b32 s97, s97, 0x1ffff\n"
"s_mov_b32 s96, 0\n"
"s_getpc_b64 s[98:99]\n"
"s_branch Lslow0_%=\n"
"s_branch Lrt4_%=\n"
"Ltr5_%=:\n"
"s_add_u32 s97, s28, 40960\n"
"s_and_b32 s97, s97, 0x1ffff\n"
"s_mov_b32 s96, 0\n"
"s_getpc_b64 s[98:99]\n"
"s_branch Lslow1_%=\n"
"s_branch Lrt5_%=\n"
"Ltr6_%=:\n"
"s_add_u32 s97, s28, 49152\n"
"s_and_b32 s97, s97, 0x1ffff\n"
"s_mov_b32 s96, 0\n"
"s_getpc_b64 s[98:99]\n"
"s_branch Lslow0_%=\n"
"s_branch Lrt6_%=\n"
"Ltr7_%=:\n"
"s_add_u32 s97, s28, 57344\n"
"s_and_b32 s97, s97, 0x1ffff\n"
"s_mov_b32 s96, 0\n"
"s_getpc_b64 s[98:99]\n"
"s_branch Lslow1_%=\n"
"s_branch Lrt7_%=\n"
"Ltr8_%=:\n"
"s_add_u32 s97, s28, 65536\n"
"s_and_b32 s97, s97, 0x1ffff\n"
"s_mov_b32 s96, 0\n"
"s_getpc_b64 s[98:99]\n"
"s_branch Lslow0_%=\n"
"s_branch Lrt8_%=\n"
"Ltr9_%=:\n"
"s_add_u32 s97, s28, 73728\n"
"s_and_b32 s97, s97, 0x1ffff\n"
"s_mov_b32 s96, 0\n"
"s_getpc_b64 s[98:99]\n"
"s_branch Lslow1_%=\n"
"s_branch Lrt9_%=\n"
"Ltr10_%=:\n"
"s_add_u32 s97, s28, 81920\n"
"s_and_b32 s97, s97, 0x1ffff\n"
"s_mov_b32 s96, 0\n"
"s_getpc_b64 s[98:99]\n"
"s_branch Lslow0_%=\n"
"s_branch Lrt10_%=\n"
"Ltr11_%=:\n"
"s_add_u32 s97, s28, 90112\n"
"s_and_b32 s97, s97, 0x1ffff\n"
"s_mov_b32 s96, 0\n"
"s_getpc_b64 s[98:99]\n"
"s_branch Lslow1_%=\n"
"s_branch Lrt11_%=\n"
"Ltr12_%=:\n"
"s_add_u32 s97, s28, 98304\n"
"s_and_b32 s97, s97, 0x1ffff\n"
"s_mov_b32 s96, 0\n"
"s_getpc_b64 s[98:99]\n"
"s_branch Lslow0_%=\n"
"s_branch Lrt12_%=\n"
"Ltr13_%=:\n"
"s_add_u32 s97, s28, 106496\n"
"s_and_b32 s97, s97, 0x1ffff\n"
"s_mov_b32 s96, 0\n"
"s_getpc_b64 s[98:99]\n"
"s_branch Lslow1_%=\n"
"s_branch Lrt13_%=\n"
"Ltr14_%=:\n"
"s_add_u32 s97, s28, 114688\n"
"s_and_b32 s97, s97, 0x1ffff\n"
"s_mov_b32 s96, 0\n"
"s_getpc_b64 s[98:99]\n"
"s_branch Lslow0_%=\n"
"s_branch Lrt14_%=\n"
"Ltr15_%=:\n"
"s_add_u32 s97, s28, 122880\n"
"s_and_b32 s97, s97, 0x1ffff\n"
"s_mov_b32 s96, 0\n"
"s_getpc_b64 s[98:99]\n"
"s_branch Lslow1_%=\n"
"s_branch Lrt15_%=\n"
"Lpolla_%=:\n"
"s_mov_b32 s93, 0\n"
"s_waitcnt vmcnt(0)\n"
"buffer_load_dword v139, v8, s[56:59], 0 offen sc1\n"
"s_sleep 2\n"
"Lpla_%=:\n"
"buffer_load_dword v140, v8, s[56:59], 0 offen sc1\n"
"s_waitcnt vmcnt(1)\n"
"v_cmp_eq_u32 vcc, v139, v10\n"
"s_nop 0\n"
"s_and_b32 s92, vcc_lo, 0xe0\n"
"s_cmp_eq_u32 s92, 0xe0\n"
"s_cbranch_scc1 Lpra_%=\n"
"buffer_load_dword v139, v8, s[56:59], 0 offen sc1\n"
"s_waitcnt vmcnt(1)\n"
"v_cmp_eq_u32 vcc, v140, v10\n"
"s_nop 0\n"
"s_and_b32 s92, vcc_lo, 0xe0\n"
"s_cmp_eq_u32 s92, 0xe0\n"
"s_cbranch_scc1 Lpra_%=\n"
"s_add_u32 s93, s93, 1\n"
"s_cmp_lt_u32 s93, 0x40000\n"
"s_cbranch_scc1 Lpla_%=\n"
"s_branch Lpra_%=\n"
"Lpollb_%=:\n"
"s_mov_b32 s93, 0\n"
"s_waitcnt vmcnt(0)\n"
"buffer_load_dword v139, v8, s[56:59], 0 offen sc1\n"
"s_sleep 2\n"
"Lplb_%=:\n"
"buffer_load_dword v140, v8, s[56:59], 0 offen sc1\n"
"s_waitcnt vmcnt(1)\n"
"v_cmp_eq_u32 vcc, v139, v10\n"
"s_nop 0\n"
"s_and_b32 s92, vcc_lo, 0xe00\n"
"s_cmp_eq_u32 s92, 0xe00\n"
"s_cbranch_scc1 Lprb_%=\n"
"buffer_load_dword v139, v8, s[56:59], 0 offen sc1\n"
"s_waitcnt vmcnt(1)\n"
"v_cmp_eq_u32 vcc, v140, v10\n"
"s_nop 0\n"
"s_and_b32 s92, vcc_lo, 0xe00\n"
"s_cmp_eq_u32 s92, 0xe00\n"
"s_cbranch_scc1 Lprb_%=\n"
"s_add_u32 s93, s93, 1\n"
"s_cmp_lt_u32 s93, 0x40000\n"
"s_cbranch_scc1 Lplb_%=\n"
"s_branch Lprb_%=\n"
"Lpollc_%=:\n"
"s_mov_b32 s93, 0\n"
"s_waitcnt vmcnt(0)\n"
"buffer_load_dword v139, v8, s[56:59], 0 offen sc1\n"
"s_sleep 2\n"
"Lplc_%=:\n"
"buffer_load_dword v140, v8, s[56:59], 0 offen sc1\n"
"s_waitcnt vmcnt(1)\n"
"v_cmp_eq_u32 vcc, v139, v10\n"
"s_nop 0\n"
"s_and_b32 s92, vcc_lo, 0xe000\n"
"s_cmp_eq_u32 s92, 0xe000\n"
"s_cbranch_scc1 Lprc_%=\n"
"buffer_load_dword v139, v8, s[56:59], 0 offen sc1\n"
"s_waitcnt vmcnt(1)\n"
"v_cmp_eq_u32 vcc, v140, v10\n"
"s_nop 0\n"
"s_and_b32 s92, vcc_lo, 0xe000\n"
"s_cmp_eq_u32 s92, 0xe000\n"
"s_cbranch_scc1 Lprc_%=\n"
"s_add_u32 s93, s93, 1\n"
"s_cmp_lt_u32 s93, 0x40000\n"
"s_cbranch_scc1 Lplc_%=\n"
"s_branch Lprc_%=\n"
"Lpolld_%=:\n"
"s_mov_b32 s93, 0\n"
"s_waitcnt vmcnt(0)\n"
"buffer_load_dword v139, v8, s[56:59], 0 offen sc1\n"
"s_sleep 2\n"
"Lpld_%=:\n"
"buffer_load_dword v140, v8, s[56:59], 0 offen sc1\n"
"s_waitcnt vmcnt(1)\n"
"v_cmp_eq_u32 vcc, v139, v10\n"
"s_nop 0\n"
"s_and_b32 s92, vcc_lo, 0xe0000\n"
"s_cmp_eq_u32 s92, 0xe0000\n"
"s_cbranch_scc1 Lprd_%=\n"
"buffer_load_dword v139, v8, s[56:59], 0 offen sc1\n"
"s_waitcnt vmcnt(1)\n"
"v_cmp_eq_u32 vcc, v140, v10\n"
"s_nop 0\n"
"s_and_b32 s92, vcc_lo, 0xe0000\n"
"s_cmp_eq_u32 s92, 0xe0000\n"
"s_cbranch_scc1 Lprd_%=\n"
"s_add_u32 s93, s93, 1\n"
"s_cmp_lt_u32 s93, 0x40000\n"
"s_cbranch_scc1 Lpld_%=\n"
"s_branch Lprd_%=\n"
"LpollA_%=:\n"
"s_mov_b32 s93, 0\n"
"s_waitcnt vmcnt(0)\n"
"buffer_load_dword v139, v8, s[56:59], 0 offen sc1\n"
"s_sleep 2\n"
"LplA_%=:\n"
"buffer_load_dword v140, v8, s[56:59], 0 offen sc1\n"
"s_waitcnt vmcnt(1)\n"
"v_cmp_eq_u32 vcc, v139, v10\n"
"s_nop 0\n"
"s_and_b32 s92, vcc_hi, 0xe0\n"
"s_cmp_eq_u32 s92, 0xe0\n"
"s_cbranch_scc1 LprA_%=\n"
"buffer_load_dword v139, v8, s[56:59], 0 offen sc1\n"
"s_waitcnt vmcnt(1)\n"
"v_cmp_eq_u32 vcc, v140, v10\n"
"s_nop 0\n"
"s_and_b32 s92, vcc_hi, 0xe0\n"
"s_cmp_eq_u32 s92, 0xe0\n"
"s_cbranch_scc1 LprA_%=\n"
"s_add_u32 s93, s93, 1\n"
"s_cmp_lt_u32 s93, 0x40000\n"
"s_cbranch_scc1 LplA_%=\n"
"s_branch LprA_%=\n"
"Lre_%=:\n"
"v_mov_b32 v11, v134\n"
"s_mov_b32 exec_lo, 0x0\n"
"s_mov_b32 exec_hi, 0xce0\n"
"buffer_store_dword v11, v9, s[56:59], 0 offen sc1\n"
"s_mov_b64 exec, -1\n"
"s_waitcnt vmcnt(0)\n"
"s_branch Lrr_%=\n"
"LpollC_%=:\n"
"s_mov_b32 s93, 0\n"
"s_waitcnt vmcnt(0)\n"
"buffer_load_dword v139, v8, s[56:59], 0 offen sc1\n"
"s_sleep 2\n"
"LplC_%=:\n"
"buffer_load_dword v140, v8, s[56:59], 0 offen sc1\n"
"s_waitcnt vmcnt(1)\n"
"v_cmp_eq_u32 vcc, v139, v10\n"
"s_nop 0\n"
"s_and_b32 s92, vcc_hi, 0xc00\n"
"s_cmp_eq_u32 s92, 0xc00\n"
"s_cbranch_scc1 LprC_%=\n"
"buffer_load_dword v139, v8, s[56:59], 0 offen sc1\n"
"s_waitcnt vmcnt(1)\n"
"v_cmp_eq_u32 vcc, v140, v10\n"
"s_nop 0\n"
"s_and_b32 s92, vcc_hi, 0xc00\n"
"s_cmp_eq_u32 s92, 0xc00\n"
"s_cbranch_scc1 LprC_%=\n"
"s_add_u32 s93, s93, 1\n"
"s_cmp_lt_u32 s93, 0x40000\n"
"s_cbranch_scc1 LplC_%=\n"
"s_branch LprC_%=\n"
"Lslow0_%=:\n"
"s_nop 15\n"
"v_and_b32 v137, 0xf0, v13\n"
"v_lshlrev_b32 v137, 4, v137\n"
"v_lshrrev_b32 v138, 8, v13\n"
"v_lshl_add_u32 v137, v138, 4, v137\n"
"s_lshl_b32 s92, s35, 17\n"
"v_add_u32 v137, s92, v137\n"
"buffer_load_dwordx4 a[224:227], v137, s[44:47], s97 offen\n"
"buffer_load_dwordx4 a[228:231], v137, s[44:47], s97 offen offset:64\n"
"buffer_load_dwordx4 a[232:235], v137, s[44:47], s97 offen offset:128\n"
"buffer_load_dwordx4 a[236:239], v137, s[44:47], s97 offen offset:192\n"
"s_add_u32 s97, s97, 4096\n"
"s_waitcnt vmcnt(0)\n"
"v_mfma_f32_16x16x32_bf16 v[16:19], a[224:227], a[192:195], v[208:211]\n"
"v_mfma_f32_16x16x32_bf16 v[24:27], a[224:227], a[196:199], v[212:215]\n"
"v_mfma_f32_16x16x32_bf16 v[32:35], a[224:227], v[176:179], v[216:219]\n"
"v_mfma_f32_16x16x32_bf16 v[40:43], a[224:227], v[180:183], v[220:223]\n"
"v_mfma_f32_16x16x32_bf16 v[48:51], a[224:227], v[144:147], v[224:227]\n"
"v_mfma_f32_16x16x32_bf16 v[56:59], a[224:227], v[148:151], v[228:231]\n"
"v_mfma_f32_16x16x32_bf16 v[16:19], a[228:231], a[200:203], v[16:19]\n"
"v_mfma_f32_16x16x32_bf16 v[24:27], a[228:231], a[204:207], v[24:27]\n"
"v_mfma_f32_16x16x32_bf16 v[32:35], a[228:231], v[184:187], v[32:35]\n"
"v_mfma_f32_16x16x32_bf16 v[40:43], a[228:231], v[188:191], v[40:43]\n"
"v_mfma_f32_16x16x32_bf16 v[48:51], a[228:231], v[152:155], v[48:51]\n"
"v_mfma_f32_16x16x32_bf16 v[56:59], a[228:231], v[156:159], v[56:59]\n"
"v_mfma_f32_16x16x32_bf16 v[16:19], a[232:235], a[208:211], v[16:19]\n"
"v_mfma_f32_16x16x32_bf16 v[24:27], a[232:235], a[212:215], v[24:27]\n"
"v_mfma_f32_16x16x32_bf16 v[32:35], a[232:235], v[192:195], v[32:35]\n"
"v_mfma_f32_16x16x32_bf16 v[40:43], a[232:235], v[196:199], v[40:43]\n"
"v_mfma_f32_16x16x32_bf16 v[48:51], a[232:235], v[160:163], v[48:51]\n"
"v_mfma_f32_16x16x32_bf16 v[56:59], a[232:235], v[164:167], v[56:59]\n"
"v_mfma_f32_16x16x32_bf16 v[16:19], a[236:239], a[216:219], v[16:19]\n"
"v_mfma_f32_16x16x32_bf16 v[24:27], a[236:239], a[220:223], v[24:27]\n"
"v_mfma_f32_16x16x32_bf16 v[32:35], a[236:239], v[200:203], v[32:35]\n"
"v_mfma_f32_16x16x32_bf16 v[40:43], a[236:239], v[204:207], v[40:43]\n"
"v_mfma_f32_16x16x32_bf16 v[48:51], a[236:239], v[168:171], v[48:51]\n"
"v_mfma_f32_16x16x32_bf16 v[56:59], a[236:239], v[172:175], v[56:59]\n"
"s_nop 15\n"
"buffer_load_dwordx4 a[224:227], v137, s[44:47], s97 offen\n"
"buffer_load_dwordx4 a[228:231], v137, s[44:47], s97 offen offset:64\n"
"buffer_load_dwordx4 a[232:235], v137, s[44:47], s97 offen offset:128\n"
"buffer_load_dwordx4 a[236:239], v137, s[44:47], s97 offen offset:192\n"
"s_add_u32 s97, s97, 4096\n"
"s_waitcnt vmcnt(0)\n"
"v_mfma_f32_16x16x32_bf16 v[20:23], a[224:227], a[192:195], v[208:211]\n"
"v_mfma_f32_16x16x32_bf16 v[28:31], a[224:227], a[196:199], v[212:215]\n"
"v_mfma_f32_16x16x32_bf16 v[36:39], a[224:227], v[176:179], v[216:219]\n"
"v_mfma_f32_16x16x32_bf16 v[44:47], a[224:227], v[180:183], v[220:223]\n"
"v_mfma_f32_16x16x32_bf16 v[52:55], a[224:227], v[144:147], v[224:227]\n"
"v_mfma_f32_16x16x32_bf16 v[60:63], a[224:227], v[148:151], v[228:231]\n"
"v_mfma_f32_16x16x32_bf16 v[20:23], a[228:231], a[200:203], v[20:23]\n"
"v_mfma_f32_16x16x32_bf16 v[28:31], a[228:231], a[204:207], v[28:31]\n"
"v_mfma_f32_16x16x32_bf16 v[36:39], a[228:231], v[184:187], v[36:39]\n"
"v_mfma_f32_16x16x32_bf16 v[44:47], a[228:231], v[188:191], v[44:47]\n"
"v_mfma_f32_16x16x32_bf16 v[52:55], a[228:231], v[152:155], v[52:55]\n"
"v_mfma_f32_16x16x32_bf16 v[60:63], a[228:231], v[156:159], v[60:63]\n"
"v_mfma_f32_16x16x32_bf16 v[20:23], a[232:235], a[208:211], v[20:23]\n"
"v_mfma_f32_16x16x32_bf16 v[28:31], a[232:235], a[212:215], v[28:31]\n"
"v_mfma_f32_16x16x32_bf16 v[36:39], a[232:235], v[192:195], v[36:39]\n"
"v_mfma_f32_16x16x32_bf16 v[44:47], a[232:235], v[196:199], v[44:47]\n"
"v_mfma_f32_16x16x32_bf16 v[52:55], a[232:235], v[160:163], v[52:55]\n"
"v_mfma_f32_16x16x32_bf16 v[60:63], a[232:235], v[164:167], v[60:63]\n"
"v_mfma_f32_16x16x32_bf16 v[20:23], a[236:239], a[216:219], v[20:23]\n"
"v_mfma_f32_16x16x32_bf16 v[28:31], a[236:239], a[220:223], v[28:31]\n"
"v_mfma_f32_16x16x32_bf16 v[36:39], a[236:239], v[200:203], v[36:39]\n"
"v_mfma_f32_16x16x32_bf16 v[44:47], a[236:239], v[204:207], v[44:47]\n"
"v_mfma_f32_16x16x32_bf16 v[52:55], a[236:239], v[168:171], v[52:55]\n"
"v_mfma_f32_16x16x32_bf16 v[60:63], a[236:239], v[172:175], v[60:63]\n"
"s_nop 15\n"
"v_max3_f32 v137, v16, v17, v18\n"
"v_max3_f32 v137, v137, v19, v20\n"
"v_max3_f32 v137, v137, v21, v22\n"
"v_max_f32 v137, v137, v23\n"
"v_mov_b32 v138, v137\n"
"s_nop 1\n"
"v_permlane32_swap_b32 v137, v138\n"
"v_max_f32 v137, v137, v138\n"
"v_mov_b32 v138, v137\n"
"s_nop 1\n"
"v_permlane16_swap_b32 v137, v138\n"
"v_max_f32 v137, v137, v138\n"
"v_max_f32 v137, s96, v137\n"
"v_max_f32 v138, 0, v137\n"
"v_sub_f32 v138, 0, v138\n"
"v_exp_f32 v138, v138\n"
"v_sub_f32 v16, v16, v137\n"
"v_sub_f32 v64, v64, v137\n"
"v_sub_f32 v17, v17, v137\n"
"v_sub_f32 v65, v65, v137\n"
"v_sub_f32 v18, v18, v137\n"
"v_sub_f32 v66, v66, v137\n"
"v_sub_f32 v19, v19, v137\n"
"v_sub_f32 v67, v67, v137\n"
"v_sub_f32 v20, v20, v137\n"
"v_sub_f32 v68, v68, v137\n"
"v_sub_f32 v21, v21, v137\n"
"v_sub_f32 v69, v69, v137\n"
"v_sub_f32 v22, v22, v137\n"
"v_sub_f32 v70, v70, v137\n"
"v_sub_f32 v23, v23, v137\n"
"v_sub_f32 v71, v71, v137\n"
"v_sub_f32 v208, v208, v137\n"
"v_sub_f32 v209, v209, v137\n"
"v_sub_f32 v210, v210, v137\n"
"v_sub_f32 v211, v211, v137\n"
"v_mul_f32 v112, v112, v138\n"
"v_accvgpr_read_b32 v118, a0\n"
"v_accvgpr_read_b32 v119, a1\n"
"v_accvgpr_read_b32 v140, a2\n"
"v_accvgpr_read_b32 v141, a3\n"
"v_mul_f32 v118, v118, v138\n"
"v_mul_f32 v119, v119, v138\n"
"v_mul_f32 v140, v140, v138\n"
"v_mul_f32 v141, v141, v138\n"
"v_accvgpr_write_b32 a0, v118\n"
"v_accvgpr_write_b32 a1, v119\n"
"v_accvgpr_write_b32 a2, v140\n"
"v_accvgpr_write_b32 a3, v141\n"
"v_accvgpr_read_b32 v118, a8\n"
"v_accvgpr_read_b32 v119, a9\n"
"v_accvgpr_read_b32 v140, a10\n"
"v_accvgpr_read_b32 v141, a11\n"
"v_mul_f32 v118, v118, v138\n"
"v_mul_f32 v119, v119, v138\n"
"v_mul_f32 v140, v140, v138\n"
"v_mul_f32 v141, v141, v138\n"
"v_accvgpr_write_b32 a8, v118\n"
"v_accvgpr_write_b32 a9, v119\n"
"v_accvgpr_write_b32 a10, v140\n"
"v_accvgpr_write_b32 a11, v141\n"
"v_accvgpr_read_b32 v118, a16\n"
"v_accvgpr_read_b32 v119, a17\n"
"v_accvgpr_read_b32 v140, a18\n"
"v_accvgpr_read_b32 v141, a19\n"
"v_mul_f32 v118, v118, v138\n"
"v_mul_f32 v119, v119, v138\n"
"v_mul_f32 v140, v140, v138\n"
"v_mul_f32 v141, v141, v138\n"
"v_accvgpr_write_b32 a16, v118\n"
"v_accvgpr_write_b32 a17, v119\n"
"v_accvgpr_write_b32 a18, v140\n"
"v_accvgpr_write_b32 a19, v141\n"
"v_accvgpr_read_b32 v118, a24\n"
"v_accvgpr_read_b32 v119, a25\n"
"v_accvgpr_read_b32 v140, a26\n"
"v_accvgpr_read_b32 v141, a27\n"
"v_mul_f32 v118, v118, v138\n"
"v_mul_f32 v119, v119, v138\n"
"v_mul_f32 v140, v140, v138\n"
"v_mul_f32 v141, v141, v138\n"
"v_accvgpr_write_b32 a24, v118\n"
"v_accvgpr_write_b32 a25, v119\n"
"v_accvgpr_write_b32 a26, v140\n"
"v_accvgpr_write_b32 a27, v141\n"
"v_accvgpr_read_b32 v118, a32\n"
"v_accvgpr_read_b32 v119, a33\n"
"v_accvgpr_read_b32 v140, a34\n"
"v_accvgpr_read_b32 v141, a35\n"
"v_mul_f32 v118, v118, v138\n"
"v_mul_f32 v119, v119, v138\n"
"v_mul_f32 v140, v140, v138\n"
"v_mul_f32 v141, v141, v138\n"
"v_accvgpr_write_b32 a32, v118\n"
"v_accvgpr_write_b32 a33, v119\n"
"v_accvgpr_write_b32 a34, v140\n"
"v_accvgpr_write_b32 a35, v141\n"
"v_accvgpr_read_b32 v118, a40\n"
"v_accvgpr_read_b32 v119, a41\n"
"v_accvgpr_read_b32 v140, a42\n"
"v_accvgpr_read_b32 v141, a43\n"
"v_mul_f32 v118, v118, v138\n"
"v_mul_f32 v119, v119, v138\n"
"v_mul_f32 v140, v140, v138\n"
"v_mul_f32 v141, v141, v138\n"
"v_accvgpr_write_b32 a40, v118\n"
"v_accvgpr_write_b32 a41, v119\n"
"v_accvgpr_write_b32 a42, v140\n"
"v_accvgpr_write_b32 a43, v141\n"
"v_accvgpr_read_b32 v118, a48\n"
"v_accvgpr_read_b32 v119, a49\n"
"v_accvgpr_read_b32 v140, a50\n"
"v_accvgpr_read_b32 v141, a51\n"
"v_mul_f32 v118, v118, v138\n"
"v_mul_f32 v119, v119, v138\n"
"v_mul_f32 v140, v140, v138\n"
"v_mul_f32 v141, v141, v138\n"
"v_accvgpr_write_b32 a48, v118\n"
"v_accvgpr_write_b32 a49, v119\n"
"v_accvgpr_write_b32 a50, v140\n"
"v_accvgpr_write_b32 a51, v141\n"
"v_accvgpr_read_b32 v118, a56\n"
"v_accvgpr_read_b32 v119, a57\n"
"v_accvgpr_read_b32 v140, a58\n"
"v_accvgpr_read_b32 v141, a59\n"
"v_mul_f32 v118, v118, v138\n"
"v_mul_f32 v119, v119, v138\n"
"v_mul_f32 v140, v140, v138\n"
"v_mul_f32 v141, v141, v138\n"
"v_accvgpr_write_b32 a56, v118\n"
"v_accvgpr_write_b32 a57, v119\n"
"v_accvgpr_write_b32 a58, v140\n"
"v_accvgpr_write_b32 a59, v141\n"
"v_exp_f32 v16, v16\n"
"v_exp_f32 v17, v17\n"
"v_exp_f32 v18, v18\n"
"v_exp_f32 v19, v19\n"
"v_exp_f32 v20, v20\n"
"v_exp_f32 v21, v21\n"
"v_exp_f32 v22, v22\n"
"v_exp_f32 v23, v23\n"
"v_add_f32 v233, v16, v17\n"
"v_add_f32 v233, v233, v18\n"
"v_add_f32 v233, v233, v19\n"
"v_add_f32 v233, v233, v20\n"
"v_add_f32 v233, v233, v21\n"
"v_add_f32 v233, v233, v22\n"
"v_add_f32 v233, v233, v23\n"
"v_cvt_pk_bf16_f32 v16, v16, v17\n"
"v_cvt_pk_bf16_f32 v17, v18, v19\n"
"v_cvt_pk_bf16_f32 v18, v20, v21\n"
"v_cvt_pk_bf16_f32 v19, v22, v23\n"
"v_max3_f32 v137, v24, v25, v26\n"
"v_max3_f32 v137, v137, v27, v28\n"
"v_max3_f32 v137, v137, v29, v30\n"
"v_max_f32 v137, v137, v31\n"
"v_mov_b32 v138, v137\n"
"s_nop 1\n"
"v_permlane32_swap_b32 v137, v138\n"
"v_max_f32 v137, v137, v138\n"
"v_mov_b32 v138, v137\n"
"s_nop 1\n"
"v_permlane16_swap_b32 v137, v138\n"
"v_max_f32 v137, v137, v138\n"
"v_max_f32 v137, s96, v137\n"
"v_max_f32 v138, 0, v137\n"
"v_sub_f32 v138, 0, v138\n"
"v_exp_f32 v138, v138\n"
"v_sub_f32 v24, v24, v137\n"
"v_sub_f32 v72, v72, v137\n"
"v_sub_f32 v25, v25, v137\n"
"v_sub_f32 v73, v73, v137\n"
"v_sub_f32 v26, v26, v137\n"
"v_sub_f32 v74, v74, v137\n"
"v_sub_f32 v27, v27, v137\n"
"v_sub_f32 v75, v75, v137\n"
"v_sub_f32 v28, v28, v137\n"
"v_sub_f32 v76, v76, v137\n"
"v_sub_f32 v29, v29, v137\n"
"v_sub_f32 v77, v77, v137\n"
"v_sub_f32 v30, v30, v137\n"
"v_sub_f32 v78, v78, v137\n"
"v_sub_f32 v31, v31, v137\n"
"v_sub_f32 v79, v79, v137\n"
"v_sub_f32 v212, v212, v137\n"
"v_sub_f32 v213, v213, v137\n"
"v_sub_f32 v214, v214, v137\n"
"v_sub_f32 v215, v215, v137\n"
"v_mul_f32 v113, v113, v138\n"
"v_accvgpr_read_b32 v118, a4\n"
"v_accvgpr_read_b32 v119, a5\n"
"v_accvgpr_read_b32 v140, a6\n"
"v_accvgpr_read_b32 v141, a7\n"
"v_mul_f32 v118, v118, v138\n"
"v_mul_f32 v119, v119, v138\n"
"v_mul_f32 v140, v140, v138\n"
"v_mul_f32 v141, v141, v138\n"
"v_accvgpr_write_b32 a4, v118\n"
"v_accvgpr_write_b32 a5, v119\n"
"v_accvgpr_write_b32 a6, v140\n"
"v_accvgpr_write_b32 a7, v141\n"
"v_accvgpr_read_b32 v118, a12\n"
"v_accvgpr_read_b32 v119, a13\n"
"v_accvgpr_read_b32 v140, a14\n"
"v_accvgpr_read_b32 v141, a15\n"
"v_mul_f32 v118, v118, v138\n"
"v_mul_f32 v119, v119, v138\n"
"v_mul_f32 v140, v140, v138\n"
"v_mul_f32 v141, v141, v138\n"
"v_accvgpr_write_b32 a12, v118\n"
"v_accvgpr_write_b32 a13, v119\n"
"v_accvgpr_write_b32 a14, v140\n"
"v_accvgpr_write_b32 a15, v141\n"
"v_accvgpr_read_b32 v118, a20\n"
"v_accvgpr_read_b32 v119, a21\n"
"v_accvgpr_read_b32 v140, a22\n"
"v_accvgpr_read_b32 v141, a23\n"
"v_mul_f32 v118, v118, v138\n"
"v_mul_f32 v119, v119, v138\n"
"v_mul_f32 v140, v140, v138\n"
"v_mul_f32 v141, v141, v138\n"
"v_accvgpr_write_b32 a20, v118\n"
"v_accvgpr_write_b32 a21, v119\n"
"v_accvgpr_write_b32 a22, v140\n"
"v_accvgpr_write_b32 a23, v141\n"
"v_accvgpr_read_b32 v118, a28\n"
"v_accvgpr_read_b32 v119, a29\n"
"v_accvgpr_read_b32 v140, a30\n"
"v_accvgpr_read_b32 v141, a31\n"
"v_mul_f32 v118, v118, v138\n"
"v_mul_f32 v119, v119, v138\n"
"v_mul_f32 v140, v140, v138\n"
"v_mul_f32 v141, v141, v138\n"
"v_accvgpr_write_b32 a28, v118\n"
"v_accvgpr_write_b32 a29, v119\n"
"v_accvgpr_write_b32 a30, v140\n"
"v_accvgpr_write_b32 a31, v141\n"
"v_accvgpr_read_b32 v118, a36\n"
"v_accvgpr_read_b32 v119, a37\n"
"v_accvgpr_read_b32 v140, a38\n"
"v_accvgpr_read_b32 v141, a39\n"
"v_mul_f32 v118, v118, v138\n"
"v_mul_f32 v119, v119, v138\n"
"v_mul_f32 v140, v140, v138\n"
"v_mul_f32 v141, v141, v138\n"
"v_accvgpr_write_b32 a36, v118\n"
"v_accvgpr_write_b32 a37, v119\n"
"v_accvgpr_write_b32 a38, v140\n"
"v_accvgpr_write_b32 a39, v141\n"
"v_accvgpr_read_b32 v118, a44\n"
"v_accvgpr_read_b32 v119, a45\n"
"v_accvgpr_read_b32 v140, a46\n"
"v_accvgpr_read_b32 v141, a47\n"
"v_mul_f32 v118, v118, v138\n"
"v_mul_f32 v119, v119, v138\n"
"v_mul_f32 v140, v140, v138\n"
"v_mul_f32 v141, v141, v138\n"
"v_accvgpr_write_b32 a44, v118\n"
"v_accvgpr_write_b32 a45, v119\n"
"v_accvgpr_write_b32 a46, v140\n"
"v_accvgpr_write_b32 a47, v141\n"
"v_accvgpr_read_b32 v118, a52\n"
"v_accvgpr_read_b32 v119, a53\n"
"v_accvgpr_read_b32 v140, a54\n"
"v_accvgpr_read_b32 v141, a55\n"
"v_mul_f32 v118, v118, v138\n"
"v_mul_f32 v119, v119, v138\n"
"v_mul_f32 v140, v140, v138\n"
"v_mul_f32 v141, v141, v138\n"
"v_accvgpr_write_b32 a52, v118\n"
"v_accvgpr_write_b32 a53, v119\n"
"v_accvgpr_write_b32 a54, v140\n"
"v_accvgpr_write_b32 a55, v141\n"
"v_accvgpr_read_b32 v118, a60\n"
"v_accvgpr_read_b32 v119, a61\n"
"v_accvgpr_read_b32 v140, a62\n"
"v_accvgpr_read_b32 v141, a63\n"
"v_mul_f32 v118, v118, v138\n"
"v_mul_f32 v119, v119, v138\n"
"v_mul_f32 v140, v140, v138\n"
"v_mul_f32 v141, v141, v138\n"
"v_accvgpr_write_b32 a60, v118\n"
"v_accvgpr_write_b32 a61, v119\n"
"v_accvgpr_write_b32 a62, v140\n"
"v_accvgpr_write_b32 a63, v141\n"
"v_exp_f32 v24, v24\n"
"v_exp_f32 v25, v25\n"
"v_exp_f32 v26, v26\n"
"v_exp_f32 v27, v27\n"
"v_exp_f32 v28, v28\n"
"v_exp_f32 v29, v29\n"
"v_exp_f32 v30, v30\n"
"v_exp_f32 v31, v31\n"
"v_add_f32 v234, v24, v25\n"
"v_add_f32 v234, v234, v26\n"
"v_add_f32 v234, v234, v27\n"
"v_add_f32 v234, v234, v28\n"
"v_add_f32 v234, v234, v29\n"
"v_add_f32 v234, v234, v30\n"
"v_add_f32 v234, v234, v31\n"
"v_cvt_pk_bf16_f32 v24, v24, v25\n"
"v_cvt_pk_bf16_f32 v25, v26, v27\n"
"v_cvt_pk_bf16_f32 v26, v28, v29\n"
"v_cvt_pk_bf16_f32 v27, v30, v31\n"
"v_max3_f32 v137, v32, v33, v34\n"
"v_max3_f32 v137, v137, v35, v36\n"
"v_max3_f32 v137, v137, v37, v38\n"
"v_max_f32 v137, v137, v39\n"
"v_mov_b32 v138, v137\n"
"s_nop 1\n"
"v_permlane32_swap_b32 v137, v138\n"
"v_max_f32 v137, v137, v138\n"
"v_mov_b32 v138, v137\n"
"s_nop 1\n"
"v_permlane16_swap_b32 v137, v138\n"
"v_max_f32 v137, v137, v138\n"
"v_max_f32 v137, s96, v137\n"
"v_max_f32 v138, 0, v137\n"
"v_sub_f32 v138, 0, v138\n"
"v_exp_f32 v138, v138\n"
"v_sub_f32 v32, v32, v137\n"
"v_sub_f32 v80, v80, v137\n"
"v_sub_f32 v33, v33, v137\n"
"v_sub_f32 v81, v81, v137\n"
"v_sub_f32 v34, v34, v137\n"
"v_sub_f32 v82, v82, v137\n"
"v_sub_f32 v35, v35, v137\n"
"v_sub_f32 v83, v83, v137\n"
"v_sub_f32 v36, v36, v137\n"
"v_sub_f32 v84, v84, v137\n"
"v_sub_f32 v37, v37, v137\n"
"v_sub_f32 v85, v85, v137\n"
"v_sub_f32 v38, v38, v137\n"
"v_sub_f32 v86, v86, v137\n"
"v_sub_f32 v39, v39, v137\n"
"v_sub_f32 v87, v87, v137\n"
"v_sub_f32 v216, v216, v137\n"
"v_sub_f32 v217, v217, v137\n"
"v_sub_f32 v218, v218, v137\n"
"v_sub_f32 v219, v219, v137\n"
"v_mul_f32 v114, v114, v138\n"
"v_accvgpr_read_b32 v118, a64\n"
"v_accvgpr_read_b32 v119, a65\n"
"v_accvgpr_read_b32 v140, a66\n"
"v_accvgpr_read_b32 v141, a67\n"
"v_mul_f32 v118, v118, v138\n"
"v_mul_f32 v119, v119, v138\n"
"v_mul_f32 v140, v140, v138\n"
"v_mul_f32 v141, v141, v138\n"
"v_accvgpr_write_b32 a64, v118\n"
"v_accvgpr_write_b32 a65, v119\n"
"v_accvgpr_write_b32 a66, v140\n"
"v_accvgpr_write_b32 a67, v141\n"
"v_accvgpr_read_b32 v118, a72\n"
"v_accvgpr_read_b32 v119, a73\n"
"v_accvgpr_read_b32 v140, a74\n"
"v_accvgpr_read_b32 v141, a75\n"
"v_mul_f32 v118, v118, v138\n"
"v_mul_f32 v119, v119, v138\n"
"v_mul_f32 v140, v140, v138\n"
"v_mul_f32 v141, v141, v138\n"
"v_accvgpr_write_b32 a72, v118\n"
"v_accvgpr_write_b32 a73, v119\n"
"v_accvgpr_write_b32 a74, v140\n"
"v_accvgpr_write_b32 a75, v141\n"
"v_accvgpr_read_b32 v118, a80\n"
"v_accvgpr_read_b32 v119, a81\n"
"v_accvgpr_read_b32 v140, a82\n"
"v_accvgpr_read_b32 v141, a83\n"
"v_mul_f32 v118, v118, v138\n"
"v_mul_f32 v119, v119, v138\n"
"v_mul_f32 v140, v140, v138\n"
"v_mul_f32 v141, v141, v138\n"
"v_accvgpr_write_b32 a80, v118\n"
"v_accvgpr_write_b32 a81, v119\n"
"v_accvgpr_write_b32 a82, v140\n"
"v_accvgpr_write_b32 a83, v141\n"
"v_accvgpr_read_b32 v118, a88\n"
"v_accvgpr_read_b32 v119, a89\n"
"v_accvgpr_read_b32 v140, a90\n"
"v_accvgpr_read_b32 v141, a91\n"
"v_mul_f32 v118, v118, v138\n"
"v_mul_f32 v119, v119, v138\n"
"v_mul_f32 v140, v140, v138\n"
"v_mul_f32 v141, v141, v138\n"
"v_accvgpr_write_b32 a88, v118\n"
"v_accvgpr_write_b32 a89, v119\n"
"v_accvgpr_write_b32 a90, v140\n"
"v_accvgpr_write_b32 a91, v141\n"
"v_accvgpr_read_b32 v118, a96\n"
"v_accvgpr_read_b32 v119, a97\n"
"v_accvgpr_read_b32 v140, a98\n"
"v_accvgpr_read_b32 v141, a99\n"
"v_mul_f32 v118, v118, v138\n"
"v_mul_f32 v119, v119, v138\n"
"v_mul_f32 v140, v140, v138\n"
"v_mul_f32 v141, v141, v138\n"
"v_accvgpr_write_b32 a96, v118\n"
"v_accvgpr_write_b32 a97, v119\n"
"v_accvgpr_write_b32 a98, v140\n"
"v_accvgpr_write_b32 a99, v141\n"
"v_accvgpr_read_b32 v118, a104\n"
"v_accvgpr_read_b32 v119, a105\n"
"v_accvgpr_read_b32 v140, a106\n"
"v_accvgpr_read_b32 v141, a107\n"
"v_mul_f32 v118, v118, v138\n"
"v_mul_f32 v119, v119, v138\n"
"v_mul_f32 v140, v140, v138\n"
"v_mul_f32 v141, v141, v138\n"
"v_accvgpr_write_b32 a104, v118\n"
"v_accvgpr_write_b32 a105, v119\n"
"v_accvgpr_write_b32 a106, v140\n"
"v_accvgpr_write_b32 a107, v141\n"
"v_accvgpr_read_b32 v118, a112\n"
"v_accvgpr_read_b32 v119, a113\n"
"v_accvgpr_read_b32 v140, a114\n"
"v_accvgpr_read_b32 v141, a115\n"
"v_mul_f32 v118, v118, v138\n"
"v_mul_f32 v119, v119, v138\n"
"v_mul_f32 v140, v140, v138\n"
"v_mul_f32 v141, v141, v138\n"
"v_accvgpr_write_b32 a112, v118\n"
"v_accvgpr_write_b32 a113, v119\n"
"v_accvgpr_write_b32 a114, v140\n"
"v_accvgpr_write_b32 a115, v141\n"
"v_accvgpr_read_b32 v118, a120\n"
"v_accvgpr_read_b32 v119, a121\n"
"v_accvgpr_read_b32 v140, a122\n"
"v_accvgpr_read_b32 v141, a123\n"
"v_mul_f32 v118, v118, v138\n"
"v_mul_f32 v119, v119, v138\n"
"v_mul_f32 v140, v140, v138\n"
"v_mul_f32 v141, v141, v138\n"
"v_accvgpr_write_b32 a120, v118\n"
"v_accvgpr_write_b32 a121, v119\n"
"v_accvgpr_write_b32 a122, v140\n"
"v_accvgpr_write_b32 a123, v141\n"
"v_exp_f32 v32, v32\n"
"v_exp_f32 v33, v33\n"
"v_exp_f32 v34, v34\n"
"v_exp_f32 v35, v35\n"
"v_exp_f32 v36, v36\n"
"v_exp_f32 v37, v37\n"
"v_exp_f32 v38, v38\n"
"v_exp_f32 v39, v39\n"
"v_add_f32 v235, v32, v33\n"
"v_add_f32 v235, v235, v34\n"
"v_add_f32 v235, v235, v35\n"
"v_add_f32 v235, v235, v36\n"
"v_add_f32 v235, v235, v37\n"
"v_add_f32 v235, v235, v38\n"
"v_add_f32 v235, v235, v39\n"
"v_cvt_pk_bf16_f32 v32, v32, v33\n"
"v_cvt_pk_bf16_f32 v33, v34, v35\n"
"v_cvt_pk_bf16_f32 v34, v36, v37\n"
"v_cvt_pk_bf16_f32 v35, v38, v39\n"
"v_max3_f32 v137, v40, v41, v42\n"
"v_max3_f32 v137, v137, v43, v44\n"
"v_max3_f32 v137, v137, v45, v46\n"
"v_max_f32 v137, v137, v47\n"
"v_mov_b32 v138, v137\n"
"s_nop 1\n"
"v_permlane32_swap_b32 v137, v138\n"
"v_max_f32 v137, v137, v138\n"
"v_mov_b32 v138, v137\n"
"s_nop 1\n"
"v_permlane16_swap_b32 v137, v138\n"
"v_max_f32 v137, v137, v138\n"
"v_max_f32 v137, s96, v137\n"
"v_max_f32 v138, 0, v137\n"
"v_sub_f32 v138, 0, v138\n"
"v_exp_f32 v138, v138\n"
"v_sub_f32 v40, v40, v137\n"
"v_sub_f32 v88, v88, v137\n"
"v_sub_f32 v41, v41, v137\n"
"v_sub_f32 v89, v89, v137\n"
"v_sub_f32 v42, v42, v137\n"
"v_sub_f32 v90, v90, v137\n"
"v_sub_f32 v43, v43, v137\n"
"v_sub_f32 v91, v91, v137\n"
"v_sub_f32 v44, v44, v137\n"
"v_sub_f32 v92, v92, v137\n"
"v_sub_f32 v45, v45, v137\n"
"v_sub_f32 v93, v93, v137\n"
"v_sub_f32 v46, v46, v137\n"
"v_sub_f32 v94, v94, v137\n"
"v_sub_f32 v47, v47, v137\n"
"v_sub_f32 v95, v95, v137\n"
"v_sub_f32 v220, v220, v137\n"
"v_sub_f32 v221, v221, v137\n"
"v_sub_f32 v222, v222, v137\n"
"v_sub_f32 v223, v223, v137\n"
"v_mul_f32 v115, v115, v138\n"
"v_accvgpr_read_b32 v118, a68\n"
"v_accvgpr_read_b32 v119, a69\n"
"v_accvgpr_read_b32 v140, a70\n"
"v_accvgpr_read_b32 v141, a71\n"
"v_mul_f32 v118, v118, v138\n"
"v_mul_f32 v119, v119, v138\n"
"v_mul_f32 v140, v140, v138\n"
"v_mul_f32 v141, v141, v138\n"
"v_accvgpr_write_b32 a68, v118\n"
"v_accvgpr_write_b32 a69, v119\n"
"v_accvgpr_write_b32 a70, v140\n"
"v_accvgpr_write_b32 a71, v141\n"
"v_accvgpr_read_b32 v118, a76\n"
"v_accvgpr_read_b32 v119, a77\n"
"v_accvgpr_read_b32 v140, a78\n"
"v_accvgpr_read_b32 v141, a79\n"
"v_mul_f32 v118, v118, v138\n"
"v_mul_f32 v119, v119, v138\n"
"v_mul_f32 v140, v140, v138\n"
"v_mul_f32 v141, v141, v138\n"
"v_accvgpr_write_b32 a76, v118\n"
"v_accvgpr_write_b32 a77, v119\n"
"v_accvgpr_write_b32 a78, v140\n"
"v_accvgpr_write_b32 a79, v141\n"
"v_accvgpr_read_b32 v118, a84\n"
"v_accvgpr_read_b32 v119, a85\n"
"v_accvgpr_read_b32 v140, a86\n"
"v_accvgpr_read_b32 v141, a87\n"
"v_mul_f32 v118, v118, v138\n"
"v_mul_f32 v119, v119, v138\n"
"v_mul_f32 v140, v140, v138\n"
"v_mul_f32 v141, v141, v138\n"
"v_accvgpr_write_b32 a84, v118\n"
"v_accvgpr_write_b32 a85, v119\n"
"v_accvgpr_write_b32 a86, v140\n"
"v_accvgpr_write_b32 a87, v141\n"
"v_accvgpr_read_b32 v118, a92\n"
"v_accvgpr_read_b32 v119, a93\n"
"v_accvgpr_read_b32 v140, a94\n"
"v_accvgpr_read_b32 v141, a95\n"
"v_mul_f32 v118, v118, v138\n"
"v_mul_f32 v119, v119, v138\n"
"v_mul_f32 v140, v140, v138\n"
"v_mul_f32 v141, v141, v138\n"
"v_accvgpr_write_b32 a92, v118\n"
"v_accvgpr_write_b32 a93, v119\n"
"v_accvgpr_write_b32 a94, v140\n"
"v_accvgpr_write_b32 a95, v141\n"
"v_accvgpr_read_b32 v118, a100\n"
"v_accvgpr_read_b32 v119, a101\n"
"v_accvgpr_read_b32 v140, a102\n"
"v_accvgpr_read_b32 v141, a103\n"
"v_mul_f32 v118, v118, v138\n"
"v_mul_f32 v119, v119, v138\n"
"v_mul_f32 v140, v140, v138\n"
"v_mul_f32 v141, v141, v138\n"
"v_accvgpr_write_b32 a100, v118\n"
"v_accvgpr_write_b32 a101, v119\n"
"v_accvgpr_write_b32 a102, v140\n"
"v_accvgpr_write_b32 a103, v141\n"
"v_accvgpr_read_b32 v118, a108\n"
"v_accvgpr_read_b32 v119, a109\n"
"v_accvgpr_read_b32 v140, a110\n"
"v_accvgpr_read_b32 v141, a111\n"
"v_mul_f32 v118, v118, v138\n"
"v_mul_f32 v119, v119, v138\n"
"v_mul_f32 v140, v140, v138\n"
"v_mul_f32 v141, v141, v138\n"
"v_accvgpr_write_b32 a108, v118\n"
"v_accvgpr_write_b32 a109, v119\n"
"v_accvgpr_write_b32 a110, v140\n"
"v_accvgpr_write_b32 a111, v141\n"
"v_accvgpr_read_b32 v118, a116\n"
"v_accvgpr_read_b32 v119, a117\n"
"v_accvgpr_read_b32 v140, a118\n"
"v_accvgpr_read_b32 v141, a119\n"
"v_mul_f32 v118, v118, v138\n"
"v_mul_f32 v119, v119, v138\n"
"v_mul_f32 v140, v140, v138\n"
"v_mul_f32 v141, v141, v138\n"
"v_accvgpr_write_b32 a116, v118\n"
"v_accvgpr_write_b32 a117, v119\n"
"v_accvgpr_write_b32 a118, v140\n"
"v_accvgpr_write_b32 a119, v141\n"
"v_accvgpr_read_b32 v118, a124\n"
"v_accvgpr_read_b32 v119, a125\n"
"v_accvgpr_read_b32 v140, a126\n"
"v_accvgpr_read_b32 v141, a127\n"
"v_mul_f32 v118, v118, v138\n"
"v_mul_f32 v119, v119, v138\n"
"v_mul_f32 v140, v140, v138\n"
"v_mul_f32 v141, v141, v138\n"
"v_accvgpr_write_b32 a124, v118\n"
"v_accvgpr_write_b32 a125, v119\n"
"v_accvgpr_write_b32 a126, v140\n"
"v_accvgpr_write_b32 a127, v141\n"
"v_exp_f32 v40, v40\n"
"v_exp_f32 v41, v41\n"
"v_exp_f32 v42, v42\n"
"v_exp_f32 v43, v43\n"
"v_exp_f32 v44, v44\n"
"v_exp_f32 v45, v45\n"
"v_exp_f32 v46, v46\n"
"v_exp_f32 v47, v47\n"
"v_add_f32 v236, v40, v41\n"
"v_add_f32 v236, v236, v42\n"
"v_add_f32 v236, v236, v43\n"
"v_add_f32 v236, v236, v44\n"
"v_add_f32 v236, v236, v45\n"
"v_add_f32 v236, v236, v46\n"
"v_add_f32 v236, v236, v47\n"
"v_cvt_pk_bf16_f32 v40, v40, v41\n"
"v_cvt_pk_bf16_f32 v41, v42, v43\n"
"v_cvt_pk_bf16_f32 v42, v44, v45\n"
"v_cvt_pk_bf16_f32 v43, v46, v47\n"
"v_max3_f32 v137, v48, v49, v50\n"
"v_max3_f32 v137, v137, v51, v52\n"
"v_max3_f32 v137, v137, v53, v54\n"
"v_max_f32 v137, v137, v55\n"
"v_mov_b32 v138, v137\n"
"s_nop 1\n"
"v_permlane32_swap_b32 v137, v138\n"
"v_max_f32 v137, v137, v138\n"
"v_mov_b32 v138, v137\n"
"s_nop 1\n"
"v_permlane16_swap_b32 v137, v138\n"
"v_max_f32 v137, v137, v138\n"
"v_max_f32 v137, s96, v137\n"
"v_max_f32 v138, 0, v137\n"
"v_sub_f32 v138, 0, v138\n"
"v_exp_f32 v138, v138\n"
"v_sub_f32 v48, v48, v137\n"
"v_sub_f32 v96, v96, v137\n"
"v_sub_f32 v49, v49, v137\n"
"v_sub_f32 v97, v97, v137\n"
"v_sub_f32 v50, v50, v137\n"
"v_sub_f32 v98, v98, v137\n"
"v_sub_f32 v51, v51, v137\n"
"v_sub_f32 v99, v99, v137\n"
"v_sub_f32 v52, v52, v137\n"
"v_sub_f32 v100, v100, v137\n"
"v_sub_f32 v53, v53, v137\n"
"v_sub_f32 v101, v101, v137\n"
"v_sub_f32 v54, v54, v137\n"
"v_sub_f32 v102, v102, v137\n"
"v_sub_f32 v55, v55, v137\n"
"v_sub_f32 v103, v103, v137\n"
"v_sub_f32 v224, v224, v137\n"
"v_sub_f32 v225, v225, v137\n"
"v_sub_f32 v226, v226, v137\n"
"v_sub_f32 v227, v227, v137\n"
"v_mul_f32 v116, v116, v138\n"
"v_accvgpr_read_b32 v118, a128\n"
"v_accvgpr_read_b32 v119, a129\n"
"v_accvgpr_read_b32 v140, a130\n"
"v_accvgpr_read_b32 v141, a131\n"
"v_mul_f32 v118, v118, v138\n"
"v_mul_f32 v119, v119, v138\n"
"v_mul_f32 v140, v140, v138\n"
"v_mul_f32 v141, v141, v138\n"
"v_accvgpr_write_b32 a128, v118\n"
"v_accvgpr_write_b32 a129, v119\n"
"v_accvgpr_write_b32 a130, v140\n"
"v_accvgpr_write_b32 a131, v141\n"
"v_accvgpr_read_b32 v118, a136\n"
"v_accvgpr_read_b32 v119, a137\n"
"v_accvgpr_read_b32 v140, a138\n"
"v_accvgpr_read_b32 v141, a139\n"
"v_mul_f32 v118, v118, v138\n"
"v_mul_f32 v119, v119, v138\n"
"v_mul_f32 v140, v140, v138\n"
"v_mul_f32 v141, v141, v138\n"
"v_accvgpr_write_b32 a136, v118\n"
"v_accvgpr_write_b32 a137, v119\n"
"v_accvgpr_write_b32 a138, v140\n"
"v_accvgpr_write_b32 a139, v141\n"
"v_accvgpr_read_b32 v118, a144\n"
"v_accvgpr_read_b32 v119, a145\n"
"v_accvgpr_read_b32 v140, a146\n"
"v_accvgpr_read_b32 v141, a147\n"
"v_mul_f32 v118, v118, v138\n"
"v_mul_f32 v119, v119, v138\n"
"v_mul_f32 v140, v140, v138\n"
"v_mul_f32 v141, v141, v138\n"
"v_accvgpr_write_b32 a144, v118\n"
"v_accvgpr_write_b32 a145, v119\n"
"v_accvgpr_write_b32 a146, v140\n"
"v_accvgpr_write_b32 a147, v141\n"
"v_accvgpr_read_b32 v118, a152\n"
"v_accvgpr_read_b32 v119, a153\n"
"v_accvgpr_read_b32 v140, a154\n"
"v_accvgpr_read_b32 v141, a155\n"
"v_mul_f32 v118, v118, v138\n"
"v_mul_f32 v119, v119, v138\n"
"v_mul_f32 v140, v140, v138\n"
"v_mul_f32 v141, v141, v138\n"
"v_accvgpr_write_b32 a152, v118\n"
"v_accvgpr_write_b32 a153, v119\n"
"v_accvgpr_write_b32 a154, v140\n"
"v_accvgpr_write_b32 a155, v141\n"
"v_accvgpr_read_b32 v118, a160\n"
"v_accvgpr_read_b32 v119, a161\n"
"v_accvgpr_read_b32 v140, a162\n"
"v_accvgpr_read_b32 v141, a163\n"
"v_mul_f32 v118, v118, v138\n"
"v_mul_f32 v119, v119, v138\n"
"v_mul_f32 v140, v140, v138\n"
"v_mul_f32 v141, v141, v138\n"
"v_accvgpr_write_b32 a160, v118\n"
"v_accvgpr_write_b32 a161, v119\n"
"v_accvgpr_write_b32 a162, v140\n"
"v_accvgpr_write_b32 a163, v141\n"
"v_accvgpr_read_b32 v118, a168\n"
"v_accvgpr_read_b32 v119, a169\n"
"v_accvgpr_read_b32 v140, a170\n"
"v_accvgpr_read_b32 v141, a171\n"
"v_mul_f32 v118, v118, v138\n"
"v_mul_f32 v119, v119, v138\n"
"v_mul_f32 v140, v140, v138\n"
"v_mul_f32 v141, v141, v138\n"
"v_accvgpr_write_b32 a168, v118\n"
"v_accvgpr_write_b32 a169, v119\n"
"v_accvgpr_write_b32 a170, v140\n"
"v_accvgpr_write_b32 a171, v141\n"
"v_accvgpr_read_b32 v118, a176\n"
"v_accvgpr_read_b32 v119, a177\n"
"v_accvgpr_read_b32 v140, a178\n"
"v_accvgpr_read_b32 v141, a179\n"
"v_mul_f32 v118, v118, v138\n"
"v_mul_f32 v119, v119, v138\n"
"v_mul_f32 v140, v140, v138\n"
"v_mul_f32 v141, v141, v138\n"
"v_accvgpr_write_b32 a176, v118\n"
"v_accvgpr_write_b32 a177, v119\n"
"v_accvgpr_write_b32 a178, v140\n"
"v_accvgpr_write_b32 a179, v141\n"
"v_accvgpr_read_b32 v118, a184\n"
"v_accvgpr_read_b32 v119, a185\n"
"v_accvgpr_read_b32 v140, a186\n"
"v_accvgpr_read_b32 v141, a187\n"
"v_mul_f32 v118, v118, v138\n"
"v_mul_f32 v119, v119, v138\n"
"v_mul_f32 v140, v140, v138\n"
"v_mul_f32 v141, v141, v138\n"
"v_accvgpr_write_b32 a184, v118\n"
"v_accvgpr_write_b32 a185, v119\n"
"v_accvgpr_write_b32 a186, v140\n"
"v_accvgpr_write_b32 a187, v141\n"
"v_exp_f32 v48, v48\n"
"v_exp_f32 v49, v49\n"
"v_exp_f32 v50, v50\n"
"v_exp_f32 v51, v51\n"
"v_exp_f32 v52, v52\n"
"v_exp_f32 v53, v53\n"
"v_exp_f32 v54, v54\n"
"v_exp_f32 v55, v55\n"
"v_add_f32 v237, v48, v49\n"
"v_add_f32 v237, v237, v50\n"
"v_add_f32 v237, v237, v51\n"
"v_add_f32 v237, v237, v52\n"
"v_add_f32 v237, v237, v53\n"
"v_add_f32 v237, v237, v54\n"
"v_add_f32 v237, v237, v55\n"
"v_cvt_pk_bf16_f32 v48, v48, v49\n"
"v_cvt_pk_bf16_f32 v49, v50, v51\n"
"v_cvt_pk_bf16_f32 v50, v52, v53\n"
"v_cvt_pk_bf16_f32 v51, v54, v55\n"
"v_max3_f32 v137, v56, v57, v58\n"
"v_max3_f32 v137, v137, v59, v60\n"
"v_max3_f32 v137, v137, v61, v62\n"
"v_max_f32 v137, v137, v63\n"
"v_mov_b32 v138, v137\n"
"s_nop 1\n"
"v_permlane32_swap_b32 v137, v138\n"
"v_max_f32 v137, v137, v138\n"
"v_mov_b32 v138, v137\n"
"s_nop 1\n"
"v_permlane16_swap_b32 v137, v138\n"
"v_max_f32 v137, v137, v138\n"
"v_max_f32 v137, s96, v137\n"
"v_max_f32 v138, 0, v137\n"
"v_sub_f32 v138, 0, v138\n"
"v_exp_f32 v138, v138\n"
"v_sub_f32 v56, v56, v137\n"
"v_sub_f32 v104, v104, v137\n"
"v_sub_f32 v57, v57, v137\n"
"v_sub_f32 v105, v105, v137\n"
"v_sub_f32 v58, v58, v137\n"
"v_sub_f32 v106, v106, v137\n"
"v_sub_f32 v59, v59, v137\n"
"v_sub_f32 v107, v107, v137\n"
"v_sub_f32 v60, v60, v137\n"
"v_sub_f32 v108, v108, v137\n"
"v_sub_f32 v61, v61, v137\n"
"v_sub_f32 v109, v109, v137\n"
"v_sub_f32 v62, v62, v137\n"
"v_sub_f32 v110, v110, v137\n"
"v_sub_f32 v63, v63, v137\n"
"v_sub_f32 v111, v111, v137\n"
"v_sub_f32 v228, v228, v137\n"
"v_sub_f32 v229, v229, v137\n"
"v_sub_f32 v230, v230, v137\n"
"v_sub_f32 v231, v231, v137\n"
"v_mul_f32 v117, v117, v138\n"
"v_accvgpr_read_b32 v118, a132\n"
"v_accvgpr_read_b32 v119, a133\n"
"v_accvgpr_read_b32 v140, a134\n"
"v_accvgpr_read_b32 v141, a135\n"
"v_mul_f32 v118, v118, v138\n"
"v_mul_f32 v119, v119, v138\n"
"v_mul_f32 v140, v140, v138\n"
"v_mul_f32 v141, v141, v138\n"
"v_accvgpr_write_b32 a132, v118\n"
"v_accvgpr_write_b32 a133, v119\n"
"v_accvgpr_write_b32 a134, v140\n"
"v_accvgpr_write_b32 a135, v141\n"
"v_accvgpr_read_b32 v118, a140\n"
"v_accvgpr_read_b32 v119, a141\n"
"v_accvgpr_read_b32 v140, a142\n"
"v_accvgpr_read_b32 v141, a143\n"
"v_mul_f32 v118, v118, v138\n"
"v_mul_f32 v119, v119, v138\n"
"v_mul_f32 v140, v140, v138\n"
"v_mul_f32 v141, v141, v138\n"
"v_accvgpr_write_b32 a140, v118\n"
"v_accvgpr_write_b32 a141, v119\n"
"v_accvgpr_write_b32 a142, v140\n"
"v_accvgpr_write_b32 a143, v141\n"
"v_accvgpr_read_b32 v118, a148\n"
"v_accvgpr_read_b32 v119, a149\n"
"v_accvgpr_read_b32 v140, a150\n"
"v_accvgpr_read_b32 v141, a151\n"
"v_mul_f32 v118, v118, v138\n"
"v_mul_f32 v119, v119, v138\n"
"v_mul_f32 v140, v140, v138\n"
"v_mul_f32 v141, v141, v138\n"
"v_accvgpr_write_b32 a148, v118\n"
"v_accvgpr_write_b32 a149, v119\n"
"v_accvgpr_write_b32 a150, v140\n"
"v_accvgpr_write_b32 a151, v141\n"
"v_accvgpr_read_b32 v118, a156\n"
"v_accvgpr_read_b32 v119, a157\n"
"v_accvgpr_read_b32 v140, a158\n"
"v_accvgpr_read_b32 v141, a159\n"
"v_mul_f32 v118, v118, v138\n"
"v_mul_f32 v119, v119, v138\n"
"v_mul_f32 v140, v140, v138\n"
"v_mul_f32 v141, v141, v138\n"
"v_accvgpr_write_b32 a156, v118\n"
"v_accvgpr_write_b32 a157, v119\n"
"v_accvgpr_write_b32 a158, v140\n"
"v_accvgpr_write_b32 a159, v141\n"
"v_accvgpr_read_b32 v118, a164\n"
"v_accvgpr_read_b32 v119, a165\n"
"v_accvgpr_read_b32 v140, a166\n"
"v_accvgpr_read_b32 v141, a167\n"
"v_mul_f32 v118, v118, v138\n"
"v_mul_f32 v119, v119, v138\n"
"v_mul_f32 v140, v140, v138\n"
"v_mul_f32 v141, v141, v138\n"
"v_accvgpr_write_b32 a164, v118\n"
"v_accvgpr_write_b32 a165, v119\n"
"v_accvgpr_write_b32 a166, v140\n"
"v_accvgpr_write_b32 a167, v141\n"
"v_accvgpr_read_b32 v118, a172\n"
"v_accvgpr_read_b32 v119, a173\n"
"v_accvgpr_read_b32 v140, a174\n"
"v_accvgpr_read_b32 v141, a175\n"
"v_mul_f32 v118, v118, v138\n"
"v_mul_f32 v119, v119, v138\n"
"v_mul_f32 v140, v140, v138\n"
"v_mul_f32 v141, v141, v138\n"
"v_accvgpr_write_b32 a172, v118\n"
"v_accvgpr_write_b32 a173, v119\n"
"v_accvgpr_write_b32 a174, v140\n"
"v_accvgpr_write_b32 a175, v141\n"
"v_accvgpr_read_b32 v118, a180\n"
"v_accvgpr_read_b32 v119, a181\n"
"v_accvgpr_read_b32 v140, a182\n"
"v_accvgpr_read_b32 v141, a183\n"
"v_mul_f32 v118, v118, v138\n"
"v_mul_f32 v119, v119, v138\n"
"v_mul_f32 v140, v140, v138\n"
"v_mul_f32 v141, v141, v138\n"
"v_accvgpr_write_b32 a180, v118\n"
"v_accvgpr_write_b32 a181, v119\n"
"v_accvgpr_write_b32 a182, v140\n"
"v_accvgpr_write_b32 a183, v141\n"
"v_accvgpr_read_b32 v118, a188\n"
"v_accvgpr_read_b32 v119, a189\n"
"v_accvgpr_read_b32 v140, a190\n"
"v_accvgpr_read_b32 v141, a191\n"
"v_mul_f32 v118, v118, v138\n"
"v_mul_f32 v119, v119, v138\n"
"v_mul_f32 v140, v140, v138\n"
"v_mul_f32 v141, v141, v138\n"
"v_accvgpr_write_b32 a188, v118\n"
"v_accvgpr_write_b32 a189, v119\n"
"v_accvgpr_write_b32 a190, v140\n"
"v_accvgpr_write_b32 a191, v141\n"
"v_exp_f32 v56, v56\n"
"v_exp_f32 v57, v57\n"
"v_exp_f32 v58, v58\n"
"v_exp_f32 v59, v59\n"
"v_exp_f32 v60, v60\n"
"v_exp_f32 v61, v61\n"
"v_exp_f32 v62, v62\n"
"v_exp_f32 v63, v63\n"
"v_add_f32 v238, v56, v57\n"
"v_add_f32 v238, v238, v58\n"
"v_add_f32 v238, v238, v59\n"
"v_add_f32 v238, v238, v60\n"
"v_add_f32 v238, v238, v61\n"
"v_add_f32 v238, v238, v62\n"
"v_add_f32 v238, v238, v63\n"
"v_cvt_pk_bf16_f32 v56, v56, v57\n"
"v_cvt_pk_bf16_f32 v57, v58, v59\n"
"v_cvt_pk_bf16_f32 v58, v60, v61\n"
"v_cvt_pk_bf16_f32 v59, v62, v63\n"
"s_add_u32 s98, s98, 4\n"
"s_addc_u32 s99, s99, 0\n"
"s_nop 3\n"
"s_setpc_b64 s[98:99]\n"
"Lslow1_%=:\n"
"s_nop 15\n"
"v_and_b32 v137, 0xf0, v13\n"
"v_lshlrev_b32 v137, 4, v137\n"
"v_lshrrev_b32 v138, 8, v13\n"
"v_lshl_add_u32 v137, v138, 4, v137\n"
"s_lshl_b32 s92, s35, 17\n"
"v_add_u32 v137, s92, v137\n"
"buffer_load_dwordx4 a[224:227], v137, s[44:47], s97 offen\n"
"buffer_load_dwordx4 a[228:231], v137, s[44:47], s97 offen offset:64\n"
"buffer_load_dwordx4 a[232:235], v137, s[44:47], s97 offen offset:128\n"
"buffer_load_dwordx4 a[236:239], v137, s[44:47], s97 offen offset:192\n"
"s_add_u32 s97, s97, 4096\n"
"s_waitcnt vmcnt(0)\n"
"v_mfma_f32_16x16x32_bf16 v[64:67], a[224:227], a[192:195], v[208:211]\n"
"v_mfma_f32_16x16x32_bf16 v[72:75], a[224:227], a[196:199], v[212:215]\n"
"v_mfma_f32_16x16x32_bf16 v[80:83], a[224:227], v[176:179], v[216:219]\n"
"v_mfma_f32_16x16x32_bf16 v[88:91], a[224:227], v[180:183], v[220:223]\n"
"v_mfma_f32_16x16x32_bf16 v[96:99], a[224:227], v[144:147], v[224:227]\n"
"v_mfma_f32_16x16x32_bf16 v[104:107], a[224:227], v[148:151], v[228:231]\n"
"v_mfma_f32_16x16x32_bf16 v[64:67], a[228:231], a[200:203], v[64:67]\n"
"v_mfma_f32_16x16x32_bf16 v[72:75], a[228:231], a[204:207], v[72:75]\n"
"v_mfma_f32_16x16x32_bf16 v[80:83], a[228:231], v[184:187], v[80:83]\n"
"v_mfma_f32_16x16x32_bf16 v[88:91], a[228:231], v[188:191], v[88:91]\n"
"v_mfma_f32_16x16x32_bf16 v[96:99], a[228:231], v[152:155], v[96:99]\n"
"v_mfma_f32_16x16x32_bf16 v[104:107], a[228:231], v[156:159], v[104:107]\n"
"v_mfma_f32_16x16x32_bf16 v[64:67], a[232:235], a[208:211], v[64:67]\n"
"v_mfma_f32_16x16x32_bf16 v[72:75], a[232:235], a[212:215], v[72:75]\n"
"v_mfma_f32_16x16x32_bf16 v[80:83], a[232:235], v[192:195], v[80:83]\n"
"v_mfma_f32_16x16x32_bf16 v[88:91], a[232:235], v[196:199], v[88:91]\n"
"v_mfma_f32_16x16x32_bf16 v[96:99], a[232:235], v[160:163], v[96:99]\n"
"v_mfma_f32_16x16x32_bf16 v[104:107], a[232:235], v[164:167], v[104:107]\n"
"v_mfma_f32_16x16x32_bf16 v[64:67], a[236:239], a[216:219], v[64:67]\n"
"v_mfma_f32_16x16x32_bf16 v[72:75], a[236:239], a[220:223], v[72:75]\n"
"v_mfma_f32_16x16x32_bf16 v[80:83], a[236:239], v[200:203], v[80:83]\n"
"v_mfma_f32_16x16x32_bf16 v[88:91], a[236:239], v[204:207], v[88:91]\n"
"v_mfma_f32_16x16x32_bf16 v[96:99], a[236:239], v[168:171], v[96:99]\n"
"v_mfma_f32_16x16x32_bf16 v[104:107], a[236:239], v[172:175], v[104:107]\n"
"s_nop 15\n"
"buffer_load_dwordx4 a[224:227], v137, s[44:47], s97 offen\n"
"buffer_load_dwordx4 a[228:231], v137, s[44:47], s97 offen offset:64\n"
"buffer_load_dwordx4 a[232:235], v137, s[44:47], s97 offen offset:128\n"
"buffer_load_dwordx4 a[236:239], v137, s[44:47], s97 offen offset:192\n"
"s_add_u32 s97, s97, 4096\n"
"s_waitcnt vmcnt(0)\n"
"v_mfma_f32_16x16x32_bf16 v[68:71], a[224:227], a[192:195], v[208:211]\n"
"v_mfma_f32_16x16x32_bf16 v[76:79], a[224:227], a[196:199], v[212:215]\n"
"v_mfma_f32_16x16x32_bf16 v[84:87], a[224:227], v[176:179], v[216:219]\n"
"v_mfma_f32_16x16x32_bf16 v[92:95], a[224:227], v[180:183], v[220:223]\n"
"v_mfma_f32_16x16x32_bf16 v[100:103], a[224:227], v[144:147], v[224:227]\n"
"v_mfma_f32_16x16x32_bf16 v[108:111], a[224:227], v[148:151], v[228:231]\n"
"v_mfma_f32_16x16x32_bf16 v[68:71], a[228:231], a[200:203], v[68:71]\n"
"v_mfma_f32_16x16x32_bf16 v[76:79], a[228:231], a[204:207], v[76:79]\n"
"v_mfma_f32_16x16x32_bf16 v[84:87], a[228:231], v[184:187], v[84:87]\n"
"v_mfma_f32_16x16x32_bf16 v[92:95], a[228:231], v[188:191], v[92:95]\n"
"v_mfma_f32_16x16x32_bf16 v[100:103], a[228:231], v[152:155], v[100:103]\n"
"v_mfma_f32_16x16x32_bf16 v[108:111], a[228:231], v[156:159], v[108:111]\n"
"v_mfma_f32_16x16x32_bf16 v[68:71], a[232:235], a[208:211], v[68:71]\n"
"v_mfma_f32_16x16x32_bf16 v[76:79], a[232:235], a[212:215], v[76:79]\n"
"v_mfma_f32_16x16x32_bf16 v[84:87], a[232:235], v[192:195], v[84:87]\n"
"v_mfma_f32_16x16x32_bf16 v[92:95], a[232:235], v[196:199], v[92:95]\n"
"v_mfma_f32_16x16x32_bf16 v[100:103], a[232:235], v[160:163], v[100:103]\n"
"v_mfma_f32_16x16x32_bf16 v[108:111], a[232:235], v[164:167], v[108:111]\n"
"v_mfma_f32_16x16x32_bf16 v[68:71], a[236:239], a[216:219], v[68:71]\n"
"v_mfma_f32_16x16x32_bf16 v[76:79], a[236:239], a[220:223], v[76:79]\n"
"v_mfma_f32_16x16x32_bf16 v[84:87], a[236:239], v[200:203], v[84:87]\n"
"v_mfma_f32_16x16x32_bf16 v[92:95], a[236:239], v[204:207], v[92:95]\n"
"v_mfma_f32_16x16x32_bf16 v[100:103], a[236:239], v[168:171], v[100:103]\n"
"v_mfma_f32_16x16x32_bf16 v[108:111], a[236:239], v[172:175], v[108:111]\n"
"s_nop 15\n"
"v_max3_f32 v137, v64, v65, v66\n"
"v_max3_f32 v137, v137, v67, v68\n"
"v_max3_f32 v137, v137, v69, v70\n"
"v_max_f32 v137, v137, v71\n"
"v_mov_b32 v138, v137\n"
"s_nop 1\n"
"v_permlane32_swap_b32 v137, v138\n"
"v_max_f32 v137, v137, v138\n"
"v_mov_b32 v138, v137\n"
"s_nop 1\n"
"v_permlane16_swap_b32 v137, v138\n"
"v_max_f32 v137, v137, v138\n"
"v_max_f32 v137, s96, v137\n"
"v_max_f32 v138, 0, v137\n"
"v_sub_f32 v138, 0, v138\n"
"v_exp_f32 v138, v138\n"
"v_sub_f32 v64, v64, v137\n"
"v_sub_f32 v16, v16, v137\n"
"v_sub_f32 v65, v65, v137\n"
"v_sub_f32 v17, v17, v137\n"
"v_sub_f32 v66, v66, v137\n"
"v_sub_f32 v18, v18, v137\n"
"v_sub_f32 v67, v67, v137\n"
"v_sub_f32 v19, v19, v137\n"
"v_sub_f32 v68, v68, v137\n"
"v_sub_f32 v20, v20, v137\n"
"v_sub_f32 v69, v69, v137\n"
"v_sub_f32 v21, v21, v137\n"
"v_sub_f32 v70, v70, v137\n"
"v_sub_f32 v22, v22, v137\n"
"v_sub_f32 v71, v71, v137\n"
"v_sub_f32 v23, v23, v137\n"
"v_sub_f32 v208, v208, v137\n"
"v_sub_f32 v209, v209, v137\n"
"v_sub_f32 v210, v210, v137\n"
"v_sub_f32 v211, v211, v137\n"
"v_mul_f32 v112, v112, v138\n"
"v_accvgpr_read_b32 v118, a0\n"
"v_accvgpr_read_b32 v119, a1\n"
"v_accvgpr_read_b32 v140, a2\n"
"v_accvgpr_read_b32 v141, a3\n"
"v_mul_f32 v118, v118, v138\n"
"v_mul_f32 v119, v119, v138\n"
"v_mul_f32 v140, v140, v138\n"
"v_mul_f32 v141, v141, v138\n"
"v_accvgpr_write_b32 a0, v118\n"
"v_accvgpr_write_b32 a1, v119\n"
"v_accvgpr_write_b32 a2, v140\n"
"v_accvgpr_write_b32 a3, v141\n"
"v_accvgpr_read_b32 v118, a8\n"
"v_accvgpr_read_b32 v119, a9\n"
"v_accvgpr_read_b32 v140, a10\n"
"v_accvgpr_read_b32 v141, a11\n"
"v_mul_f32 v118, v118, v138\n"
"v_mul_f32 v119, v119, v138\n"
"v_mul_f32 v140, v140, v138\n"
"v_mul_f32 v141, v141, v138\n"
"v_accvgpr_write_b32 a8, v118\n"
"v_accvgpr_write_b32 a9, v119\n"
"v_accvgpr_write_b32 a10, v140\n"
"v_accvgpr_write_b32 a11, v141\n"
"v_accvgpr_read_b32 v118, a16\n"
"v_accvgpr_read_b32 v119, a17\n"
"v_accvgpr_read_b32 v140, a18\n"
"v_accvgpr_read_b32 v141, a19\n"
"v_mul_f32 v118, v118, v138\n"
"v_mul_f32 v119, v119, v138\n"
"v_mul_f32 v140, v140, v138\n"
"v_mul_f32 v141, v141, v138\n"
"v_accvgpr_write_b32 a16, v118\n"
"v_accvgpr_write_b32 a17, v119\n"
"v_accvgpr_write_b32 a18, v140\n"
"v_accvgpr_write_b32 a19, v141\n"
"v_accvgpr_read_b32 v118, a24\n"
"v_accvgpr_read_b32 v119, a25\n"
"v_accvgpr_read_b32 v140, a26\n"
"v_accvgpr_read_b32 v141, a27\n"
"v_mul_f32 v118, v118, v138\n"
"v_mul_f32 v119, v119, v138\n"
"v_mul_f32 v140, v140, v138\n"
"v_mul_f32 v141, v141, v138\n"
"v_accvgpr_write_b32 a24, v118\n"
"v_accvgpr_write_b32 a25, v119\n"
"v_accvgpr_write_b32 a26, v140\n"
"v_accvgpr_write_b32 a27, v141\n"
"v_accvgpr_read_b32 v118, a32\n"
"v_accvgpr_read_b32 v119, a33\n"
"v_accvgpr_read_b32 v140, a34\n"
"v_accvgpr_read_b32 v141, a35\n"
"v_mul_f32 v118, v118, v138\n"
"v_mul_f32 v119, v119, v138\n"
"v_mul_f32 v140, v140, v138\n"
"v_mul_f32 v141, v141, v138\n"
"v_accvgpr_write_b32 a32, v118\n"
"v_accvgpr_write_b32 a33, v119\n"
"v_accvgpr_write_b32 a34, v140\n"
"v_accvgpr_write_b32 a35, v141\n"
"v_accvgpr_read_b32 v118, a40\n"
"v_accvgpr_read_b32 v119, a41\n"
"v_accvgpr_read_b32 v140, a42\n"
"v_accvgpr_read_b32 v141, a43\n"
"v_mul_f32 v118, v118, v138\n"
"v_mul_f32 v119, v119, v138\n"
"v_mul_f32 v140, v140, v138\n"
"v_mul_f32 v141, v141, v138\n"
"v_accvgpr_write_b32 a40, v118\n"
"v_accvgpr_write_b32 a41, v119\n"
"v_accvgpr_write_b32 a42, v140\n"
"v_accvgpr_write_b32 a43, v141\n"
"v_accvgpr_read_b32 v118, a48\n"
"v_accvgpr_read_b32 v119, a49\n"
"v_accvgpr_read_b32 v140, a50\n"
"v_accvgpr_read_b32 v141, a51\n"
"v_mul_f32 v118, v118, v138\n"
"v_mul_f32 v119, v119, v138\n"
"v_mul_f32 v140, v140, v138\n"
"v_mul_f32 v141, v141, v138\n"
"v_accvgpr_write_b32 a48, v118\n"
"v_accvgpr_write_b32 a49, v119\n"
"v_accvgpr_write_b32 a50, v140\n"
"v_accvgpr_write_b32 a51, v141\n"
"v_accvgpr_read_b32 v118, a56\n"
"v_accvgpr_read_b32 v119, a57\n"
"v_accvgpr_read_b32 v140, a58\n"
"v_accvgpr_read_b32 v141, a59\n"
"v_mul_f32 v118, v118, v138\n"
"v_mul_f32 v119, v119, v138\n"
"v_mul_f32 v140, v140, v138\n"
"v_mul_f32 v141, v141, v138\n"
"v_accvgpr_write_b32 a56, v118\n"
"v_accvgpr_write_b32 a57, v119\n"
"v_accvgpr_write_b32 a58, v140\n"
"v_accvgpr_write_b32 a59, v141\n"
"v_exp_f32 v64, v64\n"
"v_exp_f32 v65, v65\n"
"v_exp_f32 v66, v66\n"
"v_exp_f32 v67, v67\n"
"v_exp_f32 v68, v68\n"
"v_exp_f32 v69, v69\n"
"v_exp_f32 v70, v70\n"
"v_exp_f32 v71, v71\n"
"v_add_f32 v233, v64, v65\n"
"v_add_f32 v233, v233, v66\n"
"v_add_f32 v233, v233, v67\n"
"v_add_f32 v233, v233, v68\n"
"v_add_f32 v233, v233, v69\n"
"v_add_f32 v233, v233, v70\n"
"v_add_f32 v233, v233, v71\n"
"v_cvt_pk_bf16_f32 v64, v64, v65\n"
"v_cvt_pk_bf16_f32 v65, v66, v67\n"
"v_cvt_pk_bf16_f32 v66, v68, v69\n"
"v_cvt_pk_bf16_f32 v67, v70, v71\n"
"v_max3_f32 v137, v72, v73, v74\n"
"v_max3_f32 v137, v137, v75, v76\n"
"v_max3_f32 v137, v137, v77, v78\n"
"v_max_f32 v137, v137, v79\n"
"v_mov_b32 v138, v137\n"
"s_nop 1\n"
"v_permlane32_swap_b32 v137, v138\n"
"v_max_f32 v137, v137, v138\n"
"v_mov_b32 v138, v137\n"
"s_nop 1\n"
"v_permlane16_swap_b32 v137, v138\n"
"v_max_f32 v137, v137, v138\n"
"v_max_f32 v137, s96, v137\n"
"v_max_f32 v138, 0, v137\n"
"v_sub_f32 v138, 0, v138\n"
"v_exp_f32 v138, v138\n"
"v_sub_f32 v72, v72, v137\n"
"v_sub_f32 v24, v24, v137\n"
"v_sub_f32 v73, v73, v137\n"
"v_sub_f32 v25, v25, v137\n"
"v_sub_f32 v74, v74, v137\n"
"v_sub_f32 v26, v26, v137\n"
"v_sub_f32 v75, v75, v137\n"
"v_sub_f32 v27, v27, v137\n"
"v_sub_f32 v76, v76, v137\n"
"v_sub_f32 v28, v28, v137\n"
"v_sub_f32 v77, v77, v137\n"
"v_sub_f32 v29, v29, v137\n"
"v_sub_f32 v78, v78, v137\n"
"v_sub_f32 v30, v30, v137\n"
"v_sub_f32 v79, v79, v137\n"
"v_sub_f32 v31, v31, v137\n"
"v_sub_f32 v212, v212, v137\n"
"v_sub_f32 v213, v213, v137\n"
"v_sub_f32 v214, v214, v137\n"
"v_sub_f32 v215, v215, v137\n"
"v_mul_f32 v113, v113, v138\n"
"v_accvgpr_read_b32 v118, a4\n"
"v_accvgpr_read_b32 v119, a5\n"
"v_accvgpr_read_b32 v140, a6\n"
"v_accvgpr_read_b32 v141, a7\n"
"v_mul_f32 v118, v118, v138\n"
"v_mul_f32 v119, v119, v138\n"
"v_mul_f32 v140, v140, v138\n"
"v_mul_f32 v141, v141, v138\n"
"v_accvgpr_write_b32 a4, v118\n"
"v_accvgpr_write_b32 a5, v119\n"
"v_accvgpr_write_b32 a6, v140\n"
"v_accvgpr_write_b32 a7, v141\n"
"v_accvgpr_read_b32 v118, a12\n"
"v_accvgpr_read_b32 v119, a13\n"
"v_accvgpr_read_b32 v140, a14\n"
"v_accvgpr_read_b32 v141, a15\n"
"v_mul_f32 v118, v118, v138\n"
"v_mul_f32 v119, v119, v138\n"
"v_mul_f32 v140, v140, v138\n"
"v_mul_f32 v141, v141, v138\n"
"v_accvgpr_write_b32 a12, v118\n"
"v_accvgpr_write_b32 a13, v119\n"
"v_accvgpr_write_b32 a14, v140\n"
"v_accvgpr_write_b32 a15, v141\n"
"v_accvgpr_read_b32 v118, a20\n"
"v_accvgpr_read_b32 v119, a21\n"
"v_accvgpr_read_b32 v140, a22\n"
"v_accvgpr_read_b32 v141, a23\n"
"v_mul_f32 v118, v118, v138\n"
"v_mul_f32 v119, v119, v138\n"
"v_mul_f32 v140, v140, v138\n"
"v_mul_f32 v141, v141, v138\n"
"v_accvgpr_write_b32 a20, v118\n"
"v_accvgpr_write_b32 a21, v119\n"
"v_accvgpr_write_b32 a22, v140\n"
"v_accvgpr_write_b32 a23, v141\n"
"v_accvgpr_read_b32 v118, a28\n"
"v_accvgpr_read_b32 v119, a29\n"
"v_accvgpr_read_b32 v140, a30\n"
"v_accvgpr_read_b32 v141, a31\n"
"v_mul_f32 v118, v118, v138\n"
"v_mul_f32 v119, v119, v138\n"
"v_mul_f32 v140, v140, v138\n"
"v_mul_f32 v141, v141, v138\n"
"v_accvgpr_write_b32 a28, v118\n"
"v_accvgpr_write_b32 a29, v119\n"
"v_accvgpr_write_b32 a30, v140\n"
"v_accvgpr_write_b32 a31, v141\n"
"v_accvgpr_read_b32 v118, a36\n"
"v_accvgpr_read_b32 v119, a37\n"
"v_accvgpr_read_b32 v140, a38\n"
"v_accvgpr_read_b32 v141, a39\n"
"v_mul_f32 v118, v118, v138\n"
"v_mul_f32 v119, v119, v138\n"
"v_mul_f32 v140, v140, v138\n"
"v_mul_f32 v141, v141, v138\n"
"v_accvgpr_write_b32 a36, v118\n"
"v_accvgpr_write_b32 a37, v119\n"
"v_accvgpr_write_b32 a38, v140\n"
"v_accvgpr_write_b32 a39, v141\n"
"v_accvgpr_read_b32 v118, a44\n"
"v_accvgpr_read_b32 v119, a45\n"
"v_accvgpr_read_b32 v140, a46\n"
"v_accvgpr_read_b32 v141, a47\n"
"v_mul_f32 v118, v118, v138\n"
"v_mul_f32 v119, v119, v138\n"
"v_mul_f32 v140, v140, v138\n"
"v_mul_f32 v141, v141, v138\n"
"v_accvgpr_write_b32 a44, v118\n"
"v_accvgpr_write_b32 a45, v119\n"
"v_accvgpr_write_b32 a46, v140\n"
"v_accvgpr_write_b32 a47, v141\n"
"v_accvgpr_read_b32 v118, a52\n"
"v_accvgpr_read_b32 v119, a53\n"
"v_accvgpr_read_b32 v140, a54\n"
"v_accvgpr_read_b32 v141, a55\n"
"v_mul_f32 v118, v118, v138\n"
"v_mul_f32 v119, v119, v138\n"
"v_mul_f32 v140, v140, v138\n"
"v_mul_f32 v141, v141, v138\n"
"v_accvgpr_write_b32 a52, v118\n"
"v_accvgpr_write_b32 a53, v119\n"
"v_accvgpr_write_b32 a54, v140\n"
"v_accvgpr_write_b32 a55, v141\n"
"v_accvgpr_read_b32 v118, a60\n"
"v_accvgpr_read_b32 v119, a61\n"
"v_accvgpr_read_b32 v140, a62\n"
"v_accvgpr_read_b32 v141, a63\n"
"v_mul_f32 v118, v118, v138\n"
"v_mul_f32 v119, v119, v138\n"
"v_mul_f32 v140, v140, v138\n"
"v_mul_f32 v141, v141, v138\n"
"v_accvgpr_write_b32 a60, v118\n"
"v_accvgpr_write_b32 a61, v119\n"
"v_accvgpr_write_b32 a62, v140\n"
"v_accvgpr_write_b32 a63, v141\n"
"v_exp_f32 v72, v72\n"
"v_exp_f32 v73, v73\n"
"v_exp_f32 v74, v74\n"
"v_exp_f32 v75, v75\n"
"v_exp_f32 v76, v76\n"
"v_exp_f32 v77, v77\n"
"v_exp_f32 v78, v78\n"
"v_exp_f32 v79, v79\n"
"v_add_f32 v234, v72, v73\n"
"v_add_f32 v234, v234, v74\n"
"v_add_f32 v234, v234, v75\n"
"v_add_f32 v234, v234, v76\n"
"v_add_f32 v234, v234, v77\n"
"v_add_f32 v234, v234, v78\n"
"v_add_f32 v234, v234, v79\n"
"v_cvt_pk_bf16_f32 v72, v72, v73\n"
"v_cvt_pk_bf16_f32 v73, v74, v75\n"
"v_cvt_pk_bf16_f32 v74, v76, v77\n"
"v_cvt_pk_bf16_f32 v75, v78, v79\n"
"v_max3_f32 v137, v80, v81, v82\n"
"v_max3_f32 v137, v137, v83, v84\n"
"v_max3_f32 v137, v137, v85, v86\n"
"v_max_f32 v137, v137, v87\n"
"v_mov_b32 v138, v137\n"
"s_nop 1\n"
"v_permlane32_swap_b32 v137, v138\n"
"v_max_f32 v137, v137, v138\n"
"v_mov_b32 v138, v137\n"
"s_nop 1\n"
"v_permlane16_swap_b32 v137, v138\n"
"v_max_f32 v137, v137, v138\n"
"v_max_f32 v137, s96, v137\n"
"v_max_f32 v138, 0, v137\n"
"v_sub_f32 v138, 0, v138\n"
"v_exp_f32 v138, v138\n"
"v_sub_f32 v80, v80, v137\n"
"v_sub_f32 v32, v32, v137\n"
"v_sub_f32 v81, v81, v137\n"
"v_sub_f32 v33, v33, v137\n"
"v_sub_f32 v82, v82, v137\n"
"v_sub_f32 v34, v34, v137\n"
"v_sub_f32 v83, v83, v137\n"
"v_sub_f32 v35, v35, v137\n"
"v_sub_f32 v84, v84, v137\n"
"v_sub_f32 v36, v36, v137\n"
"v_sub_f32 v85, v85, v137\n"
"v_sub_f32 v37, v37, v137\n"
"v_sub_f32 v86, v86, v137\n"
"v_sub_f32 v38, v38, v137\n"
"v_sub_f32 v87, v87, v137\n"
"v_sub_f32 v39, v39, v137\n"
"v_sub_f32 v216, v216, v137\n"
"v_sub_f32 v217, v217, v137\n"
"v_sub_f32 v218, v218, v137\n"
"v_sub_f32 v219, v219, v137\n"
"v_mul_f32 v114, v114, v138\n"
"v_accvgpr_read_b32 v118, a64\n"
"v_accvgpr_read_b32 v119, a65\n"
"v_accvgpr_read_b32 v140, a66\n"
"v_accvgpr_read_b32 v141, a67\n"
"v_mul_f32 v118, v118, v138\n"
"v_mul_f32 v119, v119, v138\n"
"v_mul_f32 v140, v140, v138\n"
"v_mul_f32 v141, v141, v138\n"
"v_accvgpr_write_b32 a64, v118\n"
"v_accvgpr_write_b32 a65, v119\n"
"v_accvgpr_write_b32 a66, v140\n"
"v_accvgpr_write_b32 a67, v141\n"
"v_accvgpr_read_b32 v118, a72\n"
"v_accvgpr_read_b32 v119, a73\n"
"v_accvgpr_read_b32 v140, a74\n"
"v_accvgpr_read_b32 v141, a75\n"
"v_mul_f32 v118, v118, v138\n"
"v_mul_f32 v119, v119, v138\n"
"v_mul_f32 v140, v140, v138\n"
"v_mul_f32 v141, v141, v138\n"
"v_accvgpr_write_b32 a72, v118\n"
"v_accvgpr_write_b32 a73, v119\n"
"v_accvgpr_write_b32 a74, v140\n"
"v_accvgpr_write_b32 a75, v141\n"
"v_accvgpr_read_b32 v118, a80\n"
"v_accvgpr_read_b32 v119, a81\n"
"v_accvgpr_read_b32 v140, a82\n"
"v_accvgpr_read_b32 v141, a83\n"
"v_mul_f32 v118, v118, v138\n"
"v_mul_f32 v119, v119, v138\n"
"v_mul_f32 v140, v140, v138\n"
"v_mul_f32 v141, v141, v138\n"
"v_accvgpr_write_b32 a80, v118\n"
"v_accvgpr_write_b32 a81, v119\n"
"v_accvgpr_write_b32 a82, v140\n"
"v_accvgpr_write_b32 a83, v141\n"
"v_accvgpr_read_b32 v118, a88\n"
"v_accvgpr_read_b32 v119, a89\n"
"v_accvgpr_read_b32 v140, a90\n"
"v_accvgpr_read_b32 v141, a91\n"
"v_mul_f32 v118, v118, v138\n"
"v_mul_f32 v119, v119, v138\n"
"v_mul_f32 v140, v140, v138\n"
"v_mul_f32 v141, v141, v138\n"
"v_accvgpr_write_b32 a88, v118\n"
"v_accvgpr_write_b32 a89, v119\n"
"v_accvgpr_write_b32 a90, v140\n"
"v_accvgpr_write_b32 a91, v141\n"
"v_accvgpr_read_b32 v118, a96\n"
"v_accvgpr_read_b32 v119, a97\n"
"v_accvgpr_read_b32 v140, a98\n"
"v_accvgpr_read_b32 v141, a99\n"
"v_mul_f32 v118, v118, v138\n"
"v_mul_f32 v119, v119, v138\n"
"v_mul_f32 v140, v140, v138\n"
"v_mul_f32 v141, v141, v138\n"
"v_accvgpr_write_b32 a96, v118\n"
"v_accvgpr_write_b32 a97, v119\n"
"v_accvgpr_write_b32 a98, v140\n"
"v_accvgpr_write_b32 a99, v141\n"
"v_accvgpr_read_b32 v118, a104\n"
"v_accvgpr_read_b32 v119, a105\n"
"v_accvgpr_read_b32 v140, a106\n"
"v_accvgpr_read_b32 v141, a107\n"
"v_mul_f32 v118, v118, v138\n"
"v_mul_f32 v119, v119, v138\n"
"v_mul_f32 v140, v140, v138\n"
"v_mul_f32 v141, v141, v138\n"
"v_accvgpr_write_b32 a104, v118\n"
"v_accvgpr_write_b32 a105, v119\n"
"v_accvgpr_write_b32 a106, v140\n"
"v_accvgpr_write_b32 a107, v141\n"
"v_accvgpr_read_b32 v118, a112\n"
"v_accvgpr_read_b32 v119, a113\n"
"v_accvgpr_read_b32 v140, a114\n"
"v_accvgpr_read_b32 v141, a115\n"
"v_mul_f32 v118, v118, v138\n"
"v_mul_f32 v119, v119, v138\n"
"v_mul_f32 v140, v140, v138\n"
"v_mul_f32 v141, v141, v138\n"
"v_accvgpr_write_b32 a112, v118\n"
"v_accvgpr_write_b32 a113, v119\n"
"v_accvgpr_write_b32 a114, v140\n"
"v_accvgpr_write_b32 a115, v141\n"
"v_accvgpr_read_b32 v118, a120\n"
"v_accvgpr_read_b32 v119, a121\n"
"v_accvgpr_read_b32 v140, a122\n"
"v_accvgpr_read_b32 v141, a123\n"
"v_mul_f32 v118, v118, v138\n"
"v_mul_f32 v119, v119, v138\n"
"v_mul_f32 v140, v140, v138\n"
"v_mul_f32 v141, v141, v138\n"
"v_accvgpr_write_b32 a120, v118\n"
"v_accvgpr_write_b32 a121, v119\n"
"v_accvgpr_write_b32 a122, v140\n"
"v_accvgpr_write_b32 a123, v141\n"
"v_exp_f32 v80, v80\n"
"v_exp_f32 v81, v81\n"
"v_exp_f32 v82, v82\n"
"v_exp_f32 v83, v83\n"
"v_exp_f32 v84, v84\n"
"v_exp_f32 v85, v85\n"
"v_exp_f32 v86, v86\n"
"v_exp_f32 v87, v87\n"
"v_add_f32 v235, v80, v81\n"
"v_add_f32 v235, v235, v82\n"
"v_add_f32 v235, v235, v83\n"
"v_add_f32 v235, v235, v84\n"
"v_add_f32 v235, v235, v85\n"
"v_add_f32 v235, v235, v86\n"
"v_add_f32 v235, v235, v87\n"
"v_cvt_pk_bf16_f32 v80, v80, v81\n"
"v_cvt_pk_bf16_f32 v81, v82, v83\n"
"v_cvt_pk_bf16_f32 v82, v84, v85\n"
"v_cvt_pk_bf16_f32 v83, v86, v87\n"
"v_max3_f32 v137, v88, v89, v90\n"
"v_max3_f32 v137, v137, v91, v92\n"
"v_max3_f32 v137, v137, v93, v94\n"
"v_max_f32 v137, v137, v95\n"
"v_mov_b32 v138, v137\n"
"s_nop 1\n"
"v_permlane32_swap_b32 v137, v138\n"
"v_max_f32 v137, v137, v138\n"
"v_mov_b32 v138, v137\n"
"s_nop 1\n"
"v_permlane16_swap_b32 v137, v138\n"
"v_max_f32 v137, v137, v138\n"
"v_max_f32 v137, s96, v137\n"
"v_max_f32 v138, 0, v137\n"
"v_sub_f32 v138, 0, v138\n"
"v_exp_f32 v138, v138\n"
"v_sub_f32 v88, v88, v137\n"
"v_sub_f32 v40, v40, v137\n"
"v_sub_f32 v89, v89, v137\n"
"v_sub_f32 v41, v41, v137\n"
"v_sub_f32 v90, v90, v137\n"
"v_sub_f32 v42, v42, v137\n"
"v_sub_f32 v91, v91, v137\n"
"v_sub_f32 v43, v43, v137\n"
"v_sub_f32 v92, v92, v137\n"
"v_sub_f32 v44, v44, v137\n"
"v_sub_f32 v93, v93, v137\n"
"v_sub_f32 v45, v45, v137\n"
"v_sub_f32 v94, v94, v137\n"
"v_sub_f32 v46, v46, v137\n"
"v_sub_f32 v95, v95, v137\n"
"v_sub_f32 v47, v47, v137\n"
"v_sub_f32 v220, v220, v137\n"
"v_sub_f32 v221, v221, v137\n"
"v_sub_f32 v222, v222, v137\n"
"v_sub_f32 v223, v223, v137\n"
"v_mul_f32 v115, v115, v138\n"
"v_accvgpr_read_b32 v118, a68\n"
"v_accvgpr_read_b32 v119, a69\n"
"v_accvgpr_read_b32 v140, a70\n"
"v_accvgpr_read_b32 v141, a71\n"
"v_mul_f32 v118, v118, v138\n"
"v_mul_f32 v119, v119, v138\n"
"v_mul_f32 v140, v140, v138\n"
"v_mul_f32 v141, v141, v138\n"
"v_accvgpr_write_b32 a68, v118\n"
"v_accvgpr_write_b32 a69, v119\n"
"v_accvgpr_write_b32 a70, v140\n"
"v_accvgpr_write_b32 a71, v141\n"
"v_accvgpr_read_b32 v118, a76\n"
"v_accvgpr_read_b32 v119, a77\n"
"v_accvgpr_read_b32 v140, a78\n"
"v_accvgpr_read_b32 v141, a79\n"
"v_mul_f32 v118, v118, v138\n"
"v_mul_f32 v119, v119, v138\n"
"v_mul_f32 v140, v140, v138\n"
"v_mul_f32 v141, v141, v138\n"
"v_accvgpr_write_b32 a76, v118\n"
"v_accvgpr_write_b32 a77, v119\n"
"v_accvgpr_write_b32 a78, v140\n"
"v_accvgpr_write_b32 a79, v141\n"
"v_accvgpr_read_b32 v118, a84\n"
"v_accvgpr_read_b32 v119, a85\n"
"v_accvgpr_read_b32 v140, a86\n"
"v_accvgpr_read_b32 v141, a87\n"
"v_mul_f32 v118, v118, v138\n"
"v_mul_f32 v119, v119, v138\n"
"v_mul_f32 v140, v140, v138\n"
"v_mul_f32 v141, v141, v138\n"
"v_accvgpr_write_b32 a84, v118\n"
"v_accvgpr_write_b32 a85, v119\n"
"v_accvgpr_write_b32 a86, v140\n"
"v_accvgpr_write_b32 a87, v141\n"
"v_accvgpr_read_b32 v118, a92\n"
"v_accvgpr_read_b32 v119, a93\n"
"v_accvgpr_read_b32 v140, a94\n"
"v_accvgpr_read_b32 v141, a95\n"
"v_mul_f32 v118, v118, v138\n"
"v_mul_f32 v119, v119, v138\n"
"v_mul_f32 v140, v140, v138\n"
"v_mul_f32 v141, v141, v138\n"
"v_accvgpr_write_b32 a92, v118\n"
"v_accvgpr_write_b32 a93, v119\n"
"v_accvgpr_write_b32 a94, v140\n"
"v_accvgpr_write_b32 a95, v141\n"
"v_accvgpr_read_b32 v118, a100\n"
"v_accvgpr_read_b32 v119, a101\n"
"v_accvgpr_read_b32 v140, a102\n"
"v_accvgpr_read_b32 v141, a103\n"
"v_mul_f32 v118, v118, v138\n"
"v_mul_f32 v119, v119, v138\n"
"v_mul_f32 v140, v140, v138\n"
"v_mul_f32 v141, v141, v138\n"
"v_accvgpr_write_b32 a100, v118\n"
"v_accvgpr_write_b32 a101, v119\n"
"v_accvgpr_write_b32 a102, v140\n"
"v_accvgpr_write_b32 a103, v141\n"
"v_accvgpr_read_b32 v118, a108\n"
"v_accvgpr_read_b32 v119, a109\n"
"v_accvgpr_read_b32 v140, a110\n"
"v_accvgpr_read_b32 v141, a111\n"
"v_mul_f32 v118, v118, v138\n"
"v_mul_f32 v119, v119, v138\n"
"v_mul_f32 v140, v140, v138\n"
"v_mul_f32 v141, v141, v138\n"
"v_accvgpr_write_b32 a108, v118\n"
"v_accvgpr_write_b32 a109, v119\n"
"v_accvgpr_write_b32 a110, v140\n"
"v_accvgpr_write_b32 a111, v141\n"
"v_accvgpr_read_b32 v118, a116\n"
"v_accvgpr_read_b32 v119, a117\n"
"v_accvgpr_read_b32 v140, a118\n"
"v_accvgpr_read_b32 v141, a119\n"
"v_mul_f32 v118, v118, v138\n"
"v_mul_f32 v119, v119, v138\n"
"v_mul_f32 v140, v140, v138\n"
"v_mul_f32 v141, v141, v138\n"
"v_accvgpr_write_b32 a116, v118\n"
"v_accvgpr_write_b32 a117, v119\n"
"v_accvgpr_write_b32 a118, v140\n"
"v_accvgpr_write_b32 a119, v141\n"
"v_accvgpr_read_b32 v118, a124\n"
"v_accvgpr_read_b32 v119, a125\n"
"v_accvgpr_read_b32 v140, a126\n"
"v_accvgpr_read_b32 v141, a127\n"
"v_mul_f32 v118, v118, v138\n"
"v_mul_f32 v119, v119, v138\n"
"v_mul_f32 v140, v140, v138\n"
"v_mul_f32 v141, v141, v138\n"
"v_accvgpr_write_b32 a124, v118\n"
"v_accvgpr_write_b32 a125, v119\n"
"v_accvgpr_write_b32 a126, v140\n"
"v_accvgpr_write_b32 a127, v141\n"
"v_exp_f32 v88, v88\n"
"v_exp_f32 v89, v89\n"
"v_exp_f32 v90, v90\n"
"v_exp_f32 v91, v91\n"
"v_exp_f32 v92, v92\n"
"v_exp_f32 v93, v93\n"
"v_exp_f32 v94, v94\n"
"v_exp_f32 v95, v95\n"
"v_add_f32 v236, v88, v89\n"
"v_add_f32 v236, v236, v90\n"
"v_add_f32 v236, v236, v91\n"
"v_add_f32 v236, v236, v92\n"
"v_add_f32 v236, v236, v93\n"
"v_add_f32 v236, v236, v94\n"
"v_add_f32 v236, v236, v95\n"
"v_cvt_pk_bf16_f32 v88, v88, v89\n"
"v_cvt_pk_bf16_f32 v89, v90, v91\n"
"v_cvt_pk_bf16_f32 v90, v92, v93\n"
"v_cvt_pk_bf16_f32 v91, v94, v95\n"
"v_max3_f32 v137, v96, v97, v98\n"
"v_max3_f32 v137, v137, v99, v100\n"
"v_max3_f32 v137, v137, v101, v102\n"
"v_max_f32 v137, v137, v103\n"
"v_mov_b32 v138, v137\n"
"s_nop 1\n"
"v_permlane32_swap_b32 v137, v138\n"
"v_max_f32 v137, v137, v138\n"
"v_mov_b32 v138, v137\n"
"s_nop 1\n"
"v_permlane16_swap_b32 v137, v138\n"
"v_max_f32 v137, v137, v138\n"
"v_max_f32 v137, s96, v137\n"
"v_max_f32 v138, 0, v137\n"
"v_sub_f32 v138, 0, v138\n"
"v_exp_f32 v138, v138\n"
"v_sub_f32 v96, v96, v137\n"
"v_sub_f32 v48, v48, v137\n"
"v_sub_f32 v97, v97, v137\n"
"v_sub_f32 v49, v49, v137\n"
"v_sub_f32 v98, v98, v137\n"
"v_sub_f32 v50, v50, v137\n"
"v_sub_f32 v99, v99, v137\n"
"v_sub_f32 v51, v51, v137\n"
"v_sub_f32 v100, v100, v137\n"
"v_sub_f32 v52, v52, v137\n"
"v_sub_f32 v101, v101, v137\n"
"v_sub_f32 v53, v53, v137\n"
"v_sub_f32 v102, v102, v137\n"
"v_sub_f32 v54, v54, v137\n"
"v_sub_f32 v103, v103, v137\n"
"v_sub_f32 v55, v55, v137\n"
"v_sub_f32 v224, v224, v137\n"
"v_sub_f32 v225, v225, v137\n"
"v_sub_f32 v226, v226, v137\n"
"v_sub_f32 v227, v227, v137\n"
"v_mul_f32 v116, v116, v138\n"
"v_accvgpr_read_b32 v118, a128\n"
"v_accvgpr_read_b32 v119, a129\n"
"v_accvgpr_read_b32 v140, a130\n"
"v_accvgpr_read_b32 v141, a131\n"
"v_mul_f32 v118, v118, v138\n"
"v_mul_f32 v119, v119, v138\n"
"v_mul_f32 v140, v140, v138\n"
"v_mul_f32 v141, v141, v138\n"
"v_accvgpr_write_b32 a128, v118\n"
"v_accvgpr_write_b32 a129, v119\n"
"v_accvgpr_write_b32 a130, v140\n"
"v_accvgpr_write_b32 a131, v141\n"
"v_accvgpr_read_b32 v118, a136\n"
"v_accvgpr_read_b32 v119, a137\n"
"v_accvgpr_read_b32 v140, a138\n"
"v_accvgpr_read_b32 v141, a139\n"
"v_mul_f32 v118, v118, v138\n"
"v_mul_f32 v119, v119, v138\n"
"v_mul_f32 v140, v140, v138\n"
"v_mul_f32 v141, v141, v138\n"
"v_accvgpr_write_b32 a136, v118\n"
"v_accvgpr_write_b32 a137, v119\n"
"v_accvgpr_write_b32 a138, v140\n"
"v_accvgpr_write_b32 a139, v141\n"
"v_accvgpr_read_b32 v118, a144\n"
"v_accvgpr_read_b32 v119, a145\n"
"v_accvgpr_read_b32 v140, a146\n"
"v_accvgpr_read_b32 v141, a147\n"
"v_mul_f32 v118, v118, v138\n"
"v_mul_f32 v119, v119, v138\n"
"v_mul_f32 v140, v140, v138\n"
"v_mul_f32 v141, v141, v138\n"
"v_accvgpr_write_b32 a144, v118\n"
"v_accvgpr_write_b32 a145, v119\n"
"v_accvgpr_write_b32 a146, v140\n"
"v_accvgpr_write_b32 a147, v141\n"
"v_accvgpr_read_b32 v118, a152\n"
"v_accvgpr_read_b32 v119, a153\n"
"v_accvgpr_read_b32 v140, a154\n"
"v_accvgpr_read_b32 v141, a155\n"
"v_mul_f32 v118, v118, v138\n"
"v_mul_f32 v119, v119, v138\n"
"v_mul_f32 v140, v140, v138\n"
"v_mul_f32 v141, v141, v138\n"
"v_accvgpr_write_b32 a152, v118\n"
"v_accvgpr_write_b32 a153, v119\n"
"v_accvgpr_write_b32 a154, v140\n"
"v_accvgpr_write_b32 a155, v141\n"
"v_accvgpr_read_b32 v118, a160\n"
"v_accvgpr_read_b32 v119, a161\n"
"v_accvgpr_read_b32 v140, a162\n"
"v_accvgpr_read_b32 v141, a163\n"
"v_mul_f32 v118, v118, v138\n"
"v_mul_f32 v119, v119, v138\n"
"v_mul_f32 v140, v140, v138\n"
"v_mul_f32 v141, v141, v138\n"
"v_accvgpr_write_b32 a160, v118\n"
"v_accvgpr_write_b32 a161, v119\n"
"v_accvgpr_write_b32 a162, v140\n"
"v_accvgpr_write_b32 a163, v141\n"
"v_accvgpr_read_b32 v118, a168\n"
"v_accvgpr_read_b32 v119, a169\n"
"v_accvgpr_read_b32 v140, a170\n"
"v_accvgpr_read_b32 v141, a171\n"
"v_mul_f32 v118, v118, v138\n"
"v_mul_f32 v119, v119, v138\n"
"v_mul_f32 v140, v140, v138\n"
"v_mul_f32 v141, v141, v138\n"
"v_accvgpr_write_b32 a168, v118\n"
"v_accvgpr_write_b32 a169, v119\n"
"v_accvgpr_write_b32 a170, v140\n"
"v_accvgpr_write_b32 a171, v141\n"
"v_accvgpr_read_b32 v118, a176\n"
"v_accvgpr_read_b32 v119, a177\n"
"v_accvgpr_read_b32 v140, a178\n"
"v_accvgpr_read_b32 v141, a179\n"
"v_mul_f32 v118, v118, v138\n"
"v_mul_f32 v119, v119, v138\n"
"v_mul_f32 v140, v140, v138\n"
"v_mul_f32 v141, v141, v138\n"
"v_accvgpr_write_b32 a176, v118\n"
"v_accvgpr_write_b32 a177, v119\n"
"v_accvgpr_write_b32 a178, v140\n"
"v_accvgpr_write_b32 a179, v141\n"
"v_accvgpr_read_b32 v118, a184\n"
"v_accvgpr_read_b32 v119, a185\n"
"v_accvgpr_read_b32 v140, a186\n"
"v_accvgpr_read_b32 v141, a187\n"
"v_mul_f32 v118, v118, v138\n"
"v_mul_f32 v119, v119, v138\n"
"v_mul_f32 v140, v140, v138\n"
"v_mul_f32 v141, v141, v138\n"
"v_accvgpr_write_b32 a184, v118\n"
"v_accvgpr_write_b32 a185, v119\n"
"v_accvgpr_write_b32 a186, v140\n"
"v_accvgpr_write_b32 a187, v141\n"
"v_exp_f32 v96, v96\n"
"v_exp_f32 v97, v97\n"
"v_exp_f32 v98, v98\n"
"v_exp_f32 v99, v99\n"
"v_exp_f32 v100, v100\n"
"v_exp_f32 v101, v101\n"
"v_exp_f32 v102, v102\n"
"v_exp_f32 v103, v103\n"
"v_add_f32 v237, v96, v97\n"
"v_add_f32 v237, v237, v98\n"
"v_add_f32 v237, v237, v99\n"
"v_add_f32 v237, v237, v100\n"
"v_add_f32 v237, v237, v101\n"
"v_add_f32 v237, v237, v102\n"
"v_add_f32 v237, v237, v103\n"
"v_cvt_pk_bf16_f32 v96, v96, v97\n"
"v_cvt_pk_bf16_f32 v97, v98, v99\n"
"v_cvt_pk_bf16_f32 v98, v100, v101\n"
"v_cvt_pk_bf16_f32 v99, v102, v103\n"
"v_max3_f32 v137, v104, v105, v106\n"
"v_max3_f32 v137, v137, v107, v108\n"
"v_max3_f32 v137, v137, v109, v110\n"
"v_max_f32 v137, v137, v111\n"
"v_mov_b32 v138, v137\n"
"s_nop 1\n"
"v_permlane32_swap_b32 v137, v138\n"
"v_max_f32 v137, v137, v138\n"
"v_mov_b32 v138, v137\n"
"s_nop 1\n"
"v_permlane16_swap_b32 v137, v138\n"
"v_max_f32 v137, v137, v138\n"
"v_max_f32 v137, s96, v137\n"
"v_max_f32 v138, 0, v137\n"
"v_sub_f32 v138, 0, v138\n"
"v_exp_f32 v138, v138\n"
"v_sub_f32 v104, v104, v137\n"
"v_sub_f32 v56, v56, v137\n"
"v_sub_f32 v105, v105, v137\n"
"v_sub_f32 v57, v57, v137\n"
"v_sub_f32 v106, v106, v137\n"
"v_sub_f32 v58, v58, v137\n"
"v_sub_f32 v107, v107, v137\n"
"v_sub_f32 v59, v59, v137\n"
"v_sub_f32 v108, v108, v137\n"
"v_sub_f32 v60, v60, v137\n"
"v_sub_f32 v109, v109, v137\n"
"v_sub_f32 v61, v61, v137\n"
"v_sub_f32 v110, v110, v137\n"
"v_sub_f32 v62, v62, v137\n"
"v_sub_f32 v111, v111, v137\n"
"v_sub_f32 v63, v63, v137\n"
"v_sub_f32 v228, v228, v137\n"
"v_sub_f32 v229, v229, v137\n"
"v_sub_f32 v230, v230, v137\n"
"v_sub_f32 v231, v231, v137\n"
"v_mul_f32 v117, v117, v138\n"
"v_accvgpr_read_b32 v118, a132\n"
"v_accvgpr_read_b32 v119, a133\n"
"v_accvgpr_read_b32 v140, a134\n"
"v_accvgpr_read_b32 v141, a135\n"
"v_mul_f32 v118, v118, v138\n"
"v_mul_f32 v119, v119, v138\n"
"v_mul_f32 v140, v140, v138\n"
"v_mul_f32 v141, v141, v138\n"
"v_accvgpr_write_b32 a132, v118\n"
"v_accvgpr_write_b32 a133, v119\n"
"v_accvgpr_write_b32 a134, v140\n"
"v_accvgpr_write_b32 a135, v141\n"
"v_accvgpr_read_b32 v118, a140\n"
"v_accvgpr_read_b32 v119, a141\n"
"v_accvgpr_read_b32 v140, a142\n"
"v_accvgpr_read_b32 v141, a143\n"
"v_mul_f32 v118, v118, v138\n"
"v_mul_f32 v119, v119, v138\n"
"v_mul_f32 v140, v140, v138\n"
"v_mul_f32 v141, v141, v138\n"
"v_accvgpr_write_b32 a140, v118\n"
"v_accvgpr_write_b32 a141, v119\n"
"v_accvgpr_write_b32 a142, v140\n"
"v_accvgpr_write_b32 a143, v141\n"
"v_accvgpr_read_b32 v118, a148\n"
"v_accvgpr_read_b32 v119, a149\n"
"v_accvgpr_read_b32 v140, a150\n"
"v_accvgpr_read_b32 v141, a151\n"
"v_mul_f32 v118, v118, v138\n"
"v_mul_f32 v119, v119, v138\n"
"v_mul_f32 v140, v140, v138\n"
"v_mul_f32 v141, v141, v138\n"
"v_accvgpr_write_b32 a148, v118\n"
"v_accvgpr_write_b32 a149, v119\n"
"v_accvgpr_write_b32 a150, v140\n"
"v_accvgpr_write_b32 a151, v141\n"
"v_accvgpr_read_b32 v118, a156\n"
"v_accvgpr_read_b32 v119, a157\n"
"v_accvgpr_read_b32 v140, a158\n"
"v_accvgpr_read_b32 v141, a159\n"
"v_mul_f32 v118, v118, v138\n"
"v_mul_f32 v119, v119, v138\n"
"v_mul_f32 v140, v140, v138\n"
"v_mul_f32 v141, v141, v138\n"
"v_accvgpr_write_b32 a156, v118\n"
"v_accvgpr_write_b32 a157, v119\n"
"v_accvgpr_write_b32 a158, v140\n"
"v_accvgpr_write_b32 a159, v141\n"
"v_accvgpr_read_b32 v118, a164\n"
"v_accvgpr_read_b32 v119, a165\n"
"v_accvgpr_read_b32 v140, a166\n"
"v_accvgpr_read_b32 v141, a167\n"
"v_mul_f32 v118, v118, v138\n"
"v_mul_f32 v119, v119, v138\n"
"v_mul_f32 v140, v140, v138\n"
"v_mul_f32 v141, v141, v138\n"
"v_accvgpr_write_b32 a164, v118\n"
"v_accvgpr_write_b32 a165, v119\n"
"v_accvgpr_write_b32 a166, v140\n"
"v_accvgpr_write_b32 a167, v141\n"
"v_accvgpr_read_b32 v118, a172\n"
"v_accvgpr_read_b32 v119, a173\n"
"v_accvgpr_read_b32 v140, a174\n"
"v_accvgpr_read_b32 v141, a175\n"
"v_mul_f32 v118, v118, v138\n"
"v_mul_f32 v119, v119, v138\n"
"v_mul_f32 v140, v140, v138\n"
"v_mul_f32 v141, v141, v138\n"
"v_accvgpr_write_b32 a172, v118\n"
"v_accvgpr_write_b32 a173, v119\n"
"v_accvgpr_write_b32 a174, v140\n"
"v_accvgpr_write_b32 a175, v141\n"
"v_accvgpr_read_b32 v118, a180\n"
"v_accvgpr_read_b32 v119, a181\n"
"v_accvgpr_read_b32 v140, a182\n"
"v_accvgpr_read_b32 v141, a183\n"
"v_mul_f32 v118, v118, v138\n"
"v_mul_f32 v119, v119, v138\n"
"v_mul_f32 v140, v140, v138\n"
"v_mul_f32 v141, v141, v138\n"
"v_accvgpr_write_b32 a180, v118\n"
"v_accvgpr_write_b32 a181, v119\n"
"v_accvgpr_write_b32 a182, v140\n"
"v_accvgpr_write_b32 a183, v141\n"
"v_accvgpr_read_b32 v118, a188\n"
"v_accvgpr_read_b32 v119, a189\n"
"v_accvgpr_read_b32 v140, a190\n"
"v_accvgpr_read_b32 v141, a191\n"
"v_mul_f32 v118, v118, v138\n"
"v_mul_f32 v119, v119, v138\n"
"v_mul_f32 v140, v140, v138\n"
"v_mul_f32 v141, v141, v138\n"
"v_accvgpr_write_b32 a188, v118\n"
"v_accvgpr_write_b32 a189, v119\n"
"v_accvgpr_write_b32 a190, v140\n"
"v_accvgpr_write_b32 a191, v141\n"
"v_exp_f32 v104, v104\n"
"v_exp_f32 v105, v105\n"
"v_exp_f32 v106, v106\n"
"v_exp_f32 v107, v107\n"
"v_exp_f32 v108, v108\n"
"v_exp_f32 v109, v109\n"
"v_exp_f32 v110, v110\n"
"v_exp_f32 v111, v111\n"
"v_add_f32 v238, v104, v105\n"
"v_add_f32 v238, v238, v106\n"
"v_add_f32 v238, v238, v107\n"
"v_add_f32 v238, v238, v108\n"
"v_add_f32 v238, v238, v109\n"
"v_add_f32 v238, v238, v110\n"
"v_add_f32 v238, v238, v111\n"
"v_cvt_pk_bf16_f32 v104, v104, v105\n"
"v_cvt_pk_bf16_f32 v105, v106, v107\n"
"v_cvt_pk_bf16_f32 v106, v108, v109\n"
"v_cvt_pk_bf16_f32 v107, v110, v111\n"
"s_add_u32 s98, s98, 4\n"
"s_addc_u32 s99, s99, 0\n"
"s_nop 3\n"
"s_setpc_b64 s[98:99]\n"
"Lend_%=:\n"

    : [tid] "+v"(tid)
    : [bid] "s"(blockIdx.x), [lds] "s"((unsigned)(size_t)lds),
      [ka] "s"((unsigned long long)__builtin_amdgcn_kernarg_segment_ptr())
    : "memory", "vcc", "scc", "s4", "s5", "s6", "s7", "s8", "s9", "s10", "s11", "s12", "s13", "s14", "s15", "s16", "s17", "s18", "s19", "s20", "s21", "s22", "s23", "s24", "s25", "s26", "s27", "s28", "s29", "s30", "s31", "s34", "s35", "s36", "s37", "s38", "s39", "s40", "s41", "s42", "s43", "s44", "s45", "s46", "s47", "s48", "s49", "s50", "s51", "s52", "s53", "s54", "s55", "s56", "s57", "s58", "s59", "s60", "s61", "s62", "s63", "s64", "s65", "s66", "s67", "s68", "s69", "s70", "s71", "s72", "s73", "s74", "s75", "s76", "s77", "s78", "s79", "s80", "s81", "s82", "s83", "s84", "s85", "s86", "s87", "s88", "s89", "s90", "s91", "s92", "s93", "s94", "s95", "s96", "s97", "s98", "s99", "v1", "v2", "v3", "v4", "v5", "v6", "v7", "v8", "v9", "v10", "v11", "v12", "v13", "v14", "v15", "v16", "v17", "v18", "v19", "v20", "v21", "v22", "v23", "v24", "v25", "v26", "v27", "v28", "v29", "v30", "v31", "v32", "v33", "v34", "v35", "v36", "v37", "v38", "v39", "v40", "v41", "v42", "v43", "v44", "v45", "v46", "v47", "v48", "v49", "v50", "v51", "v52", "v53", "v54", "v55", "v56", "v57", "v58", "v59", "v60", "v61", "v62", "v63", "v64", "v65", "v66", "v67", "v68", "v69", "v70", "v71", "v72", "v73", "v74", "v75", "v76", "v77", "v78", "v79", "v80", "v81", "v82", "v83", "v84", "v85", "v86", "v87", "v88", "v89", "v90", "v91", "v92", "v93", "v94", "v95", "v96", "v97", "v98", "v99", "v100", "v101", "v102", "v103", "v104", "v105", "v106", "v107", "v108", "v109", "v110", "v111", "v112", "v113", "v114", "v115", "v116", "v117", "v118", "v119", "v120", "v121", "v122", "v123", "v124", "v125", "v126", "v127", "v128", "v129", "v130", "v131", "v132", "v133", "v134", "v135", "v136", "v137", "v138", "v139", "v140", "v141", "v142", "v143", "v144", "v145", "v146", "v147", "v148", "v149", "v150", "v151", "v152", "v153", "v154", "v155", "v156", "v157", "v158", "v159", "v160", "v161", "v162", "v163", "v164", "v165", "v166", "v167", "v168", "v169", "v170", "v171", "v172", "v173", "v174", "v175", "v176", "v177", "v178", "v179", "v180", "v181", "v182", "v183", "v184", "v185", "v186", "v187", "v188", "v189", "v190", "v191", "v192", "v193", "v194", "v195", "v196", "v197", "v198", "v199", "v200", "v201", "v202", "v203", "v204", "v205", "v206", "v207", "v208", "v209", "v210", "v211", "v212", "v213", "v214", "v215", "v216", "v217", "v218", "v219", "v220", "v221", "v222", "v223", "v224", "v225", "v226", "v227", "v228", "v229", "v230", "v231", "v232", "v233", "v234", "v235", "v236", "v237", "v238", "v239", "v240", "v241", "v242", "v243", "v244", "v245", "v246", "v247", "v248", "v249", "v250", "v251", "v252", "v253", "v254", "v255", "a0", "a1", "a2", "a3", "a4", "a5", "a6", "a7", "a8", "a9", "a10", "a11", "a12", "a13", "a14", "a15", "a16", "a17", "a18", "a19", "a20", "a21", "a22", "a23", "a24", "a25", "a26", "a27", "a28", "a29", "a30", "a31", "a32", "a33", "a34", "a35", "a36", "a37", "a38", "a39", "a40", "a41", "a42", "a43", "a44", "a45", "a46", "a47", "a48", "a49", "a50", "a51", "a52", "a53", "a54", "a55", "a56", "a57", "a58", "a59", "a60", "a61", "a62", "a63", "a64", "a65", "a66", "a67", "a68", "a69", "a70", "a71", "a72", "a73", "a74", "a75", "a76", "a77", "a78", "a79", "a80", "a81", "a82", "a83", "a84", "a85", "a86", "a87", "a88", "a89", "a90", "a91", "a92", "a93", "a94", "a95", "a96", "a97", "a98", "a99", "a100", "a101", "a102", "a103", "a104", "a105", "a106", "a107", "a108", "a109", "a110", "a111", "a112", "a113", "a114", "a115", "a116", "a117", "a118", "a119", "a120", "a121", "a122", "a123", "a124", "a125", "a126", "a127", "a128", "a129", "a130", "a131", "a132", "a133", "a134", "a135", "a136", "a137", "a138", "a139", "a140", "a141", "a142", "a143", "a144", "a145", "a146", "a147", "a148", "a149", "a150", "a151", "a152", "a153", "a154", "a155", "a156", "a157", "a158", "a159", "a160", "a161", "a162", "a163", "a164", "a165", "a166", "a167", "a168", "a169", "a170", "a171", "a172", "a173", "a174", "a175", "a176", "a177", "a178", "a179", "a180", "a181", "a182", "a183", "a184", "a185", "a186", "a187", "a188", "a189", "a190", "a191", "a192", "a193", "a194", "a195", "a196", "a197", "a198", "a199", "a200", "a201", "a202", "a203", "a204", "a205", "a206", "a207", "a208", "a209", "a210", "a211", "a212", "a213", "a214", "a215", "a216", "a217", "a218", "a219", "a220", "a221", "a222", "a223", "a224", "a225", "a226", "a227", "a228", "a229", "a230", "a231", "a232", "a233", "a234", "a235", "a236", "a237", "a238", "a239", "a240", "a241", "a242", "a243", "a244", "a245", "a246", "a247", "a248", "a249", "a250", "a251", "a252", "a253", "a254", "a255");
}
extern "C" void kernel_launch(void* const* d_in, const int* in_sizes, int n_in, void* d_out, int out_size, void* d_ws, size_t ws_size, hipStream_t stream) {
  Args a;
  for (int i = 0; i < 10; ++i) a.in[i] = d_in[i];
  a.out = d_out; a.ws = d_ws;
  fused_attn_kernel<<<256, 256, 0, stream>>>(a);
}
```
